```python
import torch

def _softmax(x, axis=-1):
    return torch.softmax(x, dim=axis)
def _gelu(x):
    return torch.nn.functional.gelu(x, approximate="tanh")
def _silu(x):
    return torch.nn.functional.silu(x)
def _sigmoid(x):
    return torch.sigmoid(x)
def _layernorm(x, axis=-1, eps=1e-5):
    m = x.mean(dim=axis, keepdim=True)
    v = x.var(dim=axis, keepdim=True, unbiased=False)
    return (x - m) / torch.sqrt(v + eps)
def _scatter_add(tgt, idx, src, axis=0):
    return tgt.clone().index_add_(axis, idx, src)
def _segment_sum(src, seg, n):
    out = torch.zeros((n,) + src.shape[1:], dtype=src.dtype)
    return out.index_add_(0, seg, src)
def _pad(x, pw):
    flat = tuple(v for pair in reversed(pw) for v in pair)
    return torch.nn.functional.pad(x, flat)

kernel_name = 'interleave_layers__ssm_conv_s5__ssm_conv_h3__prompt'

def setup_inputs():

    _w = (lambda a: (lambda t: t.to(torch.bfloat16) if t.is_floating_point() else t)(torch.as_tensor(a)))
    def _A_setup():
        B, S, D = 1, 2048, 640
        P = D
        Lambda_re = -0.5 * (lambda _x: torch.exp(torch.as_tensor(_x)))((lambda *s: torch.randn(*s, dtype=torch.bfloat16) * 0.2)(P))
        Lambda_im = (lambda *s: torch.randn(*s, dtype=torch.bfloat16) * 0.8)(P) * 1.0
        B_re = (lambda *s: torch.randn(*s, dtype=torch.bfloat16) * 0.2)(P, D) * (D ** -0.5)
        B_im = (lambda *s: torch.randn(*s, dtype=torch.bfloat16) * 0.2)(P, D) * (D ** -0.5)
        C_re = (lambda *s: torch.randn(*s, dtype=torch.bfloat16) * 0.2)(D, P) * (P ** -0.5)
        C_im = (lambda *s: torch.randn(*s, dtype=torch.bfloat16) * 0.2)(D, P) * (P ** -0.5)
        log_dt = (lambda *s: torch.randn(*s, dtype=torch.bfloat16) * 0.2)(P) * 0.3 - 2.0
        Dskip = (lambda *s: torch.randn(*s, dtype=torch.bfloat16) * 0.05)(D)
        return {
            "x": _w((lambda *s: torch.randn(*s, dtype=torch.bfloat16) * 0.05)(B, S, D)),
            "Lambda_re": _w(Lambda_re),
            "Lambda_im": _w(Lambda_im),
            "B_re": _w(B_re),
            "B_im": _w(B_im),
            "C_re": _w(C_re),
            "C_im": _w(C_im),
            "log_dt": _w(log_dt),
            "Dskip": _w(Dskip),
        }
    _A_in = _A_setup()

    def _B_setup():
        B, S, D = 1, 2048, 640
        ks = 4
        return {
            "q": _w((lambda *s: torch.randn(*s, dtype=torch.bfloat16) * 0.05)(B, S, D)),
            "k": _w((lambda *s: torch.randn(*s, dtype=torch.bfloat16) * 0.4)(B, S, D)),
            "v": _w((lambda *s: torch.randn(*s, dtype=torch.bfloat16) * 0.4)(B, S, D)),
            "shift_k": _w((lambda *s: torch.randn(*s, dtype=torch.bfloat16) * 0.4)(D, ks) * 0.5),
            "ssm_k": _w((lambda *s: torch.randn(*s, dtype=torch.bfloat16) * 0.4)(D, S) * (1.0 / S ** 0.5)),
            "ssm_D": _w((lambda *s: torch.randn(*s, dtype=torch.bfloat16) * 0.4)(D)),
        }
    _B_in = _B_setup()

    return {
        "A_x": _A_in["x"],
        "A_Lambda_re": _A_in["Lambda_re"],
        "A_Lambda_im": _A_in["Lambda_im"],
        "A_B_re": _A_in["B_re"],
        "A_B_im": _A_in["B_im"],
        "A_C_re": _A_in["C_re"],
        "A_C_im": _A_in["C_im"],
        "A_log_dt": _A_in["log_dt"],
        "A_Dskip": _A_in["Dskip"],
        "B_q": _B_in["q"],
        "B_k": _B_in["k"],
        "B_v": _B_in["v"],
        "B_shift_k": _B_in["shift_k"],
        "B_ssm_k": _B_in["ssm_k"],
        "B_ssm_D": _B_in["ssm_D"],
    }

def reference(A_x, A_Lambda_re, A_Lambda_im, A_B_re, A_B_im, A_C_re, A_C_im, A_log_dt, A_Dskip, B_q, B_k, B_v, B_shift_k, B_ssm_k, B_ssm_D):
    def _A_ref(x, Lambda_re, Lambda_im, B_re, B_im, C_re, C_im, log_dt, Dskip):
        Bsz, L, H = x.shape
        Lam = Lambda_re + 1j * Lambda_im
        Bmat = B_re + 1j * B_im
        Cmat = C_re + 1j * C_im
        dt = torch.exp(log_dt)
        Lam_bar = torch.exp(dt * Lam)
        B_bar = ((Lam_bar - 1.0) / Lam)[:, None] * Bmat
        Bu = torch.einsum("bld,pd->blp", x + 0j, B_bar)
        h = torch.zeros_like(Bu[:, 0, :])
        hs = []
        for t in range(L):
            h = Lam_bar[None, :] * h + Bu[:, t, :]
            hs.append(h)
        hseq = torch.stack(hs, 1)
        y = 2.0 * torch.einsum("blp,dp->bld", hseq, Cmat).real + x * Dskip[None, None, :]
        return y

    def _B_ref(q, k, v, shift_k, ssm_k, ssm_D):
        B, S, D = q.shape
        ks = shift_k.shape[-1]
        k_pad = _pad(k, ((0, 0), (ks - 1, 0), (0, 0)))
        k_shift = torch.zeros_like(k)
        for t in range(ks):
            k_shift = k_shift + k_pad[:, t:t + S, :] * shift_k[:, ks - 1 - t][None, None, :]
        kv = k_shift * v
        L = 2 * S
        kv_pad = _pad(kv, ((0, 0), (0, S), (0, 0)))
        kf_pad = _pad(ssm_k, ((0, 0), (0, S)))
        kv_f = (lambda x, axes=(-2,-1): torch.fft.rfft2(x.float(), dim=axes))(kv_pad[:, None, :, :], axes=(1, 2))
        kf_f = (lambda x, axes=(-2,-1): torch.fft.rfft2(x.float(), dim=axes))(kf_pad[:, None, :], axes=(1, 2))
        kf_f = (lambda x, *d: x.permute(*d))(kf_f, 1, 2, 0)
        y_f = kv_f * kf_f[None, :, :, :]
        y = (lambda x, s, axes=(-2,-1): torch.fft.irfft2(x, s=s, dim=axes).to(torch.bfloat16))(y_f, (1, L), axes=(1, 2))[:, 0, :S, :]
        y = y + kv * ssm_D[None, None, :]
        return q * y

    x = A_x
    x = x + _A_ref(_layernorm(x, axis=-1), A_Lambda_re, A_Lambda_im, A_B_re, A_B_im, A_C_re, A_C_im, A_log_dt, A_Dskip)
    x = x + _B_ref(_layernorm(x, axis=-1), B_k, B_v, B_shift_k, B_ssm_k, B_ssm_D)
    x = x + _A_ref(_layernorm(x, axis=-1), A_Lambda_re, A_Lambda_im, A_B_re, A_B_im, A_C_re, A_C_im, A_log_dt, A_Dskip)
    x = x + _B_ref(_layernorm(x, axis=-1), B_k, B_v, B_shift_k, B_ssm_k, B_ssm_D)
    return x
```

```cpp
#define PADS 9000,9001,2054,2,9002,4070,9,9003,6,9004,9005,6107,9006,18,19,9007,9008
#include <hip/hip_runtime.h>
#include <stdint.h>
#include <utility>

#define STAMPS 0

typedef unsigned short u16;
typedef unsigned long long u64;
typedef unsigned v4u __attribute__((ext_vector_type(4)));
typedef unsigned v2u __attribute__((ext_vector_type(2)));
typedef float f32x4 __attribute__((ext_vector_type(4)));
typedef float f32x2 __attribute__((ext_vector_type(2)));
typedef short s16x4 __attribute__((ext_vector_type(4)));
typedef __bf16 bf16x2v __attribute__((ext_vector_type(2)));
#define LAS __attribute__((address_space(3)))
#define DI __device__ __forceinline__

constexpr int S_ = 2048, D_ = 640;
#define KR1 5
#define KR2 12
constexpr int RING1 = KR1;
#define KCR1 4
constexpr int kCR1 = KCR1;
#define KXA 7
#define KN3E 17
#define KN3 12
constexpr int kXA = KXA;
constexpr int kN3E = KN3E, kN3 = KN3;
#define KFHK 9
#define KFHP 7
constexpr int kFHK = KFHK, kFHP = KFHP;
#define KGPI 29
#define KGCI 35
#define PLM 1
#define CPF 0
#define HOIST 0
#define LBE 0
#define GIN 2
#define RESG 1
#define KR20 12
constexpr int kGPI = KGPI, kGCI = KGCI;

constexpr unsigned OFF_NONCE = 0;
constexpr unsigned OFF_ST = 4096;
constexpr unsigned OFF_FW = OFF_ST + 65536;
constexpr unsigned OFF_FK = OFF_FW + 131072;
constexpr unsigned OFF_FH = OFF_FK + 81920;
constexpr unsigned OFF_FX = OFF_FH + 262144;
constexpr unsigned OFF_FG = OFF_FX + 131072;
constexpr unsigned OFF_EG = OFF_FG + 98304;
constexpr unsigned OFF_SG = OFF_EG + 327680;
constexpr unsigned OFF_PS = OFF_SG + 524288;
constexpr unsigned OFF_KVT = OFF_PS + 131072;
constexpr unsigned OFF_GT = OFF_KVT + 2621440;
constexpr unsigned OFF_W2F = OFF_GT + 2621440;
constexpr unsigned OFF_X2F = OFF_W2F + 1638400;
constexpr unsigned OFF_DUM = OFF_X2F + 2621440;
constexpr unsigned OFF_HF = OFF_DUM + 163840;
constexpr unsigned WS_BYTES = OFF_HF + 2 * 5242880;
constexpr unsigned kNone = 0x40000000u;
constexpr unsigned OFF_DBG = 0x1800000;
#define I32M 1
#define KVS 1
#define KVU 0
#define TILES() const int xq = b & 7, j = b >> 3, mt1 = 2 * xq + (j >> 4), nt1 = j & 15, mt2 = 4 * xq + (j >> 3), nt2 = j & 7, i32 = I32M == 0 ? mt2 : I32M == 1 ? 16 * (mt2 & 1) + mt1 : I32M == 2 ? 31 - mt2 : 16 * ((mt2 ^ (j >> (I32M < 3 ? 0 : I32M - 3))) & 1) + mt1;

constexpr int SC = 102400;
constexpr int REGSZ = 13056;
constexpr int L_XR = SC;
constexpr int L_CO = SC + 12288;
constexpr int L_KB = SC + 51712;
constexpr int L_TAB = SC + 55808;
constexpr int L_CW = SC + 39168;
constexpr int INBOX = 15360;
constexpr int l_rs(int l, int w) { return l ? SC + w * INBOX : L_KB + w * 256; }
constexpr int l_sg(int l, int w) { return l ? SC + w * INBOX + 256 : SC + (w == 0 ? 43264 : w == 1 ? 46080 : w == 2 ? 52736 : 56320); }
constexpr int l_tb(int l, int w) { return l ? SC + w * INBOX + 2048 : SC + 40960 + w * 5120; }
constexpr int LDS_BYTES = 163840;
constexpr int CV_E = 4608;
constexpr int CV_O = CV_E + (1056 + 15) * 4;

struct Params {
    const u16 *x0, *Lre, *Lim, *Bre, *Bim, *Cre, *Cim, *logdt, *Dskip, *k, *v, *shk, *ssmk, *ssmD;
    char* ws;
    float* out;
};

#define MF "v_mfma_f32_16x16x32_bf16 "
DI void mf(f32x4& c, v4u a, v4u b) { asm(MF "%0, %1, %2, %0" : "+v"(c) : "v"(a), "v"(b)); }
DI void mf0(f32x4& c, v4u a, v4u b) { asm(MF "%0, %1, %2, 0" : "=&v"(c) : "v"(a), "v"(b)); }
constexpr int W2R = 56;
#define WA(n) "a[%c" #n ":%c" #n "+3]"
template <int R> DI void mfa(f32x4& c, v4u a) { asm(MF "%0, " WA(2) ", %1, %0" : "+v"(c) : "v"(a), "i"(W2R + 4 * R)); }
template <int R> DI void mfa0(f32x4& c, v4u a) { asm(MF "%0, " WA(2) ", %1, 0" : "=&v"(c) : "v"(a), "i"(W2R + 4 * R)); }
#define ACC_FENCE10(c, d) asm volatile("" : "+v"(c[0]), "+v"(c[1]), "+v"(c[2]), "+v"(c[3]), "+v"(c[4]), "+v"(d[0]), "+v"(d[1]), "+v"(d[2]), "+v"(d[3]), "+v"(d[4]))
#define ACC_TIE5(c) asm volatile("" : "+v"(c[0]), "+v"(c[1]), "+v"(c[2]), "+v"(c[3]), "+v"(c[4]))
#define QP " quad_perm:[1,0,3,2] row_mask:0xf bank_mask:0xf"
struct St { f32x2 a, b; float c; };
struct Lm { f32x2 a, b; float r, s; };
#define PK1 " op_sel_hi:[0,1,1]\n\t"
#define PK2 " op_sel:[1,1,0] op_sel_hi:[1,0,1] neg_lo:[1,0,0]"
#define ST_(t0, t1, t2, h0, h1, h2, b0, b1, b2, l0, l1, l2, l3)                                                                                        \
    "v_pk_fma_f32 %" #t0 ", %" #l0 ", %" #h0 ", %" #b0 PK1 "v_pk_fma_f32 %" #t1 ", %" #l1 ", %" #h1 ", %" #b1 PK1 "v_fma_f32 %" #t2 ", %" #l2 ", %" #h2 ", %" #b2 "\n\t" \
    "v_pk_fma_f32 %" #t0 ", %" #l0 ", %" #h0 ", %" #t0 PK2 "\n\tv_pk_fma_f32 %" #t1 ", %" #l1 ", %" #h1 ", %" #t1 PK2 "\n\tv_fmac_f32_dpp %" #t2 ", %" #h2 ", %" #l3 QP "\n\t"
#define SO(t) "=&v"(t.a), "=&v"(t.b), "=&v"(t.c)
#define SI(t) "v"(t.a), "v"(t.b), "v"(t.c)
#define LI(l) "v"(l.a), "v"(l.b), "v"(l.r), "v"(l.s)
DI void run3(St& t, const St& h, const St* b, const Lm& l) {
    St u;
    asm(ST_(0, 1, 2, 6, 7, 8, 9, 10, 11, 18, 19, 20, 21) ST_(3, 4, 5, 0, 1, 2, 12, 13, 14, 18, 19, 20, 21) ST_(0, 1, 2, 3, 4, 5, 15, 16, 17, 18, 19, 20, 21)
        : SO(t), SO(u) : SI(h), SI(b[0]), SI(b[1]), SI(b[2]), LI(l));
}
DI void run4(St& t, const St& h, const St* b, const Lm& l) {
    St u;
    asm(ST_(3, 4, 5, 6, 7, 8, 9, 10, 11, 21, 22, 23, 24) ST_(0, 1, 2, 3, 4, 5, 12, 13, 14, 21, 22, 23, 24) ST_(3, 4, 5, 0, 1, 2, 15, 16, 17, 21, 22, 23, 24) ST_(0, 1, 2, 3, 4, 5, 18, 19, 20, 21, 22, 23, 24)
        : SO(t), SO(u) : SI(h), SI(b[0]), SI(b[1]), SI(b[2]), SI(b[3]), LI(l));
}
#define AC_(t0, t2, h0, h2, b0, b2, l0, l2, l3)                                                                                   \
    "v_pk_fma_f32 %" #t0 ", %" #l0 ", %" #h0 ", %" #b0 PK1 "v_fma_f32 %" #t2 ", %" #l2 ", %" #h2 ", %" #b2 "\n\t"               \
    "v_pk_fma_f32 %" #t0 ", %" #l0 ", %" #h0 ", %" #t0 PK2 "\n\tv_fmac_f32_dpp %" #t2 ", %" #h2 ", %" #l3 QP "\n\t"
#define AO(t) "=&v"(t.a), "=&v"(t.c)
#define AI(t) "v"(t.a), "v"(t.c)
DI void keep14(St* t, const St& h, const St& x, const St* b, const Lm& lq, const Lm& l) {
    St u;
    asm(AC_(8, 9, 10, 11, 12, 13, 22, 23, 24) AC_(0, 1, 8, 9, 14, 15, 25, 26, 27) AC_(2, 3, 0, 1, 16, 17, 25, 26, 27) AC_(4, 5, 2, 3, 18, 19, 25, 26, 27) AC_(6, 7, 4, 5, 20, 21, 25, 26, 27)
        : AO(t[0]), AO(t[1]), AO(t[2]), AO(t[3]), AO(u) : AI(h), AI(x), AI(b[0]), AI(b[1]), AI(b[2]), AI(b[3]), "v"(lq.a), "v"(lq.r), "v"(lq.s), "v"(l.a), "v"(l.r), "v"(l.s));
}
DI void keep4(St* t, const St& h, const St* b, const Lm& l) {
    asm(AC_(0, 1, 8, 9, 10, 11, 18, 19, 20) AC_(2, 3, 0, 1, 12, 13, 18, 19, 20) AC_(4, 5, 2, 3, 14, 15, 18, 19, 20) AC_(6, 7, 4, 5, 16, 17, 18, 19, 20)
        : AO(t[0]), AO(t[1]), AO(t[2]), AO(t[3]) : AI(h), AI(b[0]), AI(b[1]), AI(b[2]), AI(b[3]), "v"(l.a), "v"(l.r), "v"(l.s));
}
DI void b3cv(f32x2* t, unsigned* o, f32x2 h, f32x2 b0, f32x2 b1, f32x2 b2, f32x2 l0, f32x2 l, const float* e) {
    asm("v_pk_fma_f32 %0, %11, %7, %8" PK1 "v_cvt_pk_bf16_f32 %3, %13, %14\n\tv_pk_fma_f32 %0, %11, %7, %0" PK2 "\n\tv_cvt_pk_bf16_f32 %4, %15, %16\n\t"
        "v_pk_fma_f32 %1, %12, %0, %9" PK1 "v_cvt_pk_bf16_f32 %5, %17, %18\n\tv_pk_fma_f32 %1, %12, %0, %1" PK2 "\n\tv_cvt_pk_bf16_f32 %6, %19, %20\n\t"
        "v_pk_fma_f32 %2, %12, %1, %10" PK1
        : "=&v"(t[0]), "=&v"(t[1]), "=&v"(t[2]), "=&v"(o[0]), "=&v"(o[1]), "=&v"(o[2]), "=&v"(o[3])
        : "v"(h), "v"(b0), "v"(b1), "v"(b2), "v"(l0), "v"(l), "v"(e[0]), "v"(e[1]), "v"(e[2]), "v"(e[3]), "v"(e[4]), "v"(e[5]), "v"(e[6]), "v"(e[7]));
}
DI void bfin(f32x2& t, f32x2 h, f32x2 l) { asm("v_pk_fma_f32 %0, %2, %1, %0" PK2 : "+v"(t) : "v"(h), "v"(l)); }
#define UNDEF8(c) asm volatile("" : "=v"(c[0]), "=v"(c[1]), "=v"(c[2]), "=v"(c[3]), "=v"(c[4]), "=v"(c[5]), "=v"(c[6]), "=v"(c[7]))
#define PIN5(t) asm volatile("s_nop 1" : "+v"(t[0]), "+v"(t[1]), "+v"(t[2]), "+v"(t[3]), "+v"(t[4]))
DI unsigned cvtpk(float lo, float hi) {
    f32x2 v = {lo, hi};
    return __builtin_bit_cast(unsigned, __builtin_convertvector(v, bf16x2v));
}
DI float lo16f(unsigned u) { return __uint_as_float(u << 16); }
DI float hi16f(unsigned u) { return __uint_as_float(u & 0xffff0000u); }
DI float rbf(float v) { return lo16f(cvtpk(v, 0.f)); }
template <int CTRL> DI float dpp(float x) {
    return __int_as_float(__builtin_amdgcn_update_dpp(0, __float_as_int(x), CTRL, 0xF, 0xF, true));
}
DI float rowsum16(float x) {
    x += dpp<0x128>(x); x += dpp<0x124>(x); x += dpp<0x122>(x); x += dpp<0x121>(x);
    return x;
}
DI float xsum32(float x) { auto r = __builtin_amdgcn_permlane32_swap(__float_as_uint(x), __float_as_uint(x), false, false); return __uint_as_float(r[0]) + __uint_as_float(r[1]); }
DI void xsum2(float& a, float& d) {
    auto r = __builtin_amdgcn_permlane16_swap(__float_as_uint(a), __float_as_uint(d), false, false);
    const float s = __uint_as_float(r[0]) + __uint_as_float(r[1]);
    const float t = xsum32(s);
    auto u = __builtin_amdgcn_permlane16_swap(__float_as_uint(t), __float_as_uint(t), false, false);
    a = __uint_as_float(u[0]); d = __uint_as_float(u[1]);
}
DI float xsum2h(float a, float d) {
    auto r = __builtin_amdgcn_permlane16_swap(__float_as_uint(a), __float_as_uint(d), false, false);
    return xsum32(__uint_as_float(r[0]) + __uint_as_float(r[1]));
}
DI float hsum4(f32x4 v) { return (v.x + v.y) + (v.z + v.w); }
typedef __amdgpu_buffer_rsrc_t rsrc_t;
constexpr int SC1 = 16;
DI v4u ld128(rsrc_t rs, unsigned off) { return __builtin_amdgcn_raw_buffer_load_b128(rs, off, 0, SC1); }
DI v4u ld128s(rsrc_t rs, unsigned off, unsigned soff) { return __builtin_amdgcn_raw_buffer_load_b128(rs, off, soff, SC1); }
DI v2u ld64(rsrc_t rs, unsigned off) { return __builtin_amdgcn_raw_buffer_load_b64(rs, off, 0, SC1); }
DI void st128(v4u v, rsrc_t rs, unsigned off) { __builtin_amdgcn_raw_buffer_store_b128(v, rs, off, 0, SC1); }
DI void st64(v2u v, rsrc_t rs, unsigned off) { __builtin_amdgcn_raw_buffer_store_b64(v, rs, off, 0, SC1); }
#define LGKM0() { __builtin_amdgcn_s_waitcnt(0xC07F); asm volatile("" ::: "memory"); }
#define DRAIN() asm volatile("s_waitcnt vmcnt(0)" ::: "memory")
#define ORDER() __builtin_amdgcn_fence(__ATOMIC_ACQUIRE, "wavefront")
#define SCHED() __builtin_amdgcn_sched_barrier(0)
constexpr unsigned kSpinCap = 1u << 17;
#define WAVE_WAIT(...)                                                      \
    {                                                                       \
        unsigned spins_ = dead ? kSpinCap : 0u;                             \
        _Pragma("nounroll") for (;;) {                                      \
            asm volatile("" ::: "memory");                                  \
            bool ok;                                                        \
            __VA_ARGS__;                                                    \
            if (__builtin_amdgcn_ballot_w64(!ok) == 0ull) break;            \
            if (++spins_ > kSpinCap) { dead = true; break; }                \
            __builtin_amdgcn_s_sleep(1);                                    \
        }                                                                   \
        ORDER();                                                            \
    }
#define KPD1 2
#define KPDS 8
#define KPDS0 5
#define KPF0 12
#define KPF1 4
constexpr int PD1 = KPD1, PDS = KPDS, PDS0 = KPDS0;
#define PL(r) "global_load_dwordx2 " r ", %5, %6 sc1\n\t"
#define PS_(r) "s_waitcnt vmcnt(1)\n\tv_cmp_ne_u64 vcc, %7, " r "\n\ts_cbranch_vccz 2f\n\t" PL(r)
DI void pipe_flag(v2u (&t)[4], unsigned off, const void* base, v2u tag, bool& dead) {
    unsigned n = dead ? 0u : kSpinCap;
    asm volatile("s_sleep %8\n\t" PL("%0") "s_sleep %9\n\t" PL("%1")
                 "1:\n\t" PS_("%0") PS_("%1") "s_sub_u32 %4, %4, 1\n\ts_cbranch_scc0 1b\n2:"
                 : "=&v"(t[0]), "=&v"(t[1]), "=&v"(t[2]), "=&v"(t[3]), "+s"(n) : "v"(off), "s"(base), "v"(tag), "i"(KPF0), "i"(KPF1) : "memory", "vcc", "scc");
    dead = __builtin_amdgcn_readfirstlane(n) == ~0u;
}
#define WAVE_RETRY(OK, ...) { if (__builtin_expect(__builtin_amdgcn_ballot_w64(!(OK)) != 0ull, 0)) WAVE_WAIT({ __VA_ARGS__; ok = OK; }) ORDER(); }
#define STAMP(i)
#define STAMP_DUMP()

#define T1POS 27
#define TW 2
#define KLB0 0
#define KLB1 11
#define SKIP0 0
#define SKIP2 0
#define SKIP7 150
#define SKIP5 0
#define SKIP4 0
constexpr int kPads[] = {PADS};
constexpr int pad_next(int id) {
    for (unsigned i = 0; i + 1 < sizeof(kPads) / sizeof(int); ++i) if (kPads[i] == id) return kPads[i + 1];
    return -1;
}
template <int ID> DI void site() {
    if constexpr (pad_next(ID) >= 0) asm volatile("kp%c0:\n\ts_cbranch_execz kp%c1" ::"i"(ID), "i"(pad_next(ID)));
    else asm volatile("");
}
template <int T> DI void tour() {
    if constexpr (pad_next(9000 + T) >= 0 && pad_next(9000 + T) != 9001 + T) asm volatile("s_mov_b64 exec, 0\n\ts_branch kp%c0\nkp%c1:\n\ts_mov_b64 exec, -1" ::"i"(pad_next(9000 + T)), "i"(9001 + T));
}
template <int N> DI void skip() { if constexpr (N > 0) asm volatile("s_branch 1f\n\t.rept %c0\n\ts_nop 0\n\t.endr\n1:" ::"i"(N)); }
template <int N> DI void room() { asm volatile("s_branch 1f\n\t.p2align 12, , %c0\n1:" ::"i"(N)); }
template <class F, int... I> DI void static_for_impl(std::integer_sequence<int, I...>, F&& f) { (f(std::integral_constant<int, I>{}), ...); }
template <int N, class F> DI void static_for(F&& f) { static_for_impl(std::make_integer_sequence<int, N>{}, f); }
#define LAMBDA(v) [&](auto v) __attribute__((always_inline))
DI f32x2 cmul(f32x2 a, f32x2 b) {
    f32x2 t;
    asm("v_pk_mul_f32 %0, %1, %2 op_sel_hi:[0,1]\n\ts_nop 0\n\tv_pk_fma_f32 %0, %1, %2, %0" PK2 "\n\ts_nop 0" : "=&v"(t) : "v"(a), "v"(b));
    return t;
}
DI f32x2 cfma(f32x2 a, f32x2 b, f32x2 c) {
    f32x2 t;
    asm("v_pk_fma_f32 %0, %1, %2, %3 op_sel_hi:[0,1,1]\n\ts_nop 0\n\tv_pk_fma_f32 %0, %1, %2, %0" PK2 "\n\ts_nop 0" : "=&v"(t) : "v"(a), "v"(b), "v"(c));
    return t;
}
DI void cfma4(f32x2 (&t)[4], f32x2 a, f32x2 b0, f32x2 b1, f32x2 b2, f32x2 b3, f32x2 c0, f32x2 c1, f32x2 c2, f32x2 c3) {
    asm("v_pk_fma_f32 %0, %4, %5, %9 op_sel_hi:[0,1,1]\n\tv_pk_fma_f32 %1, %4, %6, %10 op_sel_hi:[0,1,1]\n\tv_pk_fma_f32 %2, %4, %7, %11 op_sel_hi:[0,1,1]\n\tv_pk_fma_f32 %3, %4, %8, %12 op_sel_hi:[0,1,1]\n\t"
        "v_pk_fma_f32 %0, %4, %5, %0" PK2 "\n\tv_pk_fma_f32 %1, %4, %6, %1" PK2 "\n\tv_pk_fma_f32 %2, %4, %7, %2" PK2 "\n\tv_pk_fma_f32 %3, %4, %8, %3" PK2 "\n\ts_nop 0"
        : "=&v"(t[0]), "=&v"(t[1]), "=&v"(t[2]), "=&v"(t[3]) : "v"(a), "v"(b0), "v"(b1), "v"(b2), "v"(b3), "v"(c0), "v"(c1), "v"(c2), "v"(c3));
}
#define C4(t0, t1, t2, t3, a, b0, b1, b2, b3, c0, c1, c2, c3)                                                                                                         \
    "v_pk_fma_f32 %" #t0 ", %" #a ", %" #b0 ", %" #c0 PK1 "v_pk_fma_f32 %" #t1 ", %" #a ", %" #b1 ", %" #c1 PK1 "v_pk_fma_f32 %" #t2 ", %" #a ", %" #b2 ", %" #c2 PK1 "v_pk_fma_f32 %" #t3 ", %" #a ", %" #b3 ", %" #c3 PK1 \
    "v_pk_fma_f32 %" #t0 ", %" #a ", %" #b0 ", %" #t0 PK2 "\n\tv_pk_fma_f32 %" #t1 ", %" #a ", %" #b1 ", %" #t1 PK2 "\n\tv_pk_fma_f32 %" #t2 ", %" #a ", %" #b2 ", %" #t2 PK2 "\n\tv_pk_fma_f32 %" #t3 ", %" #a ", %" #b3 ", %" #t3 PK2 "\n\t"
DI void cfma4n(f32x2 (&t)[4], f32x2 a, f32x2 b0, f32x2 b1, f32x2 b2, f32x2 b3, f32x2 c0, f32x2 c1, f32x2 c2, f32x2 c3) {
    asm(C4(0, 1, 2, 3, 4, 5, 6, 7, 8, 9, 10, 11, 12) : "=&v"(t[0]), "=&v"(t[1]), "=&v"(t[2]), "=&v"(t[3]) : "v"(a), "v"(b0), "v"(b1), "v"(b2), "v"(b3), "v"(c0), "v"(c1), "v"(c2), "v"(c3));
}
DI void agg(unsigned& er, unsigned& ei, f32x2 (&x)[4], f32x2& a, const f32x2 (&u)[4], const f32x2 (&s)[16], f32x2 l8, f32x2 l32, f32x2 l64) {
    f32x2 v[4], b;
    asm(C4(4, 5, 6, 7, 22, 10, 11, 12, 13, 14, 15, 16, 17) C4(0, 1, 2, 3, 22, 4, 5, 6, 7, 18, 19, 20, 21)
        "v_pk_fma_f32 %8, %23, %0, %1" PK1 "v_pk_fma_f32 %9, %23, %2, %3" PK1 "v_pk_fma_f32 %8, %23, %0, %8" PK2 "\n\tv_pk_fma_f32 %9, %23, %2, %9" PK2
        : "=&v"(x[0]), "=&v"(x[1]), "=&v"(x[2]), "=&v"(x[3]), "=&v"(v[0]), "=&v"(v[1]), "=&v"(v[2]), "=&v"(v[3]), "=&v"(a), "=&v"(b)
        : "v"(u[0]), "v"(u[1]), "v"(u[2]), "v"(u[3]), "v"(s[2]), "v"(s[6]), "v"(s[10]), "v"(s[14]), "v"(s[3]), "v"(s[7]), "v"(s[11]), "v"(s[15]), "v"(l8), "v"(l32));
    asm("s_nop 0\n\tv_fma_f32 %0, %2, %6, %4\n\tv_fma_f32 %1, %2, %7, %5\n\tv_fma_f32 %0, -%3, %7, %0\n\tv_fma_f32 %1, %3, %6, %1"
        : "=&v"(er), "=&v"(ei) : "v"(a.x), "v"(a.y), "v"(b.x), "v"(b.y), "v"(l64.x), "v"(l64.y));
}
DI void gacc2(f32x2& h0, f32x2& h1, f32x2 w0, f32x2 w1, v4u e0, v4u e1) {
    const v2u a0 = {e0.x, e0.y}, b0 = {e0.z, e0.w}, a1 = {e1.x, e1.y}, b1 = {e1.z, e1.w};
    asm("v_pk_fma_f32 %0, %4, %2, %0 op_sel_hi:[0,1,1]\n\tv_pk_fma_f32 %1, %6, %3, %1 op_sel_hi:[0,1,1]\n\t"
        "v_pk_fma_f32 %0, %5, %2, %0 op_sel:[0,1,0] op_sel_hi:[0,0,1] neg_lo:[1,0,0]\n\tv_pk_fma_f32 %1, %7, %3, %1 op_sel:[0,1,0] op_sel_hi:[0,0,1] neg_lo:[1,0,0]"
        : "+v"(h0), "+v"(h1) : "v"(w0), "v"(w1), "v"(a0), "v"(b0), "v"(a1), "v"(b1));
}
DI s16x4 trread(char* smem, int byteoff) {
    return __builtin_amdgcn_ds_read_tr16_b64_v4i16((LAS s16x4*)(smem + byteoff));
}

#define CV_LOAD_A(A, a, rr_, hf) { const unsigned* ap_ = (const unsigned*)(smem + ab[(a) >> 1] + (((a) & 1) ? 0 : 512) + 64 * (7 - (rr_)) + 8 * (hf)); A[a][2 * (hf)] = ap_[0]; A[a][2 * (hf) + 1] = ap_[1]; }
#define CV_LOAD_B(B, e, rr_) { B[e] = *(const v4u*)(smem + bb + 64 * (7 - (rr_)) + 512 * (e)); }
template <int R, bool PF, class F, class G>
DI void conv_round(f32x4 (&ca)[8], v4u (&A)[8], const v4u (&B)[8], v4u (&An)[8], v4u (&Bn)[8], const char* smem, const int (&ab)[4], int bb, F&& fill, G&& last) {
    static_for<36>(LAMBDA(g_) {
        constexpr int g = g_.value;
        constexpr int a = g < 8 ? 0 : g < 15 ? 1 : g < 21 ? 2 : g < 26 ? 3 : g < 30 ? 4 : g < 33 ? 5 : g < 35 ? 6 : 7;
        constexpr int e = g - (a * 8 - a * (a - 1) / 2);
        if constexpr (R == 0 && a == 0) mf0(ca[e], A[0], B[e]); else mf(ca[a + e], A[a], B[e]);
        SCHED();
        if constexpr (R == 0 && g < 16 && (g & 3) != 3) { constexpr int n = g - (g >> 2); CV_LOAD_A(A, 2 + (n >> 1), 0, n & 1) SCHED(); }
        if constexpr (PF && g < 32 && (g & 3) != 3) { if constexpr ((g & 3) == 2) CV_LOAD_B(Bn, g >> 2, R + 1) else CV_LOAD_A(An, g >> 2, R + 1, g & 1) SCHED(); }
        site<3000 + 36 * R + g>();
        if constexpr (R == 7) { last(g_); SCHED(); }
        if constexpr ((g & 3) == 3 && g < 28) { fill(std::integral_constant<int, 7 * R + g / 4>{}); SCHED(); }
    });
}
template <int R0, int R1, bool PFL, class F, class G>
DI void conv_part(f32x4 (&ca)[8], v4u (&A0)[8], v4u (&B0)[8], const char* smem, const int (&ab)[4], int bb, F&& fill, G&& last) {
    v4u A1[8], B1[8];
    static_for<R1 - R0>(LAMBDA(r_) {
        constexpr int r = r_.value;
        if constexpr (r & 1) conv_round<R0 + r, (R0 + r + 1 < R1 || PFL)>(ca, A1, B1, A0, B0, smem, ab, bb, fill, last);
        else conv_round<R0 + r, (R0 + r + 1 < R1 || PFL)>(ca, A0, B0, A1, B1, smem, ab, bb, fill, last);
    });
}
template <int IMM> DI void gld(v4u& dst, unsigned voff, const void* sbase) {
    asm volatile("global_load_dwordx4 %0, %1, %2 offset:%3" : "=v"(dst) : "v"(voff), "s"(sbase), "i"(IMM));
}
#define OOB 3
#define OUTP 16
#define NTM 31
template <int IMM, int BIT> DI void gldn(v4u& dst, unsigned voff, const void* sbase) {
    if constexpr ((NTM >> BIT) & 1) asm volatile("global_load_dwordx4 %0, %1, %2 offset:%3 nt" : "=v"(dst) : "v"(voff), "s"(sbase), "i"(IMM));
    else gld<IMM>(dst, voff, sbase);
}
DI void gldu16(unsigned& dst, unsigned voff, const void* sbase) { asm volatile("global_load_ushort %0, %1, %2" : "=v"(dst) : "v"(voff), "s"(sbase)); }
DI void gld1s(unsigned& dst, unsigned voff, const void* sbase) { asm volatile("global_load_dword %0, %1, %2 sc1" : "=v"(dst) : "v"(voff), "s"(sbase)); }
template <int IMM> DI void gld64(v4u& dst, const void* a) {
    if constexpr ((NTM >> 5) & 1) asm volatile("global_load_dwordx4 %0, %1, off offset:%2 nt" : "=v"(dst) : "v"(a), "i"(IMM));
    else asm volatile("global_load_dwordx4 %0, %1, off offset:%2" : "=v"(dst) : "v"(a), "i"(IMM));
}
template <int V> DI void SETM0(unsigned ldsbase) { asm volatile("s_add_u32 m0, %0, %1" ::"s"(ldsbase), "i"(V)); }
template <int GOFF> DI void GLD0(unsigned voff, const char* sbase) { asm volatile("global_load_lds_dwordx4 %0, %1 offset:%2" ::"v"(voff), "s"(sbase), "i"(GOFF) : "memory"); }
template <int GOFF, int V> DI void GLD1(unsigned voff, const char* sbase, unsigned ldsbase) {
    asm volatile("global_load_lds_dwordx4 %0, %1 offset:%3\n\ts_add_u32 m0, %2, %4" ::"v"(voff), "s"(sbase), "s"(ldsbase), "i"(GOFF), "i"(V) : "memory");
}
template <int L, int G> DI void dma_x(unsigned voff, const void* sbase, unsigned ldsbase) {
    asm volatile("s_add_u32 m0, %2, %4\n\ts_nop 0\n\tglobal_load_lds_dwordx4 %0, %1 offset:%3" ::"v"(voff), "s"(sbase), "s"(ldsbase), "i"(G), "i"(L - G) : "memory", "scc");
}
DI void ld_kv(v4u (&a)[15], int on,        unsigned kh, unsigned ko, v4u rk, const void* k, const void* v) {
    asm volatile("s_cmp_ge_u32 %11, 80\n\ts_cbranch_scc1 1f\n\t"
                 "buffer_load_dwordx4 %0, %12, %14, 0 offen nt\n\tbuffer_load_dwordx4 %1, %12, %14, 0 offen offset:1280 nt\n\tbuffer_load_dwordx4 %2, %12, %14, 0 offen offset:2560 nt\n\t"
                 "global_load_dwordx4 %3, %13, %15 nt\n\tglobal_load_dwordx4 %4, %13, %15 offset:1280 nt\n\tglobal_load_dwordx4 %5, %13, %15 offset:2560 nt\n\tglobal_load_dwordx4 %6, %13, %15 offset:3840 nt\n\t"
                 "global_load_dwordx4 %7, %13, %16 nt\n\tglobal_load_dwordx4 %8, %13, %16 offset:1280 nt\n\tglobal_load_dwordx4 %9, %13, %16 offset:2560 nt\n\tglobal_load_dwordx4 %10, %13, %16 offset:3840 nt\n1:"
                 : "=&v"(a[0]), "=&v"(a[1]), "=&v"(a[2]), "=&v"(a[3]), "=&v"(a[4]), "=&v"(a[5]), "=&v"(a[6]), "=&v"(a[7]), "=&v"(a[8]), "=&v"(a[9]), "=&v"(a[10])
                 : "s"(on), "v"(kh), "v"(ko), "s"(rk), "s"(k), "s"(v) : "scc");
}
DI void ld_wr(v4u (&a)[4], int on, unsigned o, const void* base) {
    asm volatile("s_cmp_ge_u32 %4, 80\n\ts_cbranch_scc0 1f\n\t.rept 4\n\tglobal_load_dword a[32+1], %5, %6\n\t.endr\n\ts_branch 2f\n1:\n\t"
                 "global_load_dwordx4 %0, %5, %6 nt\n\tglobal_load_dwordx4 %1, %5, %6 offset:1024 nt\n\tglobal_load_dwordx4 %2, %5, %6 offset:2048 nt\n\tglobal_load_dwordx4 %3, %5, %6 offset:3072 nt\n2:"
                 : "=&v"(a[0]), "=&v"(a[1]), "=&v"(a[2]), "=&v"(a[3]) : "s"(on), "v"(o), "s"(base) : "scc");
}
template <int L, int G> DI void dma_1(unsigned voff, const void* sbase, unsigned ldsbase) {
    asm volatile("s_add_u32 m0, %2, %4\n\ts_nop 0\n\tglobal_load_lds_dword %0, %1 offset:%3" ::"v"(voff), "s"(sbase), "s"(ldsbase), "i"(G), "i"(L - G) : "memory", "scc");
}
template <int L> DI void dma_n(unsigned voff, const void* sbase, unsigned ldsbase) {
    asm volatile("s_add_u32 m0, %2, %3\n\ts_nop 0\n\tglobal_load_lds_dwordx4 %0, %1 nt" ::"v"(voff), "s"(sbase), "s"(ldsbase), "i"(L) : "memory", "scc");
}
template <int A, int O> DI void lds2a(unsigned a) { asm volatile("ds_read_b64 a[%c1:%c1+1], %0 offset:%2" ::"v"(a), "i"(A), "i"(O) : "memory"); }
constexpr int L_EAR = L_XR + 8192;
constexpr int dma_goff(int it) { return it < 40 ? (it >> 2) * 128 : ((it - 100) / 5) * 256; }
constexpr int dma_loff(int it) { return it < 40 ? L_XR + ((it >> 2) % 3) * 4096 + (it & 3) * 1024 : ((it - 100) % 5) * 20480 + ((it - 100) / 5) * 2048; }
#define KFS 4
#define APOS 0
#define PSA 0
#define CWP 2
#define SAF 2
#define KD0 30
#define KST 59
#define KSL 5
#define KSC 11
#define WSP 1
#define XG0 5
#define XGS 1
#define WOD 1
#define USP 1
#define KSG 0
#define PKF 7
#define XDM 1
#define PSP 1
#define SBL 1
#define SMP 1
#define KQ0 0
#define KQ1 18
#define KQ2 4
#define KQ3 0
#define NPD 27
#define IMP 0
#define KSP 1
constexpr int kQ0 = KQ0, kQ1 = KQ1, kQ2 = KQ2, kQ3 = KQ3;
#define KW2S 16
#define KW2M 0x4A
#define KFKS 13
#define KFKG 6
#define KUS 17
#define KTS 19
#define KFWP 13
constexpr int kW2S = KW2S, kW2M = KW2M;
constexpr int NW2P = (20 - kW2S) * __builtin_popcount(kW2M);
constexpr int P1_PRE = NPD + 7;
constexpr int p1_dma(int i) { return i < 5 ? 100 + i : i < 13 ? i - 5 : i < 18 ? 105 + (i - 13) : i < 22 ? 8 + (i - 18) : i < NPD ? 110 + (i - 22) : 400; }
constexpr int kQ1a = KST > KD0 ? kQ1 * (KST - KD0) / (117 - KD0) : 0;
constexpr int p1_pre(int i) { return i < kQ0 + kQ1a ? p1_dma(i) : i == kQ0 + kQ1a ? 400 : p1_dma(i - 1); }
constexpr int p1_item(int s, int g) {
    if (s == KFS && g == (XG0 == 5 ? 4 : 9)) return 324;
    if (WOD ? (s & 1) && s < 10 && (g == 3 || g == 8) : g == 9 && s < 10) return WOD ? 114 + s + (g == 8) : 115 + s;
    if (s == KSL && (g == KSG || g == KSG + 1)) return 202 + g - KSG;
    if (s == KFWP && g == 1) return 208;
    if (s == KFKS && g == KFKG) return 206;
    if (USP && s == KUS && (g == 0 || g == 2 || g == 4 || g == 7)) return 210 + g / 2;
    if (s == KTS && g == 9) return 209;
    if (s >= kW2S && ((kW2M >> g) & 1)) return 500 + (s - kW2S) * __builtin_popcount(kW2M) + __builtin_popcount(kW2M & ((1 << g) - 1));
    if ((s & 1) == 0 && s <= 12 && g >= XG0 && (g - XG0) % XGS == 0 && (g - XG0) / XGS < 4) return 4 * (s / 2 + 3) + (g - XG0) / XGS;
    return -1;
}
constexpr int p1_cnt(int s, int g) {
    int n = P1_PRE;
    for (int t = 0; t <= s; ++t)
        for (int u = 0; u < 10; ++u) {
            if (t == s && u >= g) break;
            if (p1_item(t, u) >= 0) ++n;
            if (!USP && t == KUS && u == 0) n += 4;
        }
    return n;
}
constexpr int p1_pos(int code) {
    for (int i = 0; i < P1_PRE; ++i) if (p1_pre(i) == code) return i;
    for (int t = 0; t < 20; ++t)
        for (int u = 0; u < 10; ++u) if (p1_item(t, u) == code) return p1_cnt(t, u);
    return -1000;
}
constexpr int p1_lds(int s, int g) {
    constexpr int t0 = XG0 == 5 ? 0 : 5;
    if (s >= 2 && s < 10 && !(s & 1) && g >= t0 && g < t0 + 2) return 2 + s + g - t0;
    if (s == KSC + 1 && g == t0 + 2) return 38;
    if (s >= 14 && s < 19 && !(s & 1) && (g == 5 || g == 8)) return 30 + (s - 14) + g / 8;
    if (s == 19 && (g == 0 || g == 4)) return 36 + g / 4;
    if (s == 19 && g == 7) return 39;
    return -1;
}
constexpr int p1_nl(int s, int g0, int g1) {
    int n = 0;
    for (int g = g0; g < g1; ++g) n += (s < 19 && g < 5) + ((s & 1) && s < 19 && g >= 5 && g < 9) + (p1_lds(s, g) >= 0);
    return n;
}
constexpr int p1_cap(int s, int g) {
    int u = p1_nl(s, g, g + 1) + (g == 9 && s < 19) + ((s & 1) && s < 19 && g == 5) + ((s & 1) == 0 && g == XG0 - 1);
    const int it = p1_item(s, g), nt = g < 9 ? p1_item(s, g + 1) : -1;
    u += (it >= 0) + (nt >= 0 && nt < 200) + (it == 324);
    if ((s == 19 && g == 2) || (g == 9 && ((s & 3) == 2 || s == KUS - 1 || s == 18))) u += 2;
    return u < 2 ? 2 - u : 0;
}
constexpr int NFILL = 112;
constexpr int p1_from(int f) { return f < 40 ? 0 : f < 52 ? 9 : f < 72 ? KSC : 14; }
constexpr int p1_fill(int s, int g, int e) {
    int f = 0;
    for (int t = 0; t <= s; ++t)
        for (int u = 0; u < 10; ++u)
            for (int v = 0; v < p1_cap(t, u); ++v) {
                const bool go = f < NFILL && p1_from(f) <= t;
                if (t == s && u == g && v == e) return go ? f : -1;
                f += go;
                if (f == 40) f = 48;
            }
    return f;
}
static_assert(p1_fill(13, 10, 0) >= 52, "");
constexpr bool p1_all() {
    for (int i = 0; i < 40; ++i) if (p1_pos(i) < 0) return false;
    for (int i = 100; i < 125; ++i) if (p1_pos(i) < 0) return false;
    for (int i = 0; i < NW2P; ++i) if (p1_pos(500 + i) < 0) return false;
    return p1_pos(202) >= 0 && p1_pos(203) >= 0 && p1_pos(206) >= 0 && p1_pos(208) >= 0 && p1_pos(209) >= 0 && p1_pos(324) >= 0;
}
static_assert(p1_all(), "");
constexpr int p1_lim(int n) { return n > 63 ? 63 : n; }
template <int IMM> DI void gld4s(v4u& dst, unsigned voff, const void* sbase) { asm volatile("global_load_dwordx4 %0, %1, %2 offset:%3 sc1" : "=v"(dst) : "v"(voff), "s"(sbase), "i"(IMM)); }
template <int IMM> DI void gld2s(v2u& dst, unsigned voff, const void* sbase) { asm volatile("global_load_dwordx2 %0, %1, %2 offset:%3 sc1" : "=v"(dst) : "v"(voff), "s"(sbase), "i"(IMM)); }
template <class T> DI void pin(T& v) { asm volatile("" : "+v"(v)); }
DI void mul2(float& a, float& b, float x, float y, float s) { asm volatile("v_mul_f32 %0, %2, %4\n\tv_mul_f32 %1, %3, %4" : "=&v"(a), "=&v"(b) : "v"(x), "v"(y), "v"(s)); }
DI void xor2(float& a, float& b, unsigned x, unsigned y, unsigned s) { asm volatile("v_xor_b32 %0, %2, %4\n\tv_xor_b32 %1, %3, %4" : "=&v"(a), "=&v"(b) : "v"(x), "v"(y), "v"(s)); }
DI void rot2(unsigned& a, unsigned& b, unsigned x, unsigned y) { asm volatile("v_alignbit_b32 %0, %2, %2, 16\n\tv_alignbit_b32 %1, %3, %3, 16" : "=&v"(a), "=&v"(b) : "v"(x), "v"(y)); }
DI void mix2(unsigned& a, unsigned& b, unsigned x, unsigned y, unsigned z) { asm volatile("v_bfi_b32 %0, %5, %2, %3\n\tv_bfi_b32 %1, %5, %3, %4" : "=&v"(a), "=&v"(b) : "v"(x), "v"(y), "v"(z), "s"(0xffffu)); }
template <int I> DI void shr1(unsigned& o, unsigned prev, unsigned x) {
    if constexpr (I > 0) asm volatile("v_mov_b32_dpp %0, %1 wave_ror:1 row_mask:0xf bank_mask:0xf\n\tv_mov_b32_dpp %0, %2 wave_shr:1 row_mask:0xf bank_mask:0xf" : "=&v"(o) : "v"(prev), "v"(x));
    else asm volatile("v_mov_b32_dpp %0, %1 wave_shr:1 row_mask:0xf bank_mask:0xf bound_ctrl:0" : "=&v"(o) : "v"(x));
}
template <int G> DI void bc2(float& a, float& b, unsigned x) {
    asm volatile("v_mov_b32_dpp %0, %2 row_newbcast:%3 row_mask:0xf bank_mask:0xf\n\tv_mov_b32_dpp %1, %2 row_newbcast:%4 row_mask:0xf bank_mask:0xf" : "=&v"(a), "=&v"(b) : "v"(x), "i"(G), "i"(G + 8));
}
DI void rot1(unsigned& a, unsigned x) { asm volatile("v_alignbit_b32 %0, %1, %1, 16" : "=v"(a) : "v"(x)); }
DI void mix1(unsigned& a, unsigned x, unsigned y) { asm volatile("v_bfi_b32 %0, %3, %1, %2" : "=v"(a) : "v"(x), "v"(y), "s"(0xffffu)); }
DI void ror1(unsigned& o, unsigned prev) { asm volatile("v_mov_b32_dpp %0, %1 wave_ror:1 row_mask:0xf bank_mask:0xf" : "=v"(o) : "v"(prev)); }
DI void shr1b(unsigned& o, unsigned x) { asm volatile("v_mov_b32_dpp %0, %1 wave_shr:1 row_mask:0xf bank_mask:0xf" : "+v"(o) : "v"(x)); }
DI void xor1(float& a, unsigned x, unsigned s) { asm volatile("v_xor_b32 %0, %1, %2" : "=v"(a) : "v"(x), "v"(s)); }
template <int G> DI void bc1(float& a, unsigned x) { asm volatile("v_mov_b32_dpp %0, %1 row_newbcast:%2 row_mask:0xf bank_mask:0xf" : "=v"(a) : "v"(x), "i"(G)); }
template <int G> DI void mulb(float& o, unsigned x, float y) { asm volatile("v_mul_f32_dpp %0, %1, %2 row_newbcast:%3 row_mask:0xf bank_mask:0xf" : "=v"(o) : "v"(x), "v"(y), "i"(G)); }
template <int N> DI void lgkm() { __builtin_amdgcn_s_waitcnt(0xC07F | (N << 8)); asm volatile("" ::: "memory"); }
DI void unp2(float& a, float& b, unsigned x) { asm volatile("v_lshlrev_b32 %0, 16, %2\n\tv_and_b32 %1, 0xffff0000, %2" : "=&v"(a), "=&v"(b) : "v"(x)); }
#define GL(a) "global_load_dwordx4 a[" #a ":" #a "+3], %7, %8 sc1\n\tglobal_load_dwordx4 a[" #a "+4:" #a "+7], %7, %8 offset:256 sc1\n\t"
#define AR(d, a, o) "v_accvgpr_read_b32 " d ", a[" #a "+" #o "]\n\t"
#define GS_(a) "s_waitcnt vmcnt(4)\n\t" AR("%2", a, 1) AR("%3", a, 3) AR("%4", a, 5) AR("%5", a, 7) AR("%0", a, 0) AR("%1", a, 2)                              \
    "v_xor_b32 %2, %9, %2\n\tv_bitop3_b32 %4, %4, %9, %2 bitop3:0xbe\n\tv_bitop3_b32 %4, %3, %9, %4 bitop3:0xbe\n\tv_bitop3_b32 %4, %5, %9, %4 bitop3:0xbe\n\t" \
    AR("%2", a, 4) AR("%3", a, 6) "v_cmp_ne_u32 vcc, 0, %4\n\ts_cbranch_vccz 2f\n\t" GL(a)
template <int A, int D> DI void gran_req(unsigned off, const void* base) {
    if constexpr (D > 0) __builtin_amdgcn_s_sleep(D);
    asm volatile("global_load_dwordx4 a[%c2:%c2+3], %0, %1 sc1\n\tglobal_load_dwordx4 a[%c2+4:%c2+7], %0, %1 offset:256 sc1" ::"v"(off), "s"(base), "i"(A) : "memory");
}
DI void pipe_gran(float& a, float& d, unsigned off, const void* base, unsigned tag, bool& dead) {
    unsigned n = dead ? 0u : kSpinCap, y, z;
    float a1, d1;
    asm volatile("1:\n\t" GS_(32) GS_(40) GS_(48) "s_sub_u32 %6, %6, 1\n\ts_cbranch_scc0 1b\n2:"
                 : "=&v"(a), "=&v"(d), "=&v"(a1), "=&v"(d1), "=&v"(y), "=&v"(z), "+s"(n) : "v"(off), "s"(base), "s"(tag) : "memory", "vcc", "scc");
    dead = __builtin_amdgcn_readfirstlane(n) == ~0u;
    a += a1; d += d1;
}
DI void fma2(float& a, float& b, float x, float y, float s, float t, float u, float v) {
    asm volatile("v_fma_f32 %0, %2, %4, %6\n\tv_fma_f32 %1, %3, %5, %7" : "=&v"(a), "=&v"(b) : "v"(x), "v"(y), "v"(s), "v"(t), "v"(u), "v"(v));
}
DI void fma2p(f32x2& o, float x, float y, float r, float u, float v) {
    asm("v_fmac_f32 %0, %1, %2" : "+v"(u) : "v"(x), "v"(r));
    asm("v_fmac_f32 %0, %1, %2" : "+v"(v) : "v"(y), "v"(r));
    o = f32x2{u, v};
}
DI void swp2(unsigned& a, unsigned& b, unsigned& c, unsigned& d) { asm volatile("s_nop 1\n\tv_permlane16_swap_b32 %0, %1\n\tv_permlane16_swap_b32 %2, %3" : "+v"(a), "+v"(b), "+v"(c), "+v"(d)); }
DI void swp2n(unsigned& a, unsigned& b, unsigned& c, unsigned& d) { asm volatile("v_permlane16_swap_b32 %0, %1\n\tv_permlane16_swap_b32 %2, %3" : "+v"(a), "+v"(b), "+v"(c), "+v"(d)); }
DI void cp2(unsigned& a, unsigned& b, f32x4 v) { asm volatile("v_cvt_pk_bf16_f32 %0, %2, %3\n\tv_cvt_pk_bf16_f32 %1, %4, %5" : "=&v"(a), "=v"(b) : "v"(v.x), "v"(v.y), "v"(v.z), "v"(v.w)); }
DI void zero2(v2u& a) { asm volatile("v_mov_b64 %0, 0" : "=v"(a)); }
DI void mov1(unsigned& a, unsigned b) { asm volatile("v_mov_b32 %0, %1" : "=v"(a) : "v"(b)); }
DI void sw16(unsigned& a, unsigned& b) { asm volatile("v_permlane16_swap_b32 %0, %1" : "+v"(a), "+v"(b)); }
DI void sw32(unsigned& a, unsigned& b) { asm volatile("v_permlane32_swap_b32 %0, %1" : "+v"(a), "+v"(b)); }
DI void mrg8(float& a, float b) { asm volatile("v_mov_b32_dpp %0, %1 quad_perm:[0,1,2,3] row_mask:0xf bank_mask:0xc" : "+v"(a) : "v"(b)); }
DI void sel1(float& o, unsigned a, unsigned b, u64 m) { asm volatile("v_cndmask_b32 %0, %1, %2, %3" : "=v"(o) : "v"(a), "v"(b), "s"(m)); }
DI void ne1(u64& m, v2u tag, v2u a) { asm volatile("v_cmp_ne_u64 %0, %1, %2" : "=s"(m) : "v"(tag), "v"(a)); }
DI void ne2(u64& m, v2u tag, v2u a, v2u b) { asm volatile("v_cmp_ne_u64 vcc, %1, %2\n\tv_cmp_ne_u64 %0, %1, %3\n\ts_or_b64 %0, %0, vcc" : "=&s"(m) : "v"(tag), "v"(a), "v"(b) : "vcc", "scc"); }
template <int G> DI void bc2b(float& a, float& b, float x, float y) {
    asm volatile("v_mov_b32_dpp %0, %2 row_newbcast:%4 row_mask:0xf bank_mask:0xf\n\tv_mov_b32_dpp %1, %3 row_newbcast:%4 row_mask:0xf bank_mask:0xf" : "=&v"(a), "=&v"(b) : "v"(x), "v"(y), "i"(G));
}
template <class T> DI void pins(T& v) { asm volatile("" : "+s"(v)); }
template <int I> DI void gld_w2(unsigned voff, const void* sbase) {
    asm volatile("global_load_dwordx4 " WA(2) ", %0, %1 offset:%3 sc1" ::"v"(voff), "s"(sbase), "i"(W2R + 4 * I), "i"((I % 5) * 1024 - 2048));
}
DI void gld2t(v2u& dst, unsigned voff, const void* sbase) { asm volatile("global_load_dwordx2 %0, %1, %2 sc1" : "+v"(dst) : "v"(voff), "s"(sbase)); }
DI void wait2(v2u& v, v4u (&x)[3]) { asm volatile("s_waitcnt vmcnt(0)" : "+v"(v), "+v"(x[0]), "+v"(x[1]), "+v"(x[2])::"memory"); }
template <int IMM> DI void gld4p(v4u& dst, unsigned voff, const void* sbase) { asm volatile("global_load_dwordx4 %0, %1, %2 offset:%3" : "+v"(dst) : "v"(voff), "s"(sbase), "i"(IMM)); }
DI void undef4(v4u (&x)[4]) { asm volatile("" : "=v"(x[0]), "=v"(x[1]), "=v"(x[2]), "=v"(x[3])); }
DI void wait1(v2u& v) { asm volatile("s_waitcnt vmcnt(0)" : "+v"(v)::"memory"); }
template <int N> DI void wait_fkn(v2u& v, v2u& u) { asm volatile("s_waitcnt vmcnt(%2)" : "+v"(v), "+v"(u) : "i"(N) : "memory"); }
template <int A, int IMM> DI void lda2(unsigned o, const void* base) { asm volatile("global_load_dwordx2 a[%c2:%c2+1], %0, %1 offset:%3" ::"v"(o), "s"(base), "i"(A), "i"(IMM)); }
DI void wait_fk(v2u& v, v2u& u, unsigned& x) { asm volatile("s_waitcnt vmcnt(0)" : "+v"(v), "+v"(u), "+v"(x)::"memory"); }
DI void ld_ur(unsigned o, const void* base) {
    asm volatile("global_load_dwordx4 a[40:40+3], %0, %1 sc1\n\tglobal_load_dwordx4 a[44:44+3], %0, %1 offset:1024 sc1\n\t"
                 "global_load_dwordx4 a[48:48+3], %0, %1 offset:2048 sc1\n\tglobal_load_dwordx4 a[52:52+3], %0, %1 offset:3072 sc1" ::"v"(o), "s"(base));
}
template <int I> DI void ld_ur1(unsigned o, const void* base) { asm volatile("global_load_dwordx4 a[%c2:%c2+3], %0, %1 offset:%3 sc1" ::"v"(o), "s"(base), "i"(40 + 4 * I), "i"(1024 * I)); }
constexpr int kUY = USP ? p1_cnt(20, 0) - 1 - p1_pos(213) : p1_cnt(20, 0) - p1_cnt(KUS, 0) - 4;
DI void st_ur(unsigned a) {
    asm volatile("s_waitcnt vmcnt(%1)\n\tds_write_b128 %0, a[40:40+3] offset:480\n\tds_write_b128 %0, a[44:44+3] offset:1504\n\t"
                 "ds_write_b128 %0, a[48:48+3] offset:2528\n\tds_write_b128 %0, a[52:52+3] offset:3552" ::"v"(a), "i"(kUY < NW2P ? kUY : NW2P) : "memory");
}
template <int I> DI void st_ur1(unsigned a) {
    if constexpr (I == 0) asm volatile("s_waitcnt vmcnt(%0)" ::"i"(kUY) : "memory");
    asm volatile("ds_write_b128 %0, a[%c1:%c1+3] offset:%2" ::"v"(a), "i"(40 + 4 * I), "i"(480 + 1024 * I) : "memory");
}
DI void reload_b(v4u (&B)[8], unsigned a) {
    asm volatile("s_waitcnt lgkmcnt(0)\n\tds_read_b128 %0, %8 offset:448\n\tds_read_b128 %1, %8 offset:960\n\tds_read_b128 %2, %8 offset:1472\n\tds_read_b128 %3, %8 offset:1984\n\t"
                 "ds_read_b128 %4, %8 offset:2496\n\tds_read_b128 %5, %8 offset:3008\n\tds_read_b128 %6, %8 offset:3520\n\tds_read_b128 %7, %8 offset:4032\n\ts_waitcnt lgkmcnt(0)"
                 : "+v"(B[0]), "+v"(B[1]), "+v"(B[2]), "+v"(B[3]), "+v"(B[4]), "+v"(B[5]), "+v"(B[6]), "+v"(B[7]) : "v"(a) : "memory");
}
DI void wait4(v4u (&x)[4]) { asm volatile("s_waitcnt vmcnt(0)" : "+v"(x[0]), "+v"(x[1]), "+v"(x[2]), "+v"(x[3])::"memory"); }
template <int A, int IMM> DI void lda4(unsigned o, const void* base) { asm volatile("global_load_dwordx4 a[%c2:%c2+3], %0, %1 offset:%3" ::"v"(o), "s"(base), "i"(A), "i"(IMM)); }
template <int A> DI void rda(unsigned& v) { asm volatile("v_accvgpr_read_b32 %0, a[%c1+0]" : "=v"(v) : "i"(A)); }
DI void poll1(unsigned a, const void* base) { asm volatile("global_load_dwordx2 a[36:36+1], %0, %1 sc1" ::"v"(a), "s"(base)); }
DI void poll2(unsigned a, unsigned b, const void* base) { asm volatile("global_load_dwordx2 a[36:36+1], %0, %2 sc1\n\tglobal_load_dwordx2 a[36+2:36+3], %1, %2 sc1" ::"v"(a), "v"(b), "s"(base)); }
DI void touch_(unsigned a, const void* base) { asm volatile("global_load_dword a[32+1], %0, %1" ::"v"(a), "s"(base)); }
template <int N> DI void wait_st(v2u& a, v4u& b) { asm volatile("s_waitcnt vmcnt(%2)" : "+v"(a), "+v"(b) : "i"(N) : "memory"); }
DI void undef3(v4u (&x)[3]) { asm volatile("" : "=v"(x[0]), "=v"(x[1]), "=v"(x[2])); }
template <int IMM> DI void gld4t(v4u& dst, unsigned voff, const void* sbase) { asm volatile("global_load_dwordx4 %0, %1, %2 offset:%3 sc1" : "+v"(dst) : "v"(voff), "s"(sbase), "i"(IMM)); }
template <int N> DI void vm_wait() { asm volatile("s_waitcnt vmcnt(%0)" ::"i"(N) : "memory"); }
DI float dot2a(unsigned a, unsigned b, float c) {
    return __builtin_amdgcn_fdot2_f32_bf16(__builtin_bit_cast(bf16x2v, a), __builtin_bit_cast(bf16x2v, b), c, false);
}
DI void disc(unsigned l0, unsigned l1, unsigned l2, f32x2& lam, f32x2& co) {
    const float dt = rbf(__builtin_amdgcn_exp2f(lo16f(l2) * 1.44269504088896f));
    const float lre = lo16f(l0), lim = lo16f(l1);
    const float e_ = __builtin_amdgcn_exp2f(dt * lre * 1.44269504088896f);
    const float rv = __builtin_amdgcn_fractf(dt * lim * 0.159154943091895f);
    lam = f32x2{e_ * __builtin_amdgcn_cosf(rv), e_ * __builtin_amdgcn_sinf(rv)};
    const float nr = lam.x - 1.0f, ni = lam.y, id = __builtin_amdgcn_rcpf(lre * lre + lim * lim);
    co = f32x2{(nr * lre + ni * lim) * id, (ni * lre - nr * lim) * id};
}
DI f32x4 unpk4(v2u u) { return f32x4{lo16f(u.x), hi16f(u.x), lo16f(u.y), hi16f(u.y)}; }
#define D2 "v_dot2_f32_bf16 "
#define CP "v_cvt_pk_bf16_f32 "
DI void d2x4(unsigned* o, unsigned a0, unsigned a1, unsigned a2, unsigned a3, unsigned b0, unsigned b1, unsigned b2, unsigned b3) {
    asm volatile(D2 "%0, %4, %8, 0\n\t" D2 "%1, %5, %9, 0\n\t" D2 "%2, %6, %10, 0\n\t" D2 "%3, %7, %11, 0"
                 : "=&v"(o[0]), "=&v"(o[1]), "=&v"(o[2]), "=&v"(o[3]) : "v"(a0), "v"(a1), "v"(a2), "v"(a3), "v"(b0), "v"(b1), "v"(b2), "v"(b3));
}
DI void cpx4(unsigned* o, unsigned a0, unsigned a1, unsigned a2, unsigned a3, unsigned b0, unsigned b1, unsigned b2, unsigned b3) {
    asm volatile(CP "%0, %4, %8\n\t" CP "%1, %5, %9\n\t" CP "%2, %6, %10\n\t" CP "%3, %7, %11"
                 : "=&v"(o[0]), "=&v"(o[1]), "=&v"(o[2]), "=&v"(o[3]) : "v"(a0), "v"(a1), "v"(a2), "v"(a3), "v"(b0), "v"(b1), "v"(b2), "v"(b3));
}
DI void cpx4i(unsigned* t, const unsigned* ka) {
    asm volatile(CP "%0, %0, %4\n\t" CP "%1, %1, %5\n\t" CP "%2, %2, %6\n\t" CP "%3, %3, %7"
                 : "+v"(t[0]), "+v"(t[1]), "+v"(t[2]), "+v"(t[3]) : "v"(ka[0]), "v"(ka[1]), "v"(ka[2]), "v"(ka[3]));
}
template <int IMM> DI void bld4(v4u& dst, unsigned voff, v4u rsrc) {
    if constexpr ((NTM >> 4) & 1) asm volatile("buffer_load_dwordx4 %0, %1, %2, 0 offen offset:%3 nt" : "=v"(dst) : "v"(voff), "s"(rsrc), "i"(IMM));
    else asm volatile("buffer_load_dwordx4 %0, %1, %2, 0 offen offset:%3" : "=v"(dst) : "v"(voff), "s"(rsrc), "i"(IMM));
}

__global__ __launch_bounds__(256, 1) __attribute__((aligned(4096))) void k_main(Params p) {
    __shared__ __attribute__((aligned(16))) char smem[LDS_BYTES];
    const rsrc_t rs = __builtin_amdgcn_make_buffer_rsrc((void*)p.ws, 0, WS_BYTES, 0x00020000);
    const rsrc_t ro = __builtin_amdgcn_make_buffer_rsrc((void*)p.out, 0, S_ * D_ * 4, 0x00020000);
    const int w0 = __builtin_amdgcn_readfirstlane(threadIdx.x >> 6), b0 = blockIdx.x;
    bool dead = false;
    asm volatile("" ::: "a255");
    float SW[5];
    f32x4 xin[5];
    v2u gt[5];
    f32x4 acc[2][5];
    f32x4 rr[2];
    float tt[2][5][4];
    Lm lam1, lamq;
    f32x2 l8, l32, l128;
    u64 late;
    v4u cA[8], cB[8];
    int sgn_, sg4, ab[4], bb, zpa, uwa, spa, tbw, tbr, tbp, tbq, cwa, cw4, spl, dst[3];
    unsigned w2o, wro, fwo, fko, xso, gso, fgo1, fho1, fho, hso, eo, es, ho[4];
    bool cv0;
    int mt1_;
    auto addrs = [&](auto layer_, int lane, int w, int b) __attribute__((always_inline)) {
        constexpr int layer = layer_.value;
        const int c = lane & 15, q = lane >> 4;
        TILES()
        static_assert(l_sg(0, 0) == SC + 0xA900 && l_sg(0, 1) == SC + 0xB400 && l_sg(0, 2) == SC + 0xCE00 && l_sg(0, 3) == SC + 0xDC00, "");
        sgn_ = (layer ? l_sg(1, 0) + w * INBOX : SC + (int)((0xDC00CE00B400A900ull >> (16 * w)) & 0xffff)) + (q >> 1) * 640 + (q & 1) * 8;
        const int c4 = (c >> 1) * 16 + (c & 1) * 4;
        sg4 = sgn_ + c4; sgn_ += c * 16;
        const int pl = lane < 40 ? lane : 39, tbb = l_tb(layer, 0) + w * (layer ? INBOX : 5120);
        spl = SC + pl * 16;
        eo = OFF_EG + (unsigned)(layer * 163840 + nt1 * 640 + pl * 16);
        es = eo + (unsigned)(mt1 * 10240);
        spa = l_rs(layer, 0) + (2 * (mt2 & 1) + (w >> 1)) * (layer ? INBOX : 256) + (2 * (w & 1) + (c >> 3)) * 64 + (c & 7) * 4;
        tbw = tbb + c * 64 + ((q ^ ((c >> 1) & 3)) * 16);
        tbr = tbb + (2 * (c >> 2) + (q & 1)) * 64 + (((2 * (q >> 1) + ((c >> 1) & 1)) ^ (c >> 2)) * 16) + (c & 1) * 8;
        hso = OFF_HF + (unsigned)(layer * 5242880 + ((8 * mt1 + 2 * w + (q >> 1)) * 160 + 10 * nt1 + 5 * (q & 1)) * 256 + c * 16);
        tbp = tbb + (8 * (q & 1) + (c >> 2)) * 64 + (((2 * (q >> 1) + ((c >> 1) & 1)) ^ (c >> 3)) * 16) + (c & 1) * 8;
        tbq = tbp ^ 32;
        cwa = L_CW + c * 16; cw4 = L_CW + c4;
        pin(sg4); pin(tbp); pin(tbq); pin(cw4);
        fho1 = lane ? kNone : OFF_FH + (unsigned)((((layer * 32 + 2 * mt1 + (w >> 1)) * 4 + (nt1 >> 2)) * 8 + (nt1 & 3) * 2 + (w & 1)) * 128);
        fho = OFF_FH + (unsigned)(((layer * 32 + mt2) * 4 + w) * 1024 + (PLM ? lane >> 3 : lane & 7) * 128);
        pin(sgn_); pin(spl); pin(eo); pin(es); pin(spa); pin(tbw); pin(tbr); pin(hso); pin(cwa); pin(fho1); pin(fho);
        const unsigned hb_ = OFF_HF + (unsigned)(layer * 5242880 + (4 * mt2 * 160 + 40 * w) * 256);
#pragma unroll
        for (int m = 0; m < 4; ++m) { ho[m] = hb_ + (unsigned)(((m + 1 + w) & 3) * 40960 + lane * 16); pin(ho[m]); }
#pragma unroll
        for (int m = 0; m < 3; ++m) { dst[m] = SC + ((w + 1 + m) & 3) * INBOX + m * 5120 + lane * 16; pin(dst[m]); }
        mt1_ = mt1;
        if constexpr (layer == 0) {
            const int r0 = SC + (w < 3 ? w : 0) * REGSZ, y0 = 2031 + 8 * q - c, l34 = lane < 33 ? lane : 33;
#pragma unroll
            for (int i = 0; i < 4; ++i) { ab[i] = r0 + ((y0 & 1) ? CV_O : CV_E) + ((y0 + 1) >> 1) * 4 - 448 - 512 - 1024 * i; pin(ab[i]); }
            bb = r0 + 32 * c + 16 * q;
            zpa = w < 3 ? r0 + l34 * 16 + (l34 >= 30 ? CV_E + 4096 - 480 : 0) + (l34 >= 32 ? CV_O + 4100 - 32 - CV_E - 4096 : 0) : SC + 47360 + lane * 16;
            uwa = (w < 3 ? r0 : SC + 47360 - 480) + lane * 16;
            xso = XDM ? (unsigned)((64 * mt2 + 16 * w + (lane >> 2)) * D_ + 80 * nt2 + 8 * ((lane & 3) ^ q)) * 2u : (unsigned)((64 * mt2 + 16 * w + c) * D_ + 80 * nt2 + 16 * (q & 1) + 8 * (q >> 1)) * 2u;
            gso = OFF_GT + (unsigned)((80 * nt2 + i32 + 32 * w) * 4096 + (q & 1) * 512 + (c >> 2) * 128 + (q >> 1) * 64 + (c & 3) * 16);
            fgo1 = lane == 0 && i32 + 32 * w < 80 ? OFF_FG + (unsigned)((nt2 * 96 + i32 + 32 * w) * 128) : kNone;
            pin(bb); pin(zpa); pin(uwa); pin(xso); pin(gso); pin(fgo1);
        }
        SCHED();
    };
    unsigned tag0, tag1;
    v2u tagv;
#define FLAG(off) { if (lane == 0) st64(tagv, rs, off); }
#define ISTAG(f) (((f.x ^ tag0) | (f.y ^ tag1)) == 0u)
#define KW_REQ() { ld_ur(wro, p.ws + OFF_KVT); static_for<NW2P>(LAMBDA(i_) { gld_w2<i_.value>(w2o, p.ws + 2048 + (i_.value / 5) * 5120); }); }
#define FW_OK() { if (__builtin_expect(late != 0ull, 0)) { WAVE_WAIT({ const v2u fk_ = ld64(rs, fko); const v2u fw_ = ld64(rs, fwo); ok = ISTAG(fk_) && ISTAG(fw_); }) KW_REQ() } }
#define WF(g, s) *(const v4u*)(smem + wa[(s) & 1][(g) < 4 ? (g) & 1 : 2] + ((g) & 2) * 20480 + ((s) >> 1) * 1024)
#define TT_GAP(n) { constexpr int mb_ = (n) / 10, nb_ = ((n) % 10) / 2, e_ = ((n) & 1) * 2; \
        mul2(tt[mb_][nb_][e_], tt[mb_][nb_][e_ + 1], rm_[4 * mb_ + e_], rm_[4 * mb_ + e_ + 1], SW[nb_]); }
    {
        const int tid = threadIdx.x, lane = tid & 63, w = w0, b = b0;
        const int c = lane & 15, q = lane >> 4;
        TILES()
        STAMP(0)
        const unsigned lds0 = (unsigned)(size_t)(LAS char*)smem;
        unsigned nonce;
        unsigned cpf0;
        {
            u64 pc;
            int len, len2;
            asm volatile("s_getpc_b64 %0\nkpf_begin:\n\ts_mov_b32 %1, kpf_end-kpf_begin\n\ts_mov_b32 %2, kpf_end2-kpf_begin" : "=s"(pc), "=s"(len), "=s"(len2));
            len = len > len2 ? len : len2;
            const int o0 = (((j & 15) * 4 + w) + 64 * (lane >> 3)) * 128;
            asm volatile("global_load_dword %0, %1, %2" : "=v"(cpf0) : "v"(o0 < len ? o0 : 0), "s"(pc));
        }
        const bool cv = 32 * w + i32 < 80;
        const int chn = 80 * nt2 + i32 + 32 * (cv ? w : 0);
        const bool kw = 32 * w + 31 - i32 < 80;
        const int kp = w == 2 ? 256 + 8 * nt2 + ((i32 & 15) >> 1) : 32 * nt2 + i32, kh_ = w == 2 ? i32 & 1 : w & 1;
        const int kc = KVU ? kp % 10 : (80 * nt2 + (w == 2 ? 64 + (i32 & 15) : i32 + 32 * (w & 1))) % 10, kr = KVU ? 2 * (kp / 10) + kh_ : (80 * nt2 + (w == 2 ? 64 + (i32 & 15) : i32 + 32 * (w & 1))) / 10;
        const int lc = cv ? lane : 8;
        const unsigned xl = lds0 + (unsigned)(w * REGSZ);
        v4u ax[15], sa[5], cr[2], wr[4];
        unsigned uL[3], uM[3], sdq, kso;
        const int cu_ = (b < 200 ? b : 0) * 4 + w, cdb = cu_ / 20, ccg = cu_ % 20;
        {
            unsigned cpo = (unsigned)(32 * ccg + (lane & 31)) * 2u, cpm = (unsigned)(40 * nt1 + (lane < 39 ? lane : 39)) * 2u;
            unsigned ko = (unsigned)(32 * kr + 4 * (lane >> 3)) * 1280u + (unsigned)(64 * kc + 8 * (lane & 7)) * 2u, kh = ko - 3840u;
            pin(cpo); pin(cpm); pin(ko); pin(kh);
            SCHED();
            gldu16(uL[0], cpo, p.Lre); gldu16(uL[1], cpo, p.Lim); gldu16(uL[2], cpo, p.logdt);
            gldu16(uM[0], cpm, p.Lre); gldu16(uM[1], cpm, p.Lim); gldu16(uM[2], cpm, p.logdt);
            const v4u rk = {(unsigned)(u64)p.k, (unsigned)((u64)p.k >> 32) & 0xffffu, S_ * D_ * 2u, 0x00020000u};
            ld_kv(ax, 32 * w + 31 - i32, kh, ko, rk, p.k, p.v);
            SCHED();
        }
        auto small_ = [&]() __attribute__((always_inline)) {
            kso = (unsigned)(512 * kc + lane * 4);
            dma_1<L_EAR + 2048, 0>(kso, p.shk, xl); dma_1<L_EAR + 2304, 256>(kso, p.shk, xl);
            gld1s(nonce, kso & 4u, p.ws + OFF_NONCE);
            SCHED();
        };
        if (SMP == 0) small_();
        const u16* sab = w < 2 ? p.x0 : w == 2 ? p.Bre : p.Bim;
        unsigned sao;
        {
            int r0 = w < 2 ? 128 * mt1 + 8 * nt1 + 4 * w : 40 * nt1 + mt1, r1 = w < 2 ? 1 : 16, r2 = w < 2 ? 2047 : 40 * nt1 + 32 + (mt1 >> 1);
            pins(sab); pins(r0); pins(r1); pins(r2);
            const int r = r0 + q * r1;
            sao = (unsigned)((r < r2 ? r : r2) * 1280 + c * 16);
            pin(sao);
        }
        if (SAF == 0) static_for<5>(LAMBDA(i) { gld<256 * i.value>(sa[i.value], sao, sab); });
        SCHED();
        if (w == TW && !kw) tour<0>();
        STAMP(22)
        const int row8 = lane >> 3;
        const unsigned wvo = (unsigned)(40 * nt1 + row8) * 1280u + (unsigned)(((lane & 7) ^ ((w & 1) | ((row8 >> 1) << 1))) * 16 + (w >> 1) * 128);
        const char* const wbase = (const char*)((w & 1) ? p.Bim : p.Bre);
        const unsigned wl = lds0 + (unsigned)(((w & 1) * 10 + (w >> 1)) * 1024);
        const unsigned xvo0 = (unsigned)(8 * (row8 >> 1) + 2 * (row8 & 1)) * 1280u + (unsigned)(((lane & 7) ^ ((row8 >> 1) << 1)) * 16);
        const unsigned xvo1 = (xvo0 ^ 16u) + 1280u;
        const char* const xbase = (const char*)p.x0 + (size_t)(128 * mt1 + 32 * w) * 1280;
        unsigned xv[4] = {xvo0, xvo1, xvo0 + 5120u, xvo1 + 5120u}, wv[5];
#pragma unroll
        for (int i = 0; i < 5; ++i) { wv[i] = wvo + i * 10240u; pin(wv[i]); if (i < 4) pin(xv[i]); }
#define DMA_M0(it) SETM0<dma_loff(it) - dma_goff(it)>((it) < 40 ? xl : wl)
#define DMA_LD1(it, nx_) GLD1<dma_goff(it), dma_loff(nx_) - dma_goff(nx_)>((it) < 40 ? xv[(it) & 3] : wv[(it) < 40 ? 0 : ((it) - 100) % 5], (it) < 40 ? xbase : wbase, (nx_) < 40 ? xl : wl)
#define DMA_LD(it) GLD0<dma_goff(it)>((it) < 40 ? xv[(it) & 3] : wv[(it) < 40 ? 0 : ((it) - 100) % 5], (it) < 40 ? xbase : wbase)
        const int swz = (c & 1) | ((c >> 2) << 1);
        int wa[2][3];
#pragma unroll
        for (int i = 0; i < 2; ++i) {
            wa[i][0] = (c >> 3) * 20480 + (c & 7) * 128 + (((4 * i + q) ^ (c & 6)) * 16); wa[i][1] = (wa[i][0] ^ 16) + 10240;
            wa[i][2] = 81920 + (c & 1) * 10240 + (c >> 1) * 128 + (((4 * i + q) ^ ((c & 1) | ((c >> 2) << 1))) * 16);
            pin(wa[i][0]); pin(wa[i][1]); pin(wa[i][2]);
        }
        int xlane0 = L_XR + w * REGSZ + (c & 1) * 1024 + (c >> 1) * 128 + ((q ^ swz) * 16);
        int xlane1 = L_XR + w * REGSZ + (c & 1) * 1024 + (c >> 1) * 128 + (((4 + q) ^ swz) * 16);
        unsigned cvo = (unsigned)((16 * cdb + (lane >> 2)) * D_ + 32 * ccg + 8 * ((lane & 3) ^ q)) * 2u, for0, for1;
        int ear = L_EAR + w * REGSZ + c * 64 + (q ^ (c >> 2)) * 16, eas = L_EAR + 2048 + w * REGSZ + (lane & 7) * 64;
        pin(ear); pin(eas);
        {
            const int G = 4 * ccg + q, g5 = G % 5, ch = 2 * (G - g5) + 5 * (g5 & 1) + (g5 & 6), ci = ch + (g5 < 4 ? 1 : 5);
            const unsigned fo = OFF_W2F + (unsigned)(((cdb / 5) * 200 + cdb % 5) * 1024 + c * 16);
            for0 = fo + (unsigned)((ch >> 2) * 5120 + (ch & 3) * 256); for1 = fo + (unsigned)((ci >> 2) * 5120 + (ci & 3) * 256);
        }
        unsigned sto = OFF_ST + (unsigned)((mt1 * 16 + 4 * w + q) * 256 + c * 8), swo = OFF_ST + (unsigned)((PKF & 4) ? ((lane >> 2) * 16 + nt1) * 256 + 128 + (lane & 3) * 16 : (c * 16 + nt1) * 256 + 128 + q * 16);
        unsigned kvo = OFF_KVT + (unsigned)((64 * kc + 8 * (lane >> 3) + ((lane >> 2) & 1)) * 4096 + (32 * kr + 8 * (lane & 3)) * 2);
        int kbw = L_KB + w * 1024 + (lane & 7) * 128 + (((lane >> 3) * 8) ^ ((lane & 7) * 16));
        int kbr = L_KB + w * 1024 + (lane >> 3) * 128 + (((lane & 7) * 16) ^ ((lane >> 3) * 16));
        const unsigned dum = (OOB & 1) ? kNone : OFF_DUM + (unsigned)((b * 4 + w) * 128 + (lane & 7) * 16);
        unsigned fko1 = OFF_FK + (unsigned)((PKF & 1) ? (64 * kc + kr) * 32 : (10 * kr + kc) * 128);
        if (!kw) kvo = dum;
        if (b >= 200) { for0 = dum; for1 = dum; cvo = (unsigned)((b * 4 + w) * 128); }
        wro = (unsigned)(chn * 4096 + lc * 16);
        cv0 = cv;
        pin(kvo); pin(kbw); pin(kbr); pin(fko1);
        pin(xlane0); pin(xlane1); pin(cvo); pin(for0); pin(for1); pin(sto); pin(swo); pin(wro);
        SCHED();
        auto pre = LAMBDA(i_) {
            constexpr int it = p1_dma(i_.value);
            SCHED();
            DMA_M0(it);
            asm volatile("s_nop 0");
            DMA_LD(it);
            SCHED();
        };
        fko = OFF_FK + (unsigned)((PKF & 1) ? ((chn & ~63) + (cv ? lane : 0)) * 32 : ((cv ? lane : 0) * 10 + (chn >> 6)) * 128);
        { const int e_ = (lane & 7) < w ? w : (lane & 7) > w + 4 ? w + 4 : (lane & 7);
          fwo = OFF_FW + (unsigned)((PKF & 2) ? ((5 * nt2 + (lane < 40 ? lane >> 3 : 4)) * 20 + 4 * w + e_) * 32 : ((5 * nt2 + (lane < 25 ? lane / 5 : 0)) * 20 + 5 * w + (lane < 25 ? lane % 5 : 0)) * 128); }
        w2o = OFF_W2F + (unsigned)((nt2 * 40 + 10 * w) * 5120 + lane * 16);
        int ime = SC + (w < 3 ? w * REGSZ + CV_E : 43264) + 1008 - 16 * lane, imo = SC + (w < 3 ? w * REGSZ + CV_O + 4 : 47360) + 1008 - 16 * lane, im3 = w < 3 ? ime + 3072 : imo;
        int tca = L_TAB + c * 136, tcb = tca + 8 + 32 * w + 8 * q, tcc = L_TAB + (32 + (c >> 1)) * 136, tcd = tcc + 8 + 32 * w + 8 * q, tcl = L_TAB + (lane < 40 ? lane : 39) * 136, coa = L_CO + w * REGSZ + q * 64, rsa = l_rs(0, 0) + w * 256 + lane * 4;
        unsigned sgn = (c & 1) ? 0u : 0x80000000u;
        fko1 = kw && lane < 32 ? fko1 : OFF_FW + (unsigned)((b * 4 + w) * ((PKF & 2) ? 32 : 128));
        pin(fko); pin(fwo); pin(w2o); pin(fko1); pin(ime); pin(imo); pin(im3); pin(tca); pin(tcb); pin(tcc); pin(tcd); pin(tcl); pin(coa); pin(rsa); pin(sgn);
        SCHED();
        auto cw_ = [&]() __attribute__((always_inline)) {
            dma_n<L_EAR>(cvo, p.Cre, xl); dma_n<L_EAR + 1024>(cvo, p.Cim, xl);
            dma_1<L_EAR + 2560, 0>((unsigned)(160 * nt2 + (lane < 39 ? lane : 39) * 4), p.Dskip, xl);
            ld_wr(wr, 32 * w + i32, wro, p.ssmk);
            gldu16(sdq, (unsigned)chn * 2u, p.ssmD);
        };
        if (SMP == 1) small_();
        static_assert(SMP == 0 || SAF > 0, "");
        if (SAF == 1) static_for<5>(LAMBDA(i) { gld<256 * i.value>(sa[i.value], sao, sab); });
        if (CWP == 0) cw_();
        STAMP(33)
        asm volatile("s_waitcnt vmcnt(%5)\n\ts_cmp_ge_u32 %4, 80\n\ts_cbranch_scc0 1f\n\ts_waitcnt vmcnt(%6)\n1:" : "+v"(uL[0]), "+v"(uL[1]), "+v"(uL[2]), "+v"(cpf0) : "s"(32 * w + 31 - i32), "i"(14 + (SAF < 2 ? 5 : 0) + (CWP ? 0 : 8)), "i"(3 + (SAF < 2 ? 5 : 0) + (CWP ? 0 : 8)) : "memory", "scc");
        pin(uM[0]); pin(uM[1]); pin(uM[2]);
        {
            f32x2 lam, co;
            disc(uL[0], uL[1], uL[2], lam, co);
            *(f32x2*)(smem + L_CO + w * REGSZ + (lane & 31) * 8) = 2.0f * co;
        }
        SCHED();
        STAMP(35)
        if (SAF == 2) static_for<5>(LAMBDA(i) { gld<256 * i.value>(sa[i.value], sao, sab); });
        if (CWP == 1) cw_();
        if (APOS == 0) addrs(std::integral_constant<int, 0>{}, lane, w, b);
        if (SAF == 3) static_for<5>(LAMBDA(i) { gld<256 * i.value>(sa[i.value], sao, sab); });
        if (CWP == 2) cw_();
        v4u kvt[4];
        unsigned ka[8], kt[8], se[8], vs[8], ko4[4], one2 = 0x3F803F80u;
        pin(one2);
        auto kvop = LAMBDA(n_) {
            constexpr int n = n_.value, h = n ? (n - 1) / 29 : -1, u = n ? (n - 1) % 29 : 26;
            if constexpr (u < 2) d2x4(ka + 4 * u, ax[0][h], ax[1][h], ax[2][h], ax[3][h], se[u], se[u], se[u], se[u]);
            else if constexpr (u == 8) { const v4u sk = ax[11 + h]; se[4] = sk[0] >> 16; se[5] = sk[2] & 0xffff0000u; se[6] = sk[0] & 0xffffu; se[7] = sk[2] << 16; pin(se[4]); pin(se[5]); pin(se[6]); pin(se[7]); }
            else if constexpr (u < 21) {
                constexpr int v = u < 8 ? u - 2 : u - 3, tp = 1 + v / 6, st = v % 6, e = st & 1;
                if constexpr (st < 2) d2x4(kt + 4 * e, ax[tp][h], ax[tp + 1][h], ax[tp + 2][h], ax[tp + 3][h], se[2 * tp + e], se[2 * tp + e], se[2 * tp + e], se[2 * tp + e]);
                else if constexpr (st < 4) cpx4i(kt + 4 * e, ka + 4 * e);
                else d2x4(ka + 4 * e, kt[4 * e], kt[4 * e + 1], kt[4 * e + 2], kt[4 * e + 3], one2, one2, one2, one2);
            }
            else if constexpr (u == 21) { vs[0] = ax[7][h] & 0xffffu; vs[1] = ax[8][h] << 16; vs[2] = ax[9][h] & 0xffffu; vs[3] = ax[10][h] << 16; pin(vs[0]); pin(vs[1]); pin(vs[2]); pin(vs[3]); }
            else if constexpr (u == 22) { vs[4] = ax[7][h] >> 16; vs[5] = ax[8][h] & 0xffff0000u; vs[6] = ax[9][h] >> 16; vs[7] = ax[10][h] & 0xffff0000u; pin(vs[4]); pin(vs[5]); pin(vs[6]); pin(vs[7]); }
            else if constexpr (u == 23) cpx4(ko4, ka[0], ka[2], ka[4], ka[6], ka[1], ka[3], ka[5], ka[7]);
            else if constexpr (u < 26) { constexpr int e = u - 24; d2x4(kt + 4 * e, ko4[2 * e], ko4[2 * e], ko4[2 * e + 1], ko4[2 * e + 1], vs[4 * e], vs[4 * e + 1], vs[4 * e + 2], vs[4 * e + 3]); }
            else if constexpr (u == 26) {
                if constexpr (h < 3) { const v4u sk = ax[12 + h]; se[0] = sk[1] >> 16; se[1] = sk[3] & 0xffff0000u; se[2] = sk[1] & 0xffffu; se[3] = sk[3] << 16; pin(se[0]); pin(se[1]); pin(se[2]); pin(se[3]); }
                else asm volatile("s_nop 2");
            }
            else if constexpr (u == 27) {
                cpx4(ko4, kt[0], kt[2], kt[4], kt[6], kt[1], kt[3], kt[5], kt[7]);
                *(v2u*)(smem + kbw) = v2u{ko4[0], ko4[1]};
                *(v2u*)(smem + (kbw ^ 64)) = v2u{ko4[2], ko4[3]};
            }
            else kvt[h] = *(const v4u*)(smem + kbr);
            SCHED();
        };
        auto deal = [&](auto a_, auto b_, auto n_, auto i_) __attribute__((always_inline)) {
            constexpr int a = a_.value, m = b_.value - a, lo = m * i_.value / n_.value, hi = m * (i_.value + 1) / n_.value;
            static_for<hi - lo>(LAMBDA(e_) { pre(std::integral_constant<int, a + lo + e_.value>{}); });
        };
#define IC(n) std::integral_constant<int, (n)>{}
        static_for<kQ0>(pre);
        asm volatile("s_waitcnt vmcnt(%11)" : "+v"(ax[0]), "+v"(ax[1]), "+v"(ax[2]), "+v"(ax[3]), "+v"(ax[4]), "+v"(ax[5]), "+v"(ax[6]), "+v"(ax[7]), "+v"(ax[8]), "+v"(ax[9]),
                     "+v"(ax[10]) : "i"(13 + kQ0) : "memory");
#pragma unroll
        for (int i = 0; i < 4; ++i) ax[11 + i] = *(const v4u*)(smem + eas + i * 16);
        pin(nonce);
        STAMP(30)
        unsigned tch = 0u;
        auto kvs = LAMBDA(n_) {
            site<1000 + n_.value>(); SCHED();
            kvop(n_);
            if constexpr (n_.value >= KD0) deal(IC(kQ0), IC(kQ0 + kQ1), IC(117 - KD0), IC(n_.value - KD0));
        };
        static_assert((KST - 1) % 29 == 0, "");
        if (kw) static_for<KST>(kvs);
        else {
            if (w == 3) {
                f32x2 lam, co, lp[16];
                disc(uM[0], uM[1], uM[2], lam, co);
                lp[0] = cmul(lam, lam); lp[0] = cmul(lp[0], lp[0]); lp[1] = cmul(lp[0], lp[0]);
                lp[0] = f32x2{1.f, 0.f};
                lp[2] = cmul(lp[1], lp[1]); lp[3] = cmul(lp[2], lp[1]); lp[4] = cmul(lp[2], lp[2]);
                const f32x2 z2 = {0.f, 0.f};
                f32x2 t4[4];
                cfma4(t4, lp[4], lp[1], lp[2], lp[3], lp[4], z2, z2, z2, z2);
                lp[5] = t4[0]; lp[6] = t4[1]; lp[7] = t4[2]; lp[8] = t4[3];
                cfma4(t4, lp[8], lp[1], lp[2], lp[3], lp[4], z2, z2, z2, z2);
                lp[9] = t4[0]; lp[10] = t4[1]; lp[11] = t4[2]; lp[12] = t4[3];
                cfma4(t4, lp[8], lp[5], lp[6], lp[7], lp[7], z2, z2, z2, z2);
                lp[13] = t4[0]; lp[14] = t4[1]; lp[15] = t4[2];
                f32x2* tl = (f32x2*)(smem + tcl);
                tl[0] = lam;
#pragma unroll
                for (int i = 0; i < 16; ++i) tl[1 + i] = lp[i];
                SCHED();
            }
            static_for<kQ1a>(LAMBDA(i_) { pre(IC(kQ0 + i_.value)); });
            undef4(kvt);
            asm volatile("" : "=v"(se[0]), "=v"(se[1]), "=v"(se[2]), "=v"(se[3]));
        }
        tag0 = __builtin_amdgcn_readlane(nonce, 0) + 1u; tag1 = __builtin_amdgcn_readlane(nonce + (tag0 == 0u ? 1u : 0u), 1);
        tagv = v2u{tag0, tag1}; pin(tagv);
        {
            asm volatile("s_waitcnt vmcnt(%5)" : "+v"(sa[0]), "+v"(sa[1]), "+v"(sa[2]), "+v"(sa[3]), "+v"(sa[4]) : "i"(8 + kQ0 + kQ1a) : "memory");
            float s1[2] = {0.f, 0.f}, s2[2] = {0.f, 0.f};
#pragma unroll
            for (int i = 0; i < 5; ++i)
#pragma unroll
                for (int e = 0; e < 4; ++e) { s1[e & 1] = dot2a(sa[i][e], one2, s1[e & 1]); s2[e & 1] = dot2a(sa[i][e], sa[i][e], s2[e & 1]); }
            const float a1 = rowsum16(s1[0] + s1[1]), a2 = rowsum16(s2[0] + s2[1]);
            const float mean = a1 * (1.0f / D_);
            const float var = fmaxf(a2 * (1.0f / D_) - mean * mean, 0.f);
            const float ri = __builtin_amdgcn_rcpf(rbf(__builtin_amdgcn_sqrtf(rbf(rbf(var) + 1e-5f))));
            v2u sv = {__float_as_uint(w >= 2 ? a1 : c ? -ri * rbf(mean) : ri), tag0};
            int o0 = w < 2 ? (mt1 * 16 + nt1) * 256 + 32 * w : ((mt1 >> 2) * 16 + nt1) * 256 + 112 + (mt1 & 3) * 16 + 8 * w, o1 = w < 2 ? 8 : 16384, o2 = w < 2 ? 64 : 0;
            pins(o0); pins(o1); pins(o2);
            unsigned so_ = c < 2 ? OFF_ST + (unsigned)(o0 + q * o1 + c * o2) : kNone;
            pin(sv); pin(so_);
            st64(sv, rs, so_);
            SCHED();
        }
        bool kw2 = kw;
        if (kw2) {
            static_for<117 - KST>(LAMBDA(n_) { kvs(IC(KST + n_.value)); });
        } else {
            if (w == 3) {
            const int l = lane, l3 = l & 3;
            unsigned a = OFF_SG + (unsigned)((((((l >> 3) & 1) * 32 + mt2) * 4 + ((l >> 1) & 3)) * 8 + nt2) * 256 + (l & 1) * 128);
            { unsigned t_ = OFF_EG + (unsigned)((l >= 21 ? 163840 - 640 : 0) + (mt1 * 16 + nt1) * 640 + (l < 26 ? l - 16 : 5) * 128); pin(t_); a = l >= 16 ? t_ : a; }
            { unsigned t_ = OFF_PS + (unsigned)(((mt2 * 4 + l3) * 8 + nt2) * 128); pin(t_); a = l >= 28 ? t_ : a; }
            { unsigned t_ = OFF_FH + (unsigned)((((((l >> 2) & 1) * 32 + 2 * mt1 + (l3 >> 1)) * 4 + (nt1 >> 2)) * 8 + (nt1 & 3) * 2 + (l3 & 1)) * 128); pin(t_); a = l >= 32 ? t_ : a; }
            { unsigned t_ = OFF_FX + (unsigned)(((mt1 * 4 + 2 * (mt2 & 1) + (l3 >> 1)) * 16 + nt2 * 2 + (l3 & 1)) * 128); pin(t_); a = l >= 40 ? t_ : a; }
            { unsigned t_ = OFF_FG + (unsigned)((nt2 * 96 + i32 + 16 * ((l3 + 1) & 6)) * 128); pin(t_); a = l >= 48 ? t_ : a; }
            const unsigned big = (l & 7) < 2 ? OFF_HF + (unsigned)((l & 1) * 5242880 + (8 * mt1 * 160 + 10 * nt1) * 256) : (l & 7) == 2 ? OFF_X2F + (unsigned)(2 * mt2 * 40960 + 2560 * nt2)
                               : (l & 7) < 5 ? OFF_KVT + (unsigned)((l & 1) * 2621440 + (80 * nt2 + i32) * 4096) : OFF_W2F + (unsigned)(nt2 * 204800 + ((l & 7) - 5) * 98304);
            a = l >= 56 ? big : a;
            tch = a;
        }
            static_for<kQ1 - kQ1a>(LAMBDA(i_) { pre(IC(kQ0 + kQ1a + i_.value)); });
            if (w == TW) tour<1>();
            skip<SKIP0>();
            undef4(kvt);
        }
        SCHED();
        STAMP(34)
        if (APOS == 1) addrs(std::integral_constant<int, 0>{}, lane, w, b);
        v4u orr, oi;
        unsigned ie[4][4], io[4][4], iw;
        auto img = LAMBDA(n_) {
            constexpr int i = n_.value / 10, u = n_.value % 10;
            if constexpr (u == 0) { if constexpr (i > 0) ror1(iw, wr[i - 1].w); else { iw = 0u; pin(iw); } }
            else if constexpr (u < 5) rot1(ie[i][u - 1], wr[i][4 - u]);
            else if constexpr (u == 5) shr1b(iw, wr[i].w);
            else if constexpr (u < 9) mix1(io[i][u - 6], wr[i][9 - u], wr[i][8 - u]);
            else mix1(io[i][3], wr[i].x, iw);
        };
        auto tap0 = [&]() __attribute__((always_inline)) {
            wr[0].x = cvtpk(lo16f(wr[0].x) + (lane ? 0.f : lo16f(sdq)), hi16f(wr[0].x));
            pin(wr[0]);
            SCHED();
        };
        {
            static_assert(kQ0 + kQ1 <= 18, "");
            vm_wait<6 + kQ0 + kQ1>();
            cr[0] = *(const v4u*)(smem + ear); cr[1] = *(const v4u*)(smem + ear + 1024);
            static_for<5>(LAMBDA(i) { lds2a<22 + 2 * i.value, 2560 + 32 * i.value>(lds0 + (unsigned)(L_EAR + w * REGSZ + q * 8)); });
            asm volatile("s_waitcnt lgkmcnt(0)" : "+v"(cr[0]), "+v"(cr[1])::"memory");
            static_for<kQ2>(LAMBDA(i_) { pre(IC(kQ0 + kQ1 + i_.value)); });
            static_for<4>(LAMBDA(e_) {
                constexpr int e = e_.value;
                const f32x4 co = *(const f32x4*)(smem + coa + e * 16);
                const f32x2 o0 = lo16f(cr[0][e]) * f32x2{co.x, -co.y} - lo16f(cr[1][e]) * f32x2{co.y, co.x};
                const f32x2 o1 = hi16f(cr[0][e]) * f32x2{co.z, -co.w} - hi16f(cr[1][e]) * f32x2{co.w, co.z};
                orr[e] = cvtpk(o0.x, o1.x); oi[e] = cvtpk(o0.y, o1.y);
            });
            pin(orr); pin(oi);
            SCHED();
            static_for<NPD - kQ0 - kQ1 - kQ2>(LAMBDA(i_) { pre(IC(kQ0 + kQ1 + kQ2 + i_.value)); });
            for (int i = 0; i < 4; ++i) st128(kvt[i], rs, kvo + i * 8192);
            st128(orr, rs, for0); st128(oi, rs, for1);
        }
        SCHED();
        STAMP(23)
        if (w == 3) tour<7>();
        asm volatile("s_waitcnt vmcnt(%5) lgkmcnt(0)" : "+v"(wr[0]), "+v"(wr[1]), "+v"(wr[2]), "+v"(wr[3]), "+v"(sdq) : "i"(P1_PRE - 1 - p1_pos(3)) : "memory");
        __builtin_amdgcn_s_barrier();
        asm volatile("" ::: "memory");
        STAMP(24)
        site<10>();
        v4u wf[2][5], af[2][2][2];
#pragma unroll
        for (int nb = 0; nb < 5; ++nb) wf[0][nb] = WF(nb, 0);
#pragma unroll
        for (int i = 0; i < 4; ++i) af[0][i >> 1][i & 1] = *(const v4u*)(smem + ((i >> 1) ? xlane1 : xlane0) + (i & 1) * 2048);
        SCHED();
        tap0();
        site<11>();
        const char *kvtb = p.ws + OFF_KVT, *w2b0[(NW2P + 4) / 5];
        pins(kvtb);
#pragma unroll
        for (int i = 0; i < (NW2P + 4) / 5; ++i) { w2b0[i] = p.ws + 2048 + i * 5120; pins(w2b0[i]); }
        v2u sg, tc[2], fk, fw;
        v4u sw0;
        v2u zq[2];
        float rr_[8];
        unsigned sx[4], sz[4];
        u64 odd = 0xAAAAAAAAAAAAAAAAull;
        pins(odd);
        auto filler = LAMBDA(f_) {
            constexpr int f = f_.value;
            if constexpr (f < 0) {}
            else if constexpr (f < 40) img(IC(f));
            else if constexpr (f == 48) { lam1.r = __uint_as_float(tc[0].x); xor1(lam1.s, tc[0].y, sgn); }
            else if constexpr (f == 49) { lamq.r = __uint_as_float(tc[1].x); xor1(lamq.s, tc[1].y, sgn); }
            else if constexpr (f < 52) zero2(zq[f - 50]);
            else if constexpr (f == 52) { sx[0] = sw0.x; mov1(sx[1], sx[0]); }
            else if constexpr (f == 53) { sz[0] = sw0.z; mov1(sz[1], sz[0]); }
            else if constexpr (f < 56) bc1<f - 54>(rr_[f - 54], sg.x);
            else if constexpr (f == 56) sw16(sx[0], sx[1]);
            else if constexpr (f == 57) sw16(sz[0], sz[1]);
            else if constexpr (f < 60) mov1(sx[f - 56], sx[f - 58]);
            else if constexpr (f < 62) mov1(sz[f - 58], sz[f - 60]);
            else if constexpr (f < 64) sw32(sx[f - 62], sx[f - 60]);
            else if constexpr (f < 66) sw32(sz[f - 64], sz[f - 62]);
            else if constexpr (f < 72) {
                bc1<f - 64>(rr_[f - 64], sg.x);
                if constexpr (f == 66) { SW[0] = __uint_as_float(sx[0]); SW[1] = __uint_as_float(sz[0]); SW[2] = __uint_as_float(sx[1]); SW[3] = __uint_as_float(sz[1]); }
                if constexpr (f == 71) sel1(SW[4], sx[2], sz[2], odd);
            }
            else mulb<8 + (f - 72) % 8>(tt[(f - 72) % 8 / 4][(f - 72) / 8][(f - 72) % 4], sg.x, SW[(f - 72) / 8]);
        };
        auto kstep = LAMBDA(s_) {
            constexpr int s = s_.value, ph = s >> 2, s4 = s & 3, cu = s & 1, nx = cu ^ 1, jj = s >> 1, pp = jj & 1;
            if constexpr (s4 == 3 && ph < 4) {
                constexpr int pw = p1_pos(104 + 5 * (ph + 1)), px = p1_pos(4 * (jj + 1) + 3), pm = pw > px ? pw : px;
                constexpr int vm = p1_lim(p1_cnt(s, 0) - 1 - pm);
                asm volatile("" ::: "memory");
                __builtin_amdgcn_s_waitcnt((vm & 15) | ((vm >> 4) << 14) | 0x70);
                __builtin_amdgcn_s_barrier();
                asm volatile("" ::: "memory");
                STAMP(25 + ph)
                SCHED();
            }
            if constexpr (s == KSC) {
                static_assert(s4 == 3 && p1_pos(203) < p1_pos(4 * (jj + 1) + 3), "");
                pin(sg); pin(sw0);
                if (__builtin_expect(__builtin_amdgcn_ballot_w64(__builtin_amdgcn_bitop3_b32(__builtin_amdgcn_bitop3_b32(sg.y, sw0.y, tag0, 0x7E), sw0.w, tag0, 0xF6) != 0u) != 0ull, 0))
                    WAVE_WAIT({ sg = ld64(rs, sto); sw0 = ld128(rs, swo); ok = ((sg.y ^ tag0) | (sw0.y ^ tag0) | (sw0.w ^ tag0)) == 0u; })
                ORDER();
                STAMP(7)
            }
            if constexpr (s == kW2S && KUS > kW2S) { vm_wait<p1_cnt(s, 0) - 1 - p1_pos(208)>(); SCHED(); }
            if constexpr (s == KUS) {
                static_assert(p1_pos(206) > p1_pos(208), "");
                wait_fkn<p1_cnt(s, 0) - 1 - p1_pos(206)>(fk, fw);
                if (!USP) ld_ur(wro, kvtb);
                SCHED();
            }
            if constexpr (s4 != 3 || ph == 4) lgkm<(s == 0 ? 0 : p1_nl(s - 1, 5 - cu, 10) - 1)>();
            SCHED();
            static_for<10>(LAMBDA(g_) {
                constexpr int g = g_.value, mb = g / 5, k = g % 5, it = p1_item(s, g), nt_ = g < 9 ? p1_item(s, g + 1) : -1;
                constexpr bool d0 = it >= 0 && it < 200, d1 = nt_ >= 0 && nt_ < 200;
                if constexpr (s == 0) mf0(acc[mb][k], af[pp][cu][mb], wf[cu][k]); else mf(acc[mb][k], af[pp][cu][mb], wf[cu][k]);
                SCHED();
                if constexpr (s < 19) {
                    if constexpr (g < 5) wf[nx][g] = WF(g, s + 1);
                    if constexpr (cu == 1 && g == 5) vm_wait<p1_lim(p1_cnt(s, 5) - 1 - p1_pos(4 * (jj + 1) + 3))>();
                    if constexpr (cu == 1 && g >= 5 && g < 9)
                        af[pp ^ 1][(g - 5) >> 1][(g - 5) & 1] = *(const v4u*)(smem + (((g - 5) >> 1) ? xlane1 : xlane0) + ((jj + 1) % 3) * 4096 + ((g - 5) & 1) * 2048);
                }
                if constexpr (cu == 0 && g == XG0 - 1 && s > 0) lgkm<p1_nl(s - 1, 8, 10) + p1_nl(s, 0, XG0 - 1)>();
                constexpr int lq = p1_lds(s, g);
                if constexpr (lq >= 4 && lq < 8) (lq == 4 ? lam1.a : lq == 5 ? lam1.b : lq == 6 ? lamq.a : lamq.b) = *(const f32x2*)(smem + (lq < 6 ? tca : tcb) + (lq & 1) * 2176);
                else if constexpr (lq == 8 || lq == 9) tc[lq - 8] = *(const v2u*)(smem + (lq == 8 ? tcc : tcd));
                else if constexpr (lq == 10) { const f32x2* tp = (const f32x2*)(smem + tcl); l8 = tp[2]; l32 = tp[5]; }
                else if constexpr (lq == 11) l128 = *(const f32x2*)(smem + tcl + 72);
                else if constexpr (lq >= 30 && lq < 33) *(v4u*)(smem + ime + 1024 * (lq - 30)) = v4u{ie[33 - lq][0], ie[33 - lq][1], ie[33 - lq][2], ie[33 - lq][3]};
                else if constexpr (lq >= 33 && lq < 37) *(v4u*)(smem + imo + 1024 * (36 - lq)) = v4u{io[lq - 33][0], io[lq - 33][1], io[lq - 33][2], io[lq - 33][3]};
                else if constexpr (lq == 37) *(v4u*)(smem + im3) = v4u{ie[0][0], ie[0][1], ie[0][2], ie[0][3]};
                else if constexpr (lq == 38) *(unsigned*)(smem + rsa) = sg.x;
                else if constexpr (lq == 39) *(v4u*)(smem + zpa) = v4u{zq[0].x, zq[0].y, zq[1].x, zq[1].y};
                static_for<p1_cap(s, g)>(LAMBDA(e_) { filler(IC(p1_fill(s, g, e_.value))); });
                if constexpr (s == 18 && g == 9) ne2(late, tagv, fk, fw);
                if constexpr (s == 19 && g == 2) { if (__builtin_expect(late != 0ull, 0)) poll2(fko, fwo, p.ws); }
                if constexpr (it >= 0) {
                    if constexpr (d0 && d1) DMA_LD1(it, d1 ? nt_ : 0);
                    else if constexpr (d0) DMA_LD(it);
                    else if constexpr (it == 202) gld2s<0>(sg, sto, p.ws);
                    else if constexpr (it == 203) gld4s<0>(sw0, swo, p.ws);
                    else if constexpr (it == 206) gld2s<0>(fk, fko, p.ws);
                    else if constexpr (it == 208) gld2s<0>(fw, fwo, p.ws);
                    else if constexpr (it == 209) touch_(tch, p.ws);
                    else if constexpr (it >= 210 && it < 214) ld_ur1<it - 210>(wro, kvtb);
                    else if constexpr (it >= 500) gld_w2<it % 50>(w2o, w2b0[(it % 50) / 5]);
                    else { vm_wait<p1_cnt(s, g) - P1_PRE>(); st64(tagv, rs, fko1); }
                }
                if constexpr (d1 && !d0) DMA_M0(d1 ? nt_ : 0);
                site<2000 + 10 * s + g>();
                SCHED();
            });
        };
        static_for<20>(kstep);
        static_for<NFILL - p1_fill(19, 10, 0)>(LAMBDA(i_) { filler(IC(p1_fill(19, 10, 0) + i_.value)); });
        SCHED();
        rr[0] = f32x4{rr_[0], rr_[1], rr_[2], rr_[3]}; rr[1] = f32x4{rr_[4], rr_[5], rr_[6], rr_[7]};
        ACC_FENCE10(acc[0], acc[1]);
        STAMP(1)
    }

    auto layer_body = LAMBDA(layer_) {
        constexpr int layer = layer_.value;
        const int sga = sgn_;
        const bool cv = layer == 0 && cv0;
        v2u fh;
        unsigned ks[10];
        f32x4 ca[8], a2[4][5];
        float rsr, rsm;
        St bv[8];
        {
            St a0, a1;
            static_for<2>(LAMBDA(m_) {
                constexpr int mb = m_.value;
#pragma unroll
                for (int i = 0; i < 4; ++i) fma2p(bv[4 * mb + i].a, acc[mb][0][i], acc[mb][1][i], rr[mb][i], tt[mb][0][i], tt[mb][1][i]);
#pragma unroll
                for (int i = 0; i < 4; ++i) fma2p(bv[4 * mb + i].b, acc[mb][2][i], acc[mb][3][i], rr[mb][i], tt[mb][2][i], tt[mb][3][i]);
                const f32x4 v = __builtin_elementwise_fma(acc[mb][4], rr[mb], f32x4{tt[mb][4][0], tt[mb][4][1], tt[mb][4][2], tt[mb][4][3]});
#pragma unroll
                for (int i = 0; i < 4; ++i) bv[4 * mb + i].c = v[i];
                SCHED();
                if constexpr (mb == 0) run3(a0, bv[0], bv + 1, lam1); else run4(a1, a0, bv + 4, lam1);
                SCHED();
            });
            *(f32x2*)(smem + sga) = a1.a; *(f32x2*)(smem + sga + 256) = a1.b; *(float*)(smem + sg4 + 512) = a1.c;
        }
        SCHED();
        const int w_ = w0;
        {
        const int w = w_, mt1 = mt1_;
        if (layer == 0 && __builtin_expect(late != 0ull, 0)) {
            unsigned f0_, f1_, f2_, f3_;
            asm volatile("s_waitcnt vmcnt(0)\n\t" AR("%0", 36, 0) AR("%1", 36, 1) AR("%2", 36, 2) AR("%3", 36, 3) : "=v"(f0_), "=v"(f1_), "=v"(f2_), "=v"(f3_)::"memory");
            late = __builtin_amdgcn_ballot_w64(((f0_ ^ tag0) | (f1_ ^ tag1) | (f2_ ^ tag0) | (f3_ ^ tag1)) != 0u);
            ORDER();
            if (late == 0ull) KW_REQ()
        }
        asm volatile("s_waitcnt lgkmcnt(0)" ::: "memory");
        __builtin_amdgcn_s_barrier();
        asm volatile("" ::: "memory");
        STAMP(layer ? 14 : 2)
#pragma unroll
        for (int s = 0; s < 10; ++s) ks[s] = (unsigned)s * 1024u;
        constexpr int NW2A = NW2P + 7 * kCR1 + kXA, NW3 = NW2P + kN3E + kN3;
        auto w2ld = LAMBDA(i_) {
            constexpr int i = i_.value;
            gld_w2<i>(w2o, p.ws + 2048 + ks[i / 5] * 5u);
        };
        auto w2a = LAMBDA(i_) {
            constexpr int k = i_.value;
            if constexpr (k < kXA) { w2ld(std::integral_constant<int, NW2P + 2 * k>{}); w2ld(std::integral_constant<int, NW2P + 2 * k + 1>{}); }
            else w2ld(std::integral_constant<int, NW2P + kXA + k>{});
        };
        unsigned xtb = (unsigned)(size_t)(LAS char*)smem + (unsigned)(l_tb(0, 0) + w * 5120);
        auto w2x = LAMBDA(i_) {
            constexpr int i = NW2A + i_.value;
            if constexpr (i < 50) w2ld(std::integral_constant<int, i>{});
            else if constexpr (XDM) { if constexpr (i < 53) dma_x<(i - 50) * 1024, (i < 52 ? (i - 50) * 64 : 96)>(xso, p.x0, xtb); }
            else if constexpr (i < 52) lda4<40 + 4 * (i - 50), (i - 50) * 64>(xso, p.x0);
            else if constexpr (i == 52) lda4<48, 128>(xso - 2u * (unsigned)(bb & 16), p.x0);
        };
        auto w2b = LAMBDA(i_) {
            constexpr int k = i_.value - 7 * kCR1;
            w2x(std::integral_constant<int, k>{});
            if constexpr (k == kFHK) { vm_wait<(NW2A + kFHK + 1 <= 53 ? kFHK + 1 : 53 - NW2A)>(); st64(tagv, rs, fho1); STAMP(39) }
            if constexpr (k == kFHK - 1) { STAMP(38) }
            if constexpr (k == 7 * (8 - kCR1) - kFHP) poll1(fho, p.ws);
            static_assert(NW2A + 7 * (8 - kCR1) >= 53 && NW2A <= 50 && NW3 <= NW2A, "");
        };
        auto partb = [&]() __attribute__((always_inline)) {
            const float* sp = (const float*)(smem + spa);
            rsr = sp[0]; rsm = sp[8];
            if (layer == 1) { pin(rsr); pin(rsm); }
            if (layer == 1) {
                static_for<5>(LAMBDA(n_) {
                    constexpr int nb = n_.value;
                    unsigned d0, d1;
                    rda<22 + 2 * nb>(d0); rda<23 + 2 * nb>(d1);
                    a2[3][nb] = xin[nb] + (xin[nb] * rsr + rsm) * unpk4(v2u{d0, d1}); pin(a2[3][nb]);
                });
            }
        };
        constexpr int LBW = layer ? 2 : 3;
        if (w == 3) {
            site<3 + 10 * layer>(); SCHED();
            f32x4 s4[8];
#pragma unroll
            for (int i = 0; i < 8; ++i) { s4[i] = *(const f32x4*)(smem + spl + (l_sg(layer, i & 3) - SC + (i >> 2) * 640)); SCHED(); }
            f32x2 sv[16], x[4], P[4][4];
#pragma unroll
            for (int i = 0; i < 16; ++i) { const f32x4 t = s4[(i >> 2) + 2 * (i & 2)]; sv[i] = (i & 1) ? f32x2{t.z, t.w} : f32x2{t.x, t.y}; }
            unsigned er, ei;
            {
                f32x2 u[4];
                cfma4n(u, l8, sv[0], sv[4], sv[8], sv[12], sv[1], sv[5], sv[9], sv[13]);
                agg(er, ei, x, P[0][2], u, sv, l8, l32, l128);
            }
            st128(v4u{er, tag0, ei, tag0}, rs, es);
            SCHED();
            P[0][0] = f32x2{0.f, 0.f}; P[0][1] = x[0]; P[0][3] = cfma(l32, P[0][2], x[2]);
            if (layer == 0) { FW_OK() static_for<kN3E>(LAMBDA(i_) { w2ld(std::integral_constant<int, NW2P + i_.value>{}); }); }
#pragma unroll
            for (int kk = 1; kk < 4; ++kk) cfma4(P[kk], l8, P[kk - 1][0], P[kk - 1][1], P[kk - 1][2], P[kk - 1][3], sv[kk - 1], sv[kk + 3], sv[kk + 7], sv[kk + 11]);
#pragma unroll
            for (int i = 0; i < 8; ++i) { const f32x2 a = P[2 * (i >> 2)][i & 3], b_ = P[2 * (i >> 2) + 1][i & 3]; *(f32x4*)(smem + spl + (l_sg(layer, i & 3) - SC + (i >> 2) * 640)) = f32x4{a.x, a.y, b_.x, b_.y}; }
            partb();
        }
        int w1_ = w, w3_ = w;
        if (layer == 1) { pins(w1_); pins(w3_); }
        if (layer == 1 && w1_ < LBW) { site<4 + 10 * layer>(); SCHED(); partb(); if constexpr (layer == 1) { if (w == 0) tour<6>(); } }
        if (w3_ == LBW) {
            site<5 + 10 * layer>(); SCHED();
            f32x2 wg[17], t4[4];
            unsigned ea[15], tg[15];
            v4u eg[15];
            constexpr bool early = (LBE >> layer) & 1;
            const f32x2 z2 = {0.f, 0.f};
#pragma unroll
            for (int i = 0; i < 15; ++i) { ea[i] = (unsigned)(mt1 - 1 - i) * 10240u; pins(ea[i]); ea[i] = ea[i] < kNone ? ea[i] : kNone; pins(ea[i]); }
            if constexpr (early) {
                __builtin_amdgcn_s_sleep(layer ? KLB1 : KLB0);
#pragma unroll
                for (int i = 0; i < 15; ++i) eg[i] = ld128s(rs, eo, ea[i]);
                SCHED();
            }
            wg[1] = cmul(l128, l128); wg[2] = cmul(wg[1], wg[1]); wg[3] = cmul(wg[2], wg[1]); wg[4] = cmul(wg[2], wg[2]);
#pragma unroll
            for (int i = 4; i < 16; i += 4) {
                cfma4(t4, wg[i], wg[1], wg[2], wg[3], wg[4], z2, z2, z2, z2);
#pragma unroll
                for (int e = 0; e < 4; ++e) wg[i + 1 + e] = t4[e];
            }
#pragma unroll
            for (int i = 0; i < 15; ++i) { if (i) pin(wg[i]); tg[i] = mt1 - 1 - i < 0 ? 0u : tag0; pins(tg[i]); }
            if (layer == 1) partb();
            if constexpr (layer == 0) tour<3>();
            f32x2 H0, H1;
            unsigned spins = dead ? kSpinCap : 0u;
            if constexpr (layer == 1) room<1800>();
            if constexpr (!early) __builtin_amdgcn_s_sleep(layer ? KLB1 : KLB0);
            SCHED();
            auto eval = [&](const v4u (&e)[15]) __attribute__((always_inline)) {
                unsigned x = __builtin_amdgcn_bitop3_b32(e[0].y, e[0].w, tg[0], 0x7E);
                H0 = f32x2{__uint_as_float(e[0].x), __uint_as_float(e[0].z)}; H1 = f32x2{0.f, 0.f};
#pragma unroll
                for (int i = 1; i < 15; i += 2) {
                    x |= __builtin_amdgcn_bitop3_b32(e[i].y, e[i].w, tg[i], 0x7E) | __builtin_amdgcn_bitop3_b32(e[i + 1].y, e[i + 1].w, tg[i + 1], 0x7E);
                    pin(x);
                    gacc2(H0, H1, wg[i], wg[i + 1], e[i], e[i + 1]);
                    SCHED();
                }
                *(f32x2*)(smem + spl + (L_CW - SC)) = H0 + H1;
                return __builtin_amdgcn_ballot_w64(x != 0u) != 0ull;
            };
            bool again = true;
            if constexpr (early) again = eval(eg);
            if (again) {
                bool miss;
#pragma nounroll
                do {
                    asm volatile("" ::: "memory");
                    v4u e2[15];
#pragma unroll
                    for (int i = 0; i < 15; ++i) e2[i] = ld128s(rs, eo, ea[i]);
                    SCHED();
                    miss = eval(e2);
                    if (__builtin_expect(miss, 0) && ++spins > kSpinCap) { dead = true; miss = false; }
                } while (miss);
            }
            ORDER();
            STAMP(layer ? 36 : 37)
            if (layer == 0) {
                static_for<NW3 - NW2P - kN3E>(LAMBDA(i_) { w2ld(std::integral_constant<int, NW2P + kN3E + i_.value>{}); });
            }
        }
        int w2_ = w;
        pins(w2_);
        if (layer == 0) UNDEF8(ca);
        if (layer == 0 && w2_ != LBW) {
            if (cv) {
                site<2>(); SCHED();
                partb();
                {
                    FW_OK()
                    st_ur((unsigned)(size_t)(LAS char*)smem + (unsigned)uwa);
                    CV_LOAD_A(cA, 0, 0, 0) CV_LOAD_A(cA, 0, 0, 1)
                    SCHED();
                    static_for<8>(LAMBDA(a_) { constexpr int a = a_.value; CV_LOAD_B(cB, a, 0) SCHED(); });
                    CV_LOAD_A(cA, 1, 0, 0) CV_LOAD_A(cA, 1, 0, 1)
                    SCHED();
                }
                STAMP(32)
                conv_part<0, kCR1, CPF>(ca, cA, cB, smem, ab, bb, w2a, LAMBDA(i_) {});
            } else {
                if constexpr (layer == 0) tour<5>();
                FW_OK()
                static_for<NW2A - NW2P>(LAMBDA(i_) { w2ld(std::integral_constant<int, NW2P + i_.value>{}); __builtin_amdgcn_s_sleep(1); });
                partb();
                if constexpr (layer == 0) room<3900>();
            }
        }
        asm volatile("s_waitcnt lgkmcnt(0)" ::: "memory");
        __builtin_amdgcn_s_barrier();
        asm volatile("" ::: "memory");
        STAMP(layer ? 15 : 4)
        {
            St cw, ex, hv[8];
            cw.a = *(const f32x2*)(smem + cwa); cw.b = *(const f32x2*)(smem + cwa + 256); cw.c = *(const float*)(smem + cw4 + 512);
            ex.a = *(const f32x2*)(smem + sga); ex.b = *(const f32x2*)(smem + sga + 256); ex.c = *(const float*)(smem + sg4 + 512);
            keep14(hv, cw, ex, bv, lamq, lam1);
            keep4(hv + 4, hv[3], bv + 4, lam1);
            SCHED();
            v2u ta[5], td[5];
            f32x2 hb[9];
            static_for<5 + SBL>(LAMBDA(r_) {
                constexpr int r = r_.value, rd = r < 2 ? r : r == 2 ? 4 : r - 1, ro_ = r - SBL < 2 ? r - SBL : r - SBL == 2 ? 4 : r - SBL - 1;
                if constexpr (r < 5) {
                    float e[8];
                    unsigned o[4];
#pragma unroll
                    for (int i = 0; i < 8; ++i) e[i] = rd == 4 ? hv[i].c : rd < 2 ? hv[i].a[rd & 1] : hb[i + 1][rd & 1];
                    if constexpr (r == 0) b3cv(hb, o, cw.b, ex.b, bv[0].b, bv[1].b, lamq.b, lam1.b, e);
                    else if constexpr (r < 3) b3cv(hb + 3 * r, o, hb[3 * r - 1], bv[3 * r - 1].b, bv[3 * r].b, bv[3 * r + 1].b, lam1.b, lam1.b, e);
                    else for (int i = 0; i < 4; ++i) o[i] = cvtpk(e[2 * i], e[2 * i + 1]);
                    *(v4u*)(smem + tbw + rd * 1024) = v4u{o[0], o[1], o[2], o[3]};
                    SCHED();
                    if constexpr (r < 3) { bfin(hb[3 * r + 2], hb[3 * r + 1], lam1.b); SCHED(); }
                    ta[rd] = __builtin_bit_cast(v2u, trread(smem, (rd < 4 ? tbp : tbr) + rd * 1024));
                    td[rd] = __builtin_bit_cast(v2u, trread(smem, (rd < 4 ? tbq + 256 : tbr + 512) + rd * 1024));
                    SCHED();
                }
                if constexpr (r >= SBL) { st128(v4u{ta[ro_].x, ta[ro_].y, td[ro_].x, td[ro_].y}, rs, hso + ro_ * 256); SCHED(); }
            });
        }
#pragma unroll
        for (int s = 0; s < 10; ++s) pins(ks[s]);
        if (layer == 1) { DRAIN(); st64(tagv, rs, fho1); }
        STAMP(layer ? 16 : 5)
        if (cv) {
            if constexpr (layer == 0) site<6>();
            SCHED();
            unsigned gp[4];
            auto gst = LAMBDA(i_) {
                constexpr int i = i_.value >> 2, u = i_.value & 3;
                if constexpr (u < 2) cp2(gp[2 * u], gp[2 * u + 1], ca[2 * i + u]);
                else if constexpr (u == 2) swp2n(gp[0], gp[2], gp[1], gp[3]);
                else st128(v4u{gp[0], gp[1], gp[2], gp[3]}, rs, gso + i * 1024);
            };
            if constexpr (!CPF) {
                CV_LOAD_A(cA, 0, kCR1, 0) CV_LOAD_A(cA, 0, kCR1, 1)
                SCHED();
                static_for<8>(LAMBDA(a_) { constexpr int a = a_.value; CV_LOAD_B(cB, a, kCR1) SCHED(); });
                static_for<7>(LAMBDA(a_) { constexpr int a = a_.value + 1; CV_LOAD_A(cA, a, kCR1, 0) CV_LOAD_A(cA, a, kCR1, 1) });
                SCHED();
            }
            conv_part<kCR1, 8, false>(ca, cA, cB, smem, ab, bb, w2b, LAMBDA(g_) {
                constexpr int g = g_.value;
                if constexpr (GIN > 0 && (g == 14 || g == 16 || g == 18 || g == 20)) gst(std::integral_constant<int, (g - 14) / 2>{});
                if constexpr (GIN > 1 && (g == 26 || g == 28 || g == 30 || g == 32)) gst(std::integral_constant<int, 4 + (g - 26) / 2>{});
            });
            asm volatile("" : "+v"(ca[0]), "+v"(ca[1]), "+v"(ca[2]), "+v"(ca[3]), "+v"(ca[4]), "+v"(ca[5]), "+v"(ca[6]), "+v"(ca[7]));
            static_for<16 - 4 * GIN>(LAMBDA(i_) {
                constexpr int n = 4 * GIN + i_.value;
                if constexpr (n == 9 && GIN == 2) asm volatile("s_nop 0");
                if constexpr ((n & 3) == 2) asm volatile("s_nop 1");
                gst(std::integral_constant<int, n>{}); SCHED();
            });
        } else if (layer == 0) {
            static_for<53 - NW2A>(LAMBDA(i_) { w2x(i_); __builtin_amdgcn_s_sleep(1); if constexpr (i_.value == 52 - NW2A) { vm_wait<53 - NW2A>(); st64(tagv, rs, fho1); } });
            if (w == 3) { static_for<NW2A - NW3>(LAMBDA(i_) { w2ld(std::integral_constant<int, NW3 + i_.value>{}); __builtin_amdgcn_s_sleep(1); }); if constexpr (layer == 0) tour<2>(); }
            poll1(fho, p.ws);
            if constexpr (layer == 0) room<1000>();
            DRAIN();
        }
        if (layer == 0) { unsigned f0_, f1_; asm volatile("s_waitcnt vmcnt(4)\n\t" AR("%0", 36, 0) AR("%1", 36, 1) : "=v"(f0_), "=v"(f1_)::"memory"); fh = v2u{f0_, f1_}; }
        STAMP(layer ? 29 : 6)
        }

            v2u pt[4];
            if (layer == 0) {
                u64 hl;
                ne1(hl, tagv, fh);
                if (__builtin_expect(hl != 0ull, 0)) {
                    gld2t(fh, fho, p.ws); wait1(fh);
                    st64(tagv, rs, fgo1);
                    WAVE_RETRY(ISTAG(fh), fh = ld64(rs, fho))
                }
                ORDER();
            }
            else { room<3400>(); site<18>(); pipe_flag(pt, fho, p.ws, tagv, dead); ORDER(); }
            STAMP(layer ? 17 : 31)
            site<7 + 10 * layer>(); SCHED();
            constexpr int RING2 = layer ? KR2 : KR20;
            v4u ar[RING2];
#pragma unroll
            for (int i = 0; i < KR2; ++i) { ar[i] = ld128s(rs, ho[i / 10], ks[i % 10]); SCHED(); }
        int lane_o, b_o = b0, w_o = w0;
        asm volatile("v_mbcnt_lo_u32_b32 %0, -1, 0\n\tv_mbcnt_hi_u32_b32 %0, -1, %0" : "=v"(lane_o));
        asm volatile("" : "+s"(b_o), "+s"(w_o));
        const int lane = lane_o, b = b_o, w = w_o;
        const int c = lane & 15, q = lane >> 4;
        TILES()
        unsigned fgo = OFF_FG + (unsigned)((nt2 * 96 + 4 * w + (lane < 40 ? (lane & 56) * 2 + ((lane >> 1) & 3) : 64)) * 128);
        v4u gl[3];
        v2u f0;
        unsigned sgo, sgs, glo;
        u64 glate = 0ull;
        const char *gb1 = p.ws + 32 * 4096, *gb2 = p.ws + 48 * 4096;
        int gw, gr;
        {
            unsigned xs_[3][4], dq[2];
            float zx[4], zd[4], zt[4], zz[5][4];
            if (layer == 0) {
                glo = OFF_GT + (unsigned)((80 * nt2 + 4 * w + (lane >> 5) * 16 + ((lane >> 3) & 3)) * 4096 + mt2 * 128 + (lane & 7) * 16);
                gw = SC + 8192 + w * INBOX + (w & 1) * 128 + (lane >> 3) * 160 + (lane & 3) * 32 + (lane & 4) * 4;
                gr = SC + 8192 + q * INBOX + (q & 1) * 128 + (c >> 2) * 160 + w * 32 + (c & 3) * 8;
                pin(gw); pin(gr); pin(glo); pin(fgo); pins(gb1); pins(gb2);
                if constexpr (XDM) {
                    const int xr = l_tb(0, 0) + w * 5120 + c * 64 + (((q >> 1) ^ (c >> 2)) * 16) + (q & 1) * 8;
#pragma unroll
                    for (int nb = 0; nb < 5; ++nb) { const v2u t = *(const v2u*)(smem + ((nb & 5) ? xr ^ 32 : xr) + (nb >> 1) * 1024); xs_[nb >> 1][2 * (nb & 1)] = t.x; xs_[nb >> 1][2 * (nb & 1) + 1] = t.y; }
                }
            }
            sgo = OFF_SG + (unsigned)(((layer * 32 + mt2) * 4 + w) * 2048 + c * 16);
            sgs = q < 2 ? sgo + (unsigned)(nt2 * 256 + q * 8) : kNone;
            sgo += (unsigned)(q * 512);
            int srco = SC + w * INBOX + lane * 16;
            pin(sgo); pin(sgs); pin(srco);
            SCHED();
            const char* src = smem + srco;
            if constexpr (STAMPS == 1) { STAMP(layer ? 50 : 40) }
            static_for<40>(LAMBDA(i_) {
                constexpr int i = i_.value, m = i / 10, s = i % 10;
                static_for<5>(LAMBDA(k_) {
                    constexpr int kk = k_.value, GR = RING2 - KR2, pv = KR2 + i - 1 + (i - 1 < GR ? i - 1 : GR);
                    if constexpr (s == 0 && m < 3) mfa0<kk>(a2[m][kk], ar[i % RING2]); else mfa<5 * s + kk>(a2[m][kk], ar[i % RING2]);
                    SCHED();
                    if constexpr (i == 0 && kk == 0 && layer == 1) { pin(pt[0]); pin(pt[1]); pin(pt[2]); pin(pt[3]); }
                    if constexpr (layer == 0 && i >= 1 && i < 28 && (kk & 1)) {
                        constexpr int n = 2 * (i - 1) + (kk >> 1) - 9, nb = n / 9, u = n % 9 - 1;
                        if constexpr (n < 0 && XDM) {}
                        else if constexpr (n < -3) { rda<40 + 2 * (n + 9)>(xs_[(n + 9) >> 1][(2 * (n + 9)) & 3]); rda<41 + 2 * (n + 9)>(xs_[(n + 9) >> 1][(2 * (n + 9) + 1) & 3]); }
                        else if constexpr (n < 0) swp2n(xs_[n + 3][0], xs_[n + 3][2], xs_[n + 3][1], xs_[n + 3][3]);
                        else if constexpr (u < 0) { rda<22 + 2 * nb>(dq[0]); rda<23 + 2 * nb>(dq[1]); }
                        else if constexpr (u < 2) unp2(zx[2 * u], zx[2 * u + 1], xs_[nb >> 1][2 * (nb & 1) + u]);
                        else if constexpr (u < 4) unp2(zd[2 * u - 4], zd[2 * u - 3], dq[u - 2]);
                        else if constexpr (u < 6) fma2(zt[2 * u - 8], zt[2 * u - 7], zx[2 * u - 8], zx[2 * u - 7], rsr, rsr, rsm, rsm);
                        else fma2(zz[nb][2 * u - 12], zz[nb][2 * u - 11], zt[2 * u - 12], zt[2 * u - 11], zd[2 * u - 12], zd[2 * u - 11], zx[2 * u - 12], zx[2 * u - 11]);
                    }
                    if constexpr (layer == 0 && i == 0 && kk == 1) { vm_wait<KR2 - 1>(); st64(tagv, rs, fgo1); }
                    if constexpr (layer == 0 && i == kGPI && kk == 2) gld2s<0>(f0, fgo, p.ws);
                    if constexpr (layer == 0 && i == kGCI) {
                        static_assert(kGCI + KR20 > 40 && KR20 - KR2 <= 10, "");
                        if constexpr (kk == 0) { __builtin_amdgcn_s_waitcnt(0x0F70); pin(f0); gld4s<0>(gl[0], glo, p.ws); }
                        if constexpr (kk == 1) { gld4s<0>(gl[1], glo, gb1); gld4s<0>(gl[2], glo, gb2); }
                        if constexpr (kk == 3) ne1(glate, tagv, f0);
                    }
                    if constexpr (kk == 0 && i > 0 && pv < 40) ar[pv % RING2] = ld128s(rs, ho[pv / 10], ks[pv % 10]);
                    if constexpr (kk == 2 && i >= 1 && i <= GR) ar[(pv + 1) % RING2] = ld128s(rs, ho[(pv + 1) / 10], ks[(pv + 1) % 10]);
                    if constexpr (kk == 2 && m >= 1 && s >= 1 && s < 6) *(f32x4*)(smem + dst[m - 1] + (s - 1) * 1024) = a2[m - 1][s - 1];
                    site<4000 + 1000 * layer + 5 * i + kk>();
                    if constexpr (i >= 37) a2[i - 37][kk] = *(const f32x4*)(src + (i - 37) * 5120 + kk * 1024);
                    SCHED();
                });
                if constexpr (i == 10) { asm volatile("s_waitcnt lgkmcnt(0)" ::: "memory"); __builtin_amdgcn_s_barrier(); asm volatile("" ::: "memory"); }
                if constexpr (i == 28 && layer == 0) {
#pragma unroll
                    for (int nb = 0; nb < 5; ++nb) { a2[3][nb] = f32x4{zz[nb][0], zz[nb][1], zz[nb][2], zz[nb][3]}; pin(a2[3][nb]); }
                }
                if constexpr (i == 36) {
                    asm volatile("s_waitcnt lgkmcnt(0)" ::: "memory");
                    __builtin_amdgcn_s_barrier(); asm volatile("" ::: "memory");
                }
                if constexpr (STAMPS == 1 && (i == 0 || i == 9 || i == 10 || i == 15 || i == 25 || i == 35 || i == 36)) { STAMP((layer ? 51 : 41) + (i == 0 ? 0 : i == 9 ? 1 : i == 10 ? 2 : i == 15 ? 3 : i == 25 ? 4 : i == 35 ? 5 : 6)) }
                SCHED();
            });
            STAMP(layer ? 18 : 8)
        }
#pragma unroll
        for (int nb = 0; nb < 5; ++nb) a2[0][nb] = (a2[0][nb] + a2[1][nb]) + a2[2][nb];
        asm volatile("s_nop 1" : "+v"(a2[3][0]), "+v"(a2[3][1]), "+v"(a2[3][2]), "+v"(a2[3][3]), "+v"(a2[3][4]),
                     "+v"(a2[0][0]), "+v"(a2[0][1]), "+v"(a2[0][2]), "+v"(a2[0][3]), "+v"(a2[0][4]));
        float ty;
        {
            f32x4 s1, s2;
#pragma unroll
            for (int nb = 0; nb < 5; ++nb) {
                const f32x4 y = a2[3][nb] + a2[0][nb];
                xin[nb] = y;
                if (nb == 0) { s1 = y; s2 = y * y; } else { s1 += y; s2 += y * y; }
            }
            ty = xsum2h(hsum4(s1), hsum4(s2));
            st64(v2u{__float_as_uint(ty), tag0}, rs, sgs);
        }
        SCHED();
        STAMP(layer ? 19 : 9)
        if (layer == 0 && __builtin_expect(glate != 0ull, 0)) {
            asm volatile("s_waitcnt vmcnt(0)" : "+v"(gl[0]), "+v"(gl[1]), "+v"(gl[2])::"memory");
            WAVE_WAIT({ f0 = ld64(rs, fgo); ok = ISTAG(f0); })
            gld4t<0>(gl[0], glo, p.ws); gld4t<0>(gl[1], glo, gb1); gld4t<0>(gl[2], glo, gb2);
            asm volatile("s_waitcnt vmcnt(0)" : "+v"(gl[0]), "+v"(gl[1]), "+v"(gl[2])::"memory");
        }
        if (layer == 0) {
            asm volatile("s_waitcnt vmcnt(1)" : "+v"(gl[0]), "+v"(gl[1]), "+v"(gl[2])::"memory");
            *(v4u*)(smem + gw) = gl[0]; *(v4u*)(smem + gw + 1280) = gl[1]; *(v4u*)(smem + gw + 1920) = gl[2];
            asm volatile("s_waitcnt lgkmcnt(0)" ::: "memory");
            __builtin_amdgcn_s_barrier(); asm volatile("" ::: "memory");
#pragma unroll
            for (int nb = 0; nb < 5; ++nb) gt[nb] = __builtin_bit_cast(v2u, trread(smem, gr + 640 * nb));
        } else ACC_TIE5(gt);
        SCHED();
        if (PSP >> layer & 1) { gran_req<32, layer ? PDS : PDS0>(sgo, p.ws); SCHED(); }
        f32x4 gf[5], yg[5];
#pragma unroll
        for (int nb = 0; nb < 5; ++nb) { gf[nb] = unpk4(gt[nb]); yg[nb] = xin[nb] * gf[nb]; pin(gf[nb]); pin(yg[nb]); }
        SCHED();
        if (PSP >> layer & 1) { gran_req<40, 0>(sgo, p.ws); SCHED(); }
        unsigned xs = OFF_X2F + (unsigned)((2 * mt2 + (w >> 1)) * 40960 + ((c >> 2) & 1) * 20480 + (10 * nt2 + 2 * (q & 1) + (q >> 1)) * 256 + (8 * (w & 1) + 4 * (c >> 3) + (c & 3)) * 16);
        unsigned pso = q < 2 ? OFF_PS + (unsigned)(((mt2 * 4 + w) * 8 + nt2) * 128 + c * 8 + q * 4) : kNone;
        unsigned fxo1 = OFF_FX + (unsigned)(((mt1 * 4 + 2 * (mt2 & 1) + (w >> 1)) * 16 + nt2 * 2 + (w & 1)) * 128);
        unsigned oo = (unsigned)(((64 * mt2 + 16 * w + c) * D_ + 80 * nt2 + 4 * q) * 4);
        if (layer == 0) { pin(xs); pin(pso); pin(fxo1); } else pin(oo);
        float c_r, c_m, c_rr, c_rm, c_mm;
        if constexpr (layer == 0 && PSA) {
            f32x4 su = yg[0], sg_ = gf[0], syu = xin[0] * yg[0], suu = yg[0] * yg[0], sug = yg[0] * gf[0], sgg = gf[0] * gf[0];
#pragma unroll
            for (int nb = 1; nb < 5; ++nb) { su += yg[nb]; sg_ += gf[nb]; syu += xin[nb] * yg[nb]; suu += yg[nb] * yg[nb]; sug += yg[nb] * gf[nb]; sgg += gf[nb] * gf[nb]; }
            const float hu = hsum4(su);
            c_r = xsum2h(hu, 2.f * hsum4(syu)); c_m = xsum2h(hsum4(sg_), 2.f * hu);
            c_rr = xsum2h(0.f, hsum4(suu)); c_rm = xsum2h(0.f, 2.f * hsum4(sug)); c_mm = xsum2h(0.f, hsum4(sgg));
            pin(c_r); pin(c_m); pin(c_rr); pin(c_rm); pin(c_mm);
        }
        SCHED();
        {
            float a, d;
                        if (PSP >> layer & 1) gran_req<48, layer ? PD1 : 0>(sgo, p.ws);
            else { gran_req<32, layer ? PDS : PDS0>(sgo, p.ws); gran_req<40, PD1>(sgo, p.ws); gran_req<48, PD1>(sgo, p.ws); }
            room<(layer ? 900 : 1500)>(); site<9 + 10 * layer>();
            pipe_gran(a, d, sgo, p.ws, tag0, dead);
            STAMP(layer ? 20 : 10)
            xsum2(a, d);
            const float me = a * (1.0f / D_);
            const float ri = __builtin_amdgcn_rsqf(fmaxf(d * (1.0f / D_) - me * me, 0.f) + 1e-5f), mr = -me * ri;
            if constexpr (layer == 0 && PSA) {
                const float ps_ = ty + ri * (c_r + ri * c_rr + mr * c_rm) + mr * (c_m + mr * c_mm);
                __builtin_amdgcn_raw_buffer_store_b32(__float_as_uint(ps_), rs, pso, 0, SC1);
                SCHED();
            }
#pragma unroll
            for (int nb = 0; nb < 5; ++nb) xin[nb] = yg[nb] * ri + (gf[nb] * mr + xin[nb]);
        }
        if constexpr (layer == 0) {
            v2u pk[5];
#pragma unroll
            for (int nb = 0; nb < 5; ++nb) { const f32x4 x = xin[nb]; pk[nb] = v2u{cvtpk(x.x, x.y), cvtpk(x.z, x.w)}; }
#pragma unroll
            for (int i = 0; i < 3; ++i) {
                const v2u pa = pk[2 * i], pb = pk[i < 2 ? 2 * i + 1 : 4];
                const auto r0 = __builtin_amdgcn_permlane16_swap(pa.x, pb.x, false, false);
                const auto r1 = __builtin_amdgcn_permlane16_swap(pa.y, pb.y, false, false);
                if (i < 2 || (q & 1) == 0) st128(v4u{r0[0], r1[0], r0[1], r1[1]}, rs, xs + i * 1024);
            }
            if constexpr (!PSA) {
                f32x4 s1, s2;
#pragma unroll
                for (int nb = 0; nb < 5; ++nb) { const f32x4 x = xin[nb]; if (nb == 0) { s1 = x; s2 = x * x; } else { s1 += x; s2 += x * x; } }
                __builtin_amdgcn_raw_buffer_store_b32(__float_as_uint(xsum2h(hsum4(s1), hsum4(s2))), rs, pso, 0, SC1);
            }
            SCHED();
            site<12>();
        int wa[2][3];
#pragma unroll
        for (int i = 0; i < 2; ++i) {
            wa[i][0] = (c >> 3) * 20480 + (c & 7) * 128 + (((4 * i + q) ^ (c & 6)) * 16); wa[i][1] = (wa[i][0] ^ 16) + 10240;
            wa[i][2] = 81920 + (c & 1) * 10240 + (c >> 1) * 128 + (((4 * i + q) ^ ((c & 1) | ((c >> 2) << 1))) * 16);
            pin(wa[i][0]); pin(wa[i][1]); pin(wa[i][2]);
        }
            unsigned fxo = OFF_FX + (unsigned)((mt1 * 4 + w) * 2048 + (PLM ? lane >> 2 : c) * 128);
            unsigned xo = OFF_X2F + (unsigned)((4 * mt1 + w) * 40960 + lane * 16), xo1 = xo + 20480u;
            const int row = 8 * q + (c & 7);
            unsigned po = OFF_PS + (unsigned)((((2 * mt1 + (w >> 1)) * 4 + 2 * (w & 1) + (row >> 4)) * 8 + 4 * (c >> 3)) * 128 + (row & 15) * 8);
            int rsw = l_rs(1, 0) + w * INBOX + lane * 4;
            pin(rsw); pin(fxo); pin(xo); pin(xo1); pin(po);
            v4u wf[2][5];
#pragma unroll
            for (int nb = 0; nb < 5; ++nb) wf[0][nb] = WF(nb, 0);
            SCHED();
            DRAIN();
            FLAG(fxo1)
            if (b == 255 && w == 0) FLAG(OFF_NONCE)
            STAMP(11)
            if (w == 0) tour<4>();
            addrs(std::integral_constant<int, 1>{}, lane, w, b);
            v2u pt[4];
            skip<SKIP5>();
            pipe_flag(pt, fxo, p.ws, tagv, dead);
            ORDER();
            STAMP(12)
            v4u ar[RING1][2];
#pragma unroll
            for (int s = 0; s < RING1; ++s)
#pragma unroll
                for (int mb = 0; mb < 2; ++mb) { ar[s][mb] = ld128s(rs, mb ? xo1 : xo, s * 1024); SCHED(); }
            v2u ps[4];
#pragma unroll
            for (int i = 0; i < 4; ++i) ps[i] = ld64(rs, po + i * 128);
            SCHED();
            float t1, t2, rinv, mri, rr_[8], rm_[8];
            static_for<20>(LAMBDA(s_) {
                constexpr int s = s_.value, cu = s & 1, nx = cu ^ 1;
                LGKM0() SCHED();
                static_for<10>(LAMBDA(g_) {
                    constexpr int g = g_.value, mb = g / 5, k = g % 5;
                    if constexpr (s == 0) mf0(acc[mb][k], ar[0][mb], wf[0][k]); else mf(acc[mb][k], ar[s % RING1][mb], wf[cu][k]);
                    SCHED();
                    if constexpr (s == 0 && g == 0) { pin(pt[0]); pin(pt[1]); pin(pt[2]); pin(pt[3]); }
                    if constexpr (s < 19 && g < 5) wf[nx][g] = WF(g, s + 1);
                    if constexpr (g == 5 && s + RING1 < 20) ar[s % RING1][0] = ld128s(rs, xo, (s + RING1) * 1024);
                    if constexpr (g == 2 && s > 0 && s - 1 + RING1 < 20) ar[(s - 1) % RING1][1] = ld128s(rs, xo1, (s - 1 + RING1) * 1024);
                    constexpr int n = g >= 6 && g < 9 ? 3 * (s - 5) + g - 6 : -1;
                    if constexpr (n == 0) { t1 = __uint_as_float(ps[0].x) + __uint_as_float(ps[1].x); pin(t1); t2 = __uint_as_float(ps[0].y) + __uint_as_float(ps[1].y); pin(t2); }
                    if constexpr (n == 1) { t1 += __uint_as_float(ps[2].x); pin(t1); t2 += __uint_as_float(ps[2].y); pin(t2); }
                    if constexpr (n == 2) { t1 += __uint_as_float(ps[3].x); pin(t1); t2 += __uint_as_float(ps[3].y); pin(t2); }
                    if constexpr (n == 3) { t1 += dpp<0x128>(t1); t2 += dpp<0x128>(t2); pin(t1); pin(t2); }
                    if constexpr (n == 4) { t1 *= (1.0f / D_); pin(t1); t2 *= (1.0f / D_); pin(t2); }
                    if constexpr (n == 5) { t2 = fmaxf(t2 - t1 * t1, 0.f); pin(t2); }
                    if constexpr (n == 6) { rinv = __builtin_amdgcn_rsqf(t2 + 1e-5f); pin(rinv); }
                    if constexpr (n == 8) { mri = -t1 * rinv; pin(mri); }
                    if constexpr (n == 9) { t1 = rinv; mrg8(t1, mri); }
                    if constexpr (n == 10) *(float*)(smem + rsw) = t1;
                    if constexpr (n >= 11 && n < 19) bc2b<n - 11>(rr_[n - 11], rm_[n - 11], rinv, mri);
                    if constexpr (n >= 19 && n < 39) TT_GAP(n - 19)
                    site<6000 + 10 * s + g>();
                    SCHED();
                });
            });
            rr[0] = f32x4{rr_[0], rr_[1], rr_[2], rr_[3]}; rr[1] = f32x4{rr_[4], rr_[5], rr_[6], rr_[7]};
            asm volatile("kpf_end2:");
            ACC_FENCE10(acc[0], acc[1]);
            STAMP(13)
        } else {
#pragma unroll
            for (int nb = 0; nb < 5; ++nb) __builtin_amdgcn_raw_buffer_store_b128(__builtin_bit_cast(v4u, xin[nb]), ro, oo + 64 * nb, 0, OUTP);
            STAMP(21)
            STAMP_DUMP()
            asm volatile("kpf_end:");
        }
    };
    layer_body(std::integral_constant<int, 0>{});
    layer_body(std::integral_constant<int, 1>{});
}

extern "C" void kernel_launch(void* const* d_in, const int* in_sizes, int n_in,
                              void* d_out, int out_size, void* d_ws, size_t ws_size,
                              hipStream_t stream) {
    Params p;
    p.x0 = (const u16*)d_in[0];
    p.Lre = (const u16*)d_in[1];
    p.Lim = (const u16*)d_in[2];
    p.Bre = (const u16*)d_in[3];
    p.Bim = (const u16*)d_in[4];
    p.Cre = (const u16*)d_in[5];
    p.Cim = (const u16*)d_in[6];
    p.logdt = (const u16*)d_in[7];
    p.Dskip = (const u16*)d_in[8];
    p.k = (const u16*)d_in[10];
    p.v = (const u16*)d_in[11];
    p.shk = (const u16*)d_in[12];
    p.ssmk = (const u16*)d_in[13];
    p.ssmD = (const u16*)d_in[14];
    p.ws = (char*)d_ws;
    p.out = (float*)d_out;
    k_main<<<256, 256, 0, stream>>>(p);
}
```

```cpp
#define PADS 9000,9001,2055,2,9002,4070,9,9003,6,9004,9005,6113,9006,18,19,9007,9008
#include <hip/hip_runtime.h>
#include <stdint.h>
#include <utility>

#define STAMPS 0

typedef unsigned short u16;
typedef unsigned long long u64;
typedef unsigned v4u __attribute__((ext_vector_type(4)));
typedef unsigned v2u __attribute__((ext_vector_type(2)));
typedef float f32x4 __attribute__((ext_vector_type(4)));
typedef float f32x2 __attribute__((ext_vector_type(2)));
typedef short s16x4 __attribute__((ext_vector_type(4)));
typedef __bf16 bf16x2v __attribute__((ext_vector_type(2)));
#define LAS __attribute__((address_space(3)))
#define DI __device__ __forceinline__

constexpr int S_ = 2048, D_ = 640;
#define KR1 5
#define KR2 10
constexpr int RING1 = KR1;
#define KCR1 4
constexpr int kCR1 = KCR1;
#define KXA 0
#define KN3E 16
#define KN3 12
constexpr int kXA = KXA;
constexpr int kN3E = KN3E, kN3 = KN3;
#define KFHK 7
#define KFHP 7
constexpr int kFHK = KFHK, kFHP = KFHP;
#define KGPI 31
#define KGCI 36
#define PLM 1
#define CPF 0
#define HOIST 0
#define LBE 0
#define GSM 0
#define GSI 0
#define GFI 12
#define GIN 2
#define RESG 1
#define KI2 8
#define KI1 8
#define KS1 8
#define KR20 10
constexpr int kGPI = KGPI, kGCI = KGCI;

constexpr unsigned OFF_NONCE = 0;
constexpr unsigned OFF_ST = 4096;
constexpr unsigned OFF_FW = OFF_ST + 65536;
constexpr unsigned OFF_FK = OFF_FW + 131072;
constexpr unsigned OFF_FH = OFF_FK + 81920;
constexpr unsigned OFF_FX = OFF_FH + 262144;
constexpr unsigned OFF_FG = OFF_FX + 131072;
constexpr unsigned OFF_EG = OFF_FG + 98304;
constexpr unsigned OFF_SG = OFF_EG + 327680;
constexpr unsigned OFF_PS = OFF_SG + 524288;
constexpr unsigned OFF_KVT = OFF_PS + 131072;
constexpr unsigned OFF_GT = OFF_KVT + 2621440;
constexpr unsigned OFF_W2F = OFF_GT + 2621440;
constexpr unsigned OFF_X2F = OFF_W2F + 1638400;
constexpr unsigned OFF_DUM = OFF_X2F + 2621440;
constexpr unsigned OFF_HF = OFF_DUM + 163840;
constexpr unsigned WS_BYTES = OFF_HF + 2 * 5242880;
constexpr unsigned kNone = 0x40000000u;
constexpr unsigned OFF_DBG = 0x1800000;
#define I32M 1
#define KVS 1
#define KVU 0
#define TILES() const int xq = b & 7, j = b >> 3, mt1 = 2 * xq + (j >> 4), nt1 = j & 15, mt2 = 4 * xq + (j >> 3), nt2 = j & 7, i32 = I32M == 0 ? mt2 : I32M == 1 ? 16 * (mt2 & 1) + mt1 : I32M == 2 ? 31 - mt2 : 16 * ((mt2 ^ (j >> (I32M < 3 ? 0 : I32M - 3))) & 1) + mt1;

constexpr int SC = 102400;
constexpr int REGSZ = 13056;
constexpr int L_XR = SC;
constexpr int L_CO = SC + 12288;
constexpr int L_KB = SC + 51712;
constexpr int L_TAB = SC + 55808;
constexpr int L_CW = SC + 39168;
constexpr int INBOX = 15360;
constexpr int l_rs(int l, int w) { return l ? SC + w * INBOX : L_KB + w * 256; }
constexpr int l_sg(int l, int w) { return l ? SC + w * INBOX + 256 : SC + (w == 0 ? 43264 : w == 1 ? 46080 : w == 2 ? 52736 : 56320); }
constexpr int l_tb(int l, int w) { return l ? SC + w * INBOX + 2048 : SC + 40960 + w * 5120; }
constexpr int LDS_BYTES = 163840;
constexpr int CV_E = 4608;
constexpr int CV_O = CV_E + (1056 + 15) * 4;

struct Params {
    const u16 *x0, *Lre, *Lim, *Bre, *Bim, *Cre, *Cim, *logdt, *Dskip, *k, *v, *shk, *ssmk, *ssmD;
    char* ws;
    float* out;
};

#define MF "v_mfma_f32_16x16x32_bf16 "
DI void mf(f32x4& c, v4u a, v4u b) { asm(MF "%0, %1, %2, %0" : "+v"(c) : "v"(a), "v"(b)); }
DI void mf0(f32x4& c, v4u a, v4u b) { asm(MF "%0, %1, %2, 0" : "=&v"(c) : "v"(a), "v"(b)); }
constexpr int W2R = 56;
#define WA(n) "a[%c" #n ":%c" #n "+3]"
template <int R> DI void mfa(f32x4& c, v4u a) { asm(MF "%0, " WA(2) ", %1, %0" : "+v"(c) : "v"(a), "i"(W2R + 4 * R)); }
template <int R> DI void mfa0(f32x4& c, v4u a) { asm(MF "%0, " WA(2) ", %1, 0" : "=&v"(c) : "v"(a), "i"(W2R + 4 * R)); }
#define ACC_FENCE10(c, d) asm volatile("" : "+v"(c[0]), "+v"(c[1]), "+v"(c[2]), "+v"(c[3]), "+v"(c[4]), "+v"(d[0]), "+v"(d[1]), "+v"(d[2]), "+v"(d[3]), "+v"(d[4]))
#define ACC_TIE5(c) asm volatile("" : "+v"(c[0]), "+v"(c[1]), "+v"(c[2]), "+v"(c[3]), "+v"(c[4]))
#define QP " quad_perm:[1,0,3,2] row_mask:0xf bank_mask:0xf"
struct St { f32x2 a, b; float c; };
struct Lm { f32x2 a, b; float r, s; };
#define PK1 " op_sel_hi:[0,1,1]\n\t"
#define PK2 " op_sel:[1,1,0] op_sel_hi:[1,0,1] neg_lo:[1,0,0]"
#define ST_(t0, t1, t2, h0, h1, h2, b0, b1, b2, l0, l1, l2, l3)                                                                                        \
    "v_pk_fma_f32 %" #t0 ", %" #l0 ", %" #h0 ", %" #b0 PK1 "v_pk_fma_f32 %" #t1 ", %" #l1 ", %" #h1 ", %" #b1 PK1 "v_fma_f32 %" #t2 ", %" #l2 ", %" #h2 ", %" #b2 "\n\t" \
    "v_pk_fma_f32 %" #t0 ", %" #l0 ", %" #h0 ", %" #t0 PK2 "\n\tv_pk_fma_f32 %" #t1 ", %" #l1 ", %" #h1 ", %" #t1 PK2 "\n\tv_fmac_f32_dpp %" #t2 ", %" #h2 ", %" #l3 QP "\n\t"
#define SO(t) "=&v"(t.a), "=&v"(t.b), "=&v"(t.c)
#define SI(t) "v"(t.a), "v"(t.b), "v"(t.c)
#define LI(l) "v"(l.a), "v"(l.b), "v"(l.r), "v"(l.s)
DI void run3(St& t, const St& h, const St* b, const Lm& l) {
    St u;
    asm(ST_(0, 1, 2, 6, 7, 8, 9, 10, 11, 18, 19, 20, 21) ST_(3, 4, 5, 0, 1, 2, 12, 13, 14, 18, 19, 20, 21) ST_(0, 1, 2, 3, 4, 5, 15, 16, 17, 18, 19, 20, 21)
        : SO(t), SO(u) : SI(h), SI(b[0]), SI(b[1]), SI(b[2]), LI(l));
}
DI void run4(St& t, const St& h, const St* b, const Lm& l) {
    St u;
    asm(ST_(3, 4, 5, 6, 7, 8, 9, 10, 11, 21, 22, 23, 24) ST_(0, 1, 2, 3, 4, 5, 12, 13, 14, 21, 22, 23, 24) ST_(3, 4, 5, 0, 1, 2, 15, 16, 17, 21, 22, 23, 24) ST_(0, 1, 2, 3, 4, 5, 18, 19, 20, 21, 22, 23, 24)
        : SO(t), SO(u) : SI(h), SI(b[0]), SI(b[1]), SI(b[2]), SI(b[3]), LI(l));
}
#define AC_(t0, t2, h0, h2, b0, b2, l0, l2, l3)                                                                                   \
    "v_pk_fma_f32 %" #t0 ", %" #l0 ", %" #h0 ", %" #b0 PK1 "v_fma_f32 %" #t2 ", %" #l2 ", %" #h2 ", %" #b2 "\n\t"               \
    "v_pk_fma_f32 %" #t0 ", %" #l0 ", %" #h0 ", %" #t0 PK2 "\n\tv_fmac_f32_dpp %" #t2 ", %" #h2 ", %" #l3 QP "\n\t"
#define AO(t) "=&v"(t.a), "=&v"(t.c)
#define AI(t) "v"(t.a), "v"(t.c)
DI void keep14(St* t, const St& h, const St& x, const St* b, const Lm& lq, const Lm& l) {
    St u;
    asm(AC_(8, 9, 10, 11, 12, 13, 22, 23, 24) AC_(0, 1, 8, 9, 14, 15, 25, 26, 27) AC_(2, 3, 0, 1, 16, 17, 25, 26, 27) AC_(4, 5, 2, 3, 18, 19, 25, 26, 27) AC_(6, 7, 4, 5, 20, 21, 25, 26, 27)
        : AO(t[0]), AO(t[1]), AO(t[2]), AO(t[3]), AO(u) : AI(h), AI(x), AI(b[0]), AI(b[1]), AI(b[2]), AI(b[3]), "v"(lq.a), "v"(lq.r), "v"(lq.s), "v"(l.a), "v"(l.r), "v"(l.s));
}
DI void keep4(St* t, const St& h, const St* b, const Lm& l) {
    asm(AC_(0, 1, 8, 9, 10, 11, 18, 19, 20) AC_(2, 3, 0, 1, 12, 13, 18, 19, 20) AC_(4, 5, 2, 3, 14, 15, 18, 19, 20) AC_(6, 7, 4, 5, 16, 17, 18, 19, 20)
        : AO(t[0]), AO(t[1]), AO(t[2]), AO(t[3]) : AI(h), AI(b[0]), AI(b[1]), AI(b[2]), AI(b[3]), "v"(l.a), "v"(l.r), "v"(l.s));
}
DI void b3cv(f32x2* t, unsigned* o, f32x2 h, f32x2 b0, f32x2 b1, f32x2 b2, f32x2 l0, f32x2 l, const float* e) {
    asm("v_pk_fma_f32 %0, %11, %7, %8" PK1 "v_cvt_pk_bf16_f32 %3, %13, %14\n\tv_pk_fma_f32 %0, %11, %7, %0" PK2 "\n\tv_cvt_pk_bf16_f32 %4, %15, %16\n\t"
        "v_pk_fma_f32 %1, %12, %0, %9" PK1 "v_cvt_pk_bf16_f32 %5, %17, %18\n\tv_pk_fma_f32 %1, %12, %0, %1" PK2 "\n\tv_cvt_pk_bf16_f32 %6, %19, %20\n\t"
        "v_pk_fma_f32 %2, %12, %1, %10" PK1
        : "=&v"(t[0]), "=&v"(t[1]), "=&v"(t[2]), "=&v"(o[0]), "=&v"(o[1]), "=&v"(o[2]), "=&v"(o[3])
        : "v"(h), "v"(b0), "v"(b1), "v"(b2), "v"(l0), "v"(l), "v"(e[0]), "v"(e[1]), "v"(e[2]), "v"(e[3]), "v"(e[4]), "v"(e[5]), "v"(e[6]), "v"(e[7]));
}
DI void bfin(f32x2& t, f32x2 h, f32x2 l) { asm("v_pk_fma_f32 %0, %2, %1, %0" PK2 : "+v"(t) : "v"(h), "v"(l)); }
#define UNDEF8(c) asm volatile("" : "=v"(c[0]), "=v"(c[1]), "=v"(c[2]), "=v"(c[3]), "=v"(c[4]), "=v"(c[5]), "=v"(c[6]), "=v"(c[7]))
#define PIN5(t) asm volatile("s_nop 1" : "+v"(t[0]), "+v"(t[1]), "+v"(t[2]), "+v"(t[3]), "+v"(t[4]))
DI unsigned cvtpk(float lo, float hi) {
    f32x2 v = {lo, hi};
    return __builtin_bit_cast(unsigned, __builtin_convertvector(v, bf16x2v));
}
DI float lo16f(unsigned u) { return __uint_as_float(u << 16); }
DI float hi16f(unsigned u) { return __uint_as_float(u & 0xffff0000u); }
DI float rbf(float v) { return lo16f(cvtpk(v, 0.f)); }
template <int CTRL> DI float dpp(float x) {
    return __int_as_float(__builtin_amdgcn_update_dpp(0, __float_as_int(x), CTRL, 0xF, 0xF, true));
}
DI float rowsum16(float x) {
    x += dpp<0x128>(x); x += dpp<0x124>(x); x += dpp<0x122>(x); x += dpp<0x121>(x);
    return x;
}
DI float xsum32(float x) { auto r = __builtin_amdgcn_permlane32_swap(__float_as_uint(x), __float_as_uint(x), false, false); return __uint_as_float(r[0]) + __uint_as_float(r[1]); }
DI void xsum2(float& a, float& d) {
    auto r = __builtin_amdgcn_permlane16_swap(__float_as_uint(a), __float_as_uint(d), false, false);
    const float s = __uint_as_float(r[0]) + __uint_as_float(r[1]);
    const float t = xsum32(s);
    auto u = __builtin_amdgcn_permlane16_swap(__float_as_uint(t), __float_as_uint(t), false, false);
    a = __uint_as_float(u[0]); d = __uint_as_float(u[1]);
}
DI float xsum2h(float a, float d) {
    auto r = __builtin_amdgcn_permlane16_swap(__float_as_uint(a), __float_as_uint(d), false, false);
    return xsum32(__uint_as_float(r[0]) + __uint_as_float(r[1]));
}
DI float hsum4(f32x4 v) { return (v.x + v.y) + (v.z + v.w); }
typedef __amdgpu_buffer_rsrc_t rsrc_t;
constexpr int SC1 = 16;
DI v4u ld128(rsrc_t rs, unsigned off) { return __builtin_amdgcn_raw_buffer_load_b128(rs, off, 0, SC1); }
DI v4u ld128s(rsrc_t rs, unsigned off, unsigned soff) { return __builtin_amdgcn_raw_buffer_load_b128(rs, off, soff, SC1); }
DI v2u ld64(rsrc_t rs, unsigned off) { return __builtin_amdgcn_raw_buffer_load_b64(rs, off, 0, SC1); }
DI void st128(v4u v, rsrc_t rs, unsigned off) { __builtin_amdgcn_raw_buffer_store_b128(v, rs, off, 0, SC1); }
DI void st64(v2u v, rsrc_t rs, unsigned off) { __builtin_amdgcn_raw_buffer_store_b64(v, rs, off, 0, SC1); }
#define LGKM0() { __builtin_amdgcn_s_waitcnt(0xC07F); asm volatile("" ::: "memory"); }
#define DRAIN() asm volatile("s_waitcnt vmcnt(0)" ::: "memory")
#define ORDER() __builtin_amdgcn_fence(__ATOMIC_ACQUIRE, "wavefront")
#define SCHED() __builtin_amdgcn_sched_barrier(0)
constexpr unsigned kSpinCap = 1u << 17;
#define WAVE_WAIT(...)                                                      \
    {                                                                       \
        unsigned spins_ = dead ? kSpinCap : 0u;                             \
        _Pragma("nounroll") for (;;) {                                      \
            asm volatile("" ::: "memory");                                  \
            bool ok;                                                        \
            __VA_ARGS__;                                                    \
            if (__builtin_amdgcn_ballot_w64(!ok) == 0ull) break;            \
            if (++spins_ > kSpinCap) { dead = true; break; }                \
            __builtin_amdgcn_s_sleep(1);                                    \
        }                                                                   \
        ORDER();                                                            \
    }
#define KPD1 2
#define KPDS 8
#define KPDS0 5
#define KPF0 7
#define KPH0 12
#define KPF1 4
constexpr int PD1 = KPD1, PDS = KPDS, PDS0 = KPDS0;
#define PL(r) "global_load_dwordx2 " r ", %5, %6 sc1\n\t"
#define PS_(r) "s_waitcnt vmcnt(1)\n\tv_cmp_ne_u64 vcc, %7, " r "\n\ts_cbranch_vccz 2f\n\t" PL(r)
template <int D0> DI void pipe_flag(v2u (&t)[4], unsigned off, const void* base, v2u tag, bool& dead) {
    unsigned n = dead ? 0u : kSpinCap;
    asm volatile("s_sleep %8\n\t" PL("%0") "s_sleep %9\n\t" PL("%1")
                 "1:\n\t" PS_("%0") PS_("%1") "s_sub_u32 %4, %4, 1\n\ts_cbranch_scc0 1b\n2:"
                 : "=&v"(t[0]), "=&v"(t[1]), "=&v"(t[2]), "=&v"(t[3]), "+s"(n) : "v"(off), "s"(base), "v"(tag), "i"(D0), "i"(KPF1) : "memory", "vcc", "scc");
    dead = __builtin_amdgcn_readfirstlane(n) == ~0u;
}
#define WAVE_RETRY(OK, ...) { if (__builtin_expect(__builtin_amdgcn_ballot_w64(!(OK)) != 0ull, 0)) WAVE_WAIT({ __VA_ARGS__; ok = OK; }) ORDER(); }
#define STAMP(i)
#define STAMP_DUMP()

#define T1POS 27
#define TW 2
#define KLB0 0
#define KLB1 5
#define SKIP0 0
#define SKIP2 0
#define SKIP7 150
#define SKIP5 0
#define SKIP4 0
constexpr int kPads[] = {PADS};
constexpr int pad_next(int id) {
    for (unsigned i = 0; i + 1 < sizeof(kPads) / sizeof(int); ++i) if (kPads[i] == id) return kPads[i + 1];
    return -1;
}
template <int ID> DI void site() {
    if constexpr (pad_next(ID) >= 0) asm volatile("kp%c0:\n\ts_cbranch_execz kp%c1" ::"i"(ID), "i"(pad_next(ID)));
    else asm volatile("");
}
template <int T> DI void tour() {
    if constexpr (pad_next(9000 + T) >= 0 && pad_next(9000 + T) != 9001 + T) asm volatile("s_mov_b64 exec, 0\n\ts_branch kp%c0\nkp%c1:\n\ts_mov_b64 exec, -1" ::"i"(pad_next(9000 + T)), "i"(9001 + T));
}
template <int N> DI void skip() { if constexpr (N > 0) asm volatile("s_branch 1f\n\t.rept %c0\n\ts_nop 0\n\t.endr\n1:" ::"i"(N)); }
template <int N> DI void room() { asm volatile("s_branch 1f\n\t.p2align 12, , %c0\n1:" ::"i"(N)); }
template <class F, int... I> DI void static_for_impl(std::integer_sequence<int, I...>, F&& f) { (f(std::integral_constant<int, I>{}), ...); }
template <int N, class F> DI void static_for(F&& f) { static_for_impl(std::make_integer_sequence<int, N>{}, f); }
#define LAMBDA(v) [&](auto v) __attribute__((always_inline))
DI f32x2 cmul(f32x2 a, f32x2 b) {
    f32x2 t;
    asm("v_pk_mul_f32 %0, %1, %2 op_sel_hi:[0,1]\n\ts_nop 0\n\tv_pk_fma_f32 %0, %1, %2, %0" PK2 "\n\ts_nop 0" : "=&v"(t) : "v"(a), "v"(b));
    return t;
}
DI f32x2 cfma(f32x2 a, f32x2 b, f32x2 c) {
    f32x2 t;
    asm("v_pk_fma_f32 %0, %1, %2, %3 op_sel_hi:[0,1,1]\n\ts_nop 0\n\tv_pk_fma_f32 %0, %1, %2, %0" PK2 "\n\ts_nop 0" : "=&v"(t) : "v"(a), "v"(b), "v"(c));
    return t;
}
DI void cfma4(f32x2 (&t)[4], f32x2 a, f32x2 b0, f32x2 b1, f32x2 b2, f32x2 b3, f32x2 c0, f32x2 c1, f32x2 c2, f32x2 c3) {
    asm("v_pk_fma_f32 %0, %4, %5, %9 op_sel_hi:[0,1,1]\n\tv_pk_fma_f32 %1, %4, %6, %10 op_sel_hi:[0,1,1]\n\tv_pk_fma_f32 %2, %4, %7, %11 op_sel_hi:[0,1,1]\n\tv_pk_fma_f32 %3, %4, %8, %12 op_sel_hi:[0,1,1]\n\t"
        "v_pk_fma_f32 %0, %4, %5, %0" PK2 "\n\tv_pk_fma_f32 %1, %4, %6, %1" PK2 "\n\tv_pk_fma_f32 %2, %4, %7, %2" PK2 "\n\tv_pk_fma_f32 %3, %4, %8, %3" PK2 "\n\ts_nop 0"
        : "=&v"(t[0]), "=&v"(t[1]), "=&v"(t[2]), "=&v"(t[3]) : "v"(a), "v"(b0), "v"(b1), "v"(b2), "v"(b3), "v"(c0), "v"(c1), "v"(c2), "v"(c3));
}
#define C4(t0, t1, t2, t3, a, b0, b1, b2, b3, c0, c1, c2, c3)                                                                                                         \
    "v_pk_fma_f32 %" #t0 ", %" #a ", %" #b0 ", %" #c0 PK1 "v_pk_fma_f32 %" #t1 ", %" #a ", %" #b1 ", %" #c1 PK1 "v_pk_fma_f32 %" #t2 ", %" #a ", %" #b2 ", %" #c2 PK1 "v_pk_fma_f32 %" #t3 ", %" #a ", %" #b3 ", %" #c3 PK1 \
    "v_pk_fma_f32 %" #t0 ", %" #a ", %" #b0 ", %" #t0 PK2 "\n\tv_pk_fma_f32 %" #t1 ", %" #a ", %" #b1 ", %" #t1 PK2 "\n\tv_pk_fma_f32 %" #t2 ", %" #a ", %" #b2 ", %" #t2 PK2 "\n\tv_pk_fma_f32 %" #t3 ", %" #a ", %" #b3 ", %" #t3 PK2 "\n\t"
DI void cfma4n(f32x2 (&t)[4], f32x2 a, f32x2 b0, f32x2 b1, f32x2 b2, f32x2 b3, f32x2 c0, f32x2 c1, f32x2 c2, f32x2 c3) {
    asm(C4(0, 1, 2, 3, 4, 5, 6, 7, 8, 9, 10, 11, 12) : "=&v"(t[0]), "=&v"(t[1]), "=&v"(t[2]), "=&v"(t[3]) : "v"(a), "v"(b0), "v"(b1), "v"(b2), "v"(b3), "v"(c0), "v"(c1), "v"(c2), "v"(c3));
}
DI void agg(unsigned& er, unsigned& ei, f32x2 (&x)[4], f32x2& a, const f32x2 (&u)[4], const f32x2 (&s)[16], f32x2 l8, f32x2 l32, f32x2 l64) {
    f32x2 v[4], b;
    asm(C4(4, 5, 6, 7, 22, 10, 11, 12, 13, 14, 15, 16, 17) C4(0, 1, 2, 3, 22, 4, 5, 6, 7, 18, 19, 20, 21)
        "v_pk_fma_f32 %8, %23, %0, %1" PK1 "v_pk_fma_f32 %9, %23, %2, %3" PK1 "v_pk_fma_f32 %8, %23, %0, %8" PK2 "\n\tv_pk_fma_f32 %9, %23, %2, %9" PK2
        : "=&v"(x[0]), "=&v"(x[1]), "=&v"(x[2]), "=&v"(x[3]), "=&v"(v[0]), "=&v"(v[1]), "=&v"(v[2]), "=&v"(v[3]), "=&v"(a), "=&v"(b)
        : "v"(u[0]), "v"(u[1]), "v"(u[2]), "v"(u[3]), "v"(s[2]), "v"(s[6]), "v"(s[10]), "v"(s[14]), "v"(s[3]), "v"(s[7]), "v"(s[11]), "v"(s[15]), "v"(l8), "v"(l32));
    asm("s_nop 0\n\tv_fma_f32 %0, %2, %6, %4\n\tv_fma_f32 %1, %2, %7, %5\n\tv_fma_f32 %0, -%3, %7, %0\n\tv_fma_f32 %1, %3, %6, %1"
        : "=&v"(er), "=&v"(ei) : "v"(a.x), "v"(a.y), "v"(b.x), "v"(b.y), "v"(l64.x), "v"(l64.y));
}
DI void gacc2(f32x2& h0, f32x2& h1, f32x2 w0, f32x2 w1, v4u e0, v4u e1) {
    const v2u a0 = {e0.x, e0.y}, b0 = {e0.z, e0.w}, a1 = {e1.x, e1.y}, b1 = {e1.z, e1.w};
    asm("v_pk_fma_f32 %0, %4, %2, %0 op_sel_hi:[0,1,1]\n\tv_pk_fma_f32 %1, %6, %3, %1 op_sel_hi:[0,1,1]\n\t"
        "v_pk_fma_f32 %0, %5, %2, %0 op_sel:[0,1,0] op_sel_hi:[0,0,1] neg_lo:[1,0,0]\n\tv_pk_fma_f32 %1, %7, %3, %1 op_sel:[0,1,0] op_sel_hi:[0,0,1] neg_lo:[1,0,0]"
        : "+v"(h0), "+v"(h1) : "v"(w0), "v"(w1), "v"(a0), "v"(b0), "v"(a1), "v"(b1));
}
DI s16x4 trread(char* smem, int byteoff) {
    return __builtin_amdgcn_ds_read_tr16_b64_v4i16((LAS s16x4*)(smem + byteoff));
}

#define CV_LOAD_A(A, a, rr_, hf) { const unsigned* ap_ = (const unsigned*)(smem + ab[(a) >> 1] + (((a) & 1) ? 0 : 512) + 64 * (7 - (rr_)) + 8 * (hf)); A[a][2 * (hf)] = ap_[0]; A[a][2 * (hf) + 1] = ap_[1]; }
#define CV_LOAD_B(B, e, rr_) { B[e] = *(const v4u*)(smem + bb + 64 * (7 - (rr_)) + 512 * (e)); }
template <int R, bool PF, class F, class G>
DI void conv_round(f32x4 (&ca)[8], v4u (&A)[8], const v4u (&B)[8], v4u (&An)[8], v4u (&Bn)[8], const char* smem, const int (&ab)[4], int bb, F&& fill, G&& last) {
    static_for<36>(LAMBDA(g_) {
        constexpr int g = g_.value;
        constexpr int a = g < 8 ? 0 : g < 15 ? 1 : g < 21 ? 2 : g < 26 ? 3 : g < 30 ? 4 : g < 33 ? 5 : g < 35 ? 6 : 7;
        constexpr int e = g - (a * 8 - a * (a - 1) / 2);
        if constexpr (R == 0 && a == 0) mf0(ca[e], A[0], B[e]); else mf(ca[a + e], A[a], B[e]);
        SCHED();
        if constexpr (R == 0 && g < 16 && (g & 3) != 3) { constexpr int n = g - (g >> 2); CV_LOAD_A(A, 2 + (n >> 1), 0, n & 1) SCHED(); }
        if constexpr (PF && g < 32 && (g & 3) != 3) { if constexpr ((g & 3) == 2) CV_LOAD_B(Bn, g >> 2, R + 1) else CV_LOAD_A(An, g >> 2, R + 1, g & 1) SCHED(); }
        site<3000 + 36 * R + g>();
        if constexpr (R == 7) { last(g_); SCHED(); }
        if constexpr ((g & 3) == 3 && g < 28) { fill(std::integral_constant<int, 7 * R + g / 4>{}); SCHED(); }
    });
}
template <int R0, int R1, bool PFL, class F, class G>
DI void conv_part(f32x4 (&ca)[8], v4u (&A0)[8], v4u (&B0)[8], const char* smem, const int (&ab)[4], int bb, F&& fill, G&& last) {
    v4u A1[8], B1[8];
    static_for<R1 - R0>(LAMBDA(r_) {
        constexpr int r = r_.value;
        if constexpr (r & 1) conv_round<R0 + r, (R0 + r + 1 < R1 || PFL)>(ca, A1, B1, A0, B0, smem, ab, bb, fill, last);
        else conv_round<R0 + r, (R0 + r + 1 < R1 || PFL)>(ca, A0, B0, A1, B1, smem, ab, bb, fill, last);
    });
}
template <int IMM> DI void gld(v4u& dst, unsigned voff, const void* sbase) {
    asm volatile("global_load_dwordx4 %0, %1, %2 offset:%3" : "=v"(dst) : "v"(voff), "s"(sbase), "i"(IMM));
}
#define OOB 3
#define OUTP 16
#define NTM 31
template <int IMM, int BIT> DI void gldn(v4u& dst, unsigned voff, const void* sbase) {
    if constexpr ((NTM >> BIT) & 1) asm volatile("global_load_dwordx4 %0, %1, %2 offset:%3 nt" : "=v"(dst) : "v"(voff), "s"(sbase), "i"(IMM));
    else gld<IMM>(dst, voff, sbase);
}
DI void gldu16(unsigned& dst, unsigned voff, const void* sbase) { asm volatile("global_load_ushort %0, %1, %2" : "=v"(dst) : "v"(voff), "s"(sbase)); }
DI void gld1s(unsigned& dst, unsigned voff, const void* sbase) { asm volatile("global_load_dword %0, %1, %2 sc1" : "=v"(dst) : "v"(voff), "s"(sbase)); }
template <int IMM> DI void gld64(v4u& dst, const void* a) {
    if constexpr ((NTM >> 5) & 1) asm volatile("global_load_dwordx4 %0, %1, off offset:%2 nt" : "=v"(dst) : "v"(a), "i"(IMM));
    else asm volatile("global_load_dwordx4 %0, %1, off offset:%2" : "=v"(dst) : "v"(a), "i"(IMM));
}
template <int V> DI void SETM0(unsigned ldsbase) { asm volatile("s_add_u32 m0, %0, %1" ::"s"(ldsbase), "i"(V)); }
template <int GOFF> DI void GLD0(unsigned voff, const char* sbase) { asm volatile("global_load_lds_dwordx4 %0, %1 offset:%2" ::"v"(voff), "s"(sbase), "i"(GOFF) : "memory"); }
template <int GOFF, int V> DI void GLD1(unsigned voff, const char* sbase, unsigned ldsbase) {
    asm volatile("global_load_lds_dwordx4 %0, %1 offset:%3\n\ts_add_u32 m0, %2, %4" ::"v"(voff), "s"(sbase), "s"(ldsbase), "i"(GOFF), "i"(V) : "memory");
}
template <int L, int G> DI void dma_x(unsigned voff, const void* sbase, unsigned ldsbase) {
    asm volatile("s_add_u32 m0, %2, %4\n\ts_nop 0\n\tglobal_load_lds_dwordx4 %0, %1 offset:%3" ::"v"(voff), "s"(sbase), "s"(ldsbase), "i"(G), "i"(L - G) : "memory", "scc");
}
DI void ld_kv(v4u (&a)[15], int on,        unsigned kh, unsigned ko, v4u rk, const void* k, const void* v) {
    asm volatile("s_cmp_ge_u32 %11, 80\n\ts_cbranch_scc1 1f\n\t"
                 "buffer_load_dwordx4 %0, %12, %14, 0 offen nt\n\tbuffer_load_dwordx4 %1, %12, %14, 0 offen offset:1280 nt\n\tbuffer_load_dwordx4 %2, %12, %14, 0 offen offset:2560 nt\n\t"
                 "global_load_dwordx4 %3, %13, %15 nt\n\tglobal_load_dwordx4 %4, %13, %15 offset:1280 nt\n\tglobal_load_dwordx4 %5, %13, %15 offset:2560 nt\n\tglobal_load_dwordx4 %6, %13, %15 offset:3840 nt\n\t"
                 "global_load_dwordx4 %7, %13, %16 nt\n\tglobal_load_dwordx4 %8, %13, %16 offset:1280 nt\n\tglobal_load_dwordx4 %9, %13, %16 offset:2560 nt\n\tglobal_load_dwordx4 %10, %13, %16 offset:3840 nt\n1:"
                 : "=&v"(a[0]), "=&v"(a[1]), "=&v"(a[2]), "=&v"(a[3]), "=&v"(a[4]), "=&v"(a[5]), "=&v"(a[6]), "=&v"(a[7]), "=&v"(a[8]), "=&v"(a[9]), "=&v"(a[10])
                 : "s"(on), "v"(kh), "v"(ko), "s"(rk), "s"(k), "s"(v) : "scc");
}
DI void ld_wr(v4u (&a)[4], int on, unsigned o, const void* base) {
    asm volatile("s_cmp_ge_u32 %4, 80\n\ts_cbranch_scc0 1f\n\t.rept 4\n\tglobal_load_dword a[32+1], %5, %6\n\t.endr\n\ts_branch 2f\n1:\n\t"
                 "global_load_dwordx4 %0, %5, %6 nt\n\tglobal_load_dwordx4 %1, %5, %6 offset:1024 nt\n\tglobal_load_dwordx4 %2, %5, %6 offset:2048 nt\n\tglobal_load_dwordx4 %3, %5, %6 offset:3072 nt\n2:"
                 : "=&v"(a[0]), "=&v"(a[1]), "=&v"(a[2]), "=&v"(a[3]) : "s"(on), "v"(o), "s"(base) : "scc");
}
template <int L, int G> DI void dma_1(unsigned voff, const void* sbase, unsigned ldsbase) {
    asm volatile("s_add_u32 m0, %2, %4\n\ts_nop 0\n\tglobal_load_lds_dword %0, %1 offset:%3" ::"v"(voff), "s"(sbase), "s"(ldsbase), "i"(G), "i"(L - G) : "memory", "scc");
}
template <int L> DI void dma_n(unsigned voff, const void* sbase, unsigned ldsbase) {
    asm volatile("s_add_u32 m0, %2, %3\n\ts_nop 0\n\tglobal_load_lds_dwordx4 %0, %1 nt" ::"v"(voff), "s"(sbase), "s"(ldsbase), "i"(L) : "memory", "scc");
}
template <int A, int O> DI void lds2a(unsigned a) { asm volatile("ds_read_b64 a[%c1:%c1+1], %0 offset:%2" ::"v"(a), "i"(A), "i"(O) : "memory"); }
constexpr int L_EAR = L_XR + 8192;
constexpr int dma_goff(int it) { return it < 40 ? (it >> 2) * 128 : ((it - 100) / 5) * 256; }
constexpr int dma_loff(int it) { return it < 40 ? L_XR + ((it >> 2) % 3) * 4096 + (it & 3) * 1024 : ((it - 100) % 5) * 20480 + ((it - 100) / 5) * 2048; }
#define KFS 4
#define APOS 0
#define PSA 0
#define CWP 2
#define SAF 2
#define KD0 30
#define KST 59
#define KSL 5
#define KSC 11
#define WSP 1
#define XG0 5
#define XGS 1
#define WOD 1
#define USP 1
#define KSG 0
#define PKF 7
#define XDM 1
#define PSP 1
#define SBL 1
#define SMP 1
#define KW2E 0
#define KW2T 4
#define KQ0 0
#define KQ1 18
#define KQ2 4
#define KQ3 0
#define NPD 27
#define IMP 0
#define KSP 1
constexpr int kQ0 = KQ0, kQ1 = KQ1, kQ2 = KQ2, kQ3 = KQ3;
#define KW2S 16
#define KW2M 0x42
#define KFKS 13
#define KFKG 6
#define KUS 17
#define KTS 19
#define KFWP 13
constexpr int kW2S = KW2S, kW2M = KW2M;
constexpr int NW2P = (20 - kW2S) * __builtin_popcount(kW2M);
constexpr int P1_PRE = NPD + 7;
constexpr int p1_dma(int i) { return i < 5 ? 100 + i : i < 13 ? i - 5 : i < 18 ? 105 + (i - 13) : i < 22 ? 8 + (i - 18) : i < NPD ? 110 + (i - 22) : 400; }
constexpr int kQ1a = KST > KD0 ? kQ1 * (KST - KD0) / (117 - KD0) : 0;
constexpr int p1_pre(int i) { return i < kQ0 + kQ1a ? p1_dma(i) : i == kQ0 + kQ1a ? 400 : p1_dma(i - 1); }
constexpr int p1_item(int s, int g) {
    if (s == KFS && g == (XG0 == 5 ? 4 : 9)) return 324;
    if (WOD ? (s & 1) && s < 10 && (g == 3 || g == 8) : g == 9 && s < 10) return WOD ? 114 + s + (g == 8) : 115 + s;
    if (s == KSL && (g == KSG || g == KSG + 1)) return 202 + g - KSG;
    if (s == KFWP && g == 1) return 208;
    if (s == KFKS && g == KFKG) return 206;
    if (USP && s == KUS && (g == 0 || g == 2 || g == 4 || g == 7)) return 210 + g / 2;
    if (s == KTS && g == 9) return 209;
    if (s >= kW2S && ((kW2M >> g) & 1)) return 500 + (s - kW2S) * __builtin_popcount(kW2M) + __builtin_popcount(kW2M & ((1 << g) - 1));
    if ((s & 1) == 0 && s <= 12 && g >= XG0 && (g - XG0) % XGS == 0 && (g - XG0) / XGS < 4) return 4 * (s / 2 + 3) + (g - XG0) / XGS;
    return -1;
}
constexpr int p1_cnt(int s, int g) {
    int n = P1_PRE;
    for (int t = 0; t <= s; ++t)
        for (int u = 0; u < 10; ++u) {
            if (t == s && u >= g) break;
            if (p1_item(t, u) >= 0) ++n;
            if (!USP && t == KUS && u == 0) n += 4;
        }
    return n;
}
constexpr int p1_pos(int code) {
    for (int i = 0; i < P1_PRE; ++i) if (p1_pre(i) == code) return i;
    for (int t = 0; t < 20; ++t)
        for (int u = 0; u < 10; ++u) if (p1_item(t, u) == code) return p1_cnt(t, u);
    return -1000;
}
constexpr int p1_lds(int s, int g) {
    constexpr int t0 = XG0 == 5 ? 0 : 5;
    if (s >= 2 && s < 10 && !(s & 1) && g >= t0 && g < t0 + 2) return 2 + s + g - t0;
    if (s == KSC + 1 && g == t0 + 2) return 38;
    if (s >= 14 && s < 19 && !(s & 1) && (g == 5 || g == 8)) return 30 + (s - 14) + g / 8;
    if (s == 19 && (g == 0 || g == 4)) return 36 + g / 4;
    if (s == 19 && g == 7) return 39;
    return -1;
}
constexpr int p1_nl(int s, int g0, int g1) {
    int n = 0;
    for (int g = g0; g < g1; ++g) n += (s < 19 && g < 5) + ((s & 1) && s < 19 && g >= 5 && g < 9) + (p1_lds(s, g) >= 0);
    return n;
}
constexpr int p1_cap(int s, int g) {
    int u = p1_nl(s, g, g + 1) + (g == 9 && s < 19) + ((s & 1) && s < 19 && g == 5) + ((s & 1) == 0 && g == XG0 - 1);
    const int it = p1_item(s, g), nt = g < 9 ? p1_item(s, g + 1) : -1;
    u += (it >= 0) + (nt >= 0 && nt < 200) + (it == 324);
    if ((s == 19 && g == 2) || (g == 9 && ((s & 3) == 2 || s == KUS - 1 || s == 18))) u += 2;
    return u < 2 ? 2 - u : 0;
}
constexpr int NFILL = 112;
constexpr int p1_from(int f) { return f < 40 ? 0 : f < 52 ? 9 : f < 72 ? KSC : 14; }
constexpr int p1_fill(int s, int g, int e) {
    int f = 0;
    for (int t = 0; t <= s; ++t)
        for (int u = 0; u < 10; ++u)
            for (int v = 0; v < p1_cap(t, u); ++v) {
                const bool go = f < NFILL && p1_from(f) <= t;
                if (t == s && u == g && v == e) return go ? f : -1;
                f += go;
                if (f == 40) f = 48;
            }
    return f;
}
static_assert(p1_fill(13, 10, 0) >= 52, "");
constexpr bool p1_all() {
    for (int i = 0; i < 40; ++i) if (p1_pos(i) < 0) return false;
    for (int i = 100; i < 125; ++i) if (p1_pos(i) < 0) return false;
    for (int i = 0; i < NW2P; ++i) if (p1_pos(500 + i) < 0) return false;
    return p1_pos(202) >= 0 && p1_pos(203) >= 0 && p1_pos(206) >= 0 && p1_pos(208) >= 0 && p1_pos(209) >= 0 && p1_pos(324) >= 0;
}
static_assert(p1_all() && p1_lds(14, 5) == 30 && p1_lds(14, 8) == 31 && p1_lds(16, 5) == 32, "");
constexpr int ring_req(int G, int I, int R, int X) {
    int n = I, e = 0;
    for (int t = 0; t <= G; ++t) {
        int r = -1;
        if (t % 5 == 0 || t % 5 == 3) {
            if (n < 40 && 5 * (n - R) + 4 < t) r = n++;
            else if (e < X && t % 10 == 3) r = 100 + e++;
        }
        if (t == G) return r;
    }
    return -1;
}
constexpr int g2_adr(int i, int kk) {
    int n = 0;
    for (int t = 0; t <= i; ++t)
        for (int u = 0; u < 5; ++u) {
            const bool ok = u == 4 || (u == 2 && (t < 11 || t > 15)) || (u == 3 && t < 5);
            if (t == i && u == kk) return ok ? n : -1;
            n += ok;
        }
    return -1;
}
constexpr int p1_lim(int n) { return n > 63 ? 63 : n; }
template <int IMM> DI void gld4s(v4u& dst, unsigned voff, const void* sbase) { asm volatile("global_load_dwordx4 %0, %1, %2 offset:%3 sc1" : "=v"(dst) : "v"(voff), "s"(sbase), "i"(IMM)); }
template <int IMM> DI void gld2s(v2u& dst, unsigned voff, const void* sbase) { asm volatile("global_load_dwordx2 %0, %1, %2 offset:%3 sc1" : "=v"(dst) : "v"(voff), "s"(sbase), "i"(IMM)); }
template <class T> DI void pin(T& v) { asm volatile("" : "+v"(v)); }
DI void mul2(float& a, float& b, float x, float y, float s) { asm volatile("v_mul_f32 %0, %2, %4\n\tv_mul_f32 %1, %3, %4" : "=&v"(a), "=&v"(b) : "v"(x), "v"(y), "v"(s)); }
DI void xor2(float& a, float& b, unsigned x, unsigned y, unsigned s) { asm volatile("v_xor_b32 %0, %2, %4\n\tv_xor_b32 %1, %3, %4" : "=&v"(a), "=&v"(b) : "v"(x), "v"(y), "v"(s)); }
DI void rot2(unsigned& a, unsigned& b, unsigned x, unsigned y) { asm volatile("v_alignbit_b32 %0, %2, %2, 16\n\tv_alignbit_b32 %1, %3, %3, 16" : "=&v"(a), "=&v"(b) : "v"(x), "v"(y)); }
DI void mix2(unsigned& a, unsigned& b, unsigned x, unsigned y, unsigned z) { asm volatile("v_bfi_b32 %0, %5, %2, %3\n\tv_bfi_b32 %1, %5, %3, %4" : "=&v"(a), "=&v"(b) : "v"(x), "v"(y), "v"(z), "s"(0xffffu)); }
template <int I> DI void shr1(unsigned& o, unsigned prev, unsigned x) {
    if constexpr (I > 0) asm volatile("v_mov_b32_dpp %0, %1 wave_ror:1 row_mask:0xf bank_mask:0xf\n\tv_mov_b32_dpp %0, %2 wave_shr:1 row_mask:0xf bank_mask:0xf" : "=&v"(o) : "v"(prev), "v"(x));
    else asm volatile("v_mov_b32_dpp %0, %1 wave_shr:1 row_mask:0xf bank_mask:0xf bound_ctrl:0" : "=&v"(o) : "v"(x));
}
template <int G> DI void bc2(float& a, float& b, unsigned x) {
    asm volatile("v_mov_b32_dpp %0, %2 row_newbcast:%3 row_mask:0xf bank_mask:0xf\n\tv_mov_b32_dpp %1, %2 row_newbcast:%4 row_mask:0xf bank_mask:0xf" : "=&v"(a), "=&v"(b) : "v"(x), "i"(G), "i"(G + 8));
}
DI void rot1(unsigned& a, unsigned x) { asm volatile("v_alignbit_b32 %0, %1, %1, 16" : "=v"(a) : "v"(x)); }
DI void mix1(unsigned& a, unsigned x, unsigned y) { asm volatile("v_bfi_b32 %0, %3, %1, %2" : "=v"(a) : "v"(x), "v"(y), "s"(0xffffu)); }
DI void ror1(unsigned& o, unsigned prev) { asm volatile("v_mov_b32_dpp %0, %1 wave_ror:1 row_mask:0xf bank_mask:0xf" : "=v"(o) : "v"(prev)); }
DI void shr1b(unsigned& o, unsigned x) { asm volatile("v_mov_b32_dpp %0, %1 wave_shr:1 row_mask:0xf bank_mask:0xf" : "+v"(o) : "v"(x)); }
DI void xor1(float& a, unsigned x, unsigned s) { asm volatile("v_xor_b32 %0, %1, %2" : "=v"(a) : "v"(x), "v"(s)); }
template <int G> DI void bc1(float& a, unsigned x) { asm volatile("v_mov_b32_dpp %0, %1 row_newbcast:%2 row_mask:0xf bank_mask:0xf" : "=v"(a) : "v"(x), "i"(G)); }
template <int G> DI void mulb(float& o, unsigned x, float y) { asm volatile("v_mul_f32_dpp %0, %1, %2 row_newbcast:%3 row_mask:0xf bank_mask:0xf" : "=v"(o) : "v"(x), "v"(y), "i"(G)); }
template <int N> DI void lgkm() { __builtin_amdgcn_s_waitcnt(0xC07F | (N << 8)); asm volatile("" ::: "memory"); }
DI void unp2(float& a, float& b, unsigned x) { asm volatile("v_lshlrev_b32 %0, 16, %2\n\tv_and_b32 %1, 0xffff0000, %2" : "=&v"(a), "=&v"(b) : "v"(x)); }
#define GL(a) "global_load_dwordx4 a[" #a ":" #a "+3], %7, %8 sc1\n\tglobal_load_dwordx4 a[" #a "+4:" #a "+7], %7, %8 offset:256 sc1\n\t"
#define AR(d, a, o) "v_accvgpr_read_b32 " d ", a[" #a "+" #o "]\n\t"
#define GS_(a) "s_waitcnt vmcnt(4)\n\t" AR("%2", a, 1) AR("%3", a, 3) AR("%4", a, 5) AR("%5", a, 7) AR("%0", a, 0) AR("%1", a, 2)                              \
    "v_xor_b32 %2, %9, %2\n\tv_bitop3_b32 %4, %4, %9, %2 bitop3:0xbe\n\tv_bitop3_b32 %4, %3, %9, %4 bitop3:0xbe\n\tv_bitop3_b32 %4, %5, %9, %4 bitop3:0xbe\n\t" \
    AR("%2", a, 4) AR("%3", a, 6) "v_cmp_ne_u32 vcc, 0, %4\n\ts_cbranch_vccz 2f\n\t" GL(a)
template <int A, int D> DI void gran_req(unsigned off, const void* base) {
    if constexpr (D > 0) __builtin_amdgcn_s_sleep(D);
    asm volatile("global_load_dwordx4 a[%c2:%c2+3], %0, %1 sc1\n\tglobal_load_dwordx4 a[%c2+4:%c2+7], %0, %1 offset:256 sc1" ::"v"(off), "s"(base), "i"(A) : "memory");
}
DI void pipe_gran(float& a, float& d, unsigned off, const void* base, unsigned tag, bool& dead) {
    unsigned n = dead ? 0u : kSpinCap, y, z;
    float a1, d1;
    asm volatile("1:\n\t" GS_(32) GS_(40) GS_(48) "s_sub_u32 %6, %6, 1\n\ts_cbranch_scc0 1b\n2:"
                 : "=&v"(a), "=&v"(d), "=&v"(a1), "=&v"(d1), "=&v"(y), "=&v"(z), "+s"(n) : "v"(off), "s"(base), "s"(tag) : "memory", "vcc", "scc");
    dead = __builtin_amdgcn_readfirstlane(n) == ~0u;
    a += a1; d += d1;
}
DI void fma2(float& a, float& b, float x, float y, float s, float t, float u, float v) {
    asm volatile("v_fma_f32 %0, %2, %4, %6\n\tv_fma_f32 %1, %3, %5, %7" : "=&v"(a), "=&v"(b) : "v"(x), "v"(y), "v"(s), "v"(t), "v"(u), "v"(v));
}
DI void fma2p(f32x2& o, float x, float y, float r, float u, float v) {
    asm("v_fmac_f32 %0, %1, %2" : "+v"(u) : "v"(x), "v"(r));
    asm("v_fmac_f32 %0, %1, %2" : "+v"(v) : "v"(y), "v"(r));
    o = f32x2{u, v};
}
DI void swp2(unsigned& a, unsigned& b, unsigned& c, unsigned& d) { asm volatile("s_nop 1\n\tv_permlane16_swap_b32 %0, %1\n\tv_permlane16_swap_b32 %2, %3" : "+v"(a), "+v"(b), "+v"(c), "+v"(d)); }
DI void swp2n(unsigned& a, unsigned& b, unsigned& c, unsigned& d) { asm volatile("v_permlane16_swap_b32 %0, %1\n\tv_permlane16_swap_b32 %2, %3" : "+v"(a), "+v"(b), "+v"(c), "+v"(d)); }
DI void cp2(unsigned& a, unsigned& b, f32x4 v) { asm volatile("v_cvt_pk_bf16_f32 %0, %2, %3\n\tv_cvt_pk_bf16_f32 %1, %4, %5" : "=&v"(a), "=v"(b) : "v"(v.x), "v"(v.y), "v"(v.z), "v"(v.w)); }
DI void zero2(v2u& a) { asm volatile("v_mov_b64 %0, 0" : "=v"(a)); }
DI void mov1(unsigned& a, unsigned b) { asm volatile("v_mov_b32 %0, %1" : "=v"(a) : "v"(b)); }
DI void sw16(unsigned& a, unsigned& b) { asm volatile("v_permlane16_swap_b32 %0, %1" : "+v"(a), "+v"(b)); }
DI void sw32(unsigned& a, unsigned& b) { asm volatile("v_permlane32_swap_b32 %0, %1" : "+v"(a), "+v"(b)); }
DI void mrg8(float& a, float b) { asm volatile("v_mov_b32_dpp %0, %1 quad_perm:[0,1,2,3] row_mask:0xf bank_mask:0xc" : "+v"(a) : "v"(b)); }
DI void sel1(float& o, unsigned a, unsigned b, u64 m) { asm volatile("v_cndmask_b32 %0, %1, %2, %3" : "=v"(o) : "v"(a), "v"(b), "s"(m)); }
DI void ne1(u64& m, v2u tag, v2u a) { asm volatile("v_cmp_ne_u64 %0, %1, %2" : "=s"(m) : "v"(tag), "v"(a)); }
DI void ne2(u64& m, v2u tag, v2u a, v2u b) { asm volatile("v_cmp_ne_u64 vcc, %1, %2\n\tv_cmp_ne_u64 %0, %1, %3\n\ts_or_b64 %0, %0, vcc" : "=&s"(m) : "v"(tag), "v"(a), "v"(b) : "vcc", "scc"); }
template <int G> DI void bc2b(float& a, float& b, float x, float y) {
    asm volatile("v_mov_b32_dpp %0, %2 row_newbcast:%4 row_mask:0xf bank_mask:0xf\n\tv_mov_b32_dpp %1, %3 row_newbcast:%4 row_mask:0xf bank_mask:0xf" : "=&v"(a), "=&v"(b) : "v"(x), "v"(y), "i"(G));
}
template <class T> DI void pins(T& v) { asm volatile("" : "+s"(v)); }
template <int I> DI void gld_w2(unsigned voff, const void* sbase) {
    asm volatile("global_load_dwordx4 " WA(2) ", %0, %1 offset:%3 sc1" ::"v"(voff), "s"(sbase), "i"(W2R + 4 * I), "i"((I % 5) * 1024 - 2048));
}
DI void gld2t(v2u& dst, unsigned voff, const void* sbase) { asm volatile("global_load_dwordx2 %0, %1, %2 sc1" : "+v"(dst) : "v"(voff), "s"(sbase)); }
DI void wait2(v2u& v, v4u (&x)[3]) { asm volatile("s_waitcnt vmcnt(0)" : "+v"(v), "+v"(x[0]), "+v"(x[1]), "+v"(x[2])::"memory"); }
template <int IMM> DI void gld4p(v4u& dst, unsigned voff, const void* sbase) { asm volatile("global_load_dwordx4 %0, %1, %2 offset:%3" : "+v"(dst) : "v"(voff), "s"(sbase), "i"(IMM)); }
DI void undef4(v4u (&x)[4]) { asm volatile("" : "=v"(x[0]), "=v"(x[1]), "=v"(x[2]), "=v"(x[3])); }
DI void wait1(v2u& v) { asm volatile("s_waitcnt vmcnt(0)" : "+v"(v)::"memory"); }
template <int N> DI void wait_fkn(v2u& v, v2u& u) { asm volatile("s_waitcnt vmcnt(%2)" : "+v"(v), "+v"(u) : "i"(N) : "memory"); }
template <int A, int IMM> DI void lda2(unsigned o, const void* base) { asm volatile("global_load_dwordx2 a[%c2:%c2+1], %0, %1 offset:%3" ::"v"(o), "s"(base), "i"(A), "i"(IMM)); }
DI void wait_fk(v2u& v, v2u& u, unsigned& x) { asm volatile("s_waitcnt vmcnt(0)" : "+v"(v), "+v"(u), "+v"(x)::"memory"); }
DI void ld_ur(unsigned o, const void* base) {
    asm volatile("global_load_dwordx4 a[40:40+3], %0, %1 sc1\n\tglobal_load_dwordx4 a[44:44+3], %0, %1 offset:1024 sc1\n\t"
                 "global_load_dwordx4 a[48:48+3], %0, %1 offset:2048 sc1\n\tglobal_load_dwordx4 a[52:52+3], %0, %1 offset:3072 sc1" ::"v"(o), "s"(base));
}
template <int I> DI void ld_ur1(unsigned o, const void* base) { asm volatile("global_load_dwordx4 a[%c2:%c2+3], %0, %1 offset:%3 sc1" ::"v"(o), "s"(base), "i"(40 + 4 * I), "i"(1024 * I)); }
constexpr int kUY = USP ? p1_cnt(20, 0) - 1 - p1_pos(213) : p1_cnt(20, 0) - p1_cnt(KUS, 0) - 4;
DI void st_ur(unsigned a) {
    asm volatile("s_waitcnt vmcnt(%1)\n\tds_write_b128 %0, a[40:40+3] offset:480\n\tds_write_b128 %0, a[44:44+3] offset:1504\n\t"
                 "ds_write_b128 %0, a[48:48+3] offset:2528\n\tds_write_b128 %0, a[52:52+3] offset:3552" ::"v"(a), "i"(kUY < NW2P ? kUY : NW2P) : "memory");
}
template <int I> DI void st_ur1(unsigned a) {
    if constexpr (I == 0) asm volatile("s_waitcnt vmcnt(%0)" ::"i"(kUY) : "memory");
    asm volatile("ds_write_b128 %0, a[%c1:%c1+3] offset:%2" ::"v"(a), "i"(40 + 4 * I), "i"(480 + 1024 * I) : "memory");
}
DI void reload_b(v4u (&B)[8], unsigned a) {
    asm volatile("s_waitcnt lgkmcnt(0)\n\tds_read_b128 %0, %8 offset:448\n\tds_read_b128 %1, %8 offset:960\n\tds_read_b128 %2, %8 offset:1472\n\tds_read_b128 %3, %8 offset:1984\n\t"
                 "ds_read_b128 %4, %8 offset:2496\n\tds_read_b128 %5, %8 offset:3008\n\tds_read_b128 %6, %8 offset:3520\n\tds_read_b128 %7, %8 offset:4032\n\ts_waitcnt lgkmcnt(0)"
                 : "+v"(B[0]), "+v"(B[1]), "+v"(B[2]), "+v"(B[3]), "+v"(B[4]), "+v"(B[5]), "+v"(B[6]), "+v"(B[7]) : "v"(a) : "memory");
}
DI void wait4(v4u (&x)[4]) { asm volatile("s_waitcnt vmcnt(0)" : "+v"(x[0]), "+v"(x[1]), "+v"(x[2]), "+v"(x[3])::"memory"); }
template <int A, int IMM> DI void lda4(unsigned o, const void* base) { asm volatile("global_load_dwordx4 a[%c2:%c2+3], %0, %1 offset:%3" ::"v"(o), "s"(base), "i"(A), "i"(IMM)); }
template <int A> DI void rda(unsigned& v) { asm volatile("v_accvgpr_read_b32 %0, a[%c1+0]" : "=v"(v) : "i"(A)); }
DI void poll1(unsigned a, const void* base) { asm volatile("global_load_dwordx2 a[36:36+1], %0, %1 sc1" ::"v"(a), "s"(base)); }
DI void poll2(unsigned a, unsigned b, const void* base) { asm volatile("global_load_dwordx2 a[36:36+1], %0, %2 sc1\n\tglobal_load_dwordx2 a[36+2:36+3], %1, %2 sc1" ::"v"(a), "v"(b), "s"(base)); }
DI void touch_(unsigned a, const void* base) { asm volatile("global_load_dword a[32+1], %0, %1" ::"v"(a), "s"(base)); }
template <int N> DI void wait_st(v2u& a, v4u& b) { asm volatile("s_waitcnt vmcnt(%2)" : "+v"(a), "+v"(b) : "i"(N) : "memory"); }
DI void undef3(v4u (&x)[3]) { asm volatile("" : "=v"(x[0]), "=v"(x[1]), "=v"(x[2])); }
template <int IMM> DI void gld4t(v4u& dst, unsigned voff, const void* sbase) { asm volatile("global_load_dwordx4 %0, %1, %2 offset:%3 sc1" : "+v"(dst) : "v"(voff), "s"(sbase), "i"(IMM)); }
template <int N> DI void vm_wait() { asm volatile("s_waitcnt vmcnt(%0)" ::"i"(N) : "memory"); }
DI float dot2a(unsigned a, unsigned b, float c) {
    return __builtin_amdgcn_fdot2_f32_bf16(__builtin_bit_cast(bf16x2v, a), __builtin_bit_cast(bf16x2v, b), c, false);
}
DI void disc(unsigned l0, unsigned l1, unsigned l2, f32x2& lam, f32x2& co) {
    const float dt = rbf(__builtin_amdgcn_exp2f(lo16f(l2) * 1.44269504088896f));
    const float lre = lo16f(l0), lim = lo16f(l1);
    const float e_ = __builtin_amdgcn_exp2f(dt * lre * 1.44269504088896f);
    const float rv = __builtin_amdgcn_fractf(dt * lim * 0.159154943091895f);
    lam = f32x2{e_ * __builtin_amdgcn_cosf(rv), e_ * __builtin_amdgcn_sinf(rv)};
    const float nr = lam.x - 1.0f, ni = lam.y, id = __builtin_amdgcn_rcpf(lre * lre + lim * lim);
    co = f32x2{(nr * lre + ni * lim) * id, (ni * lre - nr * lim) * id};
}
DI f32x4 unpk4(v2u u) { return f32x4{lo16f(u.x), hi16f(u.x), lo16f(u.y), hi16f(u.y)}; }
#define D2 "v_dot2_f32_bf16 "
#define CP "v_cvt_pk_bf16_f32 "
DI void d2x4(unsigned* o, unsigned a0, unsigned a1, unsigned a2, unsigned a3, unsigned b0, unsigned b1, unsigned b2, unsigned b3) {
    asm volatile(D2 "%0, %4, %8, 0\n\t" D2 "%1, %5, %9, 0\n\t" D2 "%2, %6, %10, 0\n\t" D2 "%3, %7, %11, 0"
                 : "=&v"(o[0]), "=&v"(o[1]), "=&v"(o[2]), "=&v"(o[3]) : "v"(a0), "v"(a1), "v"(a2), "v"(a3), "v"(b0), "v"(b1), "v"(b2), "v"(b3));
}
DI void cpx4(unsigned* o, unsigned a0, unsigned a1, unsigned a2, unsigned a3, unsigned b0, unsigned b1, unsigned b2, unsigned b3) {
    asm volatile(CP "%0, %4, %8\n\t" CP "%1, %5, %9\n\t" CP "%2, %6, %10\n\t" CP "%3, %7, %11"
                 : "=&v"(o[0]), "=&v"(o[1]), "=&v"(o[2]), "=&v"(o[3]) : "v"(a0), "v"(a1), "v"(a2), "v"(a3), "v"(b0), "v"(b1), "v"(b2), "v"(b3));
}
DI void cpx4i(unsigned* t, const unsigned* ka) {
    asm volatile(CP "%0, %0, %4\n\t" CP "%1, %1, %5\n\t" CP "%2, %2, %6\n\t" CP "%3, %3, %7"
                 : "+v"(t[0]), "+v"(t[1]), "+v"(t[2]), "+v"(t[3]) : "v"(ka[0]), "v"(ka[1]), "v"(ka[2]), "v"(ka[3]));
}
template <int IMM> DI void bld4(v4u& dst, unsigned voff, v4u rsrc) {
    if constexpr ((NTM >> 4) & 1) asm volatile("buffer_load_dwordx4 %0, %1, %2, 0 offen offset:%3 nt" : "=v"(dst) : "v"(voff), "s"(rsrc), "i"(IMM));
    else asm volatile("buffer_load_dwordx4 %0, %1, %2, 0 offen offset:%3" : "=v"(dst) : "v"(voff), "s"(rsrc), "i"(IMM));
}

__global__ __launch_bounds__(256, 1) __attribute__((aligned(4096))) void k_main(Params p) {
    __shared__ __attribute__((aligned(16))) char smem[LDS_BYTES];
    const rsrc_t rs = __builtin_amdgcn_make_buffer_rsrc((void*)p.ws, 0, WS_BYTES, 0x00020000);
    const rsrc_t ro = __builtin_amdgcn_make_buffer_rsrc((void*)p.out, 0, S_ * D_ * 4, 0x00020000);
    const int w0 = __builtin_amdgcn_readfirstlane(threadIdx.x >> 6), b0 = blockIdx.x;
    bool dead = false;
    asm volatile("" ::: "a255");
    float SW[5];
    f32x4 xin[5];
    v2u gt[5];
    f32x4 acc[2][5];
    f32x4 rr[2];
    float tt[2][5][4];
    Lm lam1, lamq;
    f32x2 l8, l32, l128;
    u64 late;
    v4u cA[8], cB[8];
    int sgn_, sg4, ab[4], bb, zpa, uwa, spa, tbw, tbr, tbp, tbq, cwa, cw4, spl, dst[3];
    unsigned w2o, wro, fwo, fko, xso, gso, fgo1, fho1, fho, hso, eo, es, ho[4];
    bool cv0;
    int mt1_;
    auto addrs = [&](auto layer_, int lane, int w, int b) __attribute__((always_inline)) {
        constexpr int layer = layer_.value;
        const int c = lane & 15, q = lane >> 4;
        TILES()
        static_assert(l_sg(0, 0) == SC + 0xA900 && l_sg(0, 1) == SC + 0xB400 && l_sg(0, 2) == SC + 0xCE00 && l_sg(0, 3) == SC + 0xDC00, "");
        sgn_ = (layer ? l_sg(1, 0) + w * INBOX : SC + (int)((0xDC00CE00B400A900ull >> (16 * w)) & 0xffff)) + (q >> 1) * 640 + (q & 1) * 8;
        const int c4 = (c >> 1) * 16 + (c & 1) * 4;
        sg4 = sgn_ + c4; sgn_ += c * 16;
        const int pl = lane < 40 ? lane : 39, tbb = l_tb(layer, 0) + w * (layer ? INBOX : 5120);
        spl = SC + pl * 16;
        eo = OFF_EG + (unsigned)(layer * 163840 + nt1 * 640 + pl * 16);
        es = eo + (unsigned)(mt1 * 10240);
        spa = l_rs(layer, 0) + (2 * (mt2 & 1) + (w >> 1)) * (layer ? INBOX : 256) + (2 * (w & 1) + (c >> 3)) * 64 + (c & 7) * 4;
        tbw = tbb + c * 64 + ((q ^ ((c >> 1) & 3)) * 16);
        tbr = tbb + (2 * (c >> 2) + (q & 1)) * 64 + (((2 * (q >> 1) + ((c >> 1) & 1)) ^ (c >> 2)) * 16) + (c & 1) * 8;
        hso = OFF_HF + (unsigned)(layer * 5242880 + ((8 * mt1 + 2 * w + (q >> 1)) * 160 + 10 * nt1 + 5 * (q & 1)) * 256 + c * 16);
        tbp = tbb + (8 * (q & 1) + (c >> 2)) * 64 + (((2 * (q >> 1) + ((c >> 1) & 1)) ^ (c >> 3)) * 16) + (c & 1) * 8;
        tbq = tbp ^ 32;
        cwa = L_CW + c * 16; cw4 = L_CW + c4;
        pin(sg4); pin(tbp); pin(tbq); pin(cw4);
        fho1 = lane ? kNone : OFF_FH + (unsigned)((((layer * 32 + 2 * mt1 + (w >> 1)) * 4 + (nt1 >> 2)) * 8 + (nt1 & 3) * 2 + (w & 1)) * 128);
        fho = OFF_FH + (unsigned)(((layer * 32 + mt2) * 4 + w) * 1024 + (PLM ? lane >> 3 : lane & 7) * 128);
        pin(sgn_); pin(spl); pin(eo); pin(es); pin(spa); pin(tbw); pin(tbr); pin(hso); pin(cwa); pin(fho1); pin(fho);
        const unsigned hb_ = OFF_HF + (unsigned)(layer * 5242880 + (4 * mt2 * 160 + 40 * w) * 256);
#pragma unroll
        for (int m = 0; m < 4; ++m) { ho[m] = hb_ + (unsigned)(((m + 1 + w) & 3) * 40960 + lane * 16); pin(ho[m]); }
#pragma unroll
        for (int m = 0; m < 3; ++m) { dst[m] = SC + ((w + 1 + m) & 3) * INBOX + m * 5120 + lane * 16; pin(dst[m]); }
        mt1_ = mt1;
        if constexpr (layer == 0) {
            const int r0 = SC + (w < 3 ? w : 0) * REGSZ, y0 = 2031 + 8 * q - c, l34 = lane < 33 ? lane : 33;
#pragma unroll
            for (int i = 0; i < 4; ++i) { ab[i] = r0 + ((y0 & 1) ? CV_O : CV_E) + ((y0 + 1) >> 1) * 4 - 448 - 512 - 1024 * i; pin(ab[i]); }
            bb = r0 + 32 * c + 16 * q;
            zpa = w < 3 ? r0 + l34 * 16 + (l34 >= 30 ? CV_E + 4096 - 480 : 0) + (l34 >= 32 ? CV_O + 4100 - 32 - CV_E - 4096 : 0) : SC + 47360 + lane * 16;
            uwa = (w < 3 ? r0 : SC + 47360 - 480) + lane * 16;
            xso = XDM ? (unsigned)((64 * mt2 + 16 * w + (lane >> 2)) * D_ + 80 * nt2 + 8 * ((lane & 3) ^ q)) * 2u : (unsigned)((64 * mt2 + 16 * w + c) * D_ + 80 * nt2 + 16 * (q & 1) + 8 * (q >> 1)) * 2u;
            gso = OFF_GT + (unsigned)((80 * nt2 + i32 + 32 * w) * 4096 + (q & 1) * 512 + (c >> 2) * 128 + (q >> 1) * 64 + (c & 3) * 16);
            if (GSM && i32 + 32 * w >= 80) gso = kNone;
            fgo1 = lane == 0 && i32 + 32 * w < 80 ? OFF_FG + (unsigned)((nt2 * 96 + i32 + 32 * w) * 128) : kNone;
            pin(bb); pin(zpa); pin(uwa); pin(xso); pin(gso); pin(fgo1);
        }
        SCHED();
    };
    unsigned tag0, tag1;
    v2u tagv;
#define FLAG(off) { if (lane == 0) st64(tagv, rs, off); }
#define ISTAG(f) (((f.x ^ tag0) | (f.y ^ tag1)) == 0u)
#define KW_REQ() { ld_ur(wro, p.ws + OFF_KVT); static_for<NW2P>(LAMBDA(i_) { gld_w2<i_.value>(w2o, p.ws + 2048 + (i_.value / 5) * 5120); }); }
#define FW_OK() { if (__builtin_expect(late != 0ull, 0)) { WAVE_WAIT({ const v2u fk_ = ld64(rs, fko); const v2u fw_ = ld64(rs, fwo); ok = ISTAG(fk_) && ISTAG(fw_); }) KW_REQ() } }
#define WF(g, s) *(const v4u*)(smem + wa[(s) & 1][(g) < 4 ? (g) & 1 : 2] + ((g) & 2) * 20480 + ((s) >> 1) * 1024)
#define TT_GAP(n) { constexpr int mb_ = (n) / 10, nb_ = ((n) % 10) / 2, e_ = ((n) & 1) * 2; \
        mul2(tt[mb_][nb_][e_], tt[mb_][nb_][e_ + 1], rm_[4 * mb_ + e_], rm_[4 * mb_ + e_ + 1], SW[nb_]); }
    {
        const int tid = threadIdx.x, lane = tid & 63, w = w0, b = b0;
        const int c = lane & 15, q = lane >> 4;
        TILES()
        STAMP(0)
        const unsigned lds0 = (unsigned)(size_t)(LAS char*)smem;
        unsigned nonce;
        unsigned cpf0;
        {
            u64 pc;
            int len, len2;
            asm volatile("s_getpc_b64 %0\nkpf_begin:\n\ts_mov_b32 %1, kpf_end-kpf_begin\n\ts_mov_b32 %2, kpf_end2-kpf_begin" : "=s"(pc), "=s"(len), "=s"(len2));
            len = len > len2 ? len : len2;
            const int o0 = (((j & 15) * 4 + w) + 64 * (lane >> 3)) * 128;
            asm volatile("global_load_dword %0, %1, %2" : "=v"(cpf0) : "v"(o0 < len ? o0 : 0), "s"(pc));
        }
        const bool cv = 32 * w + i32 < 80;
        const int chn = 80 * nt2 + i32 + 32 * (cv ? w : 0);
        const bool kw = 32 * w + 31 - i32 < 80;
        const int kp = w == 2 ? 256 + 8 * nt2 + ((i32 & 15) >> 1) : 32 * nt2 + i32, kh_ = w == 2 ? i32 & 1 : w & 1;
        const int kc = KVU ? kp % 10 : (80 * nt2 + (w == 2 ? 64 + (i32 & 15) : i32 + 32 * (w & 1))) % 10, kr = KVU ? 2 * (kp / 10) + kh_ : (80 * nt2 + (w == 2 ? 64 + (i32 & 15) : i32 + 32 * (w & 1))) / 10;
        const int lc = cv ? lane : 8;
        const unsigned xl = lds0 + (unsigned)(w * REGSZ);
        v4u ax[15], sa[5], cr[2], wr[4];
        unsigned uL[3], uM[3], sdq, kso;
        const int cu_ = (b < 200 ? b : 0) * 4 + w, cdb = cu_ / 20, ccg = cu_ % 20;
        {
            unsigned cpo = (unsigned)(32 * ccg + (lane & 31)) * 2u, cpm = (unsigned)(40 * nt1 + (lane < 39 ? lane : 39)) * 2u;
            unsigned ko = (unsigned)(32 * kr + 4 * (lane >> 3)) * 1280u + (unsigned)(64 * kc + 8 * (lane & 7)) * 2u, kh = ko - 3840u;
            pin(cpo); pin(cpm); pin(ko); pin(kh);
            SCHED();
            gldu16(uL[0], cpo, p.Lre); gldu16(uL[1], cpo, p.Lim); gldu16(uL[2], cpo, p.logdt);
            gldu16(uM[0], cpm, p.Lre); gldu16(uM[1], cpm, p.Lim); gldu16(uM[2], cpm, p.logdt);
            const v4u rk = {(unsigned)(u64)p.k, (unsigned)((u64)p.k >> 32) & 0xffffu, S_ * D_ * 2u, 0x00020000u};
            ld_kv(ax, 32 * w + 31 - i32, kh, ko, rk, p.k, p.v);
            SCHED();
        }
        auto small_ = [&]() __attribute__((always_inline)) {
            kso = (unsigned)(512 * kc + lane * 4);
            dma_1<L_EAR + 2048, 0>(kso, p.shk, xl); dma_1<L_EAR + 2304, 256>(kso, p.shk, xl);
            gld1s(nonce, kso & 4u, p.ws + OFF_NONCE);
            SCHED();
        };
        if (SMP == 0) small_();
        const u16* sab = w < 2 ? p.x0 : w == 2 ? p.Bre : p.Bim;
        unsigned sao;
        {
            int r0 = w < 2 ? 128 * mt1 + 8 * nt1 + 4 * w : 40 * nt1 + mt1, r1 = w < 2 ? 1 : 16, r2 = w < 2 ? 2047 : 40 * nt1 + 32 + (mt1 >> 1);
            pins(sab); pins(r0); pins(r1); pins(r2);
            const int r = r0 + q * r1;
            sao = (unsigned)((r < r2 ? r : r2) * 1280 + c * 16);
            pin(sao);
        }
        if (SAF == 0) static_for<5>(LAMBDA(i) { gld<256 * i.value>(sa[i.value], sao, sab); });
        SCHED();
        if (w == TW && !kw) tour<0>();
        STAMP(22)
        const int row8 = lane >> 3;
        const unsigned wvo = (unsigned)(40 * nt1 + row8) * 1280u + (unsigned)(((lane & 7) ^ ((w & 1) | ((row8 >> 1) << 1))) * 16 + (w >> 1) * 128);
        const char* const wbase = (const char*)((w & 1) ? p.Bim : p.Bre);
        const unsigned wl = lds0 + (unsigned)(((w & 1) * 10 + (w >> 1)) * 1024);
        const unsigned xvo0 = (unsigned)(8 * (row8 >> 1) + 2 * (row8 & 1)) * 1280u + (unsigned)(((lane & 7) ^ ((row8 >> 1) << 1)) * 16);
        const unsigned xvo1 = (xvo0 ^ 16u) + 1280u;
        const char* const xbase = (const char*)p.x0 + (size_t)(128 * mt1 + 32 * w) * 1280;
        unsigned xv[4] = {xvo0, xvo1, xvo0 + 5120u, xvo1 + 5120u}, wv[5];
#pragma unroll
        for (int i = 0; i < 5; ++i) { wv[i] = wvo + i * 10240u; pin(wv[i]); if (i < 4) pin(xv[i]); }
#define DMA_M0(it) SETM0<dma_loff(it) - dma_goff(it)>((it) < 40 ? xl : wl)
#define DMA_LD1(it, nx_) GLD1<dma_goff(it), dma_loff(nx_) - dma_goff(nx_)>((it) < 40 ? xv[(it) & 3] : wv[(it) < 40 ? 0 : ((it) - 100) % 5], (it) < 40 ? xbase : wbase, (nx_) < 40 ? xl : wl)
#define DMA_LD(it) GLD0<dma_goff(it)>((it) < 40 ? xv[(it) & 3] : wv[(it) < 40 ? 0 : ((it) - 100) % 5], (it) < 40 ? xbase : wbase)
        const int swz = (c & 1) | ((c >> 2) << 1);
        int wa[2][3];
#pragma unroll
        for (int i = 0; i < 2; ++i) {
            wa[i][0] = (c >> 3) * 20480 + (c & 7) * 128 + (((4 * i + q) ^ (c & 6)) * 16); wa[i][1] = (wa[i][0] ^ 16) + 10240;
            wa[i][2] = 81920 + (c & 1) * 10240 + (c >> 1) * 128 + (((4 * i + q) ^ ((c & 1) | ((c >> 2) << 1))) * 16);
            pin(wa[i][0]); pin(wa[i][1]); pin(wa[i][2]);
        }
        int xlane0 = L_XR + w * REGSZ + (c & 1) * 1024 + (c >> 1) * 128 + ((q ^ swz) * 16);
        int xlane1 = L_XR + w * REGSZ + (c & 1) * 1024 + (c >> 1) * 128 + (((4 + q) ^ swz) * 16);
        unsigned cvo = (unsigned)((16 * cdb + (lane >> 2)) * D_ + 32 * ccg + 8 * ((lane & 3) ^ q)) * 2u, for0, for1;
        int ear = L_EAR + w * REGSZ + c * 64 + (q ^ (c >> 2)) * 16, eas = L_EAR + 2048 + w * REGSZ + (lane & 7) * 64;
        pin(ear); pin(eas);
        {
            const int G = 4 * ccg + q, g5 = G % 5, ch = 2 * (G - g5) + 5 * (g5 & 1) + (g5 & 6), ci = ch + (g5 < 4 ? 1 : 5);
            const unsigned fo = OFF_W2F + (unsigned)(((cdb / 5) * 200 + cdb % 5) * 1024 + c * 16);
            for0 = fo + (unsigned)((ch >> 2) * 5120 + (ch & 3) * 256); for1 = fo + (unsigned)((ci >> 2) * 5120 + (ci & 3) * 256);
        }
        unsigned sto = OFF_ST + (unsigned)((mt1 * 16 + 4 * w + q) * 256 + c * 8), swo = OFF_ST + (unsigned)((PKF & 4) ? ((lane >> 2) * 16 + nt1) * 256 + 128 + (lane & 3) * 16 : (c * 16 + nt1) * 256 + 128 + q * 16);
        unsigned kvo = OFF_KVT + (unsigned)((64 * kc + 8 * (lane >> 3) + ((lane >> 2) & 1)) * 4096 + (32 * kr + 8 * (lane & 3)) * 2);
        int kbw = L_KB + w * 1024 + (lane & 7) * 128 + (((lane >> 3) * 8) ^ ((lane & 7) * 16));
        int kbr = L_KB + w * 1024 + (lane >> 3) * 128 + (((lane & 7) * 16) ^ ((lane >> 3) * 16));
        const unsigned dum = (OOB & 1) ? kNone : OFF_DUM + (unsigned)((b * 4 + w) * 128 + (lane & 7) * 16);
        unsigned fko1 = OFF_FK + (unsigned)((PKF & 1) ? (64 * kc + kr) * 32 : (10 * kr + kc) * 128);
        if (!kw) kvo = dum;
        if (b >= 200) { for0 = dum; for1 = dum; cvo = (unsigned)((b * 4 + w) * 128); }
        wro = (unsigned)(chn * 4096 + lc * 16);
        cv0 = cv;
        pin(kvo); pin(kbw); pin(kbr); pin(fko1);
        pin(xlane0); pin(xlane1); pin(cvo); pin(for0); pin(for1); pin(sto); pin(swo); pin(wro);
        SCHED();
        auto pre = LAMBDA(i_) {
            constexpr int it = p1_dma(i_.value);
            SCHED();
            DMA_M0(it);
            asm volatile("s_nop 0");
            DMA_LD(it);
            SCHED();
        };
        fko = OFF_FK + (unsigned)((PKF & 1) ? ((chn & ~63) + (cv ? lane : 0)) * 32 : ((cv ? lane : 0) * 10 + (chn >> 6)) * 128);
        { const int e_ = (lane & 7) < w ? w : (lane & 7) > w + 4 ? w + 4 : (lane & 7);
          fwo = OFF_FW + (unsigned)((PKF & 2) ? ((5 * nt2 + (lane < 40 ? lane >> 3 : 4)) * 20 + 4 * w + e_) * 32 : ((5 * nt2 + (lane < 25 ? lane / 5 : 0)) * 20 + 5 * w + (lane < 25 ? lane % 5 : 0)) * 128); }
        w2o = OFF_W2F + (unsigned)((nt2 * 40 + 10 * w) * 5120 + lane * 16);
        int ime = SC + (w < 3 ? w * REGSZ + CV_E : 43264) + 1008 - 16 * lane, imo = SC + (w < 3 ? w * REGSZ + CV_O + 4 : 47360) + 1008 - 16 * lane, im3 = w < 3 ? ime + 2048 : imo;
        int tca = L_TAB + c * 136, tcb = tca + 8 + 32 * w + 8 * q, tcc = L_TAB + (32 + (c >> 1)) * 136, tcd = tcc + 8 + 32 * w + 8 * q, tcl = L_TAB + (lane < 40 ? lane : 39) * 136, coa = L_CO + w * REGSZ + q * 64, rsa = l_rs(0, 0) + w * 256 + lane * 4;
        unsigned sgn = (c & 1) ? 0u : 0x80000000u;
        fko1 = kw && lane < 32 ? fko1 : OFF_FW + (unsigned)((b * 4 + w) * ((PKF & 2) ? 32 : 128));
        pin(fko); pin(fwo); pin(w2o); pin(fko1); pin(ime); pin(imo); pin(im3); pin(tca); pin(tcb); pin(tcc); pin(tcd); pin(tcl); pin(coa); pin(rsa); pin(sgn);
        SCHED();
        auto cw_ = [&]() __attribute__((always_inline)) {
            dma_n<L_EAR>(cvo, p.Cre, xl); dma_n<L_EAR + 1024>(cvo, p.Cim, xl);
            dma_1<L_EAR + 2560, 0>((unsigned)(160 * nt2 + (lane < 39 ? lane : 39) * 4), p.Dskip, xl);
            ld_wr(wr, 32 * w + i32, wro, p.ssmk);
            gldu16(sdq, (unsigned)chn * 2u, p.ssmD);
        };
        if (SMP == 1) small_();
        static_assert(SMP == 0 || SAF > 0, "");
        if (SAF == 1) static_for<5>(LAMBDA(i) { gld<256 * i.value>(sa[i.value], sao, sab); });
        if (CWP == 0) cw_();
        STAMP(33)
        asm volatile("s_waitcnt vmcnt(%5)\n\ts_cmp_ge_u32 %4, 80\n\ts_cbranch_scc0 1f\n\ts_waitcnt vmcnt(%6)\n1:" : "+v"(uL[0]), "+v"(uL[1]), "+v"(uL[2]), "+v"(cpf0) : "s"(32 * w + 31 - i32), "i"(14 + (SAF < 2 ? 5 : 0) + (CWP ? 0 : 8)), "i"(3 + (SAF < 2 ? 5 : 0) + (CWP ? 0 : 8)) : "memory", "scc");
        pin(uM[0]); pin(uM[1]); pin(uM[2]);
        {
            f32x2 lam, co;
            disc(uL[0], uL[1], uL[2], lam, co);
            *(f32x2*)(smem + L_CO + w * REGSZ + (lane & 31) * 8) = 2.0f * co;
        }
        SCHED();
        STAMP(35)
        if (SAF == 2) static_for<5>(LAMBDA(i) { gld<256 * i.value>(sa[i.value], sao, sab); });
        if (CWP == 1) cw_();
        if (APOS == 0) addrs(std::integral_constant<int, 0>{}, lane, w, b);
        if (SAF == 3) static_for<5>(LAMBDA(i) { gld<256 * i.value>(sa[i.value], sao, sab); });
        if (CWP == 2) cw_();
        v4u kvt[4];
        unsigned ka[8], kt[8], se[8], vs[8], ko4[4], one2 = 0x3F803F80u;
        pin(one2);
        auto kvop = LAMBDA(n_) {
            constexpr int n = n_.value, h = n ? (n - 1) / 29 : -1, u = n ? (n - 1) % 29 : 26;
            if constexpr (u < 2) d2x4(ka + 4 * u, ax[0][h], ax[1][h], ax[2][h], ax[3][h], se[u], se[u], se[u], se[u]);
            else if constexpr (u == 8) { const v4u sk = ax[11 + h]; se[4] = sk[0] >> 16; se[5] = sk[2] & 0xffff0000u; se[6] = sk[0] & 0xffffu; se[7] = sk[2] << 16; pin(se[4]); pin(se[5]); pin(se[6]); pin(se[7]); }
            else if constexpr (u < 21) {
                constexpr int v = u < 8 ? u - 2 : u - 3, tp = 1 + v / 6, st = v % 6, e = st & 1;
                if constexpr (st < 2) d2x4(kt + 4 * e, ax[tp][h], ax[tp + 1][h], ax[tp + 2][h], ax[tp + 3][h], se[2 * tp + e], se[2 * tp + e], se[2 * tp + e], se[2 * tp + e]);
                else if constexpr (st < 4) cpx4i(kt + 4 * e, ka + 4 * e);
                else d2x4(ka + 4 * e, kt[4 * e], kt[4 * e + 1], kt[4 * e + 2], kt[4 * e + 3], one2, one2, one2, one2);
            }
            else if constexpr (u == 21) { vs[0] = ax[7][h] & 0xffffu; vs[1] = ax[8][h] << 16; vs[2] = ax[9][h] & 0xffffu; vs[3] = ax[10][h] << 16; pin(vs[0]); pin(vs[1]); pin(vs[2]); pin(vs[3]); }
            else if constexpr (u == 22) { vs[4] = ax[7][h] >> 16; vs[5] = ax[8][h] & 0xffff0000u; vs[6] = ax[9][h] >> 16; vs[7] = ax[10][h] & 0xffff0000u; pin(vs[4]); pin(vs[5]); pin(vs[6]); pin(vs[7]); }
            else if constexpr (u == 23) cpx4(ko4, ka[0], ka[2], ka[4], ka[6], ka[1], ka[3], ka[5], ka[7]);
            else if constexpr (u < 26) { constexpr int e = u - 24; d2x4(kt + 4 * e, ko4[2 * e], ko4[2 * e], ko4[2 * e + 1], ko4[2 * e + 1], vs[4 * e], vs[4 * e + 1], vs[4 * e + 2], vs[4 * e + 3]); }
            else if constexpr (u == 26) {
                if constexpr (h < 3) { const v4u sk = ax[12 + h]; se[0] = sk[1] >> 16; se[1] = sk[3] & 0xffff0000u; se[2] = sk[1] & 0xffffu; se[3] = sk[3] << 16; pin(se[0]); pin(se[1]); pin(se[2]); pin(se[3]); }
                else asm volatile("s_nop 2");
            }
            else if constexpr (u == 27) {
                cpx4(ko4, kt[0], kt[2], kt[4], kt[6], kt[1], kt[3], kt[5], kt[7]);
                *(v2u*)(smem + kbw) = v2u{ko4[0], ko4[1]};
                *(v2u*)(smem + (kbw ^ 64)) = v2u{ko4[2], ko4[3]};
            }
            else kvt[h] = *(const v4u*)(smem + kbr);
            SCHED();
        };
        auto deal = [&](auto a_, auto b_, auto n_, auto i_) __attribute__((always_inline)) {
            constexpr int a = a_.value, m = b_.value - a, lo = m * i_.value / n_.value, hi = m * (i_.value + 1) / n_.value;
            static_for<hi - lo>(LAMBDA(e_) { pre(std::integral_constant<int, a + lo + e_.value>{}); });
        };
#define IC(n) std::integral_constant<int, (n)>{}
        static_for<kQ0>(pre);
        asm volatile("s_waitcnt vmcnt(%11)" : "+v"(ax[0]), "+v"(ax[1]), "+v"(ax[2]), "+v"(ax[3]), "+v"(ax[4]), "+v"(ax[5]), "+v"(ax[6]), "+v"(ax[7]), "+v"(ax[8]), "+v"(ax[9]),
                     "+v"(ax[10]) : "i"(13 + kQ0) : "memory");
#pragma unroll
        for (int i = 0; i < 4; ++i) ax[11 + i] = *(const v4u*)(smem + eas + i * 16);
        pin(nonce);
        STAMP(30)
        unsigned tch = 0u;
        auto kvs = LAMBDA(n_) {
            site<1000 + n_.value>(); SCHED();
            kvop(n_);
            if constexpr (n_.value >= KD0) deal(IC(kQ0), IC(kQ0 + kQ1), IC(117 - KD0), IC(n_.value - KD0));
        };
        static_assert((KST - 1) % 29 == 0, "");
        if (kw) static_for<KST>(kvs);
        else {
            if (w == 3) {
                f32x2 lam, co, lp[16];
                disc(uM[0], uM[1], uM[2], lam, co);
                lp[0] = cmul(lam, lam); lp[0] = cmul(lp[0], lp[0]); lp[1] = cmul(lp[0], lp[0]);
                lp[0] = f32x2{1.f, 0.f};
                lp[2] = cmul(lp[1], lp[1]); lp[3] = cmul(lp[2], lp[1]); lp[4] = cmul(lp[2], lp[2]);
                const f32x2 z2 = {0.f, 0.f};
                f32x2 t4[4];
                cfma4(t4, lp[4], lp[1], lp[2], lp[3], lp[4], z2, z2, z2, z2);
                lp[5] = t4[0]; lp[6] = t4[1]; lp[7] = t4[2]; lp[8] = t4[3];
                cfma4(t4, lp[8], lp[1], lp[2], lp[3], lp[4], z2, z2, z2, z2);
                lp[9] = t4[0]; lp[10] = t4[1]; lp[11] = t4[2]; lp[12] = t4[3];
                cfma4(t4, lp[8], lp[5], lp[6], lp[7], lp[7], z2, z2, z2, z2);
                lp[13] = t4[0]; lp[14] = t4[1]; lp[15] = t4[2];
                f32x2* tl = (f32x2*)(smem + tcl);
                tl[0] = lam;
#pragma unroll
                for (int i = 0; i < 16; ++i) tl[1 + i] = lp[i];
                SCHED();
            }
            static_for<kQ1a>(LAMBDA(i_) { pre(IC(kQ0 + i_.value)); });
            undef4(kvt);
            asm volatile("" : "=v"(se[0]), "=v"(se[1]), "=v"(se[2]), "=v"(se[3]));
        }
        tag0 = __builtin_amdgcn_readlane(nonce, 0) + 1u; tag1 = __builtin_amdgcn_readlane(nonce + (tag0 == 0u ? 1u : 0u), 1);
        tagv = v2u{tag0, tag1}; pin(tagv);
        {
            asm volatile("s_waitcnt vmcnt(%5)" : "+v"(sa[0]), "+v"(sa[1]), "+v"(sa[2]), "+v"(sa[3]), "+v"(sa[4]) : "i"(8 + kQ0 + kQ1a) : "memory");
            float s1[2] = {0.f, 0.f}, s2[2] = {0.f, 0.f};
#pragma unroll
            for (int i = 0; i < 5; ++i)
#pragma unroll
                for (int e = 0; e < 4; ++e) { s1[e & 1] = dot2a(sa[i][e], one2, s1[e & 1]); s2[e & 1] = dot2a(sa[i][e], sa[i][e], s2[e & 1]); }
            const float a1 = rowsum16(s1[0] + s1[1]), a2 = rowsum16(s2[0] + s2[1]);
            const float mean = a1 * (1.0f / D_);
            const float var = fmaxf(a2 * (1.0f / D_) - mean * mean, 0.f);
            const float ri = __builtin_amdgcn_rcpf(rbf(__builtin_amdgcn_sqrtf(rbf(rbf(var) + 1e-5f))));
            v2u sv = {__float_as_uint(w >= 2 ? a1 : c ? -ri * rbf(mean) : ri), tag0};
            int o0 = w < 2 ? (mt1 * 16 + nt1) * 256 + 32 * w : ((mt1 >> 2) * 16 + nt1) * 256 + 112 + (mt1 & 3) * 16 + 8 * w, o1 = w < 2 ? 8 : 16384, o2 = w < 2 ? 64 : 0;
            pins(o0); pins(o1); pins(o2);
            unsigned so_ = c < 2 ? OFF_ST + (unsigned)(o0 + q * o1 + c * o2) : kNone;
            pin(sv); pin(so_);
            st64(sv, rs, so_);
            SCHED();
        }
        bool kw2 = kw;
        if (kw2) {
            static_for<117 - KST>(LAMBDA(n_) { kvs(IC(KST + n_.value)); });
        } else {
            if (w == 3) {
            const int l = lane, l3 = l & 3;
            unsigned a = OFF_SG + (unsigned)((((((l >> 3) & 1) * 32 + mt2) * 4 + ((l >> 1) & 3)) * 8 + nt2) * 256 + (l & 1) * 128);
            { unsigned t_ = OFF_EG + (unsigned)((l >= 21 ? 163840 - 640 : 0) + (mt1 * 16 + nt1) * 640 + (l < 26 ? l - 16 : 5) * 128); pin(t_); a = l >= 16 ? t_ : a; }
            { unsigned t_ = OFF_PS + (unsigned)(((mt2 * 4 + l3) * 8 + nt2) * 128); pin(t_); a = l >= 28 ? t_ : a; }
            { unsigned t_ = OFF_FH + (unsigned)((((((l >> 2) & 1) * 32 + 2 * mt1 + (l3 >> 1)) * 4 + (nt1 >> 2)) * 8 + (nt1 & 3) * 2 + (l3 & 1)) * 128); pin(t_); a = l >= 32 ? t_ : a; }
            { unsigned t_ = OFF_FX + (unsigned)(((mt1 * 4 + 2 * (mt2 & 1) + (l3 >> 1)) * 16 + nt2 * 2 + (l3 & 1)) * 128); pin(t_); a = l >= 40 ? t_ : a; }
            { unsigned t_ = OFF_FG + (unsigned)((nt2 * 96 + i32 + 16 * ((l3 + 1) & 6)) * 128); pin(t_); a = l >= 48 ? t_ : a; }
            const unsigned big = (l & 7) < 2 ? OFF_HF + (unsigned)((l & 1) * 5242880 + (8 * mt1 * 160 + 10 * nt1) * 256) : (l & 7) == 2 ? OFF_X2F + (unsigned)(2 * mt2 * 40960 + 2560 * nt2)
                               : (l & 7) < 5 ? OFF_KVT + (unsigned)((l & 1) * 2621440 + (80 * nt2 + i32) * 4096) : OFF_W2F + (unsigned)(nt2 * 204800 + ((l & 7) - 5) * 98304);
            a = l >= 56 ? big : a;
            tch = a;
        }
            static_for<kQ1 - kQ1a>(LAMBDA(i_) { pre(IC(kQ0 + kQ1a + i_.value)); });
            if (w == TW) tour<1>();
            skip<SKIP0>();
            undef4(kvt);
        }
        SCHED();
        STAMP(34)
        if (APOS == 1) addrs(std::integral_constant<int, 0>{}, lane, w, b);
        v4u orr, oi;
        unsigned ie[4][4], io[4][4], iw;
        auto img = LAMBDA(n_) {
            constexpr int i = n_.value / 10, u = n_.value % 10;
            if constexpr (u == 0) { if constexpr (i > 0) ror1(iw, wr[i - 1].w); else { iw = 0u; pin(iw); } }
            else if constexpr (u < 5) rot1(ie[i][u - 1], wr[i][4 - u]);
            else if constexpr (u == 5) shr1b(iw, wr[i].w);
            else if constexpr (u < 9) mix1(io[i][u - 6], wr[i][9 - u], wr[i][8 - u]);
            else mix1(io[i][3], wr[i].x, iw);
        };
        auto tap0 = [&]() __attribute__((always_inline)) {
            wr[0].x = cvtpk(lo16f(wr[0].x) + (lane ? 0.f : lo16f(sdq)), hi16f(wr[0].x));
            pin(wr[0]);
            SCHED();
        };
        {
            static_assert(kQ0 + kQ1 <= 18, "");
            vm_wait<6 + kQ0 + kQ1>();
            cr[0] = *(const v4u*)(smem + ear); cr[1] = *(const v4u*)(smem + ear + 1024);
            static_for<5>(LAMBDA(i) { lds2a<22 + 2 * i.value, 2560 + 32 * i.value>(lds0 + (unsigned)(L_EAR + w * REGSZ + q * 8)); });
            asm volatile("s_waitcnt lgkmcnt(0)" : "+v"(cr[0]), "+v"(cr[1])::"memory");
            static_for<kQ2>(LAMBDA(i_) { pre(IC(kQ0 + kQ1 + i_.value)); });
            static_for<4>(LAMBDA(e_) {
                constexpr int e = e_.value;
                const f32x4 co = *(const f32x4*)(smem + coa + e * 16);
                const f32x2 o0 = lo16f(cr[0][e]) * f32x2{co.x, -co.y} - lo16f(cr[1][e]) * f32x2{co.y, co.x};
                const f32x2 o1 = hi16f(cr[0][e]) * f32x2{co.z, -co.w} - hi16f(cr[1][e]) * f32x2{co.w, co.z};
                orr[e] = cvtpk(o0.x, o1.x); oi[e] = cvtpk(o0.y, o1.y);
            });
            pin(orr); pin(oi);
            SCHED();
            static_for<NPD - kQ0 - kQ1 - kQ2>(LAMBDA(i_) { pre(IC(kQ0 + kQ1 + kQ2 + i_.value)); });
            for (int i = 0; i < 4; ++i) st128(kvt[i], rs, kvo + i * 8192);
            st128(orr, rs, for0); st128(oi, rs, for1);
        }
        SCHED();
        STAMP(23)
        if (w == 3) tour<7>();
        asm volatile("s_waitcnt vmcnt(%5) lgkmcnt(0)" : "+v"(wr[0]), "+v"(wr[1]), "+v"(wr[2]), "+v"(wr[3]), "+v"(sdq) : "i"(P1_PRE - 1 - p1_pos(3)) : "memory");
        __builtin_amdgcn_s_barrier();
        asm volatile("" ::: "memory");
        STAMP(24)
        site<10>();
        v4u wf[2][5], af[2][2][2];
#pragma unroll
        for (int nb = 0; nb < 5; ++nb) wf[0][nb] = WF(nb, 0);
#pragma unroll
        for (int i = 0; i < 4; ++i) af[0][i >> 1][i & 1] = *(const v4u*)(smem + ((i >> 1) ? xlane1 : xlane0) + (i & 1) * 2048);
        SCHED();
        tap0();
        site<11>();
        const char *kvtb = p.ws + OFF_KVT, *w2b0[(NW2P + 4) / 5];
        pins(kvtb);
#pragma unroll
        for (int i = 0; i < (NW2P + 4) / 5; ++i) { w2b0[i] = p.ws + 2048 + i * 5120; pins(w2b0[i]); }
        v2u sg, tc[2], fk, fw;
        v4u sw0;
        v2u zq[2];
        float rr_[8];
        unsigned sx[4], sz[4];
        u64 odd = 0xAAAAAAAAAAAAAAAAull;
        pins(odd);
        auto filler = LAMBDA(f_) {
            constexpr int f = f_.value;
            if constexpr (f < 0) {}
            else if constexpr (f < 40) img(IC(f));
            else if constexpr (f == 48) { lam1.r = __uint_as_float(tc[0].x); xor1(lam1.s, tc[0].y, sgn); }
            else if constexpr (f == 49) { lamq.r = __uint_as_float(tc[1].x); xor1(lamq.s, tc[1].y, sgn); }
            else if constexpr (f < 52) zero2(zq[f - 50]);
            else if constexpr (f == 52) { sx[0] = sw0.x; mov1(sx[1], sx[0]); }
            else if constexpr (f == 53) { sz[0] = sw0.z; mov1(sz[1], sz[0]); }
            else if constexpr (f < 56) bc1<f - 54>(rr_[f - 54], sg.x);
            else if constexpr (f == 56) sw16(sx[0], sx[1]);
            else if constexpr (f == 57) sw16(sz[0], sz[1]);
            else if constexpr (f < 60) mov1(sx[f - 56], sx[f - 58]);
            else if constexpr (f < 62) mov1(sz[f - 58], sz[f - 60]);
            else if constexpr (f < 64) sw32(sx[f - 62], sx[f - 60]);
            else if constexpr (f < 66) sw32(sz[f - 64], sz[f - 62]);
            else if constexpr (f < 72) {
                bc1<f - 64>(rr_[f - 64], sg.x);
                if constexpr (f == 66) { SW[0] = __uint_as_float(sx[0]); SW[1] = __uint_as_float(sz[0]); SW[2] = __uint_as_float(sx[1]); SW[3] = __uint_as_float(sz[1]); }
                if constexpr (f == 71) sel1(SW[4], sx[2], sz[2], odd);
            }
            else mulb<8 + (f - 72) % 8>(tt[(f - 72) % 8 / 4][(f - 72) / 8][(f - 72) % 4], sg.x, SW[(f - 72) / 8]);
        };
        auto kstep = LAMBDA(s_) {
            constexpr int s = s_.value, ph = s >> 2, s4 = s & 3, cu = s & 1, nx = cu ^ 1, jj = s >> 1, pp = jj & 1;
            if constexpr (s4 == 3 && ph < 4) {
                constexpr int pw = p1_pos(104 + 5 * (ph + 1)), px = p1_pos(4 * (jj + 1) + 3), pm = pw > px ? pw : px;
                constexpr int vm = p1_lim(p1_cnt(s, 0) - 1 - pm);
                asm volatile("" ::: "memory");
                __builtin_amdgcn_s_waitcnt((vm & 15) | ((vm >> 4) << 14) | 0x70);
                __builtin_amdgcn_s_barrier();
                asm volatile("" ::: "memory");
                STAMP(25 + ph)
                SCHED();
            }
            if constexpr (s == KSC) {
                static_assert(s4 == 3 && p1_pos(203) < p1_pos(4 * (jj + 1) + 3), "");
                pin(sg); pin(sw0);
                if (__builtin_expect(__builtin_amdgcn_ballot_w64(__builtin_amdgcn_bitop3_b32(__builtin_amdgcn_bitop3_b32(sg.y, sw0.y, tag0, 0x7E), sw0.w, tag0, 0xF6) != 0u) != 0ull, 0))
                    WAVE_WAIT({ sg = ld64(rs, sto); sw0 = ld128(rs, swo); ok = ((sg.y ^ tag0) | (sw0.y ^ tag0) | (sw0.w ^ tag0)) == 0u; })
                ORDER();
                STAMP(7)
            }
            if constexpr (s == kW2S && KUS > kW2S) { vm_wait<p1_cnt(s, 0) - 1 - p1_pos(208)>(); SCHED(); }
            if constexpr (s == KUS) {
                static_assert(p1_pos(206) > p1_pos(208), "");
                wait_fkn<p1_cnt(s, 0) - 1 - p1_pos(206)>(fk, fw);
                if (!USP) ld_ur(wro, kvtb);
                SCHED();
            }
            if constexpr (s4 != 3 || ph == 4) lgkm<(s == 0 ? 0 : p1_nl(s - 1, 5 - cu, 10) - 1)>();
            SCHED();
            static_for<10>(LAMBDA(g_) {
                constexpr int g = g_.value, mb = g / 5, k = g % 5, it = p1_item(s, g), nt_ = g < 9 ? p1_item(s, g + 1) : -1;
                constexpr bool d0 = it >= 0 && it < 200, d1 = nt_ >= 0 && nt_ < 200;
                if constexpr (s == 0) mf0(acc[mb][k], af[pp][cu][mb], wf[cu][k]); else mf(acc[mb][k], af[pp][cu][mb], wf[cu][k]);
                SCHED();
                if constexpr (s < 19) {
                    if constexpr (g < 5) wf[nx][g] = WF(g, s + 1);
                    if constexpr (cu == 1 && g == 5) vm_wait<p1_lim(p1_cnt(s, 5) - 1 - p1_pos(4 * (jj + 1) + 3))>();
                    if constexpr (cu == 1 && g >= 5 && g < 9)
                        af[pp ^ 1][(g - 5) >> 1][(g - 5) & 1] = *(const v4u*)(smem + (((g - 5) >> 1) ? xlane1 : xlane0) + ((jj + 1) % 3) * 4096 + ((g - 5) & 1) * 2048);
                }
                if constexpr (cu == 0 && g == XG0 - 1 && s > 0) lgkm<p1_nl(s - 1, 8, 10) + p1_nl(s, 0, XG0 - 1)>();
                constexpr int lq = p1_lds(s, g);
                if constexpr (lq >= 4 && lq < 8) (lq == 4 ? lam1.a : lq == 5 ? lam1.b : lq == 6 ? lamq.a : lamq.b) = *(const f32x2*)(smem + (lq < 6 ? tca : tcb) + (lq & 1) * 2176);
                else if constexpr (lq == 8 || lq == 9) tc[lq - 8] = *(const v2u*)(smem + (lq == 8 ? tcc : tcd));
                else if constexpr (lq == 10) { const f32x2* tp = (const f32x2*)(smem + tcl); l8 = tp[2]; l32 = tp[5]; }
                else if constexpr (lq == 11) l128 = *(const f32x2*)(smem + tcl + 72);
                else if constexpr (lq >= 30 && lq < 33) *(v4u*)(smem + (lq < 32 ? ime + 1024 * (lq - 30) : im3)) = v4u{ie[33 - lq][0], ie[33 - lq][1], ie[33 - lq][2], ie[33 - lq][3]};
                else if constexpr (lq >= 33 && lq < 37) *(v4u*)(smem + imo + 1024 * (36 - lq)) = v4u{io[lq - 33][0], io[lq - 33][1], io[lq - 33][2], io[lq - 33][3]};
                else if constexpr (lq == 37) *(v4u*)(smem + im3 + 1024) = v4u{ie[0][0], ie[0][1], ie[0][2], ie[0][3]};
                else if constexpr (lq == 38) *(unsigned*)(smem + rsa) = sg.x;
                else if constexpr (lq == 39) *(v4u*)(smem + zpa) = v4u{zq[0].x, zq[0].y, zq[1].x, zq[1].y};
                static_for<p1_cap(s, g)>(LAMBDA(e_) { filler(IC(p1_fill(s, g, e_.value))); });
                if constexpr (s == 18 && g == 9) ne2(late, tagv, fk, fw);
                if constexpr (s == 19 && g == 2) { if (__builtin_expect(late != 0ull, 0)) poll2(fko, fwo, p.ws); }
                if constexpr (it >= 0) {
                    if constexpr (d0 && d1) DMA_LD1(it, d1 ? nt_ : 0);
                    else if constexpr (d0) DMA_LD(it);
                    else if constexpr (it == 202) gld2s<0>(sg, sto, p.ws);
                    else if constexpr (it == 203) gld4s<0>(sw0, swo, p.ws);
                    else if constexpr (it == 206) gld2s<0>(fk, fko, p.ws);
                    else if constexpr (it == 208) gld2s<0>(fw, fwo, p.ws);
                    else if constexpr (it == 209) touch_(tch, p.ws);
                    else if constexpr (it >= 210 && it < 214) ld_ur1<it - 210>(wro, kvtb);
                    else if constexpr (it >= 500) gld_w2<it % 50>(w2o, w2b0[(it % 50) / 5]);
                    else { vm_wait<p1_cnt(s, g) - P1_PRE>(); st64(tagv, rs, fko1); }
                }
                if constexpr (d1 && !d0) DMA_M0(d1 ? nt_ : 0);
                site<2000 + 10 * s + g>();
                SCHED();
            });
        };
        static_for<20>(kstep);
        static_for<NFILL - p1_fill(19, 10, 0)>(LAMBDA(i_) { filler(IC(p1_fill(19, 10, 0) + i_.value)); });
        SCHED();
        rr[0] = f32x4{rr_[0], rr_[1], rr_[2], rr_[3]}; rr[1] = f32x4{rr_[4], rr_[5], rr_[6], rr_[7]};
        ACC_FENCE10(acc[0], acc[1]);
        STAMP(1)
    }

    auto layer_body = LAMBDA(layer_) {
        constexpr int layer = layer_.value;
        const int sga = sgn_;
        const bool cv = layer == 0 && cv0;
        v2u fh;
        unsigned ks[10];
        f32x4 ca[8], a2[4][5];
        unsigned gp[4][4];
        v4u gq[4];
        auto gst = LAMBDA(i_) {
            constexpr int i = i_.value >> 2, u = i_.value & 3;
            if constexpr (u < 2) cp2(gp[i][2 * u], gp[i][2 * u + 1], ca[2 * i + u]);
            else if constexpr (u == 2) swp2n(gp[i][0], gp[i][2], gp[i][1], gp[i][3]);
            else if constexpr (GSM) { gq[i] = v4u{gp[i][0], gp[i][1], gp[i][2], gp[i][3]}; pin(gq[i]); }
            else st128(v4u{gp[i][0], gp[i][1], gp[i][2], gp[i][3]}, rs, gso + i * 1024);
        };
        float rsr, rsm;
        St bv[8];
        {
            St a0, a1;
            static_for<2>(LAMBDA(m_) {
                constexpr int mb = m_.value;
#pragma unroll
                for (int i = 0; i < 4; ++i) fma2p(bv[4 * mb + i].a, acc[mb][0][i], acc[mb][1][i], rr[mb][i], tt[mb][0][i], tt[mb][1][i]);
#pragma unroll
                for (int i = 0; i < 4; ++i) fma2p(bv[4 * mb + i].b, acc[mb][2][i], acc[mb][3][i], rr[mb][i], tt[mb][2][i], tt[mb][3][i]);
                const f32x4 v = __builtin_elementwise_fma(acc[mb][4], rr[mb], f32x4{tt[mb][4][0], tt[mb][4][1], tt[mb][4][2], tt[mb][4][3]});
#pragma unroll
                for (int i = 0; i < 4; ++i) bv[4 * mb + i].c = v[i];
                SCHED();
                if constexpr (mb == 0) run3(a0, bv[0], bv + 1, lam1); else run4(a1, a0, bv + 4, lam1);
                SCHED();
            });
            *(f32x2*)(smem + sga) = a1.a; *(f32x2*)(smem + sga + 256) = a1.b; *(float*)(smem + sg4 + 512) = a1.c;
        }
        SCHED();
        const int w_ = w0;
        {
        const int w = w_, mt1 = mt1_;
        if (layer == 0 && __builtin_expect(late != 0ull, 0)) {
            unsigned f0_, f1_, f2_, f3_;
            asm volatile("s_waitcnt vmcnt(0)\n\t" AR("%0", 36, 0) AR("%1", 36, 1) AR("%2", 36, 2) AR("%3", 36, 3) : "=v"(f0_), "=v"(f1_), "=v"(f2_), "=v"(f3_)::"memory");
            late = __builtin_amdgcn_ballot_w64(((f0_ ^ tag0) | (f1_ ^ tag1) | (f2_ ^ tag0) | (f3_ ^ tag1)) != 0u);
            ORDER();
            if (late == 0ull) KW_REQ()
        }
        asm volatile("s_waitcnt lgkmcnt(0)" ::: "memory");
        __builtin_amdgcn_s_barrier();
        asm volatile("" ::: "memory");
        STAMP(layer ? 14 : 2)
#pragma unroll
        for (int s = 0; s < 10; ++s) ks[s] = (unsigned)s * 1024u;
        constexpr int NW2A = NW2P + 7 * kCR1 + kXA, NW3 = NW2P + kN3E + kN3;
        auto w2ld = LAMBDA(i_) {
            constexpr int i = i_.value;
            gld_w2<i>(w2o, p.ws + 2048 + ks[i / 5] * 5u);
        };
        auto w2a = LAMBDA(i_) {
            constexpr int k = i_.value;
            if constexpr (k < kXA) { w2ld(std::integral_constant<int, NW2P + 2 * k>{}); w2ld(std::integral_constant<int, NW2P + 2 * k + 1>{}); }
            else w2ld(std::integral_constant<int, NW2P + kXA + k>{});
        };
        unsigned xtb = (unsigned)(size_t)(LAS char*)smem + (unsigned)(l_tb(0, 0) + w * 5120);
        auto w2x = LAMBDA(i_) {
            constexpr int i = NW2A + i_.value;
            if constexpr (i < 50) w2ld(std::integral_constant<int, i>{});
            else if constexpr (XDM) { if constexpr (i < 53) dma_x<(i - 50) * 1024, (i < 52 ? (i - 50) * 64 : 96)>(xso, p.x0, xtb); }
            else if constexpr (i < 52) lda4<40 + 4 * (i - 50), (i - 50) * 64>(xso, p.x0);
            else if constexpr (i == 52) lda4<48, 128>(xso - 2u * (unsigned)(bb & 16), p.x0);
        };
        auto w2b = LAMBDA(i_) {
            constexpr int k = i_.value - 7 * kCR1;
            constexpr int ne = KW2E < 53 - NW2A ? KW2E : 53 - NW2A;
            if constexpr (k < ne) w2x(std::integral_constant<int, k>{});
            constexpr int k1 = k - kFHK - 1;
            if constexpr (k1 >= 0 && k1 < KW2T) { w2x(std::integral_constant<int, ne + 2 * k1>{}); w2x(std::integral_constant<int, ne + 2 * k1 + 1>{}); }
            else if constexpr (k1 >= 0) w2x(std::integral_constant<int, ne + KW2T + k1>{});
            static_assert(ne <= kFHK + 1 && 53 - NW2A - ne - KW2T + kFHK < 7 * (8 - kCR1) - kFHP + 1, "");
            if constexpr (k == kFHK) { vm_wait<ne>(); st64(tagv, rs, fho1); STAMP(39) }
            if constexpr (k == kFHK - 1) { STAMP(38) }
            if constexpr (k == 7 * (8 - kCR1) - kFHP) poll1(fho, p.ws);
            static_assert(NW2A + 7 * (8 - kCR1) >= 53 && NW2A <= 50 && NW3 <= NW2A, "");
        };
        auto partb = [&]() __attribute__((always_inline)) {
            const float* sp = (const float*)(smem + spa);
            rsr = sp[0]; rsm = sp[8];
            if (layer == 1) { pin(rsr); pin(rsm); }
            if (layer == 1) {
                static_for<5>(LAMBDA(n_) {
                    constexpr int nb = n_.value;
                    unsigned d0, d1;
                    rda<22 + 2 * nb>(d0); rda<23 + 2 * nb>(d1);
                    a2[3][nb] = xin[nb] + (xin[nb] * rsr + rsm) * unpk4(v2u{d0, d1}); pin(a2[3][nb]);
                });
            }
        };
        constexpr int LBW = layer ? 2 : 3;
        if (w == 3) {
            site<3 + 10 * layer>(); SCHED();
            f32x4 s4[8];
#pragma unroll
            for (int i = 0; i < 8; ++i) { s4[i] = *(const f32x4*)(smem + spl + (l_sg(layer, i & 3) - SC + (i >> 2) * 640)); SCHED(); }
            f32x2 sv[16], x[4], P[4][4];
#pragma unroll
            for (int i = 0; i < 16; ++i) { const f32x4 t = s4[(i >> 2) + 2 * (i & 2)]; sv[i] = (i & 1) ? f32x2{t.z, t.w} : f32x2{t.x, t.y}; }
            unsigned er, ei;
            {
                f32x2 u[4];
                cfma4n(u, l8, sv[0], sv[4], sv[8], sv[12], sv[1], sv[5], sv[9], sv[13]);
                agg(er, ei, x, P[0][2], u, sv, l8, l32, l128);
            }
            st128(v4u{er, tag0, ei, tag0}, rs, es);
            SCHED();
            P[0][0] = f32x2{0.f, 0.f}; P[0][1] = x[0]; P[0][3] = cfma(l32, P[0][2], x[2]);
            if (layer == 0) { FW_OK() static_for<kN3E>(LAMBDA(i_) { w2ld(std::integral_constant<int, NW2P + i_.value>{}); }); }
#pragma unroll
            for (int kk = 1; kk < 4; ++kk) cfma4(P[kk], l8, P[kk - 1][0], P[kk - 1][1], P[kk - 1][2], P[kk - 1][3], sv[kk - 1], sv[kk + 3], sv[kk + 7], sv[kk + 11]);
#pragma unroll
            for (int i = 0; i < 8; ++i) { const f32x2 a = P[2 * (i >> 2)][i & 3], b_ = P[2 * (i >> 2) + 1][i & 3]; *(f32x4*)(smem + spl + (l_sg(layer, i & 3) - SC + (i >> 2) * 640)) = f32x4{a.x, a.y, b_.x, b_.y}; }
            partb();
        }
        int w1_ = w, w3_ = w;
        if (layer == 1) { pins(w1_); pins(w3_); }
        if (layer == 1 && w1_ < LBW) { site<4 + 10 * layer>(); SCHED(); partb(); if constexpr (layer == 1) { if (w == 0) tour<6>(); } }
        if (w3_ == LBW) {
            site<5 + 10 * layer>(); SCHED();
            f32x2 wg[17], t4[4];
            unsigned ea[15], tg[15];
            v4u eg[15];
            constexpr bool early = (LBE >> layer) & 1;
            const f32x2 z2 = {0.f, 0.f};
#pragma unroll
            for (int i = 0; i < 15; ++i) { ea[i] = (unsigned)(mt1 - 1 - i) * 10240u; pins(ea[i]); ea[i] = ea[i] < kNone ? ea[i] : kNone; pins(ea[i]); }
            if constexpr (early) {
                __builtin_amdgcn_s_sleep(layer ? KLB1 : KLB0);
#pragma unroll
                for (int i = 0; i < 15; ++i) eg[i] = ld128s(rs, eo, ea[i]);
                SCHED();
            }
            wg[1] = cmul(l128, l128); wg[2] = cmul(wg[1], wg[1]); wg[3] = cmul(wg[2], wg[1]); wg[4] = cmul(wg[2], wg[2]);
#pragma unroll
            for (int i = 4; i < 16; i += 4) {
                cfma4(t4, wg[i], wg[1], wg[2], wg[3], wg[4], z2, z2, z2, z2);
#pragma unroll
                for (int e = 0; e < 4; ++e) wg[i + 1 + e] = t4[e];
            }
#pragma unroll
            for (int i = 0; i < 15; ++i) { if (i) pin(wg[i]); tg[i] = mt1 - 1 - i < 0 ? 0u : tag0; pins(tg[i]); }
            if (layer == 1) partb();
            if constexpr (layer == 0) tour<3>();
            f32x2 H0, H1;
            unsigned spins = dead ? kSpinCap : 0u;
            if constexpr (layer == 1) room<1800>();
            if constexpr (!early) __builtin_amdgcn_s_sleep(layer ? KLB1 : KLB0);
            SCHED();
            auto eval = [&](const v4u (&e)[15]) __attribute__((always_inline)) {
                unsigned x = __builtin_amdgcn_bitop3_b32(e[0].y, e[0].w, tg[0], 0x7E);
                H0 = f32x2{__uint_as_float(e[0].x), __uint_as_float(e[0].z)}; H1 = f32x2{0.f, 0.f};
#pragma unroll
                for (int i = 1; i < 15; i += 2) {
                    x |= __builtin_amdgcn_bitop3_b32(e[i].y, e[i].w, tg[i], 0x7E) | __builtin_amdgcn_bitop3_b32(e[i + 1].y, e[i + 1].w, tg[i + 1], 0x7E);
                    pin(x);
                    gacc2(H0, H1, wg[i], wg[i + 1], e[i], e[i + 1]);
                    SCHED();
                }
                *(f32x2*)(smem + spl + (L_CW - SC)) = H0 + H1;
                return __builtin_amdgcn_ballot_w64(x != 0u) != 0ull;
            };
            bool again = true;
            if constexpr (early) again = eval(eg);
            if (again) {
                bool miss;
#pragma nounroll
                do {
                    asm volatile("" ::: "memory");
                    v4u e2[15];
#pragma unroll
                    for (int i = 0; i < 15; ++i) e2[i] = ld128s(rs, eo, ea[i]);
                    SCHED();
                    miss = eval(e2);
                    if (__builtin_expect(miss, 0) && ++spins > kSpinCap) { dead = true; miss = false; }
                } while (miss);
            }
            ORDER();
            STAMP(layer ? 36 : 37)
            if (layer == 0) {
                static_for<NW3 - NW2P - kN3E>(LAMBDA(i_) { w2ld(std::integral_constant<int, NW2P + kN3E + i_.value>{}); });
            }
        }
        int w2_ = w;
        pins(w2_);
        if (layer == 0) UNDEF8(ca);
        if (layer == 0 && w2_ != LBW) {
            if (cv) {
                site<2>(); SCHED();
                partb();
                {
                    FW_OK()
                    st_ur((unsigned)(size_t)(LAS char*)smem + (unsigned)uwa);
                    CV_LOAD_A(cA, 0, 0, 0) CV_LOAD_A(cA, 0, 0, 1)
                    SCHED();
                    static_for<8>(LAMBDA(a_) { constexpr int a = a_.value; CV_LOAD_B(cB, a, 0) SCHED(); });
                    CV_LOAD_A(cA, 1, 0, 0) CV_LOAD_A(cA, 1, 0, 1)
                    SCHED();
                }
                STAMP(32)
                conv_part<0, kCR1, CPF>(ca, cA, cB, smem, ab, bb, w2a, LAMBDA(i_) {});
            } else {
                if constexpr (layer == 0) tour<5>();
                FW_OK()
                static_for<NW2A - NW2P>(LAMBDA(i_) { w2ld(std::integral_constant<int, NW2P + i_.value>{}); __builtin_amdgcn_s_sleep(1); });
                partb();
                if constexpr (layer == 0) room<3900>();
            }
        }
        asm volatile("s_waitcnt lgkmcnt(0)" ::: "memory");
        __builtin_amdgcn_s_barrier();
        asm volatile("" ::: "memory");
        STAMP(layer ? 15 : 4)
        {
            St cw, ex, hv[8];
            cw.a = *(const f32x2*)(smem + cwa); cw.b = *(const f32x2*)(smem + cwa + 256); cw.c = *(const float*)(smem + cw4 + 512);
            ex.a = *(const f32x2*)(smem + sga); ex.b = *(const f32x2*)(smem + sga + 256); ex.c = *(const float*)(smem + sg4 + 512);
            keep14(hv, cw, ex, bv, lamq, lam1);
            keep4(hv + 4, hv[3], bv + 4, lam1);
            SCHED();
            v2u ta[5], td[5];
            f32x2 hb[9];
            static_for<5 + SBL>(LAMBDA(r_) {
                constexpr int r = r_.value, rd = r < 2 ? r : r == 2 ? 4 : r - 1, ro_ = r - SBL < 2 ? r - SBL : r - SBL == 2 ? 4 : r - SBL - 1;
                if constexpr (r < 5) {
                    float e[8];
                    unsigned o[4];
#pragma unroll
                    for (int i = 0; i < 8; ++i) e[i] = rd == 4 ? hv[i].c : rd < 2 ? hv[i].a[rd & 1] : hb[i + 1][rd & 1];
                    if constexpr (r == 0) b3cv(hb, o, cw.b, ex.b, bv[0].b, bv[1].b, lamq.b, lam1.b, e);
                    else if constexpr (r < 3) b3cv(hb + 3 * r, o, hb[3 * r - 1], bv[3 * r - 1].b, bv[3 * r].b, bv[3 * r + 1].b, lam1.b, lam1.b, e);
                    else for (int i = 0; i < 4; ++i) o[i] = cvtpk(e[2 * i], e[2 * i + 1]);
                    *(v4u*)(smem + tbw + rd * 1024) = v4u{o[0], o[1], o[2], o[3]};
                    SCHED();
                    if constexpr (r < 3) { bfin(hb[3 * r + 2], hb[3 * r + 1], lam1.b); SCHED(); }
                    ta[rd] = __builtin_bit_cast(v2u, trread(smem, (rd < 4 ? tbp : tbr) + rd * 1024));
                    td[rd] = __builtin_bit_cast(v2u, trread(smem, (rd < 4 ? tbq + 256 : tbr + 512) + rd * 1024));
                    SCHED();
                }
                if constexpr (r >= SBL) { st128(v4u{ta[ro_].x, ta[ro_].y, td[ro_].x, td[ro_].y}, rs, hso + ro_ * 256); SCHED(); }
            });
        }
#pragma unroll
        for (int s = 0; s < 10; ++s) pins(ks[s]);
        if (layer == 1) { DRAIN(); st64(tagv, rs, fho1); }
        STAMP(layer ? 16 : 5)
        if (cv) {
            if constexpr (layer == 0) site<6>();
            SCHED();
            if constexpr (!CPF) {
                CV_LOAD_A(cA, 0, kCR1, 0) CV_LOAD_A(cA, 0, kCR1, 1)
                SCHED();
                static_for<8>(LAMBDA(a_) { constexpr int a = a_.value; CV_LOAD_B(cB, a, kCR1) SCHED(); });
                static_for<7>(LAMBDA(a_) { constexpr int a = a_.value + 1; CV_LOAD_A(cA, a, kCR1, 0) CV_LOAD_A(cA, a, kCR1, 1) });
                SCHED();
            }
            conv_part<kCR1, 8, false>(ca, cA, cB, smem, ab, bb, w2b, LAMBDA(g_) {
                constexpr int g = g_.value;
                if constexpr (GIN > 0 && (g == 14 || g == 16 || g == 18 || g == 20)) gst(std::integral_constant<int, (g - 14) / 2>{});
                if constexpr (GIN > 1 && (g == 26 || g == 28 || g == 30 || g == 32)) gst(std::integral_constant<int, 4 + (g - 26) / 2>{});
            });
            asm volatile("" : "+v"(ca[0]), "+v"(ca[1]), "+v"(ca[2]), "+v"(ca[3]), "+v"(ca[4]), "+v"(ca[5]), "+v"(ca[6]), "+v"(ca[7]));
            static_for<(GSM ? 0 : 16 - 4 * GIN)>(LAMBDA(i_) {
                constexpr int n = 4 * GIN + i_.value;
                if constexpr (n == 9 && GIN == 2) asm volatile("s_nop 0");
                if constexpr ((n & 3) == 2) asm volatile("s_nop 1");
                gst(std::integral_constant<int, n>{}); SCHED();
            });
        } else if (layer == 0) {
            constexpr int ne = KW2E < 53 - NW2A ? KW2E : 53 - NW2A;
            static_for<54 - NW2A>(LAMBDA(i_) {
                if constexpr (i_.value == ne) { vm_wait<ne>(); st64(tagv, rs, fho1); }
                if constexpr (i_.value < 53 - NW2A) { w2x(i_); __builtin_amdgcn_s_sleep(1); }
            });
            if (w == 3) { static_for<NW2A - NW3>(LAMBDA(i_) { w2ld(std::integral_constant<int, NW3 + i_.value>{}); __builtin_amdgcn_s_sleep(1); }); if constexpr (layer == 0) tour<2>(); }
            poll1(fho, p.ws);
            if constexpr (layer == 0) room<1000>();
            DRAIN();
            asm volatile("" : "=v"(gq[0]), "=v"(gq[1]));
        }
        if (layer == 0) { unsigned f0_, f1_; asm volatile("s_waitcnt vmcnt(%2)\n\t" AR("%0", 36, 0) AR("%1", 36, 1) : "=v"(f0_), "=v"(f1_) : "i"(GSM ? 0 : 4) : "memory"); fh = v2u{f0_, f1_}; }
        STAMP(layer ? 29 : 6)
        }

            v2u pt[4];
            if (layer == 0) {
                u64 hl;
                ne1(hl, tagv, fh);
                if (__builtin_expect(hl != 0ull, 0)) {
                    if constexpr (!GSM) { gld2t(fh, fho, p.ws); wait1(fh); st64(tagv, rs, fgo1); }
                    WAVE_RETRY(ISTAG(fh), fh = ld64(rs, fho))
                }
                ORDER();
            }
            else { room<3400>(); site<18>(); pipe_flag<KPH0>(pt, fho, p.ws, tagv, dead); ORDER(); }
            STAMP(layer ? 17 : 31)
            site<7 + 10 * layer>(); SCHED();
            constexpr int RING2 = layer ? KR2 : KR20;
            v4u ar[RING2];
#pragma unroll
            for (int i = 0; i < KI2; ++i) { ar[i] = ld128s(rs, ho[i / 10], ks[i % 10]); SCHED(); }
        if constexpr (layer == 0 && GSM) { static_assert(GIN == 2, ""); gst(IC(8)); gst(IC(9)); gst(IC(12)); gst(IC(13)); gst(IC(10)); gst(IC(14)); gst(IC(11)); gst(IC(15)); SCHED(); }
        int b_o = b0, w_o = w0;
        asm volatile("" : "+s"(b_o), "+s"(w_o));
        const int b = b_o, w = w_o;
        TILES()
        v4u gl[3];
        v2u f0;
        unsigned fgo, sgo, sgs, glo, gw, gr, srco;
        u64 glate = 0ull;
        const char *gb1 = p.ws + 32 * 4096, *gb2 = p.ws + 48 * 4096;
        {
            unsigned xs_[3][4], dq[2];
            float zx[4], zd[4], zt[4], zz[5][4];
            unsigned sA = SC + w * INBOX, s1 = OFF_SG + ((layer * 32 + mt2) * 4 + w) * 2048, s2 = s1 + nt2 * 256, sF = OFF_FG + (nt2 * 96 + 4 * w) * 128, sG = OFF_GT + (80 * nt2 + 4 * w) * 4096 + mt2 * 128,
                     sW = sA + 8192 + (w & 1) * 128, sR = SC + 8192 + w * 32, sX = l_tb(0, 0) + w * 5120, k160 = 160, kIn = INBOX;
            pins(sA); pins(s1); pins(s2);
            if (layer == 0) { pins(sF); pins(sG); pins(sW); pins(sR); pins(sX); pins(k160); pins(kIn); pins(gb1); pins(gb2); }
            unsigned l16, c16, q0, q1, c2, xr0, xr1, ta_, tb_;
#define A1(n_, v_, e_) else if constexpr (n == n_) { v_ = e_; pin(v_); }
            auto adr = LAMBDA(n_) {
                constexpr int n = n_.value + (layer && n_.value >= 4 ? 12 : 0);
                if constexpr (n == 0) { l16 = (unsigned)dst[0] & 1023u; pin(l16); }
                A1(1, c16, l16 & 0xf0u) A1(2, q0, (l16 >> 8) & 1u) A1(3, q1, l16 >> 9)
                A1(4, c2, (l16 >> 6) & 3u) A1(5, xr0, (c16 << 2) + sX) A1(6, ta_, q1 ^ c2) A1(7, xr0, (q0 << 3) + xr0) A1(8, xr0, (ta_ << 4) + xr0) A1(9, xr1, xr0 ^ 32u)
                else if constexpr (n >= 10 && n < 15) { constexpr int nb = n - 10; const v2u t = *(const v2u*)(smem + ((nb & 5) ? xr1 : xr0) + (nb >> 1) * 1024); xs_[nb >> 1][2 * (nb & 1)] = t.x; xs_[nb >> 1][2 * (nb & 1) + 1] = t.y; }
                A1(16, srco, l16 + sA) A1(17, ta_, l16 & 0x300u) A1(18, sgo, s1 + l16 + ta_) A1(19, sgs, (q0 << 3) + c16) A1(20, ta_, (q1 << 30) + s2) A1(21, sgs, sgs + ta_)
                else if constexpr (layer == 1) {}
                A1(22, ta_, l16 < 624u ? l16 : 624u) A1(23, tb_, ta_ & 0x380u) A1(24, ta_, ta_ & 0x60u) A1(25, fgo, (tb_ << 4) + sF) A1(26, fgo, (ta_ << 2) + fgo)
                A1(27, glo, (q1 << 16) + sG) A1(28, ta_, l16 & 0x180u) A1(29, tb_, l16 & 0x70u) A1(30, glo, (ta_ << 5) + glo) A1(31, glo, glo + tb_)
                A1(32, ta_, l16 >> 7) A1(33, tb_, l16 & 0x30u) A1(34, gw, (tb_ << 1) + sW) A1(35, gw, __umul24(ta_, k160) + gw) A1(36, ta_, c2 & 1u) A1(37, gw, (ta_ << 4) + gw)
                A1(38, ta_, l16 >> 8) A1(39, gr, (q0 << 7) + sR) A1(40, gr, __umul24(ta_, kIn) + gr) A1(41, gr, __umul24(c2, k160) + gr) A1(42, tb_, tb_ >> 1) A1(43, gr, gr + tb_)
            };
            SCHED();
            if constexpr (STAMPS == 1) { STAMP(layer ? 50 : 40) }
            static_for<40>(LAMBDA(i_) {
                constexpr int i = i_.value, m = i / 10, s = i % 10;
                static_for<5>(LAMBDA(k_) {
                    constexpr int kk = k_.value, pv = ring_req(5 * i + kk, KI2, RING2, 0);
                    if constexpr (s == 0 && m < 3) mfa0<kk>(a2[m][kk], ar[i % RING2]); else mfa<5 * s + kk>(a2[m][kk], ar[i % RING2]);
                    SCHED();
                    if constexpr (i == 0 && kk == 0 && layer == 1) { pin(pt[0]); pin(pt[1]); pin(pt[2]); pin(pt[3]); }
                    if constexpr (g2_adr(i, kk) >= 0) adr(IC(g2_adr(i, kk)));
                    if constexpr (layer == 0 && GSM && kk == 1 && i >= GSI && i < GSI + 4) st128(gq[i - GSI], rs, gso + (i - GSI) * 1024);
                    if constexpr (layer == 0 && GSM && kk == 0 && i == GFI) { vm_wait<GFI - GSI - 4>(); st64(tagv, rs, fgo1); }
                    if constexpr (layer == 0 && i >= 1 && i < 28 && (kk & 1)) {
                        constexpr int n = 2 * (i - 1) + (kk >> 1) - 9, nb = n / 9, u = n % 9 - 1;
                        if constexpr (n < 0 && XDM) {}
                        else if constexpr (n < -3) { rda<40 + 2 * (n + 9)>(xs_[(n + 9) >> 1][(2 * (n + 9)) & 3]); rda<41 + 2 * (n + 9)>(xs_[(n + 9) >> 1][(2 * (n + 9) + 1) & 3]); }
                        else if constexpr (n < 0) swp2n(xs_[n + 3][0], xs_[n + 3][2], xs_[n + 3][1], xs_[n + 3][3]);
                        else if constexpr (u < 0) { rda<22 + 2 * nb>(dq[0]); rda<23 + 2 * nb>(dq[1]); }
                        else if constexpr (u < 2) unp2(zx[2 * u], zx[2 * u + 1], xs_[nb >> 1][2 * (nb & 1) + u]);
                        else if constexpr (u < 4) unp2(zd[2 * u - 4], zd[2 * u - 3], dq[u - 2]);
                        else if constexpr (u < 6) fma2(zt[2 * u - 8], zt[2 * u - 7], zx[2 * u - 8], zx[2 * u - 7], rsr, rsr, rsm, rsm);
                        else fma2(zz[nb][2 * u - 12], zz[nb][2 * u - 11], zt[2 * u - 12], zt[2 * u - 11], zd[2 * u - 12], zd[2 * u - 11], zx[2 * u - 12], zx[2 * u - 11]);
                    }
                    if constexpr (layer == 0 && !GSM && i == 0 && kk == 1) { vm_wait<KI2 - 1 + (ring_req(0, KI2, RING2, 0) >= 0)>(); st64(tagv, rs, fgo1); }
                    if constexpr (layer == 0 && i == kGPI && kk == 2) gld2s<0>(f0, fgo, p.ws);
                    if constexpr (layer == 0 && i == kGCI) {
                        static_assert(kGCI + KR20 > 40 && !GSM, "");
                        if constexpr (kk == 0) { __builtin_amdgcn_s_waitcnt(0x0F70); pin(f0); gld4s<0>(gl[0], glo, p.ws); }
                        if constexpr (kk == 1) { gld4s<0>(gl[1], glo, gb1); gld4s<0>(gl[2], glo, gb2); }
                        if constexpr (kk == 3) ne1(glate, tagv, f0);
                    }
                    if constexpr (pv >= 0) ar[pv % RING2] = ld128s(rs, ho[pv / 10], ks[pv % 10]);
                    if constexpr (kk == 2 && m >= 1 && s >= 1 && s < 6) *(f32x4*)(smem + dst[m - 1] + (s - 1) * 1024) = a2[m - 1][s - 1];
                    site<4000 + 1000 * layer + 5 * i + kk>();
                    if constexpr (i >= 37) a2[i - 37][kk] = *(const f32x4*)(smem + srco + (i - 37) * 5120 + kk * 1024);
                    SCHED();
                });
                if constexpr (i == 10) { asm volatile("s_waitcnt lgkmcnt(0)" ::: "memory"); __builtin_amdgcn_s_barrier(); asm volatile("" ::: "memory"); }
                if constexpr (i == 28 && layer == 0) {
#pragma unroll
                    for (int nb = 0; nb < 5; ++nb) { a2[3][nb] = f32x4{zz[nb][0], zz[nb][1], zz[nb][2], zz[nb][3]}; pin(a2[3][nb]); }
                }
                if constexpr (i == 36) {
                    asm volatile("s_waitcnt lgkmcnt(0)" ::: "memory");
                    __builtin_amdgcn_s_barrier(); asm volatile("" ::: "memory");
                }
                if constexpr (STAMPS == 1 && (i == 0 || i == 9 || i == 10 || i == 15 || i == 25 || i == 35 || i == 36)) { STAMP((layer ? 51 : 41) + (i == 0 ? 0 : i == 9 ? 1 : i == 10 ? 2 : i == 15 ? 3 : i == 25 ? 4 : i == 35 ? 5 : 6)) }
                SCHED();
            });
            STAMP(layer ? 18 : 8)
        }
#pragma unroll
        for (int nb = 0; nb < 5; ++nb) a2[0][nb] = (a2[0][nb] + a2[1][nb]) + a2[2][nb];
        asm volatile("s_nop 1" : "+v"(a2[3][0]), "+v"(a2[3][1]), "+v"(a2[3][2]), "+v"(a2[3][3]), "+v"(a2[3][4]),
                     "+v"(a2[0][0]), "+v"(a2[0][1]), "+v"(a2[0][2]), "+v"(a2[0][3]), "+v"(a2[0][4]));
        float ty;
        {
            f32x4 s1, s2;
#pragma unroll
            for (int nb = 0; nb < 5; ++nb) {
                const f32x4 y = a2[3][nb] + a2[0][nb];
                xin[nb] = y;
                if (nb == 0) { s1 = y; s2 = y * y; } else { s1 += y; s2 += y * y; }
            }
            ty = xsum2h(hsum4(s1), hsum4(s2));
            st64(v2u{__float_as_uint(ty), tag0}, rs, sgs);
        }
        SCHED();
        STAMP(layer ? 19 : 9)
        if (layer == 0 && __builtin_expect(glate != 0ull, 0)) {
            asm volatile("s_waitcnt vmcnt(0)" : "+v"(gl[0]), "+v"(gl[1]), "+v"(gl[2])::"memory");
            WAVE_WAIT({ f0 = ld64(rs, fgo); ok = ISTAG(f0); })
            gld4t<0>(gl[0], glo, p.ws); gld4t<0>(gl[1], glo, gb1); gld4t<0>(gl[2], glo, gb2);
            asm volatile("s_waitcnt vmcnt(0)" : "+v"(gl[0]), "+v"(gl[1]), "+v"(gl[2])::"memory");
        }
        if (layer == 0) {
            asm volatile("s_waitcnt vmcnt(1)" : "+v"(gl[0]), "+v"(gl[1]), "+v"(gl[2])::"memory");
            *(v4u*)(smem + gw) = gl[0]; *(v4u*)(smem + gw + 1280) = gl[1]; *(v4u*)(smem + gw + 1920) = gl[2];
            asm volatile("s_waitcnt lgkmcnt(0)" ::: "memory");
            __builtin_amdgcn_s_barrier(); asm volatile("" ::: "memory");
#pragma unroll
            for (int nb = 0; nb < 5; ++nb) gt[nb] = __builtin_bit_cast(v2u, trread(smem, gr + 640 * nb));
        } else ACC_TIE5(gt);
        SCHED();
        if (PSP >> layer & 1) { gran_req<32, layer ? PDS : PDS0>(sgo, p.ws); SCHED(); }
        f32x4 gf[5], yg[5];
#pragma unroll
        for (int nb = 0; nb < 5; ++nb) { gf[nb] = unpk4(gt[nb]); yg[nb] = xin[nb] * gf[nb]; pin(gf[nb]); pin(yg[nb]); }
        SCHED();
        if (PSP >> layer & 1) { gran_req<40, 0>(sgo, p.ws); SCHED(); }
        int lane_o;
        asm volatile("v_mbcnt_lo_u32_b32 %0, -1, 0\n\tv_mbcnt_hi_u32_b32 %0, -1, %0" : "=v"(lane_o));
        const int lane = lane_o, c = lane & 15, q = lane >> 4;
        unsigned xs = OFF_X2F + (unsigned)((2 * mt2 + (w >> 1)) * 40960 + ((c >> 2) & 1) * 20480 + (10 * nt2 + 2 * (q & 1) + (q >> 1)) * 256 + (8 * (w & 1) + 4 * (c >> 3) + (c & 3)) * 16);
        unsigned pso = q < 2 ? OFF_PS + (unsigned)(((mt2 * 4 + w) * 8 + nt2) * 128 + c * 8 + q * 4) : kNone;
        unsigned fxo1 = OFF_FX + (unsigned)(((mt1 * 4 + 2 * (mt2 & 1) + (w >> 1)) * 16 + nt2 * 2 + (w & 1)) * 128);
        unsigned oo = (unsigned)(((64 * mt2 + 16 * w + c) * D_ + 80 * nt2 + 4 * q) * 4);
        if (layer == 0) { pin(xs); pin(pso); pin(fxo1); } else pin(oo);
        float c_r, c_m, c_rr, c_rm, c_mm;
        if constexpr (layer == 0 && PSA) {
            f32x4 su = yg[0], sg_ = gf[0], syu = xin[0] * yg[0], suu = yg[0] * yg[0], sug = yg[0] * gf[0], sgg = gf[0] * gf[0];
#pragma unroll
            for (int nb = 1; nb < 5; ++nb) { su += yg[nb]; sg_ += gf[nb]; syu += xin[nb] * yg[nb]; suu += yg[nb] * yg[nb]; sug += yg[nb] * gf[nb]; sgg += gf[nb] * gf[nb]; }
            const float hu = hsum4(su);
            c_r = xsum2h(hu, 2.f * hsum4(syu)); c_m = xsum2h(hsum4(sg_), 2.f * hu);
            c_rr = xsum2h(0.f, hsum4(suu)); c_rm = xsum2h(0.f, 2.f * hsum4(sug)); c_mm = xsum2h(0.f, hsum4(sgg));
            pin(c_r); pin(c_m); pin(c_rr); pin(c_rm); pin(c_mm);
        }
        SCHED();
        {
            float a, d;
                        if (PSP >> layer & 1) gran_req<48, layer ? PD1 : 0>(sgo, p.ws);
            else { gran_req<32, layer ? PDS : PDS0>(sgo, p.ws); gran_req<40, PD1>(sgo, p.ws); gran_req<48, PD1>(sgo, p.ws); }
            room<(layer ? 900 : 1500)>(); site<9 + 10 * layer>();
            pipe_gran(a, d, sgo, p.ws, tag0, dead);
            STAMP(layer ? 20 : 10)
            xsum2(a, d);
            const float me = a * (1.0f / D_);
            const float ri = __builtin_amdgcn_rsqf(fmaxf(d * (1.0f / D_) - me * me, 0.f) + 1e-5f), mr = -me * ri;
            if constexpr (layer == 0 && PSA) {
                const float ps_ = ty + ri * (c_r + ri * c_rr + mr * c_rm) + mr * (c_m + mr * c_mm);
                __builtin_amdgcn_raw_buffer_store_b32(__float_as_uint(ps_), rs, pso, 0, SC1);
                SCHED();
            }
#pragma unroll
            for (int nb = 0; nb < 5; ++nb) xin[nb] = yg[nb] * ri + (gf[nb] * mr + xin[nb]);
        }
        if constexpr (layer == 0) {
            v2u pk[5];
#pragma unroll
            for (int nb = 0; nb < 5; ++nb) { const f32x4 x = xin[nb]; pk[nb] = v2u{cvtpk(x.x, x.y), cvtpk(x.z, x.w)}; }
#pragma unroll
            for (int i = 0; i < 3; ++i) {
                const v2u pa = pk[2 * i], pb = pk[i < 2 ? 2 * i + 1 : 4];
                const auto r0 = __builtin_amdgcn_permlane16_swap(pa.x, pb.x, false, false);
                const auto r1 = __builtin_amdgcn_permlane16_swap(pa.y, pb.y, false, false);
                if (i < 2 || (q & 1) == 0) st128(v4u{r0[0], r1[0], r0[1], r1[1]}, rs, xs + i * 1024);
            }
            if constexpr (!PSA) {
                f32x4 s1, s2;
#pragma unroll
                for (int nb = 0; nb < 5; ++nb) { const f32x4 x = xin[nb]; if (nb == 0) { s1 = x; s2 = x * x; } else { s1 += x; s2 += x * x; } }
                __builtin_amdgcn_raw_buffer_store_b32(__float_as_uint(xsum2h(hsum4(s1), hsum4(s2))), rs, pso, 0, SC1);
            }
            SCHED();
            site<12>();
        int wa[2][3];
#pragma unroll
        for (int i = 0; i < 2; ++i) {
            wa[i][0] = (c >> 3) * 20480 + (c & 7) * 128 + (((4 * i + q) ^ (c & 6)) * 16); wa[i][1] = (wa[i][0] ^ 16) + 10240;
            wa[i][2] = 81920 + (c & 1) * 10240 + (c >> 1) * 128 + (((4 * i + q) ^ ((c & 1) | ((c >> 2) << 1))) * 16);
            pin(wa[i][0]); pin(wa[i][1]); pin(wa[i][2]);
        }
            unsigned fxo = OFF_FX + (unsigned)((mt1 * 4 + w) * 2048 + (PLM ? lane >> 2 : c) * 128);
            unsigned xo = OFF_X2F + (unsigned)((4 * mt1 + w) * 40960 + lane * 16), xo1 = xo + 20480u;
            const int row = 8 * q + (c & 7);
            unsigned po = OFF_PS + (unsigned)((((2 * mt1 + (w >> 1)) * 4 + 2 * (w & 1) + (row >> 4)) * 8 + 4 * (c >> 3)) * 128 + (row & 15) * 8);
            int rsw = l_rs(1, 0) + w * INBOX + lane * 4;
            pin(rsw); pin(fxo); pin(xo); pin(xo1); pin(po);
            v4u wf[2][5];
#pragma unroll
            for (int nb = 0; nb < 5; ++nb) wf[0][nb] = WF(nb, 0);
            SCHED();
            DRAIN();
            FLAG(fxo1)
            if (b == 255 && w == 0) FLAG(OFF_NONCE)
            STAMP(11)
            if (w == 0) tour<4>();
            addrs(std::integral_constant<int, 1>{}, lane, w, b);
            v2u pt[4];
            skip<SKIP5>();
            pipe_flag<KPF0>(pt, fxo, p.ws, tagv, dead);
            ORDER();
            STAMP(12)
            v4u ar[2 * RING1];
#define XF(n) ld128s(rs, (((n) & 1) ? xo1 : xo) + (((n) >> 1) & 3) * 1024, ((n) >> 3) * 4096)
#pragma unroll
            for (int n = 0; n < KI1; ++n) { ar[n] = XF(n); SCHED(); }
            v2u ps[4];
            float t1, t2, rinv, mri, rr_[8], rm_[8];
            static_for<20>(LAMBDA(s_) {
                constexpr int s = s_.value, cu = s & 1, nx = cu ^ 1;
                LGKM0() SCHED();
                static_for<10>(LAMBDA(g_) {
                    constexpr int g = g_.value, mb = g / 5, k = g % 5;
                    if constexpr (s == 0) mf0(acc[mb][k], ar[mb], wf[0][k]); else mf(acc[mb][k], ar[(2 * s + mb) % (2 * RING1)], wf[cu][k]);
                    SCHED();
                    if constexpr (s == 0 && g == 0) { pin(pt[0]); pin(pt[1]); pin(pt[2]); pin(pt[3]); }
                    if constexpr (s < 19 && g < 5) wf[nx][g] = WF(g, s + 1);
                    constexpr int rq = ring_req(10 * s + g, KI1, 2 * RING1, 4);
                    if constexpr (rq >= 100) ps[rq - 100] = ld64(rs, po + (rq - 100) * 128);
                    else if constexpr (rq >= 0) ar[rq % (2 * RING1)] = XF(rq);
                    constexpr int n = g >= 6 ? 4 * (s - KS1) + g - 6 : -1;
                    if constexpr (n == 0) { t1 = __uint_as_float(ps[0].x) + __uint_as_float(ps[1].x); pin(t1); t2 = __uint_as_float(ps[0].y) + __uint_as_float(ps[1].y); pin(t2); }
                    if constexpr (n == 1) { t1 += __uint_as_float(ps[2].x); pin(t1); t2 += __uint_as_float(ps[2].y); pin(t2); }
                    if constexpr (n == 2) { t1 += __uint_as_float(ps[3].x); pin(t1); t2 += __uint_as_float(ps[3].y); pin(t2); }
                    if constexpr (n == 3) { t1 += dpp<0x128>(t1); t2 += dpp<0x128>(t2); pin(t1); pin(t2); }
                    if constexpr (n == 4) { t1 *= (1.0f / D_); pin(t1); t2 *= (1.0f / D_); pin(t2); }
                    if constexpr (n == 5) { t2 = fmaxf(t2 - t1 * t1, 0.f); pin(t2); }
                    if constexpr (n == 6) { rinv = __builtin_amdgcn_rsqf(t2 + 1e-5f); pin(rinv); }
                    if constexpr (n == 8) { mri = -t1 * rinv; pin(mri); }
                    if constexpr (n == 9) { t1 = rinv; mrg8(t1, mri); }
                    if constexpr (n == 10) *(float*)(smem + rsw) = t1;
                    if constexpr (n >= 11 && n < 19) bc2b<n - 11>(rr_[n - 11], rm_[n - 11], rinv, mri);
                    if constexpr (n >= 19 && n < 39) TT_GAP(n - 19)
                    site<6000 + 10 * s + g>();
                    SCHED();
                });
            });
            rr[0] = f32x4{rr_[0], rr_[1], rr_[2], rr_[3]}; rr[1] = f32x4{rr_[4], rr_[5], rr_[6], rr_[7]};
            asm volatile("kpf_end2:");
            ACC_FENCE10(acc[0], acc[1]);
            STAMP(13)
        } else {
#pragma unroll
            for (int nb = 0; nb < 5; ++nb) __builtin_amdgcn_raw_buffer_store_b128(__builtin_bit_cast(v4u, xin[nb]), ro, oo + 64 * nb, 0, OUTP);
            STAMP(21)
            STAMP_DUMP()
            asm volatile("kpf_end:");
        }
    };
    layer_body(std::integral_constant<int, 0>{});
    layer_body(std::integral_constant<int, 1>{});
}

extern "C" void kernel_launch(void* const* d_in, const int* in_sizes, int n_in,
                              void* d_out, int out_size, void* d_ws, size_t ws_size,
                              hipStream_t stream) {
    Params p;
    p.x0 = (const u16*)d_in[0];
    p.Lre = (const u16*)d_in[1];
    p.Lim = (const u16*)d_in[2];
    p.Bre = (const u16*)d_in[3];
    p.Bim = (const u16*)d_in[4];
    p.Cre = (const u16*)d_in[5];
    p.Cim = (const u16*)d_in[6];
    p.logdt = (const u16*)d_in[7];
    p.Dskip = (const u16*)d_in[8];
    p.k = (const u16*)d_in[10];
    p.v = (const u16*)d_in[11];
    p.shk = (const u16*)d_in[12];
    p.ssmk = (const u16*)d_in[13];
    p.ssmD = (const u16*)d_in[14];
    p.ws = (char*)d_ws;
    p.out = (float*)d_out;
    k_main<<<256, 256, 0, stream>>>(p);
}
```

```cpp
#define PADS 9000,9001,2055,2,9002,4070,9,9003,6,9004,9005,6105,9006,18,19,9007,9008
#include <hip/hip_runtime.h>
#include <stdint.h>
#include <utility>

#define STAMPS 0

typedef unsigned short u16;
typedef unsigned long long u64;
typedef unsigned v4u __attribute__((ext_vector_type(4)));
typedef unsigned v2u __attribute__((ext_vector_type(2)));
typedef float f32x4 __attribute__((ext_vector_type(4)));
typedef float f32x2 __attribute__((ext_vector_type(2)));
typedef short s16x4 __attribute__((ext_vector_type(4)));
typedef __bf16 bf16x2v __attribute__((ext_vector_type(2)));
#define LAS __attribute__((address_space(3)))
#define DI __device__ __forceinline__

constexpr int S_ = 2048, D_ = 640;
#define KR1 5
#define KR2 10
constexpr int RING1 = KR1;
#define KCR1 4
constexpr int kCR1 = KCR1;
#define KXA 0
#define KN3E 16
#define KN3 12
constexpr int kXA = KXA;
constexpr int kN3E = KN3E, kN3 = KN3;
#define KFHK 7
#define KFHP 7
constexpr int kFHK = KFHK, kFHP = KFHP;
#define KGPI 31
#define KGCI 36
#define PLM 1
#define CPF 0
#define HOIST 0
#define LBE 0
#define GSM 0
#define GSI 0
#define GFI 12
#define GIN 2
#define RESG 1
#define KI2 8
#define KI1 8
#define KS1 8
#define EST 1
#define ESF 0
#define KR20 10
constexpr int kGPI = KGPI, kGCI = KGCI;

constexpr unsigned OFF_NONCE = 0;
constexpr unsigned OFF_ST = 4096;
constexpr unsigned OFF_FW = OFF_ST + 65536;
constexpr unsigned OFF_FK = OFF_FW + 131072;
constexpr unsigned OFF_FH = OFF_FK + 81920;
constexpr unsigned OFF_FX = OFF_FH + 262144;
constexpr unsigned OFF_FG = OFF_FX + 131072;
constexpr unsigned OFF_EG = OFF_FG + 98304;
constexpr unsigned OFF_SG = OFF_EG + 327680;
constexpr unsigned OFF_PS = OFF_SG + 524288;
constexpr unsigned OFF_KVT = OFF_PS + 131072;
constexpr unsigned OFF_GT = OFF_KVT + 2621440;
constexpr unsigned OFF_W2F = OFF_GT + 2621440;
constexpr unsigned OFF_X2F = OFF_W2F + 1638400;
constexpr unsigned OFF_DUM = OFF_X2F + 2621440;
constexpr unsigned OFF_HF = OFF_DUM + 163840;
constexpr unsigned WS_BYTES = OFF_HF + 2 * 5242880;
constexpr unsigned kNone = 0x40000000u;
constexpr unsigned OFF_DBG = 0x1800000;
#define I32M 1
#define KVS 1
#define KVU 0
#define TILES() const int xq = b & 7, j = b >> 3, mt1 = 2 * xq + (j >> 4), nt1 = j & 15, mt2 = 4 * xq + (j >> 3), nt2 = j & 7, i32 = I32M == 0 ? mt2 : I32M == 1 ? 16 * (mt2 & 1) + mt1 : I32M == 2 ? 31 - mt2 : 16 * ((mt2 ^ (j >> (I32M < 3 ? 0 : I32M - 3))) & 1) + mt1;

constexpr int SC = 102400;
constexpr int REGSZ = 13056;
constexpr int L_XR = SC;
constexpr int L_CO = SC + 12288;
constexpr int L_KB = SC + 51712;
constexpr int L_TAB = SC + 55808;
constexpr int L_CW = SC + 39168;
constexpr int INBOX = 15360;
constexpr int l_rs(int l, int w) { return l ? SC + w * INBOX : L_KB + w * 256; }
constexpr int l_sg(int l, int w) { return l ? SC + w * INBOX + 256 : SC + (w == 0 ? 43264 : w == 1 ? 46080 : w == 2 ? 52736 : 56320); }
constexpr int l_tb(int l, int w) { return l ? SC + w * INBOX + 2048 : SC + 40960 + w * 5120; }
constexpr int LDS_BYTES = 163840;
constexpr int CV_E = 4608;
constexpr int CV_O = CV_E + (1056 + 15) * 4;

struct Params {
    const u16 *x0, *Lre, *Lim, *Bre, *Bim, *Cre, *Cim, *logdt, *Dskip, *k, *v, *shk, *ssmk, *ssmD;
    char* ws;
    float* out;
};

#define MF "v_mfma_f32_16x16x32_bf16 "
DI void mf(f32x4& c, v4u a, v4u b) { asm(MF "%0, %1, %2, %0" : "+v"(c) : "v"(a), "v"(b)); }
DI void mf0(f32x4& c, v4u a, v4u b) { asm(MF "%0, %1, %2, 0" : "=&v"(c) : "v"(a), "v"(b)); }
constexpr int W2R = 56;
#define WA(n) "a[%c" #n ":%c" #n "+3]"
template <int R> DI void mfa(f32x4& c, v4u a) { asm(MF "%0, " WA(2) ", %1, %0" : "+v"(c) : "v"(a), "i"(W2R + 4 * R)); }
template <int R> DI void mfa0(f32x4& c, v4u a) { asm(MF "%0, " WA(2) ", %1, 0" : "=&v"(c) : "v"(a), "i"(W2R + 4 * R)); }
#define ACC_FENCE10(c, d) asm volatile("" : "+v"(c[0]), "+v"(c[1]), "+v"(c[2]), "+v"(c[3]), "+v"(c[4]), "+v"(d[0]), "+v"(d[1]), "+v"(d[2]), "+v"(d[3]), "+v"(d[4]))
#define ACC_TIE5(c) asm volatile("" : "+v"(c[0]), "+v"(c[1]), "+v"(c[2]), "+v"(c[3]), "+v"(c[4]))
#define QP " quad_perm:[1,0,3,2] row_mask:0xf bank_mask:0xf"
struct St { f32x2 a, b; float c; };
struct Lm { f32x2 a, b; float r, s; };
#define PK1 " op_sel_hi:[0,1,1]\n\t"
#define PK2 " op_sel:[1,1,0] op_sel_hi:[1,0,1] neg_lo:[1,0,0]"
#define ST_(t0, t1, t2, h0, h1, h2, b0, b1, b2, l0, l1, l2, l3)                                                                                        \
    "v_pk_fma_f32 %" #t0 ", %" #l0 ", %" #h0 ", %" #b0 PK1 "v_pk_fma_f32 %" #t1 ", %" #l1 ", %" #h1 ", %" #b1 PK1 "v_fma_f32 %" #t2 ", %" #l2 ", %" #h2 ", %" #b2 "\n\t" \
    "v_pk_fma_f32 %" #t0 ", %" #l0 ", %" #h0 ", %" #t0 PK2 "\n\tv_pk_fma_f32 %" #t1 ", %" #l1 ", %" #h1 ", %" #t1 PK2 "\n\tv_fmac_f32_dpp %" #t2 ", %" #h2 ", %" #l3 QP "\n\t"
#define SO(t) "=&v"(t.a), "=&v"(t.b), "=&v"(t.c)
#define SI(t) "v"(t.a), "v"(t.b), "v"(t.c)
#define LI(l) "v"(l.a), "v"(l.b), "v"(l.r), "v"(l.s)
DI void run3(St& t, const St& h, const St* b, const Lm& l) {
    St u;
    asm(ST_(0, 1, 2, 6, 7, 8, 9, 10, 11, 18, 19, 20, 21) ST_(3, 4, 5, 0, 1, 2, 12, 13, 14, 18, 19, 20, 21) ST_(0, 1, 2, 3, 4, 5, 15, 16, 17, 18, 19, 20, 21)
        : SO(t), SO(u) : SI(h), SI(b[0]), SI(b[1]), SI(b[2]), LI(l));
}
DI void run4(St& t, const St& h, const St* b, const Lm& l) {
    St u;
    asm(ST_(3, 4, 5, 6, 7, 8, 9, 10, 11, 21, 22, 23, 24) ST_(0, 1, 2, 3, 4, 5, 12, 13, 14, 21, 22, 23, 24) ST_(3, 4, 5, 0, 1, 2, 15, 16, 17, 21, 22, 23, 24) ST_(0, 1, 2, 3, 4, 5, 18, 19, 20, 21, 22, 23, 24)
        : SO(t), SO(u) : SI(h), SI(b[0]), SI(b[1]), SI(b[2]), SI(b[3]), LI(l));
}
#define AC_(t0, t2, h0, h2, b0, b2, l0, l2, l3)                                                                                   \
    "v_pk_fma_f32 %" #t0 ", %" #l0 ", %" #h0 ", %" #b0 PK1 "v_fma_f32 %" #t2 ", %" #l2 ", %" #h2 ", %" #b2 "\n\t"               \
    "v_pk_fma_f32 %" #t0 ", %" #l0 ", %" #h0 ", %" #t0 PK2 "\n\tv_fmac_f32_dpp %" #t2 ", %" #h2 ", %" #l3 QP "\n\t"
#define AO(t) "=&v"(t.a), "=&v"(t.c)
#define AI(t) "v"(t.a), "v"(t.c)
DI void keep14(St* t, const St& h, const St& x, const St* b, const Lm& lq, const Lm& l) {
    St u;
    asm(AC_(8, 9, 10, 11, 12, 13, 22, 23, 24) AC_(0, 1, 8, 9, 14, 15, 25, 26, 27) AC_(2, 3, 0, 1, 16, 17, 25, 26, 27) AC_(4, 5, 2, 3, 18, 19, 25, 26, 27) AC_(6, 7, 4, 5, 20, 21, 25, 26, 27)
        : AO(t[0]), AO(t[1]), AO(t[2]), AO(t[3]), AO(u) : AI(h), AI(x), AI(b[0]), AI(b[1]), AI(b[2]), AI(b[3]), "v"(lq.a), "v"(lq.r), "v"(lq.s), "v"(l.a), "v"(l.r), "v"(l.s));
}
DI void keep4(St* t, const St& h, const St* b, const Lm& l) {
    asm(AC_(0, 1, 8, 9, 10, 11, 18, 19, 20) AC_(2, 3, 0, 1, 12, 13, 18, 19, 20) AC_(4, 5, 2, 3, 14, 15, 18, 19, 20) AC_(6, 7, 4, 5, 16, 17, 18, 19, 20)
        : AO(t[0]), AO(t[1]), AO(t[2]), AO(t[3]) : AI(h), AI(b[0]), AI(b[1]), AI(b[2]), AI(b[3]), "v"(l.a), "v"(l.r), "v"(l.s));
}
DI void b3cv(f32x2* t, unsigned* o, f32x2 h, f32x2 b0, f32x2 b1, f32x2 b2, f32x2 l0, f32x2 l, const float* e) {
    asm("v_pk_fma_f32 %0, %11, %7, %8" PK1 "v_cvt_pk_bf16_f32 %3, %13, %14\n\tv_pk_fma_f32 %0, %11, %7, %0" PK2 "\n\tv_cvt_pk_bf16_f32 %4, %15, %16\n\t"
        "v_pk_fma_f32 %1, %12, %0, %9" PK1 "v_cvt_pk_bf16_f32 %5, %17, %18\n\tv_pk_fma_f32 %1, %12, %0, %1" PK2 "\n\tv_cvt_pk_bf16_f32 %6, %19, %20\n\t"
        "v_pk_fma_f32 %2, %12, %1, %10" PK1
        : "=&v"(t[0]), "=&v"(t[1]), "=&v"(t[2]), "=&v"(o[0]), "=&v"(o[1]), "=&v"(o[2]), "=&v"(o[3])
        : "v"(h), "v"(b0), "v"(b1), "v"(b2), "v"(l0), "v"(l), "v"(e[0]), "v"(e[1]), "v"(e[2]), "v"(e[3]), "v"(e[4]), "v"(e[5]), "v"(e[6]), "v"(e[7]));
}
DI void bfin(f32x2& t, f32x2 h, f32x2 l) { asm("v_pk_fma_f32 %0, %2, %1, %0" PK2 : "+v"(t) : "v"(h), "v"(l)); }
#define UNDEF8(c) asm volatile("" : "=v"(c[0]), "=v"(c[1]), "=v"(c[2]), "=v"(c[3]), "=v"(c[4]), "=v"(c[5]), "=v"(c[6]), "=v"(c[7]))
#define PIN5(t) asm volatile("s_nop 1" : "+v"(t[0]), "+v"(t[1]), "+v"(t[2]), "+v"(t[3]), "+v"(t[4]))
DI unsigned cvtpk(float lo, float hi) {
    f32x2 v = {lo, hi};
    return __builtin_bit_cast(unsigned, __builtin_convertvector(v, bf16x2v));
}
DI float lo16f(unsigned u) { return __uint_as_float(u << 16); }
DI float hi16f(unsigned u) { return __uint_as_float(u & 0xffff0000u); }
DI float rbf(float v) { return lo16f(cvtpk(v, 0.f)); }
template <int CTRL> DI float dpp(float x) {
    return __int_as_float(__builtin_amdgcn_update_dpp(0, __float_as_int(x), CTRL, 0xF, 0xF, true));
}
DI float rowsum16(float x) {
    x += dpp<0x128>(x); x += dpp<0x124>(x); x += dpp<0x122>(x); x += dpp<0x121>(x);
    return x;
}
DI float xsum32(float x) { auto r = __builtin_amdgcn_permlane32_swap(__float_as_uint(x), __float_as_uint(x), false, false); return __uint_as_float(r[0]) + __uint_as_float(r[1]); }
DI void xsum2(float& a, float& d) {
    auto r = __builtin_amdgcn_permlane16_swap(__float_as_uint(a), __float_as_uint(d), false, false);
    const float s = __uint_as_float(r[0]) + __uint_as_float(r[1]);
    const float t = xsum32(s);
    auto u = __builtin_amdgcn_permlane16_swap(__float_as_uint(t), __float_as_uint(t), false, false);
    a = __uint_as_float(u[0]); d = __uint_as_float(u[1]);
}
DI float xsum2h(float a, float d) {
    auto r = __builtin_amdgcn_permlane16_swap(__float_as_uint(a), __float_as_uint(d), false, false);
    return xsum32(__uint_as_float(r[0]) + __uint_as_float(r[1]));
}
DI float hsum4(f32x4 v) { return (v.x + v.y) + (v.z + v.w); }
typedef __amdgpu_buffer_rsrc_t rsrc_t;
constexpr int SC1 = 16;
DI v4u ld128(rsrc_t rs, unsigned off) { return __builtin_amdgcn_raw_buffer_load_b128(rs, off, 0, SC1); }
DI v4u ld128s(rsrc_t rs, unsigned off, unsigned soff) { return __builtin_amdgcn_raw_buffer_load_b128(rs, off, soff, SC1); }
DI v2u ld64(rsrc_t rs, unsigned off) { return __builtin_amdgcn_raw_buffer_load_b64(rs, off, 0, SC1); }
DI void st128(v4u v, rsrc_t rs, unsigned off) { __builtin_amdgcn_raw_buffer_store_b128(v, rs, off, 0, SC1); }
DI void st64(v2u v, rsrc_t rs, unsigned off) { __builtin_amdgcn_raw_buffer_store_b64(v, rs, off, 0, SC1); }
#define LGKM0() { __builtin_amdgcn_s_waitcnt(0xC07F); asm volatile("" ::: "memory"); }
#define DRAIN() asm volatile("s_waitcnt vmcnt(0)" ::: "memory")
#define ORDER() __builtin_amdgcn_fence(__ATOMIC_ACQUIRE, "wavefront")
#define SCHED() __builtin_amdgcn_sched_barrier(0)
constexpr unsigned kSpinCap = 1u << 17;
#define WAVE_WAIT(...)                                                      \
    {                                                                       \
        unsigned spins_ = dead ? kSpinCap : 0u;                             \
        _Pragma("nounroll") for (;;) {                                      \
            asm volatile("" ::: "memory");                                  \
            bool ok;                                                        \
            __VA_ARGS__;                                                    \
            if (__builtin_amdgcn_ballot_w64(!ok) == 0ull) break;            \
            if (++spins_ > kSpinCap) { dead = true; break; }                \
            __builtin_amdgcn_s_sleep(1);                                    \
        }                                                                   \
        ORDER();                                                            \
    }
#define KPD1 2
#define KPDS 5
#define KPDS0 5
#define KPF0 5
#define KPH0 12
#define KPF1 4
constexpr int PD1 = KPD1, PDS = KPDS, PDS0 = KPDS0;
#define PL(r) "global_load_dwordx2 " r ", %5, %6 sc1\n\t"
#define PS_(r) "s_waitcnt vmcnt(1)\n\tv_cmp_ne_u64 vcc, %7, " r "\n\ts_cbranch_vccz 2f\n\t" PL(r)
template <int D0> DI void pipe_flag(v2u (&t)[4], unsigned off, const void* base, v2u tag, bool& dead) {
    unsigned n = dead ? 0u : kSpinCap;
    asm volatile("s_sleep %8\n\t" PL("%0") "s_sleep %9\n\t" PL("%1")
                 "1:\n\t" PS_("%0") PS_("%1") "s_sub_u32 %4, %4, 1\n\ts_cbranch_scc0 1b\n2:"
                 : "=&v"(t[0]), "=&v"(t[1]), "=&v"(t[2]), "=&v"(t[3]), "+s"(n) : "v"(off), "s"(base), "v"(tag), "i"(D0), "i"(KPF1) : "memory", "vcc", "scc");
    dead = __builtin_amdgcn_readfirstlane(n) == ~0u;
}
#define WAVE_RETRY(OK, ...) { if (__builtin_expect(__builtin_amdgcn_ballot_w64(!(OK)) != 0ull, 0)) WAVE_WAIT({ __VA_ARGS__; ok = OK; }) ORDER(); }
#define STAMP(i)
#define STAMP_DUMP()

#define T1POS 27
#define TW 2
#define KLB0 0
#define KLB1 5
#define SKIP0 0
#define SKIP2 0
#define SKIP7 150
#define SKIP5 0
#define SKIP4 0
constexpr int kPads[] = {PADS};
constexpr int pad_next(int id) {
    for (unsigned i = 0; i + 1 < sizeof(kPads) / sizeof(int); ++i) if (kPads[i] == id) return kPads[i + 1];
    return -1;
}
template <int ID> DI void site() {
    if constexpr (pad_next(ID) >= 0) asm volatile("kp%c0:\n\ts_cbranch_execz kp%c1" ::"i"(ID), "i"(pad_next(ID)));
    else asm volatile("");
}
template <int T> DI void tour() {
    if constexpr (pad_next(9000 + T) >= 0 && pad_next(9000 + T) != 9001 + T) asm volatile("s_mov_b64 exec, 0\n\ts_branch kp%c0\nkp%c1:\n\ts_mov_b64 exec, -1" ::"i"(pad_next(9000 + T)), "i"(9001 + T));
}
template <int N> DI void skip() { if constexpr (N > 0) asm volatile("s_branch 1f\n\t.rept %c0\n\ts_nop 0\n\t.endr\n1:" ::"i"(N)); }
template <int N> DI void room() { asm volatile("s_branch 1f\n\t.p2align 12, , %c0\n1:" ::"i"(N)); }
template <class F, int... I> DI void static_for_impl(std::integer_sequence<int, I...>, F&& f) { (f(std::integral_constant<int, I>{}), ...); }
template <int N, class F> DI void static_for(F&& f) { static_for_impl(std::make_integer_sequence<int, N>{}, f); }
#define LAMBDA(v) [&](auto v) __attribute__((always_inline))
DI f32x2 cmul(f32x2 a, f32x2 b) {
    f32x2 t;
    asm("v_pk_mul_f32 %0, %1, %2 op_sel_hi:[0,1]\n\ts_nop 0\n\tv_pk_fma_f32 %0, %1, %2, %0" PK2 "\n\ts_nop 0" : "=&v"(t) : "v"(a), "v"(b));
    return t;
}
DI f32x2 cfma(f32x2 a, f32x2 b, f32x2 c) {
    f32x2 t;
    asm("v_pk_fma_f32 %0, %1, %2, %3 op_sel_hi:[0,1,1]\n\ts_nop 0\n\tv_pk_fma_f32 %0, %1, %2, %0" PK2 "\n\ts_nop 0" : "=&v"(t) : "v"(a), "v"(b), "v"(c));
    return t;
}
DI void cfma4(f32x2 (&t)[4], f32x2 a, f32x2 b0, f32x2 b1, f32x2 b2, f32x2 b3, f32x2 c0, f32x2 c1, f32x2 c2, f32x2 c3) {
    asm("v_pk_fma_f32 %0, %4, %5, %9 op_sel_hi:[0,1,1]\n\tv_pk_fma_f32 %1, %4, %6, %10 op_sel_hi:[0,1,1]\n\tv_pk_fma_f32 %2, %4, %7, %11 op_sel_hi:[0,1,1]\n\tv_pk_fma_f32 %3, %4, %8, %12 op_sel_hi:[0,1,1]\n\t"
        "v_pk_fma_f32 %0, %4, %5, %0" PK2 "\n\tv_pk_fma_f32 %1, %4, %6, %1" PK2 "\n\tv_pk_fma_f32 %2, %4, %7, %2" PK2 "\n\tv_pk_fma_f32 %3, %4, %8, %3" PK2 "\n\ts_nop 0"
        : "=&v"(t[0]), "=&v"(t[1]), "=&v"(t[2]), "=&v"(t[3]) : "v"(a), "v"(b0), "v"(b1), "v"(b2), "v"(b3), "v"(c0), "v"(c1), "v"(c2), "v"(c3));
}
#define C4(t0, t1, t2, t3, a, b0, b1, b2, b3, c0, c1, c2, c3)                                                                                                         \
    "v_pk_fma_f32 %" #t0 ", %" #a ", %" #b0 ", %" #c0 PK1 "v_pk_fma_f32 %" #t1 ", %" #a ", %" #b1 ", %" #c1 PK1 "v_pk_fma_f32 %" #t2 ", %" #a ", %" #b2 ", %" #c2 PK1 "v_pk_fma_f32 %" #t3 ", %" #a ", %" #b3 ", %" #c3 PK1 \
    "v_pk_fma_f32 %" #t0 ", %" #a ", %" #b0 ", %" #t0 PK2 "\n\tv_pk_fma_f32 %" #t1 ", %" #a ", %" #b1 ", %" #t1 PK2 "\n\tv_pk_fma_f32 %" #t2 ", %" #a ", %" #b2 ", %" #t2 PK2 "\n\tv_pk_fma_f32 %" #t3 ", %" #a ", %" #b3 ", %" #t3 PK2 "\n\t"
DI void cfma4n(f32x2 (&t)[4], f32x2 a, f32x2 b0, f32x2 b1, f32x2 b2, f32x2 b3, f32x2 c0, f32x2 c1, f32x2 c2, f32x2 c3) {
    asm(C4(0, 1, 2, 3, 4, 5, 6, 7, 8, 9, 10, 11, 12) : "=&v"(t[0]), "=&v"(t[1]), "=&v"(t[2]), "=&v"(t[3]) : "v"(a), "v"(b0), "v"(b1), "v"(b2), "v"(b3), "v"(c0), "v"(c1), "v"(c2), "v"(c3));
}
DI void agg(unsigned& er, unsigned& ei, f32x2 (&x)[4], f32x2& a, const f32x2 (&u)[4], const f32x2 (&s)[16], f32x2 l8, f32x2 l32, f32x2 l64) {
    f32x2 v[4], b;
    asm(C4(4, 5, 6, 7, 22, 10, 11, 12, 13, 14, 15, 16, 17) C4(0, 1, 2, 3, 22, 4, 5, 6, 7, 18, 19, 20, 21)
        "v_pk_fma_f32 %8, %23, %0, %1" PK1 "v_pk_fma_f32 %9, %23, %2, %3" PK1 "v_pk_fma_f32 %8, %23, %0, %8" PK2 "\n\tv_pk_fma_f32 %9, %23, %2, %9" PK2
        : "=&v"(x[0]), "=&v"(x[1]), "=&v"(x[2]), "=&v"(x[3]), "=&v"(v[0]), "=&v"(v[1]), "=&v"(v[2]), "=&v"(v[3]), "=&v"(a), "=&v"(b)
        : "v"(u[0]), "v"(u[1]), "v"(u[2]), "v"(u[3]), "v"(s[2]), "v"(s[6]), "v"(s[10]), "v"(s[14]), "v"(s[3]), "v"(s[7]), "v"(s[11]), "v"(s[15]), "v"(l8), "v"(l32));
    asm("s_nop 0\n\tv_fma_f32 %0, %2, %6, %4\n\tv_fma_f32 %1, %2, %7, %5\n\tv_fma_f32 %0, -%3, %7, %0\n\tv_fma_f32 %1, %3, %6, %1"
        : "=&v"(er), "=&v"(ei) : "v"(a.x), "v"(a.y), "v"(b.x), "v"(b.y), "v"(l64.x), "v"(l64.y));
}
DI void gacc2(f32x2& h0, f32x2& h1, f32x2 w0, f32x2 w1, v4u e0, v4u e1) {
    const v2u a0 = {e0.x, e0.y}, b0 = {e0.z, e0.w}, a1 = {e1.x, e1.y}, b1 = {e1.z, e1.w};
    asm("v_pk_fma_f32 %0, %4, %2, %0 op_sel_hi:[0,1,1]\n\tv_pk_fma_f32 %1, %6, %3, %1 op_sel_hi:[0,1,1]\n\t"
        "v_pk_fma_f32 %0, %5, %2, %0 op_sel:[0,1,0] op_sel_hi:[0,0,1] neg_lo:[1,0,0]\n\tv_pk_fma_f32 %1, %7, %3, %1 op_sel:[0,1,0] op_sel_hi:[0,0,1] neg_lo:[1,0,0]"
        : "+v"(h0), "+v"(h1) : "v"(w0), "v"(w1), "v"(a0), "v"(b0), "v"(a1), "v"(b1));
}
DI s16x4 trread(char* smem, int byteoff) {
    return __builtin_amdgcn_ds_read_tr16_b64_v4i16((LAS s16x4*)(smem + byteoff));
}

#define CV_LOAD_A(A, a, rr_, hf) { const unsigned* ap_ = (const unsigned*)(smem + ab[(a) >> 1] + (((a) & 1) ? 0 : 512) + 64 * (7 - (rr_)) + 8 * (hf)); A[a][2 * (hf)] = ap_[0]; A[a][2 * (hf) + 1] = ap_[1]; }
#define CV_LOAD_B(B, e, rr_) { B[e] = *(const v4u*)(smem + bb + 64 * (7 - (rr_)) + 512 * (e)); }
template <int R, bool PF, class F, class G>
DI void conv_round(f32x4 (&ca)[8], v4u (&A)[8], const v4u (&B)[8], v4u (&An)[8], v4u (&Bn)[8], const char* smem, const int (&ab)[4], int bb, F&& fill, G&& last) {
    static_for<36>(LAMBDA(g_) {
        constexpr int g = g_.value;
        constexpr int a = g < 8 ? 0 : g < 15 ? 1 : g < 21 ? 2 : g < 26 ? 3 : g < 30 ? 4 : g < 33 ? 5 : g < 35 ? 6 : 7;
        constexpr int e = g - (a * 8 - a * (a - 1) / 2);
        if constexpr (R == 0 && a == 0) mf0(ca[e], A[0], B[e]); else mf(ca[a + e], A[a], B[e]);
        SCHED();
        if constexpr (R == 0 && g < 16 && (g & 3) != 3) { constexpr int n = g - (g >> 2); CV_LOAD_A(A, 2 + (n >> 1), 0, n & 1) SCHED(); }
        if constexpr (PF && g < 32 && (g & 3) != 3) { if constexpr ((g & 3) == 2) CV_LOAD_B(Bn, g >> 2, R + 1) else CV_LOAD_A(An, g >> 2, R + 1, g & 1) SCHED(); }
        site<3000 + 36 * R + g>();
        if constexpr (R == 7) { last(g_); SCHED(); }
        if constexpr ((g & 3) == 3 && g < 28) { fill(std::integral_constant<int, 7 * R + g / 4>{}); SCHED(); }
    });
}
template <int R0, int R1, bool PFL, class F, class G>
DI void conv_part(f32x4 (&ca)[8], v4u (&A0)[8], v4u (&B0)[8], const char* smem, const int (&ab)[4], int bb, F&& fill, G&& last) {
    v4u A1[8], B1[8];
    static_for<R1 - R0>(LAMBDA(r_) {
        constexpr int r = r_.value;
        if constexpr (r & 1) conv_round<R0 + r, (R0 + r + 1 < R1 || PFL)>(ca, A1, B1, A0, B0, smem, ab, bb, fill, last);
        else conv_round<R0 + r, (R0 + r + 1 < R1 || PFL)>(ca, A0, B0, A1, B1, smem, ab, bb, fill, last);
    });
}
template <int IMM> DI void gld(v4u& dst, unsigned voff, const void* sbase) {
    asm volatile("global_load_dwordx4 %0, %1, %2 offset:%3" : "=v"(dst) : "v"(voff), "s"(sbase), "i"(IMM));
}
#define OOB 3
#define OUTP 16
#define NTM 31
template <int IMM, int BIT> DI void gldn(v4u& dst, unsigned voff, const void* sbase) {
    if constexpr ((NTM >> BIT) & 1) asm volatile("global_load_dwordx4 %0, %1, %2 offset:%3 nt" : "=v"(dst) : "v"(voff), "s"(sbase), "i"(IMM));
    else gld<IMM>(dst, voff, sbase);
}
DI void gldu16(unsigned& dst, unsigned voff, const void* sbase) { asm volatile("global_load_ushort %0, %1, %2" : "=v"(dst) : "v"(voff), "s"(sbase)); }
DI void gld1s(unsigned& dst, unsigned voff, const void* sbase) { asm volatile("global_load_dword %0, %1, %2 sc1" : "=v"(dst) : "v"(voff), "s"(sbase)); }
template <int IMM> DI void gld64(v4u& dst, const void* a) {
    if constexpr ((NTM >> 5) & 1) asm volatile("global_load_dwordx4 %0, %1, off offset:%2 nt" : "=v"(dst) : "v"(a), "i"(IMM));
    else asm volatile("global_load_dwordx4 %0, %1, off offset:%2" : "=v"(dst) : "v"(a), "i"(IMM));
}
template <int V> DI void SETM0(unsigned ldsbase) { asm volatile("s_add_u32 m0, %0, %1" ::"s"(ldsbase), "i"(V)); }
template <int GOFF> DI void GLD0(unsigned voff, const char* sbase) { asm volatile("global_load_lds_dwordx4 %0, %1 offset:%2" ::"v"(voff), "s"(sbase), "i"(GOFF) : "memory"); }
template <int GOFF, int V> DI void GLD1(unsigned voff, const char* sbase, unsigned ldsbase) {
    asm volatile("global_load_lds_dwordx4 %0, %1 offset:%3\n\ts_add_u32 m0, %2, %4" ::"v"(voff), "s"(sbase), "s"(ldsbase), "i"(GOFF), "i"(V) : "memory");
}
template <int L, int G> DI void dma_x(unsigned voff, const void* sbase, unsigned ldsbase) {
    asm volatile("s_add_u32 m0, %2, %4\n\ts_nop 0\n\tglobal_load_lds_dwordx4 %0, %1 offset:%3" ::"v"(voff), "s"(sbase), "s"(ldsbase), "i"(G), "i"(L - G) : "memory", "scc");
}
DI void ld_kv(v4u (&a)[15], int on,        unsigned kh, unsigned ko, v4u rk, const void* k, const void* v) {
    asm volatile("s_cmp_ge_u32 %11, 80\n\ts_cbranch_scc1 1f\n\t"
                 "buffer_load_dwordx4 %0, %12, %14, 0 offen nt\n\tbuffer_load_dwordx4 %1, %12, %14, 0 offen offset:1280 nt\n\tbuffer_load_dwordx4 %2, %12, %14, 0 offen offset:2560 nt\n\t"
                 "global_load_dwordx4 %3, %13, %15 nt\n\tglobal_load_dwordx4 %4, %13, %15 offset:1280 nt\n\tglobal_load_dwordx4 %5, %13, %15 offset:2560 nt\n\tglobal_load_dwordx4 %6, %13, %15 offset:3840 nt\n\t"
                 "global_load_dwordx4 %7, %13, %16 nt\n\tglobal_load_dwordx4 %8, %13, %16 offset:1280 nt\n\tglobal_load_dwordx4 %9, %13, %16 offset:2560 nt\n\tglobal_load_dwordx4 %10, %13, %16 offset:3840 nt\n1:"
                 : "=&v"(a[0]), "=&v"(a[1]), "=&v"(a[2]), "=&v"(a[3]), "=&v"(a[4]), "=&v"(a[5]), "=&v"(a[6]), "=&v"(a[7]), "=&v"(a[8]), "=&v"(a[9]), "=&v"(a[10])
                 : "s"(on), "v"(kh), "v"(ko), "s"(rk), "s"(k), "s"(v) : "scc");
}
DI void ld_wr(v4u (&a)[4], int on, unsigned o, const void* base) {
    asm volatile("s_cmp_ge_u32 %4, 80\n\ts_cbranch_scc0 1f\n\t.rept 4\n\tglobal_load_dword a[32+1], %5, %6\n\t.endr\n\ts_branch 2f\n1:\n\t"
                 "global_load_dwordx4 %0, %5, %6 nt\n\tglobal_load_dwordx4 %1, %5, %6 offset:1024 nt\n\tglobal_load_dwordx4 %2, %5, %6 offset:2048 nt\n\tglobal_load_dwordx4 %3, %5, %6 offset:3072 nt\n2:"
                 : "=&v"(a[0]), "=&v"(a[1]), "=&v"(a[2]), "=&v"(a[3]) : "s"(on), "v"(o), "s"(base) : "scc");
}
template <int L, int G> DI void dma_1(unsigned voff, const void* sbase, unsigned ldsbase) {
    asm volatile("s_add_u32 m0, %2, %4\n\ts_nop 0\n\tglobal_load_lds_dword %0, %1 offset:%3" ::"v"(voff), "s"(sbase), "s"(ldsbase), "i"(G), "i"(L - G) : "memory", "scc");
}
template <int L> DI void dma_n(unsigned voff, const void* sbase, unsigned ldsbase) {
    asm volatile("s_add_u32 m0, %2, %3\n\ts_nop 0\n\tglobal_load_lds_dwordx4 %0, %1 nt" ::"v"(voff), "s"(sbase), "s"(ldsbase), "i"(L) : "memory", "scc");
}
template <int A, int O> DI void lds2a(unsigned a) { asm volatile("ds_read_b64 a[%c1:%c1+1], %0 offset:%2" ::"v"(a), "i"(A), "i"(O) : "memory"); }
constexpr int L_EAR = L_XR + 8192;
constexpr int dma_goff(int it) { return it < 40 ? (it >> 2) * 128 : ((it - 100) / 5) * 256; }
constexpr int dma_loff(int it) { return it < 40 ? L_XR + ((it >> 2) % 3) * 4096 + (it & 3) * 1024 : ((it - 100) % 5) * 20480 + ((it - 100) / 5) * 2048; }
#define KFS 4
#define APOS 0
#define PSA 0
#define CWP 2
#define SAF 2
#define KD0 30
#define KST 59
#define KSL 5
#define KSC 11
#define WSP 1
#define XG0 5
#define XGS 1
#define WOD 1
#define USP 1
#define KSG 0
#define PKF 7
#define XDM 1
#define PSP 3
#define SBL 1
#define SMP 1
#define KW2E 0
#define KW2T 4
#define OTR 1
#define KQ0 0
#define KQ1 18
#define KQ2 4
#define KQ3 0
#define NPD 27
#define IMP 0
#define KSP 1
constexpr int kQ0 = KQ0, kQ1 = KQ1, kQ2 = KQ2, kQ3 = KQ3;
#define KW2S 16
#define KW2M 0x42
#define KFKS 13
#define KFKG 6
#define KUS 17
#define KTS 19
#define KFWP 13
constexpr int kW2S = KW2S, kW2M = KW2M;
constexpr int NW2P = (20 - kW2S) * __builtin_popcount(kW2M);
constexpr int P1_PRE = NPD + 7;
constexpr int p1_dma(int i) { return i < 5 ? 100 + i : i < 13 ? i - 5 : i < 18 ? 105 + (i - 13) : i < 22 ? 8 + (i - 18) : i < NPD ? 110 + (i - 22) : 400; }
constexpr int kQ1a = KST > KD0 ? kQ1 * (KST - KD0) / (117 - KD0) : 0;
constexpr int p1_pre(int i) { return i < kQ0 + kQ1a ? p1_dma(i) : i == kQ0 + kQ1a ? 400 : p1_dma(i - 1); }
constexpr int p1_item(int s, int g) {
    if (s == KFS && g == (XG0 == 5 ? 4 : 9)) return 324;
    if (WOD ? (s & 1) && s < 10 && (g == 3 || g == 8) : g == 9 && s < 10) return WOD ? 114 + s + (g == 8) : 115 + s;
    if (s == KSL && (g == KSG || g == KSG + 1)) return 202 + g - KSG;
    if (s == KFWP && g == 1) return 208;
    if (s == KFKS && g == KFKG) return 206;
    if (USP && s == KUS && (g == 0 || g == 2 || g == 4 || g == 7)) return 210 + g / 2;
    if (s == KTS && g == 9) return 209;
    if (s >= kW2S && ((kW2M >> g) & 1)) return 500 + (s - kW2S) * __builtin_popcount(kW2M) + __builtin_popcount(kW2M & ((1 << g) - 1));
    if ((s & 1) == 0 && s <= 12 && g >= XG0 && (g - XG0) % XGS == 0 && (g - XG0) / XGS < 4) return 4 * (s / 2 + 3) + (g - XG0) / XGS;
    return -1;
}
constexpr int p1_cnt(int s, int g) {
    int n = P1_PRE;
    for (int t = 0; t <= s; ++t)
        for (int u = 0; u < 10; ++u) {
            if (t == s && u >= g) break;
            if (p1_item(t, u) >= 0) ++n;
            if (!USP && t == KUS && u == 0) n += 4;
        }
    return n;
}
constexpr int p1_pos(int code) {
    for (int i = 0; i < P1_PRE; ++i) if (p1_pre(i) == code) return i;
    for (int t = 0; t < 20; ++t)
        for (int u = 0; u < 10; ++u) if (p1_item(t, u) == code) return p1_cnt(t, u);
    return -1000;
}
constexpr int p1_lds(int s, int g) {
    constexpr int t0 = XG0 == 5 ? 0 : 5;
    if (s >= 2 && s < 10 && !(s & 1) && g >= t0 && g < t0 + 2) return 2 + s + g - t0;
    if (s == KSC + 1 && g == t0 + 2) return 38;
    if (s >= 14 && s < 19 && !(s & 1) && (g == 5 || g == 8)) return 30 + (s - 14) + g / 8;
    if (s == 19 && (g == 0 || g == 4)) return 36 + g / 4;
    if (s == 19 && g == 7) return 39;
    return -1;
}
constexpr int p1_nl(int s, int g0, int g1) {
    int n = 0;
    for (int g = g0; g < g1; ++g) n += (s < 19 && g < 5) + ((s & 1) && s < 19 && g >= 5 && g < 9) + (p1_lds(s, g) >= 0);
    return n;
}
constexpr int p1_cap(int s, int g) {
    int u = p1_nl(s, g, g + 1) + (g == 9 && s < 19) + ((s & 1) && s < 19 && g == 5) + ((s & 1) == 0 && g == XG0 - 1);
    const int it = p1_item(s, g), nt = g < 9 ? p1_item(s, g + 1) : -1;
    u += (it >= 0) + (nt >= 0 && nt < 200) + (it == 324);
    if ((s == 19 && g == 2) || (g == 9 && ((s & 3) == 2 || s == KUS - 1 || s == 18))) u += 2;
    return u < 2 ? 2 - u : 0;
}
constexpr int NFILL = 112;
constexpr int p1_from(int f) { return f < 40 ? 0 : f < 52 ? 9 : f < 72 ? KSC : 14; }
constexpr int p1_fill(int s, int g, int e) {
    int f = 0;
    for (int t = 0; t <= s; ++t)
        for (int u = 0; u < 10; ++u)
            for (int v = 0; v < p1_cap(t, u); ++v) {
                const bool go = f < NFILL && p1_from(f) <= t;
                if (t == s && u == g && v == e) return go ? f : -1;
                f += go;
                if (f == 40) f = 48;
            }
    return f;
}
static_assert(p1_fill(13, 10, 0) >= 52, "");
constexpr bool p1_all() {
    for (int i = 0; i < 40; ++i) if (p1_pos(i) < 0) return false;
    for (int i = 100; i < 125; ++i) if (p1_pos(i) < 0) return false;
    for (int i = 0; i < NW2P; ++i) if (p1_pos(500 + i) < 0) return false;
    return p1_pos(202) >= 0 && p1_pos(203) >= 0 && p1_pos(206) >= 0 && p1_pos(208) >= 0 && p1_pos(209) >= 0 && p1_pos(324) >= 0;
}
static_assert(p1_all() && p1_lds(14, 5) == 30 && p1_lds(14, 8) == 31 && p1_lds(16, 5) == 32, "");
constexpr int ring_req(int G, int I, int R, int X) {
    int n = I, e = 0;
    for (int t = 0; t <= G; ++t) {
        int r = -1;
        if (t % 5 == 0 || t % 5 == 3) {
            if (n < 40 && 5 * (n - R) + 4 < t) r = n++;
            else if (e < X && t % 10 == 3) r = 100 + e++;
        }
        if (t == G) return r;
    }
    return -1;
}
constexpr int g2_adr(int i, int kk) {
    int n = 0;
    for (int t = 0; t <= i; ++t)
        for (int u = 0; u < 5; ++u) {
            const bool ok = u == 4 || (u == 2 && (t < 11 || t > 15)) || (u == 3 && t < 5);
            if (t == i && u == kk) return ok ? n : -1;
            n += ok;
        }
    return -1;
}
constexpr int p1_lim(int n) { return n > 63 ? 63 : n; }
template <int IMM> DI void gld4s(v4u& dst, unsigned voff, const void* sbase) { asm volatile("global_load_dwordx4 %0, %1, %2 offset:%3 sc1" : "=v"(dst) : "v"(voff), "s"(sbase), "i"(IMM)); }
template <int IMM> DI void gld2s(v2u& dst, unsigned voff, const void* sbase) { asm volatile("global_load_dwordx2 %0, %1, %2 offset:%3 sc1" : "=v"(dst) : "v"(voff), "s"(sbase), "i"(IMM)); }
template <class T> DI void pin(T& v) { asm volatile("" : "+v"(v)); }
DI void mul2(float& a, float& b, float x, float y, float s) { asm volatile("v_mul_f32 %0, %2, %4\n\tv_mul_f32 %1, %3, %4" : "=&v"(a), "=&v"(b) : "v"(x), "v"(y), "v"(s)); }
DI void xor2(float& a, float& b, unsigned x, unsigned y, unsigned s) { asm volatile("v_xor_b32 %0, %2, %4\n\tv_xor_b32 %1, %3, %4" : "=&v"(a), "=&v"(b) : "v"(x), "v"(y), "v"(s)); }
DI void rot2(unsigned& a, unsigned& b, unsigned x, unsigned y) { asm volatile("v_alignbit_b32 %0, %2, %2, 16\n\tv_alignbit_b32 %1, %3, %3, 16" : "=&v"(a), "=&v"(b) : "v"(x), "v"(y)); }
DI void mix2(unsigned& a, unsigned& b, unsigned x, unsigned y, unsigned z) { asm volatile("v_bfi_b32 %0, %5, %2, %3\n\tv_bfi_b32 %1, %5, %3, %4" : "=&v"(a), "=&v"(b) : "v"(x), "v"(y), "v"(z), "s"(0xffffu)); }
template <int I> DI void shr1(unsigned& o, unsigned prev, unsigned x) {
    if constexpr (I > 0) asm volatile("v_mov_b32_dpp %0, %1 wave_ror:1 row_mask:0xf bank_mask:0xf\n\tv_mov_b32_dpp %0, %2 wave_shr:1 row_mask:0xf bank_mask:0xf" : "=&v"(o) : "v"(prev), "v"(x));
    else asm volatile("v_mov_b32_dpp %0, %1 wave_shr:1 row_mask:0xf bank_mask:0xf bound_ctrl:0" : "=&v"(o) : "v"(x));
}
template <int G> DI void bc2(float& a, float& b, unsigned x) {
    asm volatile("v_mov_b32_dpp %0, %2 row_newbcast:%3 row_mask:0xf bank_mask:0xf\n\tv_mov_b32_dpp %1, %2 row_newbcast:%4 row_mask:0xf bank_mask:0xf" : "=&v"(a), "=&v"(b) : "v"(x), "i"(G), "i"(G + 8));
}
DI void rot1(unsigned& a, unsigned x) { asm volatile("v_alignbit_b32 %0, %1, %1, 16" : "=v"(a) : "v"(x)); }
DI void mix1(unsigned& a, unsigned x, unsigned y) { asm volatile("v_bfi_b32 %0, %3, %1, %2" : "=v"(a) : "v"(x), "v"(y), "s"(0xffffu)); }
DI void ror1(unsigned& o, unsigned prev) { asm volatile("v_mov_b32_dpp %0, %1 wave_ror:1 row_mask:0xf bank_mask:0xf" : "=v"(o) : "v"(prev)); }
DI void shr1b(unsigned& o, unsigned x) { asm volatile("v_mov_b32_dpp %0, %1 wave_shr:1 row_mask:0xf bank_mask:0xf" : "+v"(o) : "v"(x)); }
DI void xor1(float& a, unsigned x, unsigned s) { asm volatile("v_xor_b32 %0, %1, %2" : "=v"(a) : "v"(x), "v"(s)); }
template <int G> DI void bc1(float& a, unsigned x) { asm volatile("v_mov_b32_dpp %0, %1 row_newbcast:%2 row_mask:0xf bank_mask:0xf" : "=v"(a) : "v"(x), "i"(G)); }
template <int G> DI void mulb(float& o, unsigned x, float y) { asm volatile("v_mul_f32_dpp %0, %1, %2 row_newbcast:%3 row_mask:0xf bank_mask:0xf" : "=v"(o) : "v"(x), "v"(y), "i"(G)); }
template <int N> DI void lgkm() { __builtin_amdgcn_s_waitcnt(0xC07F | (N << 8)); asm volatile("" ::: "memory"); }
DI void unp2(float& a, float& b, unsigned x) { asm volatile("v_lshlrev_b32 %0, 16, %2\n\tv_and_b32 %1, 0xffff0000, %2" : "=&v"(a), "=&v"(b) : "v"(x)); }
#define GL(a) "global_load_dwordx4 a[" #a ":" #a "+3], %7, %8 sc1\n\tglobal_load_dwordx4 a[" #a "+4:" #a "+7], %7, %8 offset:256 sc1\n\t"
#define AR(d, a, o) "v_accvgpr_read_b32 " d ", a[" #a "+" #o "]\n\t"
#define GS_(a) "s_waitcnt vmcnt(4)\n\t" AR("%2", a, 1) AR("%3", a, 3) AR("%4", a, 5) AR("%5", a, 7) AR("%0", a, 0) AR("%1", a, 2)                              \
    "v_xor_b32 %2, %9, %2\n\tv_bitop3_b32 %4, %4, %9, %2 bitop3:0xbe\n\tv_bitop3_b32 %4, %3, %9, %4 bitop3:0xbe\n\tv_bitop3_b32 %4, %5, %9, %4 bitop3:0xbe\n\t" \
    AR("%2", a, 4) AR("%3", a, 6) "v_cmp_ne_u32 vcc, 0, %4\n\ts_cbranch_vccz 2f\n\t" GL(a)
template <int A, int D> DI void gran_req(unsigned off, const void* base) {
    if constexpr (D > 0) __builtin_amdgcn_s_sleep(D);
    asm volatile("global_load_dwordx4 a[%c2:%c2+3], %0, %1 sc1\n\tglobal_load_dwordx4 a[%c2+4:%c2+7], %0, %1 offset:256 sc1" ::"v"(off), "s"(base), "i"(A) : "memory");
}
DI void pipe_gran(float& a, float& d, unsigned off, const void* base, unsigned tag, bool& dead) {
    unsigned n = dead ? 0u : kSpinCap, y, z;
    float a1, d1;
    asm volatile("1:\n\t" GS_(32) GS_(40) GS_(48) "s_sub_u32 %6, %6, 1\n\ts_cbranch_scc0 1b\n2:"
                 : "=&v"(a), "=&v"(d), "=&v"(a1), "=&v"(d1), "=&v"(y), "=&v"(z), "+s"(n) : "v"(off), "s"(base), "s"(tag) : "memory", "vcc", "scc");
    dead = __builtin_amdgcn_readfirstlane(n) == ~0u;
    a += a1; d += d1;
}
DI void fma2(float& a, float& b, float x, float y, float s, float t, float u, float v) {
    asm volatile("v_fma_f32 %0, %2, %4, %6\n\tv_fma_f32 %1, %3, %5, %7" : "=&v"(a), "=&v"(b) : "v"(x), "v"(y), "v"(s), "v"(t), "v"(u), "v"(v));
}
DI void fma2p(f32x2& o, float x, float y, float r, float u, float v) {
    asm("v_fmac_f32 %0, %1, %2" : "+v"(u) : "v"(x), "v"(r));
    asm("v_fmac_f32 %0, %1, %2" : "+v"(v) : "v"(y), "v"(r));
    o = f32x2{u, v};
}
DI void swp2(unsigned& a, unsigned& b, unsigned& c, unsigned& d) { asm volatile("s_nop 1\n\tv_permlane16_swap_b32 %0, %1\n\tv_permlane16_swap_b32 %2, %3" : "+v"(a), "+v"(b), "+v"(c), "+v"(d)); }
DI void swp2n(unsigned& a, unsigned& b, unsigned& c, unsigned& d) { asm volatile("v_permlane16_swap_b32 %0, %1\n\tv_permlane16_swap_b32 %2, %3" : "+v"(a), "+v"(b), "+v"(c), "+v"(d)); }
DI void cp2(unsigned& a, unsigned& b, f32x4 v) { asm volatile("v_cvt_pk_bf16_f32 %0, %2, %3\n\tv_cvt_pk_bf16_f32 %1, %4, %5" : "=&v"(a), "=v"(b) : "v"(v.x), "v"(v.y), "v"(v.z), "v"(v.w)); }
DI void zero2(v2u& a) { asm volatile("v_mov_b64 %0, 0" : "=v"(a)); }
DI void mov1(unsigned& a, unsigned b) { asm volatile("v_mov_b32 %0, %1" : "=v"(a) : "v"(b)); }
DI void sw16(unsigned& a, unsigned& b) { asm volatile("v_permlane16_swap_b32 %0, %1" : "+v"(a), "+v"(b)); }
DI void sw32(unsigned& a, unsigned& b) { asm volatile("v_permlane32_swap_b32 %0, %1" : "+v"(a), "+v"(b)); }
DI void mrg8(float& a, float b) { asm volatile("v_mov_b32_dpp %0, %1 quad_perm:[0,1,2,3] row_mask:0xf bank_mask:0xc" : "+v"(a) : "v"(b)); }
DI void sel1(float& o, unsigned a, unsigned b, u64 m) { asm volatile("v_cndmask_b32 %0, %1, %2, %3" : "=v"(o) : "v"(a), "v"(b), "s"(m)); }
DI void ne1(u64& m, v2u tag, v2u a) { asm volatile("v_cmp_ne_u64 %0, %1, %2" : "=s"(m) : "v"(tag), "v"(a)); }
DI void ne2(u64& m, v2u tag, v2u a, v2u b) { asm volatile("v_cmp_ne_u64 vcc, %1, %2\n\tv_cmp_ne_u64 %0, %1, %3\n\ts_or_b64 %0, %0, vcc" : "=&s"(m) : "v"(tag), "v"(a), "v"(b) : "vcc", "scc"); }
template <int G> DI void bc2b(float& a, float& b, float x, float y) {
    asm volatile("v_mov_b32_dpp %0, %2 row_newbcast:%4 row_mask:0xf bank_mask:0xf\n\tv_mov_b32_dpp %1, %3 row_newbcast:%4 row_mask:0xf bank_mask:0xf" : "=&v"(a), "=&v"(b) : "v"(x), "v"(y), "i"(G));
}
template <class T> DI void pins(T& v) { asm volatile("" : "+s"(v)); }
template <int I> DI void gld_w2(unsigned voff, const void* sbase) {
    asm volatile("global_load_dwordx4 " WA(2) ", %0, %1 offset:%3 sc1" ::"v"(voff), "s"(sbase), "i"(W2R + 4 * I), "i"((I % 5) * 1024 - 2048));
}
DI void gld2t(v2u& dst, unsigned voff, const void* sbase) { asm volatile("global_load_dwordx2 %0, %1, %2 sc1" : "+v"(dst) : "v"(voff), "s"(sbase)); }
DI void wait2(v2u& v, v4u (&x)[3]) { asm volatile("s_waitcnt vmcnt(0)" : "+v"(v), "+v"(x[0]), "+v"(x[1]), "+v"(x[2])::"memory"); }
template <int IMM> DI void gld4p(v4u& dst, unsigned voff, const void* sbase) { asm volatile("global_load_dwordx4 %0, %1, %2 offset:%3" : "+v"(dst) : "v"(voff), "s"(sbase), "i"(IMM)); }
DI void undef4(v4u (&x)[4]) { asm volatile("" : "=v"(x[0]), "=v"(x[1]), "=v"(x[2]), "=v"(x[3])); }
DI void wait1(v2u& v) { asm volatile("s_waitcnt vmcnt(0)" : "+v"(v)::"memory"); }
template <int N> DI void wait_fkn(v2u& v, v2u& u) { asm volatile("s_waitcnt vmcnt(%2)" : "+v"(v), "+v"(u) : "i"(N) : "memory"); }
template <int A, int IMM> DI void lda2(unsigned o, const void* base) { asm volatile("global_load_dwordx2 a[%c2:%c2+1], %0, %1 offset:%3" ::"v"(o), "s"(base), "i"(A), "i"(IMM)); }
DI void wait_fk(v2u& v, v2u& u, unsigned& x) { asm volatile("s_waitcnt vmcnt(0)" : "+v"(v), "+v"(u), "+v"(x)::"memory"); }
DI void ld_ur(unsigned o, const void* base) {
    asm volatile("global_load_dwordx4 a[40:40+3], %0, %1 sc1\n\tglobal_load_dwordx4 a[44:44+3], %0, %1 offset:1024 sc1\n\t"
                 "global_load_dwordx4 a[48:48+3], %0, %1 offset:2048 sc1\n\tglobal_load_dwordx4 a[52:52+3], %0, %1 offset:3072 sc1" ::"v"(o), "s"(base));
}
template <int I> DI void ld_ur1(unsigned o, const void* base) { asm volatile("global_load_dwordx4 a[%c2:%c2+3], %0, %1 offset:%3 sc1" ::"v"(o), "s"(base), "i"(40 + 4 * I), "i"(1024 * I)); }
constexpr int kUY = USP ? p1_cnt(20, 0) - 1 - p1_pos(213) : p1_cnt(20, 0) - p1_cnt(KUS, 0) - 4;
DI void st_ur(unsigned a) {
    asm volatile("s_waitcnt vmcnt(%1)\n\tds_write_b128 %0, a[40:40+3] offset:480\n\tds_write_b128 %0, a[44:44+3] offset:1504\n\t"
                 "ds_write_b128 %0, a[48:48+3] offset:2528\n\tds_write_b128 %0, a[52:52+3] offset:3552" ::"v"(a), "i"(kUY < NW2P ? kUY : NW2P) : "memory");
}
template <int I> DI void st_ur1(unsigned a) {
    if constexpr (I == 0) asm volatile("s_waitcnt vmcnt(%0)" ::"i"(kUY) : "memory");
    asm volatile("ds_write_b128 %0, a[%c1:%c1+3] offset:%2" ::"v"(a), "i"(40 + 4 * I), "i"(480 + 1024 * I) : "memory");
}
DI void reload_b(v4u (&B)[8], unsigned a) {
    asm volatile("s_waitcnt lgkmcnt(0)\n\tds_read_b128 %0, %8 offset:448\n\tds_read_b128 %1, %8 offset:960\n\tds_read_b128 %2, %8 offset:1472\n\tds_read_b128 %3, %8 offset:1984\n\t"
                 "ds_read_b128 %4, %8 offset:2496\n\tds_read_b128 %5, %8 offset:3008\n\tds_read_b128 %6, %8 offset:3520\n\tds_read_b128 %7, %8 offset:4032\n\ts_waitcnt lgkmcnt(0)"
                 : "+v"(B[0]), "+v"(B[1]), "+v"(B[2]), "+v"(B[3]), "+v"(B[4]), "+v"(B[5]), "+v"(B[6]), "+v"(B[7]) : "v"(a) : "memory");
}
DI void wait4(v4u (&x)[4]) { asm volatile("s_waitcnt vmcnt(0)" : "+v"(x[0]), "+v"(x[1]), "+v"(x[2]), "+v"(x[3])::"memory"); }
template <int A, int IMM> DI void lda4(unsigned o, const void* base) { asm volatile("global_load_dwordx4 a[%c2:%c2+3], %0, %1 offset:%3" ::"v"(o), "s"(base), "i"(A), "i"(IMM)); }
template <int A> DI void rda(unsigned& v) { asm volatile("v_accvgpr_read_b32 %0, a[%c1+0]" : "=v"(v) : "i"(A)); }
DI void poll1(unsigned a, const void* base) { asm volatile("global_load_dwordx2 a[36:36+1], %0, %1 sc1" ::"v"(a), "s"(base)); }
DI void poll2(unsigned a, unsigned b, const void* base) { asm volatile("global_load_dwordx2 a[36:36+1], %0, %2 sc1\n\tglobal_load_dwordx2 a[36+2:36+3], %1, %2 sc1" ::"v"(a), "v"(b), "s"(base)); }
DI void touch_(unsigned a, const void* base) { asm volatile("global_load_dword a[32+1], %0, %1" ::"v"(a), "s"(base)); }
template <int N> DI void wait_st(v2u& a, v4u& b) { asm volatile("s_waitcnt vmcnt(%2)" : "+v"(a), "+v"(b) : "i"(N) : "memory"); }
DI void undef3(v4u (&x)[3]) { asm volatile("" : "=v"(x[0]), "=v"(x[1]), "=v"(x[2])); }
template <int IMM> DI void gld4t(v4u& dst, unsigned voff, const void* sbase) { asm volatile("global_load_dwordx4 %0, %1, %2 offset:%3 sc1" : "+v"(dst) : "v"(voff), "s"(sbase), "i"(IMM)); }
template <int N> DI void vm_wait() { asm volatile("s_waitcnt vmcnt(%0)" ::"i"(N) : "memory"); }
DI float dot2a(unsigned a, unsigned b, float c) {
    return __builtin_amdgcn_fdot2_f32_bf16(__builtin_bit_cast(bf16x2v, a), __builtin_bit_cast(bf16x2v, b), c, false);
}
DI void disc(unsigned l0, unsigned l1, unsigned l2, f32x2& lam, f32x2& co) {
    const float dt = rbf(__builtin_amdgcn_exp2f(lo16f(l2) * 1.44269504088896f));
    const float lre = lo16f(l0), lim = lo16f(l1);
    const float e_ = __builtin_amdgcn_exp2f(dt * lre * 1.44269504088896f);
    const float rv = __builtin_amdgcn_fractf(dt * lim * 0.159154943091895f);
    lam = f32x2{e_ * __builtin_amdgcn_cosf(rv), e_ * __builtin_amdgcn_sinf(rv)};
    const float nr = lam.x - 1.0f, ni = lam.y, id = __builtin_amdgcn_rcpf(lre * lre + lim * lim);
    co = f32x2{(nr * lre + ni * lim) * id, (ni * lre - nr * lim) * id};
}
DI f32x4 unpk4(v2u u) { return f32x4{lo16f(u.x), hi16f(u.x), lo16f(u.y), hi16f(u.y)}; }
#define D2 "v_dot2_f32_bf16 "
#define CP "v_cvt_pk_bf16_f32 "
DI void d2x4(unsigned* o, unsigned a0, unsigned a1, unsigned a2, unsigned a3, unsigned b0, unsigned b1, unsigned b2, unsigned b3) {
    asm volatile(D2 "%0, %4, %8, 0\n\t" D2 "%1, %5, %9, 0\n\t" D2 "%2, %6, %10, 0\n\t" D2 "%3, %7, %11, 0"
                 : "=&v"(o[0]), "=&v"(o[1]), "=&v"(o[2]), "=&v"(o[3]) : "v"(a0), "v"(a1), "v"(a2), "v"(a3), "v"(b0), "v"(b1), "v"(b2), "v"(b3));
}
DI void cpx4(unsigned* o, unsigned a0, unsigned a1, unsigned a2, unsigned a3, unsigned b0, unsigned b1, unsigned b2, unsigned b3) {
    asm volatile(CP "%0, %4, %8\n\t" CP "%1, %5, %9\n\t" CP "%2, %6, %10\n\t" CP "%3, %7, %11"
                 : "=&v"(o[0]), "=&v"(o[1]), "=&v"(o[2]), "=&v"(o[3]) : "v"(a0), "v"(a1), "v"(a2), "v"(a3), "v"(b0), "v"(b1), "v"(b2), "v"(b3));
}
DI void cpx4i(unsigned* t, const unsigned* ka) {
    asm volatile(CP "%0, %0, %4\n\t" CP "%1, %1, %5\n\t" CP "%2, %2, %6\n\t" CP "%3, %3, %7"
                 : "+v"(t[0]), "+v"(t[1]), "+v"(t[2]), "+v"(t[3]) : "v"(ka[0]), "v"(ka[1]), "v"(ka[2]), "v"(ka[3]));
}
template <int IMM> DI void bld4(v4u& dst, unsigned voff, v4u rsrc) {
    if constexpr ((NTM >> 4) & 1) asm volatile("buffer_load_dwordx4 %0, %1, %2, 0 offen offset:%3 nt" : "=v"(dst) : "v"(voff), "s"(rsrc), "i"(IMM));
    else asm volatile("buffer_load_dwordx4 %0, %1, %2, 0 offen offset:%3" : "=v"(dst) : "v"(voff), "s"(rsrc), "i"(IMM));
}

__global__ __launch_bounds__(256, 1) __attribute__((aligned(4096))) void k_main(Params p) {
    __shared__ __attribute__((aligned(16))) char smem[LDS_BYTES];
    const rsrc_t rs = __builtin_amdgcn_make_buffer_rsrc((void*)p.ws, 0, WS_BYTES, 0x00020000);
    const rsrc_t ro = __builtin_amdgcn_make_buffer_rsrc((void*)p.out, 0, S_ * D_ * 4, 0x00020000);
    const int w0 = __builtin_amdgcn_readfirstlane(threadIdx.x >> 6), b0 = blockIdx.x;
    bool dead = false;
    asm volatile("" ::: "a255");
    float SW[5];
    f32x4 xin[5];
    v2u gt[5];
    f32x4 acc[2][5];
    f32x4 rr[2];
    float tt[2][5][4];
    Lm lam1, lamq;
    f32x2 l8, l32, l128;
    u64 late;
    v4u cA[8], cB[8];
    int sgn_, sg4, ab[4], bb, zpa, uwa, spa, tbw, tbr, tbp, tbq, cwa, cw4, spl, dst[3];
    unsigned w2o, wro, fwo, fko, xso, gso, fgo1, fho1, fho, hso, eo, es, ho[4];
    bool cv0;
    int otw, otr0, otr4;
    int mt1_;
    auto addrs = [&](auto layer_, int lane, int w, int b) __attribute__((always_inline)) {
        constexpr int layer = layer_.value;
        const int c = lane & 15, q = lane >> 4;
        TILES()
        static_assert(l_sg(0, 0) == SC + 0xA900 && l_sg(0, 1) == SC + 0xB400 && l_sg(0, 2) == SC + 0xCE00 && l_sg(0, 3) == SC + 0xDC00, "");
        sgn_ = (layer ? l_sg(1, 0) + w * INBOX : SC + (int)((0xDC00CE00B400A900ull >> (16 * w)) & 0xffff)) + (q >> 1) * 640 + (q & 1) * 8;
        const int c4 = (c >> 1) * 16 + (c & 1) * 4;
        sg4 = sgn_ + c4; sgn_ += c * 16;
        const int pl = lane < 40 ? lane : 39, tbb = l_tb(layer, 0) + w * (layer ? INBOX : 5120);
        spl = SC + pl * 16;
        eo = OFF_EG + (unsigned)(layer * 163840 + nt1 * 640 + pl * 16);
        es = eo + (unsigned)(mt1 * 10240);
        spa = l_rs(layer, 0) + (2 * (mt2 & 1) + (w >> 1)) * (layer ? INBOX : 256) + (2 * (w & 1) + (c >> 3)) * 64 + (c & 7) * 4;
        tbw = tbb + c * 64 + ((q ^ ((c >> 1) & 3)) * 16);
        tbr = tbb + (2 * (c >> 2) + (q & 1)) * 64 + (((2 * (q >> 1) + ((c >> 1) & 1)) ^ (c >> 2)) * 16) + (c & 1) * 8;
        hso = OFF_HF + (unsigned)(layer * 5242880 + ((8 * mt1 + 2 * w + (q >> 1)) * 160 + 10 * nt1 + 5 * (q & 1)) * 256 + c * 16);
        tbp = tbb + (8 * (q & 1) + (c >> 2)) * 64 + (((2 * (q >> 1) + ((c >> 1) & 1)) ^ (c >> 3)) * 16) + (c & 1) * 8;
        tbq = tbp ^ 32;
        cwa = L_CW + c * 16; cw4 = L_CW + c4;
        pin(sg4); pin(tbp); pin(tbq); pin(cw4);
        fho1 = lane ? kNone : OFF_FH + (unsigned)((((layer * 32 + 2 * mt1 + (w >> 1)) * 4 + (nt1 >> 2)) * 8 + (nt1 & 3) * 2 + (w & 1)) * 128);
        fho = OFF_FH + (unsigned)(((layer * 32 + mt2) * 4 + w) * 1024 + (PLM ? lane >> 3 : lane & 7) * 128);
        pin(sgn_); pin(spl); pin(eo); pin(es); pin(spa); pin(tbw); pin(tbr); pin(hso); pin(cwa); pin(fho1); pin(fho);
        const unsigned hb_ = OFF_HF + (unsigned)(layer * 5242880 + (4 * mt2 * 160 + 40 * w) * 256);
#pragma unroll
        for (int m = 0; m < 4; ++m) { ho[m] = hb_ + (unsigned)(((m + 1 + w) & 3) * 40960 + lane * 16); pin(ho[m]); }
#pragma unroll
        for (int m = 0; m < 3; ++m) { dst[m] = SC + ((w + 1 + m) & 3) * INBOX + m * 5120 + lane * 16; pin(dst[m]); }
        mt1_ = mt1;
        if constexpr (layer == 0) {
            const int r0 = SC + (w < 3 ? w : 0) * REGSZ, y0 = 2031 + 8 * q - c, l34 = lane < 33 ? lane : 33;
#pragma unroll
            for (int i = 0; i < 4; ++i) { ab[i] = r0 + ((y0 & 1) ? CV_O : CV_E) + ((y0 + 1) >> 1) * 4 - 448 - 512 - 1024 * i; pin(ab[i]); }
            bb = r0 + 32 * c + 16 * q;
            zpa = w < 3 ? r0 + l34 * 16 + (l34 >= 30 ? CV_E + 4096 - 480 : 0) + (l34 >= 32 ? CV_O + 4100 - 32 - CV_E - 4096 : 0) : SC + 47360 + lane * 16;
            uwa = (w < 3 ? r0 : SC + 47360 - 480) + lane * 16;
            xso = XDM ? (unsigned)((64 * mt2 + 16 * w + (lane >> 2)) * D_ + 80 * nt2 + 8 * ((lane & 3) ^ q)) * 2u : (unsigned)((64 * mt2 + 16 * w + c) * D_ + 80 * nt2 + 16 * (q & 1) + 8 * (q >> 1)) * 2u;
            gso = OFF_GT + (unsigned)((80 * nt2 + i32 + 32 * w) * 4096 + (q & 1) * 512 + (c >> 2) * 128 + (q >> 1) * 64 + (c & 3) * 16);
            if (GSM && i32 + 32 * w >= 80) gso = kNone;
            fgo1 = lane == 0 && i32 + 32 * w < 80 ? OFF_FG + (unsigned)((nt2 * 96 + i32 + 32 * w) * 128) : kNone;
            pin(bb); pin(zpa); pin(uwa); pin(xso); pin(gso); pin(fgo1);
        }
        SCHED();
    };
    unsigned tag0, tag1;
    v2u tagv;
#define FLAG(off) { if (lane == 0) st64(tagv, rs, off); }
#define ISTAG(f) (((f.x ^ tag0) | (f.y ^ tag1)) == 0u)
#define KW_REQ() { ld_ur(wro, p.ws + OFF_KVT); static_for<NW2P>(LAMBDA(i_) { gld_w2<i_.value>(w2o, p.ws + 2048 + (i_.value / 5) * 5120); }); }
#define FW_OK() { if (__builtin_expect(late != 0ull, 0)) { WAVE_WAIT({ const v2u fk_ = ld64(rs, fko); const v2u fw_ = ld64(rs, fwo); ok = ISTAG(fk_) && ISTAG(fw_); }) KW_REQ() } }
#define WF(g, s) *(const v4u*)(smem + wa[(s) & 1][(g) < 4 ? (g) & 1 : 2] + ((g) & 2) * 20480 + ((s) >> 1) * 1024)
#define TT_GAP(n) { constexpr int mb_ = (n) / 10, nb_ = ((n) % 10) / 2, e_ = ((n) & 1) * 2; \
        mul2(tt[mb_][nb_][e_], tt[mb_][nb_][e_ + 1], rm_[4 * mb_ + e_], rm_[4 * mb_ + e_ + 1], SW[nb_]); }
    {
        const int tid = threadIdx.x, lane = tid & 63, w = w0, b = b0;
        const int c = lane & 15, q = lane >> 4;
        TILES()
        STAMP(0)
        const unsigned lds0 = (unsigned)(size_t)(LAS char*)smem;
        unsigned nonce;
        unsigned cpf0;
        {
            u64 pc;
            int len, len2;
            asm volatile("s_getpc_b64 %0\nkpf_begin:\n\ts_mov_b32 %1, kpf_end-kpf_begin\n\ts_mov_b32 %2, kpf_end2-kpf_begin" : "=s"(pc), "=s"(len), "=s"(len2));
            len = len > len2 ? len : len2;
            const int o0 = (((j & 15) * 4 + w) + 64 * (lane >> 3)) * 128;
            asm volatile("global_load_dword %0, %1, %2" : "=v"(cpf0) : "v"(o0 < len ? o0 : 0), "s"(pc));
        }
        const bool cv = 32 * w + i32 < 80;
        const int chn = 80 * nt2 + i32 + 32 * (cv ? w : 0);
        const bool kw = 32 * w + 31 - i32 < 80;
        const int kp = w == 2 ? 256 + 8 * nt2 + ((i32 & 15) >> 1) : 32 * nt2 + i32, kh_ = w == 2 ? i32 & 1 : w & 1;
        const int kc = KVU ? kp % 10 : (80 * nt2 + (w == 2 ? 64 + (i32 & 15) : i32 + 32 * (w & 1))) % 10, kr = KVU ? 2 * (kp / 10) + kh_ : (80 * nt2 + (w == 2 ? 64 + (i32 & 15) : i32 + 32 * (w & 1))) / 10;
        const int lc = cv ? lane : 8;
        const unsigned xl = lds0 + (unsigned)(w * REGSZ);
        v4u ax[15], sa[5], cr[2], wr[4];
        unsigned uL[3], uM[3], sdq, kso;
        const int cu_ = (b < 200 ? b : 0) * 4 + w, cdb = cu_ / 20, ccg = cu_ % 20;
        {
            unsigned cpo = (unsigned)(32 * ccg + (lane & 31)) * 2u, cpm = (unsigned)(40 * nt1 + (lane < 39 ? lane : 39)) * 2u;
            unsigned ko = (unsigned)(32 * kr + 4 * (lane >> 3)) * 1280u + (unsigned)(64 * kc + 8 * (lane & 7)) * 2u, kh = ko - 3840u;
            pin(cpo); pin(cpm); pin(ko); pin(kh);
            SCHED();
            gldu16(uL[0], cpo, p.Lre); gldu16(uL[1], cpo, p.Lim); gldu16(uL[2], cpo, p.logdt);
            gldu16(uM[0], cpm, p.Lre); gldu16(uM[1], cpm, p.Lim); gldu16(uM[2], cpm, p.logdt);
            const v4u rk = {(unsigned)(u64)p.k, (unsigned)((u64)p.k >> 32) & 0xffffu, S_ * D_ * 2u, 0x00020000u};
            ld_kv(ax, 32 * w + 31 - i32, kh, ko, rk, p.k, p.v);
            SCHED();
        }
        auto small_ = [&]() __attribute__((always_inline)) {
            kso = (unsigned)(512 * kc + lane * 4);
            dma_1<L_EAR + 2048, 0>(kso, p.shk, xl); dma_1<L_EAR + 2304, 256>(kso, p.shk, xl);
            gld1s(nonce, kso & 4u, p.ws + OFF_NONCE);
            SCHED();
        };
        if (SMP == 0) small_();
        const u16* sab = w < 2 ? p.x0 : w == 2 ? p.Bre : p.Bim;
        unsigned sao;
        {
            int r0 = w < 2 ? 128 * mt1 + 8 * nt1 + 4 * w : 40 * nt1 + mt1, r1 = w < 2 ? 1 : 16, r2 = w < 2 ? 2047 : 40 * nt1 + 32 + (mt1 >> 1);
            pins(sab); pins(r0); pins(r1); pins(r2);
            const int r = r0 + q * r1;
            sao = (unsigned)((r < r2 ? r : r2) * 1280 + c * 16);
            pin(sao);
        }
        if (SAF == 0) static_for<5>(LAMBDA(i) { gld<256 * i.value>(sa[i.value], sao, sab); });
        SCHED();
        if (w == TW && !kw) tour<0>();
        STAMP(22)
        const int row8 = lane >> 3;
        const unsigned wvo = (unsigned)(40 * nt1 + row8) * 1280u + (unsigned)(((lane & 7) ^ ((w & 1) | ((row8 >> 1) << 1))) * 16 + (w >> 1) * 128);
        const char* const wbase = (const char*)((w & 1) ? p.Bim : p.Bre);
        const unsigned wl = lds0 + (unsigned)(((w & 1) * 10 + (w >> 1)) * 1024);
        const unsigned xvo0 = (unsigned)(8 * (row8 >> 1) + 2 * (row8 & 1)) * 1280u + (unsigned)(((lane & 7) ^ ((row8 >> 1) << 1)) * 16);
        const unsigned xvo1 = (xvo0 ^ 16u) + 1280u;
        const char* const xbase = (const char*)p.x0 + (size_t)(128 * mt1 + 32 * w) * 1280;
        unsigned xv[4] = {xvo0, xvo1, xvo0 + 5120u, xvo1 + 5120u}, wv[5];
#pragma unroll
        for (int i = 0; i < 5; ++i) { wv[i] = wvo + i * 10240u; pin(wv[i]); if (i < 4) pin(xv[i]); }
#define DMA_M0(it) SETM0<dma_loff(it) - dma_goff(it)>((it) < 40 ? xl : wl)
#define DMA_LD1(it, nx_) GLD1<dma_goff(it), dma_loff(nx_) - dma_goff(nx_)>((it) < 40 ? xv[(it) & 3] : wv[(it) < 40 ? 0 : ((it) - 100) % 5], (it) < 40 ? xbase : wbase, (nx_) < 40 ? xl : wl)
#define DMA_LD(it) GLD0<dma_goff(it)>((it) < 40 ? xv[(it) & 3] : wv[(it) < 40 ? 0 : ((it) - 100) % 5], (it) < 40 ? xbase : wbase)
        const int swz = (c & 1) | ((c >> 2) << 1);
        int wa[2][3];
#pragma unroll
        for (int i = 0; i < 2; ++i) {
            wa[i][0] = (c >> 3) * 20480 + (c & 7) * 128 + (((4 * i + q) ^ (c & 6)) * 16); wa[i][1] = (wa[i][0] ^ 16) + 10240;
            wa[i][2] = 81920 + (c & 1) * 10240 + (c >> 1) * 128 + (((4 * i + q) ^ ((c & 1) | ((c >> 2) << 1))) * 16);
            pin(wa[i][0]); pin(wa[i][1]); pin(wa[i][2]);
        }
        int xlane0 = L_XR + w * REGSZ + (c & 1) * 1024 + (c >> 1) * 128 + ((q ^ swz) * 16);
        int xlane1 = L_XR + w * REGSZ + (c & 1) * 1024 + (c >> 1) * 128 + (((4 + q) ^ swz) * 16);
        unsigned cvo = (unsigned)((16 * cdb + (lane >> 2)) * D_ + 32 * ccg + 8 * ((lane & 3) ^ q)) * 2u, for0, for1;
        int ear = L_EAR + w * REGSZ + c * 64 + (q ^ (c >> 2)) * 16, eas = L_EAR + 2048 + w * REGSZ + (lane & 7) * 64;
        pin(ear); pin(eas);
        {
            const int G = 4 * ccg + q, g5 = G % 5, ch = 2 * (G - g5) + 5 * (g5 & 1) + (g5 & 6), ci = ch + (g5 < 4 ? 1 : 5);
            const unsigned fo = OFF_W2F + (unsigned)(((cdb / 5) * 200 + cdb % 5) * 1024 + c * 16);
            for0 = fo + (unsigned)((ch >> 2) * 5120 + (ch & 3) * 256); for1 = fo + (unsigned)((ci >> 2) * 5120 + (ci & 3) * 256);
        }
        unsigned sto = OFF_ST + (unsigned)((mt1 * 16 + 4 * w + q) * 256 + c * 8), swo = OFF_ST + (unsigned)((PKF & 4) ? ((lane >> 2) * 16 + nt1) * 256 + 128 + (lane & 3) * 16 : (c * 16 + nt1) * 256 + 128 + q * 16);
        unsigned kvo = OFF_KVT + (unsigned)((64 * kc + 8 * (lane >> 3) + ((lane >> 2) & 1)) * 4096 + (32 * kr + 8 * (lane & 3)) * 2);
        int kbw = L_KB + w * 1024 + (lane & 7) * 128 + (((lane >> 3) * 8) ^ ((lane & 7) * 16));
        int kbr = L_KB + w * 1024 + (lane >> 3) * 128 + (((lane & 7) * 16) ^ ((lane >> 3) * 16));
        const unsigned dum = (OOB & 1) ? kNone : OFF_DUM + (unsigned)((b * 4 + w) * 128 + (lane & 7) * 16);
        unsigned fko1 = OFF_FK + (unsigned)((PKF & 1) ? (64 * kc + kr) * 32 : (10 * kr + kc) * 128);
        if (!kw) kvo = dum;
        if (b >= 200) { for0 = dum; for1 = dum; cvo = (unsigned)((b * 4 + w) * 128); }
        wro = (unsigned)(chn * 4096 + lc * 16);
        cv0 = cv;
        pin(kvo); pin(kbw); pin(kbr); pin(fko1);
        pin(xlane0); pin(xlane1); pin(cvo); pin(for0); pin(for1); pin(sto); pin(swo); pin(wro);
        SCHED();
        auto pre = LAMBDA(i_) {
            constexpr int it = p1_dma(i_.value);
            SCHED();
            DMA_M0(it);
            asm volatile("s_nop 0");
            DMA_LD(it);
            SCHED();
        };
        fko = OFF_FK + (unsigned)((PKF & 1) ? ((chn & ~63) + (cv ? lane : 0)) * 32 : ((cv ? lane : 0) * 10 + (chn >> 6)) * 128);
        { const int e_ = (lane & 7) < w ? w : (lane & 7) > w + 4 ? w + 4 : (lane & 7);
          fwo = OFF_FW + (unsigned)((PKF & 2) ? ((5 * nt2 + (lane < 40 ? lane >> 3 : 4)) * 20 + 4 * w + e_) * 32 : ((5 * nt2 + (lane < 25 ? lane / 5 : 0)) * 20 + 5 * w + (lane < 25 ? lane % 5 : 0)) * 128); }
        w2o = OFF_W2F + (unsigned)((nt2 * 40 + 10 * w) * 5120 + lane * 16);
        int ime = SC + (w < 3 ? w * REGSZ + CV_E : 43264) + 1008 - 16 * lane, imo = SC + (w < 3 ? w * REGSZ + CV_O + 4 : 47360) + 1008 - 16 * lane, im3 = w < 3 ? ime + 2048 : imo;
        int tca = L_TAB + c * 136, tcb = tca + 8 + 32 * w + 8 * q, tcc = L_TAB + (32 + (c >> 1)) * 136, tcd = tcc + 8 + 32 * w + 8 * q, tcl = L_TAB + (lane < 40 ? lane : 39) * 136, coa = L_CO + w * REGSZ + q * 64, rsa = l_rs(0, 0) + w * 256 + lane * 4;
        unsigned sgn = (c & 1) ? 0u : 0x80000000u;
        fko1 = kw && lane < 32 ? fko1 : OFF_FW + (unsigned)((b * 4 + w) * ((PKF & 2) ? 32 : 128));
        pin(fko); pin(fwo); pin(w2o); pin(fko1); pin(ime); pin(imo); pin(im3); pin(tca); pin(tcb); pin(tcc); pin(tcd); pin(tcl); pin(coa); pin(rsa); pin(sgn);
        SCHED();
        auto cw_ = [&]() __attribute__((always_inline)) {
            dma_n<L_EAR>(cvo, p.Cre, xl); dma_n<L_EAR + 1024>(cvo, p.Cim, xl);
            dma_1<L_EAR + 2560, 0>((unsigned)(160 * nt2 + (lane < 39 ? lane : 39) * 4), p.Dskip, xl);
            ld_wr(wr, 32 * w + i32, wro, p.ssmk);
            gldu16(sdq, (unsigned)chn * 2u, p.ssmD);
        };
        if (SMP == 1) small_();
        static_assert(SMP == 0 || SAF > 0, "");
        if (SAF == 1) static_for<5>(LAMBDA(i) { gld<256 * i.value>(sa[i.value], sao, sab); });
        if (CWP == 0) cw_();
        STAMP(33)
        asm volatile("s_waitcnt vmcnt(%5)\n\ts_cmp_ge_u32 %4, 80\n\ts_cbranch_scc0 1f\n\ts_waitcnt vmcnt(%6)\n1:" : "+v"(uL[0]), "+v"(uL[1]), "+v"(uL[2]), "+v"(cpf0) : "s"(32 * w + 31 - i32), "i"(14 + (SAF < 2 ? 5 : 0) + (CWP ? 0 : 8)), "i"(3 + (SAF < 2 ? 5 : 0) + (CWP ? 0 : 8)) : "memory", "scc");
        pin(uM[0]); pin(uM[1]); pin(uM[2]);
        {
            f32x2 lam, co;
            disc(uL[0], uL[1], uL[2], lam, co);
            *(f32x2*)(smem + L_CO + w * REGSZ + (lane & 31) * 8) = 2.0f * co;
        }
        SCHED();
        STAMP(35)
        if (SAF == 2) static_for<5>(LAMBDA(i) { gld<256 * i.value>(sa[i.value], sao, sab); });
        if (CWP == 1) cw_();
        if (APOS == 0) addrs(std::integral_constant<int, 0>{}, lane, w, b);
        if (SAF == 3) static_for<5>(LAMBDA(i) { gld<256 * i.value>(sa[i.value], sao, sab); });
        if (CWP == 2) cw_();
        v4u kvt[4];
        unsigned ka[8], kt[8], se[8], vs[8], ko4[4], one2 = 0x3F803F80u;
        pin(one2);
        auto kvop = LAMBDA(n_) {
            constexpr int n = n_.value, h = n ? (n - 1) / 29 : -1, u = n ? (n - 1) % 29 : 26;
            if constexpr (u < 2) d2x4(ka + 4 * u, ax[0][h], ax[1][h], ax[2][h], ax[3][h], se[u], se[u], se[u], se[u]);
            else if constexpr (u == 8) { const v4u sk = ax[11 + h]; se[4] = sk[0] >> 16; se[5] = sk[2] & 0xffff0000u; se[6] = sk[0] & 0xffffu; se[7] = sk[2] << 16; pin(se[4]); pin(se[5]); pin(se[6]); pin(se[7]); }
            else if constexpr (u < 21) {
                constexpr int v = u < 8 ? u - 2 : u - 3, tp = 1 + v / 6, st = v % 6, e = st & 1;
                if constexpr (st < 2) d2x4(kt + 4 * e, ax[tp][h], ax[tp + 1][h], ax[tp + 2][h], ax[tp + 3][h], se[2 * tp + e], se[2 * tp + e], se[2 * tp + e], se[2 * tp + e]);
                else if constexpr (st < 4) cpx4i(kt + 4 * e, ka + 4 * e);
                else d2x4(ka + 4 * e, kt[4 * e], kt[4 * e + 1], kt[4 * e + 2], kt[4 * e + 3], one2, one2, one2, one2);
            }
            else if constexpr (u == 21) { vs[0] = ax[7][h] & 0xffffu; vs[1] = ax[8][h] << 16; vs[2] = ax[9][h] & 0xffffu; vs[3] = ax[10][h] << 16; pin(vs[0]); pin(vs[1]); pin(vs[2]); pin(vs[3]); }
            else if constexpr (u == 22) { vs[4] = ax[7][h] >> 16; vs[5] = ax[8][h] & 0xffff0000u; vs[6] = ax[9][h] >> 16; vs[7] = ax[10][h] & 0xffff0000u; pin(vs[4]); pin(vs[5]); pin(vs[6]); pin(vs[7]); }
            else if constexpr (u == 23) cpx4(ko4, ka[0], ka[2], ka[4], ka[6], ka[1], ka[3], ka[5], ka[7]);
            else if constexpr (u < 26) { constexpr int e = u - 24; d2x4(kt + 4 * e, ko4[2 * e], ko4[2 * e], ko4[2 * e + 1], ko4[2 * e + 1], vs[4 * e], vs[4 * e + 1], vs[4 * e + 2], vs[4 * e + 3]); }
            else if constexpr (u == 26) {
                if constexpr (h < 3) { const v4u sk = ax[12 + h]; se[0] = sk[1] >> 16; se[1] = sk[3] & 0xffff0000u; se[2] = sk[1] & 0xffffu; se[3] = sk[3] << 16; pin(se[0]); pin(se[1]); pin(se[2]); pin(se[3]); }
                else asm volatile("s_nop 2");
            }
            else if constexpr (u == 27) {
                cpx4(ko4, kt[0], kt[2], kt[4], kt[6], kt[1], kt[3], kt[5], kt[7]);
                *(v2u*)(smem + kbw) = v2u{ko4[0], ko4[1]};
                *(v2u*)(smem + (kbw ^ 64)) = v2u{ko4[2], ko4[3]};
            }
            else kvt[h] = *(const v4u*)(smem + kbr);
            SCHED();
        };
        auto deal = [&](auto a_, auto b_, auto n_, auto i_) __attribute__((always_inline)) {
            constexpr int a = a_.value, m = b_.value - a, lo = m * i_.value / n_.value, hi = m * (i_.value + 1) / n_.value;
            static_for<hi - lo>(LAMBDA(e_) { pre(std::integral_constant<int, a + lo + e_.value>{}); });
        };
#define IC(n) std::integral_constant<int, (n)>{}
        static_for<kQ0>(pre);
        asm volatile("s_waitcnt vmcnt(%11)" : "+v"(ax[0]), "+v"(ax[1]), "+v"(ax[2]), "+v"(ax[3]), "+v"(ax[4]), "+v"(ax[5]), "+v"(ax[6]), "+v"(ax[7]), "+v"(ax[8]), "+v"(ax[9]),
                     "+v"(ax[10]) : "i"(13 + kQ0) : "memory");
#pragma unroll
        for (int i = 0; i < 4; ++i) ax[11 + i] = *(const v4u*)(smem + eas + i * 16);
        pin(nonce);
        STAMP(30)
        unsigned tch = 0u;
        auto kvs = LAMBDA(n_) {
            site<1000 + n_.value>(); SCHED();
            kvop(n_);
            if constexpr (n_.value >= KD0) deal(IC(kQ0), IC(kQ0 + kQ1), IC(117 - KD0), IC(n_.value - KD0));
        };
        static_assert((KST - 1) % 29 == 0, "");
        if (kw) static_for<KST>(kvs);
        else {
            if (w == 3) {
                f32x2 lam, co, lp[16];
                disc(uM[0], uM[1], uM[2], lam, co);
                lp[0] = cmul(lam, lam); lp[0] = cmul(lp[0], lp[0]); lp[1] = cmul(lp[0], lp[0]);
                lp[0] = f32x2{1.f, 0.f};
                lp[2] = cmul(lp[1], lp[1]); lp[3] = cmul(lp[2], lp[1]); lp[4] = cmul(lp[2], lp[2]);
                const f32x2 z2 = {0.f, 0.f};
                f32x2 t4[4];
                cfma4(t4, lp[4], lp[1], lp[2], lp[3], lp[4], z2, z2, z2, z2);
                lp[5] = t4[0]; lp[6] = t4[1]; lp[7] = t4[2]; lp[8] = t4[3];
                cfma4(t4, lp[8], lp[1], lp[2], lp[3], lp[4], z2, z2, z2, z2);
                lp[9] = t4[0]; lp[10] = t4[1]; lp[11] = t4[2]; lp[12] = t4[3];
                cfma4(t4, lp[8], lp[5], lp[6], lp[7], lp[7], z2, z2, z2, z2);
                lp[13] = t4[0]; lp[14] = t4[1]; lp[15] = t4[2];
                f32x2* tl = (f32x2*)(smem + tcl);
                tl[0] = lam;
#pragma unroll
                for (int i = 0; i < 16; ++i) tl[1 + i] = lp[i];
                SCHED();
            }
            static_for<kQ1a>(LAMBDA(i_) { pre(IC(kQ0 + i_.value)); });
            undef4(kvt);
            asm volatile("" : "=v"(se[0]), "=v"(se[1]), "=v"(se[2]), "=v"(se[3]));
        }
        tag0 = __builtin_amdgcn_readlane(nonce, 0) + 1u; tag1 = __builtin_amdgcn_readlane(nonce + (tag0 == 0u ? 1u : 0u), 1);
        tagv = v2u{tag0, tag1}; pin(tagv);
        {
            asm volatile("s_waitcnt vmcnt(%5)" : "+v"(sa[0]), "+v"(sa[1]), "+v"(sa[2]), "+v"(sa[3]), "+v"(sa[4]) : "i"(8 + kQ0 + kQ1a) : "memory");
            float s1[2] = {0.f, 0.f}, s2[2] = {0.f, 0.f};
#pragma unroll
            for (int i = 0; i < 5; ++i)
#pragma unroll
                for (int e = 0; e < 4; ++e) { s1[e & 1] = dot2a(sa[i][e], one2, s1[e & 1]); s2[e & 1] = dot2a(sa[i][e], sa[i][e], s2[e & 1]); }
            const float a1 = rowsum16(s1[0] + s1[1]), a2 = rowsum16(s2[0] + s2[1]);
            const float mean = a1 * (1.0f / D_);
            const float var = fmaxf(a2 * (1.0f / D_) - mean * mean, 0.f);
            const float ri = __builtin_amdgcn_rcpf(rbf(__builtin_amdgcn_sqrtf(rbf(rbf(var) + 1e-5f))));
            v2u sv = {__float_as_uint(w >= 2 ? a1 : c ? -ri * rbf(mean) : ri), tag0};
            int o0 = w < 2 ? (mt1 * 16 + nt1) * 256 + 32 * w : ((mt1 >> 2) * 16 + nt1) * 256 + 112 + (mt1 & 3) * 16 + 8 * w, o1 = w < 2 ? 8 : 16384, o2 = w < 2 ? 64 : 0;
            pins(o0); pins(o1); pins(o2);
            unsigned so_ = c < 2 ? OFF_ST + (unsigned)(o0 + q * o1 + c * o2) : kNone;
            pin(sv); pin(so_);
            st64(sv, rs, so_);
            SCHED();
        }
        bool kw2 = kw;
        if (kw2) {
            static_for<117 - KST>(LAMBDA(n_) { kvs(IC(KST + n_.value)); });
        } else {
            if (w == 3) {
            const int l = lane, l3 = l & 3;
            unsigned a = OFF_SG + (unsigned)((((((l >> 3) & 1) * 32 + mt2) * 4 + ((l >> 1) & 3)) * 8 + nt2) * 256 + (l & 1) * 128);
            { unsigned t_ = OFF_EG + (unsigned)((l >= 21 ? 163840 - 640 : 0) + (mt1 * 16 + nt1) * 640 + (l < 26 ? l - 16 : 5) * 128); pin(t_); a = l >= 16 ? t_ : a; }
            { unsigned t_ = OFF_PS + (unsigned)(((mt2 * 4 + l3) * 8 + nt2) * 128); pin(t_); a = l >= 28 ? t_ : a; }
            { unsigned t_ = OFF_FH + (unsigned)((((((l >> 2) & 1) * 32 + 2 * mt1 + (l3 >> 1)) * 4 + (nt1 >> 2)) * 8 + (nt1 & 3) * 2 + (l3 & 1)) * 128); pin(t_); a = l >= 32 ? t_ : a; }
            { unsigned t_ = OFF_FX + (unsigned)(((mt1 * 4 + 2 * (mt2 & 1) + (l3 >> 1)) * 16 + nt2 * 2 + (l3 & 1)) * 128); pin(t_); a = l >= 40 ? t_ : a; }
            { unsigned t_ = OFF_FG + (unsigned)((nt2 * 96 + i32 + 16 * ((l3 + 1) & 6)) * 128); pin(t_); a = l >= 48 ? t_ : a; }
            const unsigned big = (l & 7) < 2 ? OFF_HF + (unsigned)((l & 1) * 5242880 + (8 * mt1 * 160 + 10 * nt1) * 256) : (l & 7) == 2 ? OFF_X2F + (unsigned)(2 * mt2 * 40960 + 2560 * nt2)
                               : (l & 7) < 5 ? OFF_KVT + (unsigned)((l & 1) * 2621440 + (80 * nt2 + i32) * 4096) : OFF_W2F + (unsigned)(nt2 * 204800 + ((l & 7) - 5) * 98304);
            a = l >= 56 ? big : a;
            tch = a;
        }
            static_for<kQ1 - kQ1a>(LAMBDA(i_) { pre(IC(kQ0 + kQ1a + i_.value)); });
            if (w == TW) tour<1>();
            skip<SKIP0>();
            undef4(kvt);
        }
        SCHED();
        STAMP(34)
        if (APOS == 1) addrs(std::integral_constant<int, 0>{}, lane, w, b);
        v4u orr, oi;
        unsigned ie[4][4], io[4][4], iw;
        auto img = LAMBDA(n_) {
            constexpr int i = n_.value / 10, u = n_.value % 10;
            if constexpr (u == 0) { if constexpr (i > 0) ror1(iw, wr[i - 1].w); else { iw = 0u; pin(iw); } }
            else if constexpr (u < 5) rot1(ie[i][u - 1], wr[i][4 - u]);
            else if constexpr (u == 5) shr1b(iw, wr[i].w);
            else if constexpr (u < 9) mix1(io[i][u - 6], wr[i][9 - u], wr[i][8 - u]);
            else mix1(io[i][3], wr[i].x, iw);
        };
        auto tap0 = [&]() __attribute__((always_inline)) {
            wr[0].x = cvtpk(lo16f(wr[0].x) + (lane ? 0.f : lo16f(sdq)), hi16f(wr[0].x));
            pin(wr[0]);
            SCHED();
        };
        {
            static_assert(kQ0 + kQ1 <= 18, "");
            vm_wait<6 + kQ0 + kQ1>();
            cr[0] = *(const v4u*)(smem + ear); cr[1] = *(const v4u*)(smem + ear + 1024);
            static_for<5>(LAMBDA(i) { lds2a<22 + 2 * i.value, 2560 + 32 * i.value>(lds0 + (unsigned)(L_EAR + w * REGSZ + q * 8)); });
            asm volatile("s_waitcnt lgkmcnt(0)" : "+v"(cr[0]), "+v"(cr[1])::"memory");
            static_for<kQ2>(LAMBDA(i_) { pre(IC(kQ0 + kQ1 + i_.value)); });
            static_for<4>(LAMBDA(e_) {
                constexpr int e = e_.value;
                const f32x4 co = *(const f32x4*)(smem + coa + e * 16);
                const f32x2 o0 = lo16f(cr[0][e]) * f32x2{co.x, -co.y} - lo16f(cr[1][e]) * f32x2{co.y, co.x};
                const f32x2 o1 = hi16f(cr[0][e]) * f32x2{co.z, -co.w} - hi16f(cr[1][e]) * f32x2{co.w, co.z};
                orr[e] = cvtpk(o0.x, o1.x); oi[e] = cvtpk(o0.y, o1.y);
            });
            pin(orr); pin(oi);
            SCHED();
            static_for<NPD - kQ0 - kQ1 - kQ2>(LAMBDA(i_) { pre(IC(kQ0 + kQ1 + kQ2 + i_.value)); });
            for (int i = 0; i < 4; ++i) st128(kvt[i], rs, kvo + i * 8192);
            st128(orr, rs, for0); st128(oi, rs, for1);
        }
        SCHED();
        STAMP(23)
        if (w == 3) tour<7>();
        asm volatile("s_waitcnt vmcnt(%5) lgkmcnt(0)" : "+v"(wr[0]), "+v"(wr[1]), "+v"(wr[2]), "+v"(wr[3]), "+v"(sdq) : "i"(P1_PRE - 1 - p1_pos(3)) : "memory");
        __builtin_amdgcn_s_barrier();
        asm volatile("" ::: "memory");
        STAMP(24)
        site<10>();
        v4u wf[2][5], af[2][2][2];
#pragma unroll
        for (int nb = 0; nb < 5; ++nb) wf[0][nb] = WF(nb, 0);
#pragma unroll
        for (int i = 0; i < 4; ++i) af[0][i >> 1][i & 1] = *(const v4u*)(smem + ((i >> 1) ? xlane1 : xlane0) + (i & 1) * 2048);
        SCHED();
        tap0();
        site<11>();
        const char *kvtb = p.ws + OFF_KVT, *w2b0[(NW2P + 4) / 5];
        pins(kvtb);
#pragma unroll
        for (int i = 0; i < (NW2P + 4) / 5; ++i) { w2b0[i] = p.ws + 2048 + i * 5120; pins(w2b0[i]); }
        v2u sg, tc[2], fk, fw;
        v4u sw0;
        v2u zq[2];
        float rr_[8];
        unsigned sx[4], sz[4];
        u64 odd = 0xAAAAAAAAAAAAAAAAull;
        pins(odd);
        auto filler = LAMBDA(f_) {
            constexpr int f = f_.value;
            if constexpr (f < 0) {}
            else if constexpr (f < 40) img(IC(f));
            else if constexpr (f == 48) { lam1.r = __uint_as_float(tc[0].x); xor1(lam1.s, tc[0].y, sgn); }
            else if constexpr (f == 49) { lamq.r = __uint_as_float(tc[1].x); xor1(lamq.s, tc[1].y, sgn); }
            else if constexpr (f < 52) zero2(zq[f - 50]);
            else if constexpr (f == 52) { sx[0] = sw0.x; mov1(sx[1], sx[0]); }
            else if constexpr (f == 53) { sz[0] = sw0.z; mov1(sz[1], sz[0]); }
            else if constexpr (f < 56) bc1<f - 54>(rr_[f - 54], sg.x);
            else if constexpr (f == 56) sw16(sx[0], sx[1]);
            else if constexpr (f == 57) sw16(sz[0], sz[1]);
            else if constexpr (f < 60) mov1(sx[f - 56], sx[f - 58]);
            else if constexpr (f < 62) mov1(sz[f - 58], sz[f - 60]);
            else if constexpr (f < 64) sw32(sx[f - 62], sx[f - 60]);
            else if constexpr (f < 66) sw32(sz[f - 64], sz[f - 62]);
            else if constexpr (f < 72) {
                bc1<f - 64>(rr_[f - 64], sg.x);
                if constexpr (f == 66) { SW[0] = __uint_as_float(sx[0]); SW[1] = __uint_as_float(sz[0]); SW[2] = __uint_as_float(sx[1]); SW[3] = __uint_as_float(sz[1]); }
                if constexpr (f == 71) sel1(SW[4], sx[2], sz[2], odd);
            }
            else mulb<8 + (f - 72) % 8>(tt[(f - 72) % 8 / 4][(f - 72) / 8][(f - 72) % 4], sg.x, SW[(f - 72) / 8]);
        };
        auto kstep = LAMBDA(s_) {
            constexpr int s = s_.value, ph = s >> 2, s4 = s & 3, cu = s & 1, nx = cu ^ 1, jj = s >> 1, pp = jj & 1;
            if constexpr (s4 == 3 && ph < 4) {
                constexpr int pw = p1_pos(104 + 5 * (ph + 1)), px = p1_pos(4 * (jj + 1) + 3), pm = pw > px ? pw : px;
                constexpr int vm = p1_lim(p1_cnt(s, 0) - 1 - pm);
                asm volatile("" ::: "memory");
                __builtin_amdgcn_s_waitcnt((vm & 15) | ((vm >> 4) << 14) | 0x70);
                __builtin_amdgcn_s_barrier();
                asm volatile("" ::: "memory");
                STAMP(25 + ph)
                SCHED();
            }
            if constexpr (s == KSC) {
                static_assert(s4 == 3 && p1_pos(203) < p1_pos(4 * (jj + 1) + 3), "");
                pin(sg); pin(sw0);
                if (__builtin_expect(__builtin_amdgcn_ballot_w64(__builtin_amdgcn_bitop3_b32(__builtin_amdgcn_bitop3_b32(sg.y, sw0.y, tag0, 0x7E), sw0.w, tag0, 0xF6) != 0u) != 0ull, 0))
                    WAVE_WAIT({ sg = ld64(rs, sto); sw0 = ld128(rs, swo); ok = ((sg.y ^ tag0) | (sw0.y ^ tag0) | (sw0.w ^ tag0)) == 0u; })
                ORDER();
                STAMP(7)
            }
            if constexpr (s == kW2S && KUS > kW2S) { vm_wait<p1_cnt(s, 0) - 1 - p1_pos(208)>(); SCHED(); }
            if constexpr (s == KUS) {
                static_assert(p1_pos(206) > p1_pos(208), "");
                wait_fkn<p1_cnt(s, 0) - 1 - p1_pos(206)>(fk, fw);
                if (!USP) ld_ur(wro, kvtb);
                SCHED();
            }
            if constexpr (s4 != 3 || ph == 4) lgkm<(s == 0 ? 0 : p1_nl(s - 1, 5 - cu, 10) - 1)>();
            SCHED();
            static_for<10>(LAMBDA(g_) {
                constexpr int g = g_.value, mb = g / 5, k = g % 5, it = p1_item(s, g), nt_ = g < 9 ? p1_item(s, g + 1) : -1;
                constexpr bool d0 = it >= 0 && it < 200, d1 = nt_ >= 0 && nt_ < 200;
                if constexpr (s == 0) mf0(acc[mb][k], af[pp][cu][mb], wf[cu][k]); else mf(acc[mb][k], af[pp][cu][mb], wf[cu][k]);
                SCHED();
                if constexpr (s < 19) {
                    if constexpr (g < 5) wf[nx][g] = WF(g, s + 1);
                    if constexpr (cu == 1 && g == 5) vm_wait<p1_lim(p1_cnt(s, 5) - 1 - p1_pos(4 * (jj + 1) + 3))>();
                    if constexpr (cu == 1 && g >= 5 && g < 9)
                        af[pp ^ 1][(g - 5) >> 1][(g - 5) & 1] = *(const v4u*)(smem + (((g - 5) >> 1) ? xlane1 : xlane0) + ((jj + 1) % 3) * 4096 + ((g - 5) & 1) * 2048);
                }
                if constexpr (cu == 0 && g == XG0 - 1 && s > 0) lgkm<p1_nl(s - 1, 8, 10) + p1_nl(s, 0, XG0 - 1)>();
                constexpr int lq = p1_lds(s, g);
                if constexpr (lq >= 4 && lq < 8) (lq == 4 ? lam1.a : lq == 5 ? lam1.b : lq == 6 ? lamq.a : lamq.b) = *(const f32x2*)(smem + (lq < 6 ? tca : tcb) + (lq & 1) * 2176);
                else if constexpr (lq == 8 || lq == 9) tc[lq - 8] = *(const v2u*)(smem + (lq == 8 ? tcc : tcd));
                else if constexpr (lq == 10) { const f32x2* tp = (const f32x2*)(smem + tcl); l8 = tp[2]; l32 = tp[5]; }
                else if constexpr (lq == 11) l128 = *(const f32x2*)(smem + tcl + 72);
                else if constexpr (lq >= 30 && lq < 33) *(v4u*)(smem + (lq < 32 ? ime + 1024 * (lq - 30) : im3)) = v4u{ie[33 - lq][0], ie[33 - lq][1], ie[33 - lq][2], ie[33 - lq][3]};
                else if constexpr (lq >= 33 && lq < 37) *(v4u*)(smem + imo + 1024 * (36 - lq)) = v4u{io[lq - 33][0], io[lq - 33][1], io[lq - 33][2], io[lq - 33][3]};
                else if constexpr (lq == 37) *(v4u*)(smem + im3 + 1024) = v4u{ie[0][0], ie[0][1], ie[0][2], ie[0][3]};
                else if constexpr (lq == 38) *(unsigned*)(smem + rsa) = sg.x;
                else if constexpr (lq == 39) *(v4u*)(smem + zpa) = v4u{zq[0].x, zq[0].y, zq[1].x, zq[1].y};
                static_for<p1_cap(s, g)>(LAMBDA(e_) { filler(IC(p1_fill(s, g, e_.value))); });
                if constexpr (s == 18 && g == 9) ne2(late, tagv, fk, fw);
                if constexpr (s == 19 && g == 2) { if (__builtin_expect(late != 0ull, 0)) poll2(fko, fwo, p.ws); }
                if constexpr (it >= 0) {
                    if constexpr (d0 && d1) DMA_LD1(it, d1 ? nt_ : 0);
                    else if constexpr (d0) DMA_LD(it);
                    else if constexpr (it == 202) gld2s<0>(sg, sto, p.ws);
                    else if constexpr (it == 203) gld4s<0>(sw0, swo, p.ws);
                    else if constexpr (it == 206) gld2s<0>(fk, fko, p.ws);
                    else if constexpr (it == 208) gld2s<0>(fw, fwo, p.ws);
                    else if constexpr (it == 209) touch_(tch, p.ws);
                    else if constexpr (it >= 210 && it < 214) ld_ur1<it - 210>(wro, kvtb);
                    else if constexpr (it >= 500) gld_w2<it % 50>(w2o, w2b0[(it % 50) / 5]);
                    else { vm_wait<p1_cnt(s, g) - P1_PRE>(); st64(tagv, rs, fko1); }
                }
                if constexpr (d1 && !d0) DMA_M0(d1 ? nt_ : 0);
                site<2000 + 10 * s + g>();
                SCHED();
            });
        };
        static_for<20>(kstep);
        static_for<NFILL - p1_fill(19, 10, 0)>(LAMBDA(i_) { filler(IC(p1_fill(19, 10, 0) + i_.value)); });
        SCHED();
        rr[0] = f32x4{rr_[0], rr_[1], rr_[2], rr_[3]}; rr[1] = f32x4{rr_[4], rr_[5], rr_[6], rr_[7]};
        ACC_FENCE10(acc[0], acc[1]);
        STAMP(1)
    }

    auto layer_body = LAMBDA(layer_) {
        constexpr int layer = layer_.value;
        const int sga = sgn_;
        const bool cv = layer == 0 && cv0;
        v2u fh;
        unsigned ks[10];
        f32x4 ca[8], a2[4][5];
        unsigned gp[4][4];
        v4u gq[4];
        auto gst = LAMBDA(i_) {
            constexpr int i = i_.value >> 2, u = i_.value & 3;
            if constexpr (u < 2) cp2(gp[i][2 * u], gp[i][2 * u + 1], ca[2 * i + u]);
            else if constexpr (u == 2) swp2n(gp[i][0], gp[i][2], gp[i][1], gp[i][3]);
            else if constexpr (GSM) { gq[i] = v4u{gp[i][0], gp[i][1], gp[i][2], gp[i][3]}; pin(gq[i]); }
            else st128(v4u{gp[i][0], gp[i][1], gp[i][2], gp[i][3]}, rs, gso + i * 1024);
        };
        float rsr, rsm;
        St bv[8];
        {
            St a0, a1;
            static_for<2>(LAMBDA(m_) {
                constexpr int mb = m_.value;
#pragma unroll
                for (int i = 0; i < 4; ++i) fma2p(bv[4 * mb + i].a, acc[mb][0][i], acc[mb][1][i], rr[mb][i], tt[mb][0][i], tt[mb][1][i]);
#pragma unroll
                for (int i = 0; i < 4; ++i) fma2p(bv[4 * mb + i].b, acc[mb][2][i], acc[mb][3][i], rr[mb][i], tt[mb][2][i], tt[mb][3][i]);
                const f32x4 v = __builtin_elementwise_fma(acc[mb][4], rr[mb], f32x4{tt[mb][4][0], tt[mb][4][1], tt[mb][4][2], tt[mb][4][3]});
#pragma unroll
                for (int i = 0; i < 4; ++i) bv[4 * mb + i].c = v[i];
                SCHED();
                if constexpr (mb == 0) run3(a0, bv[0], bv + 1, lam1); else run4(a1, a0, bv + 4, lam1);
                SCHED();
            });
            *(f32x2*)(smem + sga) = a1.a; *(f32x2*)(smem + sga + 256) = a1.b; *(float*)(smem + sg4 + 512) = a1.c;
        }
        SCHED();
        const int w_ = w0;
        {
        const int w = w_, mt1 = mt1_;
        if (layer == 0 && __builtin_expect(late != 0ull, 0)) {
            unsigned f0_, f1_, f2_, f3_;
            asm volatile("s_waitcnt vmcnt(0)\n\t" AR("%0", 36, 0) AR("%1", 36, 1) AR("%2", 36, 2) AR("%3", 36, 3) : "=v"(f0_), "=v"(f1_), "=v"(f2_), "=v"(f3_)::"memory");
            late = __builtin_amdgcn_ballot_w64(((f0_ ^ tag0) | (f1_ ^ tag1) | (f2_ ^ tag0) | (f3_ ^ tag1)) != 0u);
            ORDER();
            if (late == 0ull) KW_REQ()
        }
        asm volatile("s_waitcnt lgkmcnt(0)" ::: "memory");
        __builtin_amdgcn_s_barrier();
        asm volatile("" ::: "memory");
        STAMP(layer ? 14 : 2)
#pragma unroll
        for (int s = 0; s < 10; ++s) ks[s] = (unsigned)s * 1024u;
        constexpr int NW2A = NW2P + 7 * kCR1 + kXA, NW3 = NW2P + kN3E + kN3;
        auto w2ld = LAMBDA(i_) {
            constexpr int i = i_.value;
            gld_w2<i>(w2o, p.ws + 2048 + ks[i / 5] * 5u);
        };
        auto w2a = LAMBDA(i_) {
            constexpr int k = i_.value;
            if constexpr (k < kXA) { w2ld(std::integral_constant<int, NW2P + 2 * k>{}); w2ld(std::integral_constant<int, NW2P + 2 * k + 1>{}); }
            else w2ld(std::integral_constant<int, NW2P + kXA + k>{});
        };
        unsigned xtb = (unsigned)(size_t)(LAS char*)smem + (unsigned)(l_tb(0, 0) + w * 5120);
        auto w2x = LAMBDA(i_) {
            constexpr int i = NW2A + i_.value;
            if constexpr (i < 50) w2ld(std::integral_constant<int, i>{});
            else if constexpr (XDM) { if constexpr (i < 53) dma_x<(i - 50) * 1024, (i < 52 ? (i - 50) * 64 : 96)>(xso, p.x0, xtb); }
            else if constexpr (i < 52) lda4<40 + 4 * (i - 50), (i - 50) * 64>(xso, p.x0);
            else if constexpr (i == 52) lda4<48, 128>(xso - 2u * (unsigned)(bb & 16), p.x0);
        };
        auto w2b = LAMBDA(i_) {
            constexpr int k = i_.value - 7 * kCR1;
            constexpr int ne = KW2E < 53 - NW2A ? KW2E : 53 - NW2A;
            if constexpr (k < ne) w2x(std::integral_constant<int, k>{});
            constexpr int k1 = k - kFHK - 1;
            if constexpr (k1 >= 0 && k1 < KW2T) { w2x(std::integral_constant<int, ne + 2 * k1>{}); w2x(std::integral_constant<int, ne + 2 * k1 + 1>{}); }
            else if constexpr (k1 >= 0) w2x(std::integral_constant<int, ne + KW2T + k1>{});
            static_assert(ne <= kFHK + 1 && 53 - NW2A - ne - KW2T + kFHK < 7 * (8 - kCR1) - kFHP + 1, "");
            if constexpr (k == kFHK) { vm_wait<ne>(); st64(tagv, rs, fho1); STAMP(39) }
            if constexpr (k == kFHK - 1) { STAMP(38) }
            if constexpr (k == 7 * (8 - kCR1) - kFHP) poll1(fho, p.ws);
            static_assert(NW2A + 7 * (8 - kCR1) >= 53 && NW2A <= 50 && NW3 <= NW2A, "");
        };
        auto partb = [&]() __attribute__((always_inline)) {
            const float* sp = (const float*)(smem + spa);
            rsr = sp[0]; rsm = sp[8];
            if (layer == 1) { pin(rsr); pin(rsm); }
            if (layer == 1) {
                static_for<5>(LAMBDA(n_) {
                    constexpr int nb = n_.value;
                    unsigned d0, d1;
                    rda<22 + 2 * nb>(d0); rda<23 + 2 * nb>(d1);
                    a2[3][nb] = xin[nb] + (xin[nb] * rsr + rsm) * unpk4(v2u{d0, d1}); pin(a2[3][nb]);
                });
            }
        };
        constexpr int LBW = layer ? 2 : 3;
        if (w == 3) {
            site<3 + 10 * layer>(); SCHED();
            f32x4 s4[8];
#pragma unroll
            for (int i = 0; i < 8; ++i) { s4[i] = *(const f32x4*)(smem + spl + (l_sg(layer, i & 3) - SC + (i >> 2) * 640)); SCHED(); }
            f32x2 sv[16], x[4], P[4][4];
#pragma unroll
            for (int i = 0; i < 16; ++i) { const f32x4 t = s4[(i >> 2) + 2 * (i & 2)]; sv[i] = (i & 1) ? f32x2{t.z, t.w} : f32x2{t.x, t.y}; }
            unsigned er, ei;
            {
                f32x2 u[4];
                cfma4n(u, l8, sv[0], sv[4], sv[8], sv[12], sv[1], sv[5], sv[9], sv[13]);
                agg(er, ei, x, P[0][2], u, sv, l8, l32, l128);
            }
            st128(v4u{er, tag0, ei, tag0}, rs, es);
            SCHED();
            P[0][0] = f32x2{0.f, 0.f}; P[0][1] = x[0]; P[0][3] = cfma(l32, P[0][2], x[2]);
            if (layer == 0) { FW_OK() static_for<kN3E>(LAMBDA(i_) { w2ld(std::integral_constant<int, NW2P + i_.value>{}); }); }
#pragma unroll
            for (int kk = 1; kk < 4; ++kk) cfma4(P[kk], l8, P[kk - 1][0], P[kk - 1][1], P[kk - 1][2], P[kk - 1][3], sv[kk - 1], sv[kk + 3], sv[kk + 7], sv[kk + 11]);
#pragma unroll
            for (int i = 0; i < 8; ++i) { const f32x2 a = P[2 * (i >> 2)][i & 3], b_ = P[2 * (i >> 2) + 1][i & 3]; *(f32x4*)(smem + spl + (l_sg(layer, i & 3) - SC + (i >> 2) * 640)) = f32x4{a.x, a.y, b_.x, b_.y}; }
            partb();
        }
        int w1_ = w, w3_ = w;
        if (layer == 1) { pins(w1_); pins(w3_); }
        if (layer == 1 && w1_ < LBW) { site<4 + 10 * layer>(); SCHED(); partb(); if constexpr (layer == 1) { if (w == 0) tour<6>(); } }
        if (w3_ == LBW) {
            site<5 + 10 * layer>(); SCHED();
            f32x2 wg[17], t4[4];
            unsigned ea[15], tg[15];
            v4u eg[15];
            constexpr bool early = (LBE >> layer) & 1;
            const f32x2 z2 = {0.f, 0.f};
#pragma unroll
            for (int i = 0; i < 15; ++i) { ea[i] = (unsigned)(mt1 - 1 - i) * 10240u; pins(ea[i]); ea[i] = ea[i] < kNone ? ea[i] : kNone; pins(ea[i]); }
            if constexpr (early) {
                __builtin_amdgcn_s_sleep(layer ? KLB1 : KLB0);
#pragma unroll
                for (int i = 0; i < 15; ++i) eg[i] = ld128s(rs, eo, ea[i]);
                SCHED();
            }
            wg[1] = cmul(l128, l128); wg[2] = cmul(wg[1], wg[1]); wg[3] = cmul(wg[2], wg[1]); wg[4] = cmul(wg[2], wg[2]);
#pragma unroll
            for (int i = 4; i < 16; i += 4) {
                cfma4(t4, wg[i], wg[1], wg[2], wg[3], wg[4], z2, z2, z2, z2);
#pragma unroll
                for (int e = 0; e < 4; ++e) wg[i + 1 + e] = t4[e];
            }
#pragma unroll
            for (int i = 0; i < 15; ++i) { if (i) pin(wg[i]); tg[i] = mt1 - 1 - i < 0 ? 0u : tag0; pins(tg[i]); }
            if (layer == 1) partb();
            if constexpr (layer == 0) tour<3>();
            f32x2 H0, H1;
            unsigned spins = dead ? kSpinCap : 0u;
            if constexpr (layer == 1) room<1800>();
            if constexpr (!early) __builtin_amdgcn_s_sleep(layer ? KLB1 : KLB0);
            SCHED();
            auto eval = [&](const v4u (&e)[15]) __attribute__((always_inline)) {
                unsigned x = __builtin_amdgcn_bitop3_b32(e[0].y, e[0].w, tg[0], 0x7E);
                H0 = f32x2{__uint_as_float(e[0].x), __uint_as_float(e[0].z)}; H1 = f32x2{0.f, 0.f};
#pragma unroll
                for (int i = 1; i < 15; i += 2) {
                    x |= __builtin_amdgcn_bitop3_b32(e[i].y, e[i].w, tg[i], 0x7E) | __builtin_amdgcn_bitop3_b32(e[i + 1].y, e[i + 1].w, tg[i + 1], 0x7E);
                    pin(x);
                    gacc2(H0, H1, wg[i], wg[i + 1], e[i], e[i + 1]);
                    SCHED();
                }
                *(f32x2*)(smem + spl + (L_CW - SC)) = H0 + H1;
                return __builtin_amdgcn_ballot_w64(x != 0u) != 0ull;
            };
            bool again = true;
            if constexpr (early) again = eval(eg);
            if (again) {
                bool miss;
#pragma nounroll
                do {
                    asm volatile("" ::: "memory");
                    v4u e2[15];
#pragma unroll
                    for (int i = 0; i < 15; ++i) e2[i] = ld128s(rs, eo, ea[i]);
                    SCHED();
                    miss = eval(e2);
                    if (__builtin_expect(miss, 0) && ++spins > kSpinCap) { dead = true; miss = false; }
                } while (miss);
            }
            ORDER();
            STAMP(layer ? 36 : 37)
            if (layer == 0) {
                static_for<NW3 - NW2P - kN3E>(LAMBDA(i_) { w2ld(std::integral_constant<int, NW2P + kN3E + i_.value>{}); });
            }
        }
        int w2_ = w;
        pins(w2_);
        if (layer == 0) UNDEF8(ca);
        if (layer == 0 && w2_ != LBW) {
            if (cv) {
                site<2>(); SCHED();
                partb();
                {
                    FW_OK()
                    st_ur((unsigned)(size_t)(LAS char*)smem + (unsigned)uwa);
                    CV_LOAD_A(cA, 0, 0, 0) CV_LOAD_A(cA, 0, 0, 1)
                    SCHED();
                    static_for<8>(LAMBDA(a_) { constexpr int a = a_.value; CV_LOAD_B(cB, a, 0) SCHED(); });
                    CV_LOAD_A(cA, 1, 0, 0) CV_LOAD_A(cA, 1, 0, 1)
                    SCHED();
                }
                STAMP(32)
                conv_part<0, kCR1, CPF>(ca, cA, cB, smem, ab, bb, w2a, LAMBDA(i_) {});
            } else {
                if constexpr (layer == 0) tour<5>();
                FW_OK()
                static_for<NW2A - NW2P>(LAMBDA(i_) { w2ld(std::integral_constant<int, NW2P + i_.value>{}); __builtin_amdgcn_s_sleep(1); });
                partb();
                if constexpr (layer == 0) room<3900>();
            }
        }
        asm volatile("s_waitcnt lgkmcnt(0)" ::: "memory");
        __builtin_amdgcn_s_barrier();
        asm volatile("" ::: "memory");
        STAMP(layer ? 15 : 4)
        {
            St cw, ex, hv[8];
            cw.a = *(const f32x2*)(smem + cwa); cw.b = *(const f32x2*)(smem + cwa + 256); cw.c = *(const float*)(smem + cw4 + 512);
            ex.a = *(const f32x2*)(smem + sga); ex.b = *(const f32x2*)(smem + sga + 256); ex.c = *(const float*)(smem + sg4 + 512);
            keep14(hv, cw, ex, bv, lamq, lam1);
            keep4(hv + 4, hv[3], bv + 4, lam1);
            SCHED();
            v2u ta[5], td[5];
            f32x2 hb[9];
            static_for<5 + SBL>(LAMBDA(r_) {
                constexpr int r = r_.value, rd = r < 2 ? r : r == 2 ? 4 : r - 1, ro_ = r - SBL < 2 ? r - SBL : r - SBL == 2 ? 4 : r - SBL - 1;
                if constexpr (r < 5) {
                    float e[8];
                    unsigned o[4];
#pragma unroll
                    for (int i = 0; i < 8; ++i) e[i] = rd == 4 ? hv[i].c : rd < 2 ? hv[i].a[rd & 1] : hb[i + 1][rd & 1];
                    if constexpr (r == 0) b3cv(hb, o, cw.b, ex.b, bv[0].b, bv[1].b, lamq.b, lam1.b, e);
                    else if constexpr (r < 3) b3cv(hb + 3 * r, o, hb[3 * r - 1], bv[3 * r - 1].b, bv[3 * r].b, bv[3 * r + 1].b, lam1.b, lam1.b, e);
                    else for (int i = 0; i < 4; ++i) o[i] = cvtpk(e[2 * i], e[2 * i + 1]);
                    *(v4u*)(smem + tbw + rd * 1024) = v4u{o[0], o[1], o[2], o[3]};
                    SCHED();
                    if constexpr (r < 3) { bfin(hb[3 * r + 2], hb[3 * r + 1], lam1.b); SCHED(); }
                    ta[rd] = __builtin_bit_cast(v2u, trread(smem, (rd < 4 ? tbp : tbr) + rd * 1024));
                    td[rd] = __builtin_bit_cast(v2u, trread(smem, (rd < 4 ? tbq + 256 : tbr + 512) + rd * 1024));
                    SCHED();
                }
                if constexpr (r >= SBL) { st128(v4u{ta[ro_].x, ta[ro_].y, td[ro_].x, td[ro_].y}, rs, hso + ro_ * 256); SCHED(); }
            });
        }
#pragma unroll
        for (int s = 0; s < 10; ++s) pins(ks[s]);
        if (layer == 1) { DRAIN(); st64(tagv, rs, fho1); }
        STAMP(layer ? 16 : 5)
        if (cv) {
            if constexpr (layer == 0) site<6>();
            SCHED();
            if constexpr (!CPF) {
                CV_LOAD_A(cA, 0, kCR1, 0) CV_LOAD_A(cA, 0, kCR1, 1)
                SCHED();
                static_for<8>(LAMBDA(a_) { constexpr int a = a_.value; CV_LOAD_B(cB, a, kCR1) SCHED(); });
                static_for<7>(LAMBDA(a_) { constexpr int a = a_.value + 1; CV_LOAD_A(cA, a, kCR1, 0) CV_LOAD_A(cA, a, kCR1, 1) });
                SCHED();
            }
            conv_part<kCR1, 8, false>(ca, cA, cB, smem, ab, bb, w2b, LAMBDA(g_) {
                constexpr int g = g_.value;
                if constexpr (GIN > 0 && (g == 14 || g == 16 || g == 18 || g == 20)) gst(std::integral_constant<int, (g - 14) / 2>{});
                if constexpr (GIN > 1 && (g == 26 || g == 28 || g == 30 || g == 32)) gst(std::integral_constant<int, 4 + (g - 26) / 2>{});
            });
            asm volatile("" : "+v"(ca[0]), "+v"(ca[1]), "+v"(ca[2]), "+v"(ca[3]), "+v"(ca[4]), "+v"(ca[5]), "+v"(ca[6]), "+v"(ca[7]));
            static_for<(GSM ? 0 : 16 - 4 * GIN)>(LAMBDA(i_) {
                constexpr int n = 4 * GIN + i_.value;
                if constexpr (n == 9 && GIN == 2) asm volatile("s_nop 0");
                if constexpr ((n & 3) == 2) asm volatile("s_nop 1");
                gst(std::integral_constant<int, n>{}); SCHED();
            });
        } else if (layer == 0) {
            constexpr int ne = KW2E < 53 - NW2A ? KW2E : 53 - NW2A;
            static_for<54 - NW2A>(LAMBDA(i_) {
                if constexpr (i_.value == ne) { vm_wait<ne>(); st64(tagv, rs, fho1); }
                if constexpr (i_.value < 53 - NW2A) { w2x(i_); __builtin_amdgcn_s_sleep(1); }
            });
            if (w == 3) { static_for<NW2A - NW3>(LAMBDA(i_) { w2ld(std::integral_constant<int, NW3 + i_.value>{}); __builtin_amdgcn_s_sleep(1); }); if constexpr (layer == 0) tour<2>(); }
            poll1(fho, p.ws);
            if constexpr (layer == 0) room<1000>();
            DRAIN();
            asm volatile("" : "=v"(gq[0]), "=v"(gq[1]));
        }
        if (layer == 0) { unsigned f0_, f1_; asm volatile("s_waitcnt vmcnt(%2)\n\t" AR("%0", 36, 0) AR("%1", 36, 1) : "=v"(f0_), "=v"(f1_) : "i"(GSM ? 0 : 4) : "memory"); fh = v2u{f0_, f1_}; }
        STAMP(layer ? 29 : 6)
        }

            v2u pt[4];
            if (layer == 0) {
                u64 hl;
                ne1(hl, tagv, fh);
                if (__builtin_expect(hl != 0ull, 0)) {
                    if constexpr (!GSM) { gld2t(fh, fho, p.ws); wait1(fh); st64(tagv, rs, fgo1); }
                    WAVE_RETRY(ISTAG(fh), fh = ld64(rs, fho))
                }
                ORDER();
            }
            else { room<3400>(); site<18>(); pipe_flag<KPH0>(pt, fho, p.ws, tagv, dead); ORDER(); }
            STAMP(layer ? 17 : 31)
            site<7 + 10 * layer>(); SCHED();
            constexpr int RING2 = layer ? KR2 : KR20;
            v4u ar[RING2];
#pragma unroll
            for (int i = 0; i < KI2; ++i) { ar[i] = ld128s(rs, ho[i / 10], ks[i % 10]); SCHED(); }
        if constexpr (layer == 0 && GSM) { static_assert(GIN == 2, ""); gst(IC(8)); gst(IC(9)); gst(IC(12)); gst(IC(13)); gst(IC(10)); gst(IC(14)); gst(IC(11)); gst(IC(15)); SCHED(); }
        int b_o = b0, w_o = w0;
        asm volatile("" : "+s"(b_o), "+s"(w_o));
        const int b = b_o, w = w_o;
        TILES()
        v4u gl[3];
        v2u f0;
        unsigned fgo, sgo, sgs, glo, gw, gr, srco;
        u64 glate = 0ull;
        const char *gb1 = p.ws + 32 * 4096, *gb2 = p.ws + 48 * 4096;
        {
            unsigned xs_[3][4], dq[2];
            float zx[4], zd[4], zt[4], zz[5][4];
            unsigned sA = SC + w * INBOX, s1 = OFF_SG + ((layer * 32 + mt2) * 4 + w) * 2048, s2 = s1 + nt2 * 256, sF = OFF_FG + (nt2 * 96 + 4 * w) * 128, sG = OFF_GT + (80 * nt2 + 4 * w) * 4096 + mt2 * 128,
                     sW = sA + 8192 + (w & 1) * 128, sR = SC + 8192 + w * 32, sX = l_tb(0, 0) + w * 5120, k160 = 160, kIn = INBOX;
            pins(sA); pins(s1); pins(s2);
            if (layer == 0) { pins(sF); pins(sG); pins(sW); pins(sR); pins(sX); pins(k160); pins(kIn); pins(gb1); pins(gb2); }
            unsigned l16, c16, q0, q1, c2, xr0, xr1, ta_, tb_;
#define A1(n_, v_, e_) else if constexpr (n == n_) { v_ = e_; pin(v_); }
            auto adr = LAMBDA(n_) {
                constexpr int n = n_.value + (layer && n_.value >= 4 ? 12 : 0);
                if constexpr (n == 0) { l16 = (unsigned)dst[0] & 1023u; pin(l16); }
                A1(1, c16, l16 & 0xf0u) A1(2, q0, (l16 >> 8) & 1u) A1(3, q1, l16 >> 9)
                A1(4, c2, (l16 >> 6) & 3u) A1(5, xr0, (c16 << 2) + sX) A1(6, ta_, q1 ^ c2) A1(7, xr0, (q0 << 3) + xr0) A1(8, xr0, (ta_ << 4) + xr0) A1(9, xr1, xr0 ^ 32u)
                else if constexpr (n >= 10 && n < 15) { constexpr int nb = n - 10; const v2u t = *(const v2u*)(smem + ((nb & 5) ? xr1 : xr0) + (nb >> 1) * 1024); xs_[nb >> 1][2 * (nb & 1)] = t.x; xs_[nb >> 1][2 * (nb & 1) + 1] = t.y; }
                A1(16, srco, l16 + sA) A1(17, ta_, l16 & 0x300u) A1(18, sgo, s1 + l16 + ta_) A1(19, sgs, (q0 << 3) + c16) A1(20, ta_, (q1 << 30) + s2) A1(21, sgs, sgs + ta_)
                else if constexpr (layer == 1) {}
                A1(22, ta_, l16 < 624u ? l16 : 624u) A1(23, tb_, ta_ & 0x380u) A1(24, ta_, ta_ & 0x60u) A1(25, fgo, (tb_ << 4) + sF) A1(26, fgo, (ta_ << 2) + fgo)
                A1(27, glo, (q1 << 16) + sG) A1(28, ta_, l16 & 0x180u) A1(29, tb_, l16 & 0x70u) A1(30, glo, (ta_ << 5) + glo) A1(31, glo, glo + tb_)
                A1(32, ta_, l16 >> 7) A1(33, tb_, l16 & 0x30u) A1(34, gw, (tb_ << 1) + sW) A1(35, gw, __umul24(ta_, k160) + gw) A1(36, ta_, c2 & 1u) A1(37, gw, (ta_ << 4) + gw)
                A1(38, ta_, l16 >> 8) A1(39, gr, (q0 << 7) + sR) A1(40, gr, __umul24(ta_, kIn) + gr) A1(41, gr, __umul24(c2, k160) + gr) A1(42, tb_, tb_ >> 1) A1(43, gr, gr + tb_)
            };
            SCHED();
            if constexpr (STAMPS == 1) { STAMP(layer ? 50 : 40) }
            static_for<40>(LAMBDA(i_) {
                constexpr int i = i_.value, m = i / 10, s = i % 10;
                static_for<5>(LAMBDA(k_) {
                    constexpr int kk = k_.value, pv = ring_req(5 * i + kk, KI2, RING2, 0);
                    if constexpr (s == 0 && m < 3) mfa0<kk>(a2[m][kk], ar[i % RING2]); else mfa<5 * s + kk>(a2[m][kk], ar[i % RING2]);
                    SCHED();
                    if constexpr (i == 0 && kk == 0 && layer == 1) { pin(pt[0]); pin(pt[1]); pin(pt[2]); pin(pt[3]); }
                    if constexpr (g2_adr(i, kk) >= 0) adr(IC(g2_adr(i, kk)));
                    if constexpr (layer == 0 && GSM && kk == 1 && i >= GSI && i < GSI + 4) st128(gq[i - GSI], rs, gso + (i - GSI) * 1024);
                    if constexpr (layer == 0 && GSM && kk == 0 && i == GFI) { vm_wait<GFI - GSI - 4>(); st64(tagv, rs, fgo1); }
                    if constexpr (layer == 0 && i >= 1 && i < 28 && (kk & 1)) {
                        constexpr int n = 2 * (i - 1) + (kk >> 1) - 9, nb = n / 9, u = n % 9 - 1;
                        if constexpr (n < 0 && XDM) {}
                        else if constexpr (n < -3) { rda<40 + 2 * (n + 9)>(xs_[(n + 9) >> 1][(2 * (n + 9)) & 3]); rda<41 + 2 * (n + 9)>(xs_[(n + 9) >> 1][(2 * (n + 9) + 1) & 3]); }
                        else if constexpr (n < 0) swp2n(xs_[n + 3][0], xs_[n + 3][2], xs_[n + 3][1], xs_[n + 3][3]);
                        else if constexpr (u < 0) { rda<22 + 2 * nb>(dq[0]); rda<23 + 2 * nb>(dq[1]); }
                        else if constexpr (u < 2) unp2(zx[2 * u], zx[2 * u + 1], xs_[nb >> 1][2 * (nb & 1) + u]);
                        else if constexpr (u < 4) unp2(zd[2 * u - 4], zd[2 * u - 3], dq[u - 2]);
                        else if constexpr (u < 6) fma2(zt[2 * u - 8], zt[2 * u - 7], zx[2 * u - 8], zx[2 * u - 7], rsr, rsr, rsm, rsm);
                        else fma2(zz[nb][2 * u - 12], zz[nb][2 * u - 11], zt[2 * u - 12], zt[2 * u - 11], zd[2 * u - 12], zd[2 * u - 11], zx[2 * u - 12], zx[2 * u - 11]);
                    }
                    if constexpr (layer == 0 && !GSM && i == 0 && kk == 1) { vm_wait<KI2 - 1 + (ring_req(0, KI2, RING2, 0) >= 0)>(); st64(tagv, rs, fgo1); }
                    if constexpr (layer == 0 && i == kGPI && kk == 2) gld2s<0>(f0, fgo, p.ws);
                    if constexpr (layer == 0 && i == kGCI) {
                        static_assert(kGCI + KR20 > 40 && !GSM, "");
                        if constexpr (kk == 0) { __builtin_amdgcn_s_waitcnt(0x0F70); pin(f0); gld4s<0>(gl[0], glo, p.ws); }
                        if constexpr (kk == 1) { gld4s<0>(gl[1], glo, gb1); gld4s<0>(gl[2], glo, gb2); }
                        if constexpr (kk == 3) ne1(glate, tagv, f0);
                    }
                    if constexpr (pv >= 0) ar[pv % RING2] = ld128s(rs, ho[pv / 10], ks[pv % 10]);
                    if constexpr (kk == 2 && m >= 1 && s >= 1 && s < 6) *(f32x4*)(smem + dst[m - 1] + (s - 1) * 1024) = a2[m - 1][s - 1];
                    site<4000 + 1000 * layer + 5 * i + kk>();
                    if constexpr (i >= 37) a2[i - 37][kk] = *(const f32x4*)(smem + srco + (i - 37) * 5120 + kk * 1024);
                    SCHED();
                });
                if constexpr (i == 10) { asm volatile("s_waitcnt lgkmcnt(0)" ::: "memory"); __builtin_amdgcn_s_barrier(); asm volatile("" ::: "memory"); }
                if constexpr (i == 28 && layer == 0) {
#pragma unroll
                    for (int nb = 0; nb < 5; ++nb) { a2[3][nb] = f32x4{zz[nb][0], zz[nb][1], zz[nb][2], zz[nb][3]}; pin(a2[3][nb]); }
                }
                if constexpr (i == 36) {
                    asm volatile("s_waitcnt lgkmcnt(0)" ::: "memory");
                    __builtin_amdgcn_s_barrier(); asm volatile("" ::: "memory");
                }
                if constexpr (STAMPS == 1 && (i == 0 || i == 9 || i == 10 || i == 15 || i == 25 || i == 35 || i == 36)) { STAMP((layer ? 51 : 41) + (i == 0 ? 0 : i == 9 ? 1 : i == 10 ? 2 : i == 15 ? 3 : i == 25 ? 4 : i == 35 ? 5 : 6)) }
                SCHED();
            });
            STAMP(layer ? 18 : 8)
        }
#pragma unroll
        for (int nb = 0; nb < 5; ++nb) a2[0][nb] = (a2[0][nb] + a2[1][nb]) + a2[2][nb];
        asm volatile("s_nop 1" : "+v"(a2[3][0]), "+v"(a2[3][1]), "+v"(a2[3][2]), "+v"(a2[3][3]), "+v"(a2[3][4]),
                     "+v"(a2[0][0]), "+v"(a2[0][1]), "+v"(a2[0][2]), "+v"(a2[0][3]), "+v"(a2[0][4]));
        float ty;
        {
            f32x4 s1, s2;
#pragma unroll
            for (int nb = 0; nb < 5; ++nb) {
                const f32x4 y = a2[3][nb] + a2[0][nb];
                xin[nb] = y;
                if (nb == 0) { s1 = y; s2 = y * y; } else { s1 += y; s2 += y * y; }
            }
            ty = xsum2h(hsum4(s1), hsum4(s2));
            st64(v2u{__float_as_uint(ty), tag0}, rs, sgs);
        }
        SCHED();
        STAMP(layer ? 19 : 9)
        if (layer == 0 && __builtin_expect(glate != 0ull, 0)) {
            asm volatile("s_waitcnt vmcnt(0)" : "+v"(gl[0]), "+v"(gl[1]), "+v"(gl[2])::"memory");
            WAVE_WAIT({ f0 = ld64(rs, fgo); ok = ISTAG(f0); })
            gld4t<0>(gl[0], glo, p.ws); gld4t<0>(gl[1], glo, gb1); gld4t<0>(gl[2], glo, gb2);
            asm volatile("s_waitcnt vmcnt(0)" : "+v"(gl[0]), "+v"(gl[1]), "+v"(gl[2])::"memory");
        }
        if (layer == 0) {
            asm volatile("s_waitcnt vmcnt(1)" : "+v"(gl[0]), "+v"(gl[1]), "+v"(gl[2])::"memory");
            *(v4u*)(smem + gw) = gl[0]; *(v4u*)(smem + gw + 1280) = gl[1]; *(v4u*)(smem + gw + 1920) = gl[2];
            asm volatile("s_waitcnt lgkmcnt(0)" ::: "memory");
            __builtin_amdgcn_s_barrier(); asm volatile("" ::: "memory");
#pragma unroll
            for (int nb = 0; nb < 5; ++nb) gt[nb] = __builtin_bit_cast(v2u, trread(smem, gr + 640 * nb));
        } else {
            ACC_TIE5(gt);
            if constexpr (OTR) {
#pragma unroll
                for (int nb = 0; nb < 5; ++nb) *(f32x4*)(smem + otw + nb * 64) = xin[nb];
#pragma unroll
                for (int nb = 0; nb < 5; ++nb) xin[nb] = *(const f32x4*)(smem + (nb < 4 ? otr0 + (nb & 1) * 8 * 336 + (nb >> 1) * 128 : otr4));
            }
        }
        SCHED();
        if (PSP >> layer & 1) { gran_req<32, layer ? PDS : PDS0>(sgo, p.ws); SCHED(); }
        f32x4 gf[5], yg[5];
#pragma unroll
        for (int nb = 0; nb < 5; ++nb) { gf[nb] = unpk4(gt[nb]); yg[nb] = xin[nb] * gf[nb]; pin(gf[nb]); pin(yg[nb]); }
        SCHED();
        if (PSP >> layer & 1) { gran_req<40, 0>(sgo, p.ws); SCHED(); }
        int lane_o;
        asm volatile("v_mbcnt_lo_u32_b32 %0, -1, 0\n\tv_mbcnt_hi_u32_b32 %0, -1, %0" : "=v"(lane_o));
        const int lane = lane_o, c = lane & 15, q = lane >> 4;
        unsigned xs = OFF_X2F + (unsigned)((2 * mt2 + (w >> 1)) * 40960 + ((c >> 2) & 1) * 20480 + (10 * nt2 + 2 * (q & 1) + (q >> 1)) * 256 + (8 * (w & 1) + 4 * (c >> 3) + (c & 3)) * 16);
        unsigned pso = q < 2 ? OFF_PS + (unsigned)(((mt2 * 4 + w) * 8 + nt2) * 128 + c * 8 + q * 4) : kNone;
        unsigned fxo1 = OFF_FX + (unsigned)(((mt1 * 4 + 2 * (mt2 & 1) + (w >> 1)) * 16 + nt2 * 2 + (w & 1)) * 128);
        unsigned oo = OTR ? (unsigned)(((64 * mt2 + 16 * w + (lane >> 3)) * D_ + 80 * nt2) * 4 + (nt2 & 1) * 64 + (lane & 7) * 16) : (unsigned)(((64 * mt2 + 16 * w + c) * D_ + 80 * nt2 + 4 * q) * 4);
        unsigned oo1 = oo + 8 * D_ * 4, oo4 = (unsigned)(((64 * mt2 + 16 * w + (lane >> 3) + (lane & 4) * 2) * D_ + 80 * nt2) * 4 + 256 - (nt2 & 1) * 256 + (lane & 3) * 16);
        int bpi = (lane >> 3) * 4;
        u64 up = 0xF0F0F0F0F0F0F0F0ull;
        unsigned xs2 = (q & 1) ? kNone : xs;
        fxo1 = lane ? kNone : fxo1;
        if (layer == 0) { pin(xs); pin(xs2); pin(pso); pin(fxo1); } else { pin(oo); if (OTR) { pin(oo1); pin(oo4); pin(bpi); pins(up); } }
        float c_r, c_m, c_rr, c_rm, c_mm;
        if constexpr (layer == 0 && PSA) {
            f32x4 su = yg[0], sg_ = gf[0], syu = xin[0] * yg[0], suu = yg[0] * yg[0], sug = yg[0] * gf[0], sgg = gf[0] * gf[0];
#pragma unroll
            for (int nb = 1; nb < 5; ++nb) { su += yg[nb]; sg_ += gf[nb]; syu += xin[nb] * yg[nb]; suu += yg[nb] * yg[nb]; sug += yg[nb] * gf[nb]; sgg += gf[nb] * gf[nb]; }
            const float hu = hsum4(su);
            c_r = xsum2h(hu, 2.f * hsum4(syu)); c_m = xsum2h(hsum4(sg_), 2.f * hu);
            c_rr = xsum2h(0.f, hsum4(suu)); c_rm = xsum2h(0.f, 2.f * hsum4(sug)); c_mm = xsum2h(0.f, hsum4(sgg));
            pin(c_r); pin(c_m); pin(c_rr); pin(c_rm); pin(c_mm);
        }
        SCHED();
        {
            float a, d;
                        if (PSP >> layer & 1) gran_req<48, layer ? PD1 : 0>(sgo, p.ws);
            else { gran_req<32, layer ? PDS : PDS0>(sgo, p.ws); gran_req<40, PD1>(sgo, p.ws); gran_req<48, PD1>(sgo, p.ws); }
            room<(layer ? 900 : 1500)>(); site<9 + 10 * layer>();
            pipe_gran(a, d, sgo, p.ws, tag0, dead);
            STAMP(layer ? 20 : 10)
            xsum2(a, d);
            const float me = a * (1.0f / D_);
            const float ri = __builtin_amdgcn_rsqf(fmaxf(d * (1.0f / D_) - me * me, 0.f) + 1e-5f), mr = -me * ri;
            if constexpr (layer == 0 && PSA) {
                const float ps_ = ty + ri * (c_r + ri * c_rr + mr * c_rm) + mr * (c_m + mr * c_mm);
                __builtin_amdgcn_raw_buffer_store_b32(__float_as_uint(ps_), rs, pso, 0, SC1);
                SCHED();
            }
            float rv[3] = {ri, ri, ri}, mv[3] = {mr, mr, mr};
            if constexpr (layer == 1 && OTR) {
#pragma unroll
                for (int i = 0; i < 2; ++i) { rv[i] = __int_as_float(__builtin_amdgcn_ds_bpermute(bpi + 32 * i, __float_as_int(ri))); mv[i] = __int_as_float(__builtin_amdgcn_ds_bpermute(bpi + 32 * i, __float_as_int(mr))); }
                SCHED();
            }
            static_for<5>(LAMBDA(n_) {
                constexpr int nb = layer ? n_.value : (n_.value + 4) % 5;
                if constexpr (layer == 1 && OTR && nb == 4) { sel1(rv[2], __float_as_uint(rv[0]), __float_as_uint(rv[1]), up); sel1(mv[2], __float_as_uint(mv[0]), __float_as_uint(mv[1]), up); }
                xin[nb] = yg[nb] * rv[nb < 4 ? nb & 1 : 2] + (gf[nb] * mv[nb < 4 ? nb & 1 : 2] + xin[nb]);
                if constexpr (layer == 1) { __builtin_amdgcn_raw_buffer_store_b128(__builtin_bit_cast(v4u, xin[nb]), ro, !OTR ? oo + 64 * nb : nb == 4 ? oo4 : ((nb & 1) ? oo1 : oo) + (nb >> 1) * 128, 0, OUTP); if (EST) SCHED(); }
                else if constexpr (nb & 5) {
                    const f32x4 xa = xin[nb & 6], xb = xin[nb];
                    const auto r0 = __builtin_amdgcn_permlane16_swap(cvtpk(xa.x, xa.y), cvtpk(xb.x, xb.y), false, false);
                    const auto r1 = __builtin_amdgcn_permlane16_swap(cvtpk(xa.z, xa.w), cvtpk(xb.z, xb.w), false, false);
                    st128(v4u{r0[0], r1[0], r0[1], r1[1]}, rs, (nb == 4 ? xs2 : xs) + (nb >> 1) * 1024);
                    if (EST) SCHED();
                }
            });
        }
        if constexpr (layer == 0) {
            if constexpr (!PSA) {
                f32x4 s1, s2;
#pragma unroll
                for (int nb = 0; nb < 5; ++nb) { const f32x4 x = xin[nb]; if (nb == 0) { s1 = x; s2 = x * x; } else { s1 += x; s2 += x * x; } }
                __builtin_amdgcn_raw_buffer_store_b32(__float_as_uint(xsum2h(hsum4(s1), hsum4(s2))), rs, pso, 0, SC1);
            }
            SCHED();
            site<12>();
        int wa[2][3];
#pragma unroll
        for (int i = 0; i < 2; ++i) {
            wa[i][0] = (c >> 3) * 20480 + (c & 7) * 128 + (((4 * i + q) ^ (c & 6)) * 16); wa[i][1] = (wa[i][0] ^ 16) + 10240;
            wa[i][2] = 81920 + (c & 1) * 10240 + (c >> 1) * 128 + (((4 * i + q) ^ ((c & 1) | ((c >> 2) << 1))) * 16);
            pin(wa[i][0]); pin(wa[i][1]); pin(wa[i][2]);
        }
            unsigned fxo = OFF_FX + (unsigned)((mt1 * 4 + w) * 2048 + (PLM ? lane >> 2 : c) * 128);
            unsigned xo = OFF_X2F + (unsigned)((4 * mt1 + w) * 40960 + lane * 16), xo1 = xo + 20480u;
            const int row = 8 * q + (c & 7);
            unsigned po = OFF_PS + (unsigned)((((2 * mt1 + (w >> 1)) * 4 + 2 * (w & 1) + (row >> 4)) * 8 + 4 * (c >> 3)) * 128 + (row & 15) * 8);
            int rsw = l_rs(1, 0) + w * INBOX + lane * 4;
            pin(rsw); pin(fxo); pin(xo); pin(xo1); pin(po);
            v4u wf[2][5];
#pragma unroll
            for (int nb = 0; nb < 5; ++nb) wf[0][nb] = WF(nb, 0);
            SCHED();
            DRAIN();
            st64(tagv, rs, fxo1);
            if (b == 255 && w == 0) FLAG(OFF_NONCE)
            STAMP(11)
            if (w == 0) tour<4>();
            if constexpr (OTR) {
                const int sh = (nt2 & 1) * 64, r4 = (lane >> 3) + (lane & 4) * 2;
                const int a0 = c * 336 + q * 16, a1 = (lane >> 3) * 336 + sh + (lane & 7) * 16, a2_ = r4 * 336 + 256 - 4 * sh + (lane & 3) * 16;
                char* tw = smem + SC + w * INBOX + 12288;
#pragma unroll
                for (int nb = 0; nb < 5; ++nb) *(v2u*)(tw + a0 / 2 + nb * 32) = gt[nb];
#pragma unroll
                for (int nb = 0; nb < 5; ++nb) gt[nb] = *(const v2u*)(tw + (nb < 4 ? a1 / 2 + (nb & 1) * 8 * 168 + (nb >> 1) * 64 : a2_ / 2));
                otw = SC + w * INBOX + a0; otr0 = SC + w * INBOX + a1; otr4 = SC + w * INBOX + a2_;
                pin(otw); pin(otr0); pin(otr4);
            }
            addrs(std::integral_constant<int, 1>{}, lane, w, b);
            ACC_TIE5(gt);
            v2u pt[4];
            skip<SKIP5>();
            pipe_flag<KPF0>(pt, fxo, p.ws, tagv, dead);
            ORDER();
            STAMP(12)
            v4u ar[2 * RING1];
#define XF(n) ld128s(rs, (((n) & 1) ? xo1 : xo) + (((n) >> 1) & 3) * 1024, ((n) >> 3) * 4096)
#pragma unroll
            for (int n = 0; n < KI1; ++n) { ar[n] = XF(n); SCHED(); }
            v2u ps[4];
            float t1, t2, rinv, mri, rr_[8], rm_[8];
            static_for<20>(LAMBDA(s_) {
                constexpr int s = s_.value, cu = s & 1, nx = cu ^ 1;
                LGKM0() SCHED();
                static_for<10>(LAMBDA(g_) {
                    constexpr int g = g_.value, mb = g / 5, k = g % 5;
                    if constexpr (s == 0) mf0(acc[mb][k], ar[mb], wf[0][k]); else mf(acc[mb][k], ar[(2 * s + mb) % (2 * RING1)], wf[cu][k]);
                    SCHED();
                    if constexpr (s == 0 && g == 0) { pin(pt[0]); pin(pt[1]); pin(pt[2]); pin(pt[3]); }
                    if constexpr (s < 19 && g < 5) wf[nx][g] = WF(g, s + 1);
                    constexpr int rq = ring_req(10 * s + g, KI1, 2 * RING1, 4);
                    if constexpr (rq >= 100) ps[rq - 100] = ld64(rs, po + (rq - 100) * 128);
                    else if constexpr (rq >= 0) ar[rq % (2 * RING1)] = XF(rq);
                    constexpr int n = g >= 6 ? 4 * (s - KS1) + g - 6 : -1;
                    if constexpr (n == 0) { t1 = __uint_as_float(ps[0].x) + __uint_as_float(ps[1].x); pin(t1); t2 = __uint_as_float(ps[0].y) + __uint_as_float(ps[1].y); pin(t2); }
                    if constexpr (n == 1) { t1 += __uint_as_float(ps[2].x); pin(t1); t2 += __uint_as_float(ps[2].y); pin(t2); }
                    if constexpr (n == 2) { t1 += __uint_as_float(ps[3].x); pin(t1); t2 += __uint_as_float(ps[3].y); pin(t2); }
                    if constexpr (n == 3) { t1 += dpp<0x128>(t1); t2 += dpp<0x128>(t2); pin(t1); pin(t2); }
                    if constexpr (n == 4) { t1 *= (1.0f / D_); pin(t1); t2 *= (1.0f / D_); pin(t2); }
                    if constexpr (n == 5) { t2 = fmaxf(t2 - t1 * t1, 0.f); pin(t2); }
                    if constexpr (n == 6) { rinv = __builtin_amdgcn_rsqf(t2 + 1e-5f); pin(rinv); }
                    if constexpr (n == 8) { mri = -t1 * rinv; pin(mri); }
                    if constexpr (n == 9) { t1 = rinv; mrg8(t1, mri); }
                    if constexpr (n == 10) *(float*)(smem + rsw) = t1;
                    if constexpr (n >= 11 && n < 19) bc2b<n - 11>(rr_[n - 11], rm_[n - 11], rinv, mri);
                    if constexpr (n >= 19 && n < 39) TT_GAP(n - 19)
                    site<6000 + 10 * s + g>();
                    SCHED();
                });
            });
            rr[0] = f32x4{rr_[0], rr_[1], rr_[2], rr_[3]}; rr[1] = f32x4{rr_[4], rr_[5], rr_[6], rr_[7]};
            asm volatile("kpf_end2:");
            ACC_FENCE10(acc[0], acc[1]);
            STAMP(13)
        } else {
            STAMP(21)
            STAMP_DUMP()
            asm volatile("kpf_end:");
        }
    };
    layer_body(std::integral_constant<int, 0>{});
    layer_body(std::integral_constant<int, 1>{});
}

extern "C" void kernel_launch(void* const* d_in, const int* in_sizes, int n_in,
                              void* d_out, int out_size, void* d_ws, size_t ws_size,
                              hipStream_t stream) {
    Params p;
    p.x0 = (const u16*)d_in[0];
    p.Lre = (const u16*)d_in[1];
    p.Lim = (const u16*)d_in[2];
    p.Bre = (const u16*)d_in[3];
    p.Bim = (const u16*)d_in[4];
    p.Cre = (const u16*)d_in[5];
    p.Cim = (const u16*)d_in[6];
    p.logdt = (const u16*)d_in[7];
    p.Dskip = (const u16*)d_in[8];
    p.k = (const u16*)d_in[10];
    p.v = (const u16*)d_in[11];
    p.shk = (const u16*)d_in[12];
    p.ssmk = (const u16*)d_in[13];
    p.ssmD = (const u16*)d_in[14];
    p.ws = (char*)d_ws;
    p.out = (float*)d_out;
    k_main<<<256, 256, 0, stream>>>(p);
}
```

```cpp
#define PADS 9000,1004,1059,1095,2096,9001,9002,4072,9,9003,6,9004,9005,6105,14,15,9006,18,19,9007,21,2,9008
#include <hip/hip_runtime.h>
#include <stdint.h>
#include <utility>

#define STAMPS 0

typedef unsigned short u16;
typedef unsigned long long u64;
typedef unsigned v4u __attribute__((ext_vector_type(4)));
typedef unsigned v2u __attribute__((ext_vector_type(2)));
typedef float f32x4 __attribute__((ext_vector_type(4)));
typedef float f32x2 __attribute__((ext_vector_type(2)));
typedef short s16x4 __attribute__((ext_vector_type(4)));
typedef __bf16 bf16x2v __attribute__((ext_vector_type(2)));
#define LAS __attribute__((address_space(3)))
#define DI __device__ __forceinline__

constexpr int S_ = 2048, D_ = 640;
#define KR1 5
#define KR2 10
constexpr int RING1 = KR1;
#define KCR1 4
constexpr int kCR1 = KCR1;
#define KXA 0
#define KN3E 16
#define KN3 12
constexpr int kXA = KXA;
constexpr int kN3E = KN3E, kN3 = KN3;
#define KFHK 9
#define KFHP 7
constexpr int kFHK = KFHK, kFHP = KFHP;
#define KGPI 31
#define KGCI 36
#define PLM 1
#define CPF 0
#define HOIST 0
#define LBE 0
#define GSM 0
#define GSI 0
#define GFI 12
#define LBR 1
#define W3O 1
#define GIN 2
#define RESG 1
#define KI2 8
#define KI1 8
#define KS1 8
#define EST 1
#define ESF 0
#define KR20 10
constexpr int kGPI = KGPI, kGCI = KGCI;

#define KPAD 0
#define KPS 0
#define GPAD 0
constexpr int KSTR = 4096 + KPAD, GSTR = 4096 + GPAD;
constexpr unsigned OFF_NONCE = 0;
constexpr unsigned OFF_ST = 4096;
constexpr unsigned OFF_FW = OFF_ST + 65536;
constexpr unsigned OFF_FK = OFF_FW + 131072;
constexpr unsigned OFF_FH = OFF_FK + 81920;
constexpr unsigned OFF_FX = OFF_FH + 262144;
constexpr unsigned OFF_FG = OFF_FX + 131072;
constexpr unsigned OFF_EG = OFF_FG + 98304;
constexpr unsigned OFF_SG = OFF_EG + 327680;
constexpr unsigned OFF_PS = OFF_SG + 524288;
constexpr unsigned OFF_KVT = OFF_PS + 131072;
constexpr unsigned OFF_GT = OFF_KVT + 640 * 4608;
constexpr unsigned OFF_W2F = OFF_GT + 640 * 4608;
constexpr unsigned OFF_X2F = OFF_W2F + 1638400;
constexpr unsigned OFF_DUM = OFF_X2F + 2621440;
constexpr unsigned OFF_HF = OFF_DUM + 163840;
constexpr unsigned WS_BYTES = OFF_HF + 2 * 5242880;
constexpr unsigned kNone = 0x40000000u;
constexpr unsigned OFF_DBG = 0x1800000;
#define I32M 1
#define KVS 1
#define KVM 1
#define KVU 4
#define TILES() const int xq = b & 7, j = b >> 3, mt1 = 2 * xq + (j >> 4), nt1 = j & 15, mt2 = 4 * xq + (j >> 3), nt2 = j & 7, i32 = I32M == 0 ? mt2 : I32M == 1 ? 16 * (mt2 & 1) + mt1 : I32M == 2 ? 31 - mt2 : 16 * ((mt2 ^ (j >> (I32M < 3 ? 0 : I32M - 3))) & 1) + mt1;

constexpr int SC = 102400;
constexpr int REGSZ = 13056;
constexpr int L_XR = SC;
constexpr int L_CO = SC + 12288;
constexpr int L_KB = SC + 51712;
constexpr int L_TAB = SC + 55808;
constexpr int L_CW = SC + 39168;
constexpr int INBOX = 15360;
constexpr int l_rs(int l, int w) { return l ? SC + w * INBOX : L_KB + w * 256; }
constexpr int l_sg(int l, int w) { return l ? SC + w * INBOX + 256 : SC + (w == 0 ? 43264 : w == 1 ? 46080 : w == 2 ? 52736 : 56320); }
constexpr int l_tb(int l, int w) { return l ? SC + w * INBOX + 2048 : SC + 40960 + w * 5120; }
constexpr int LDS_BYTES = 163840;
constexpr int CV_E = 4608;
constexpr int CV_O = CV_E + (1056 + 15) * 4;

struct Params {
    const u16 *x0, *Lre, *Lim, *Bre, *Bim, *Cre, *Cim, *logdt, *Dskip, *k, *v, *shk, *ssmk, *ssmD;
    char* ws;
    float* out;
};

#define MF "v_mfma_f32_16x16x32_bf16 "
DI void mf(f32x4& c, v4u a, v4u b) { asm(MF "%0, %1, %2, %0" : "+v"(c) : "v"(a), "v"(b)); }
DI void mf0(f32x4& c, v4u a, v4u b) { asm(MF "%0, %1, %2, 0" : "=&v"(c) : "v"(a), "v"(b)); }
constexpr int W2R = 56;
#define WA(n) "a[%c" #n ":%c" #n "+3]"
template <int R> DI void mfa(f32x4& c, v4u a) { asm(MF "%0, " WA(2) ", %1, %0" : "+v"(c) : "v"(a), "i"(W2R + 4 * R)); }
template <int R> DI void mfa0(f32x4& c, v4u a) { asm(MF "%0, " WA(2) ", %1, 0" : "=&v"(c) : "v"(a), "i"(W2R + 4 * R)); }
#define ACC_FENCE10(c, d) asm volatile("" : "+v"(c[0]), "+v"(c[1]), "+v"(c[2]), "+v"(c[3]), "+v"(c[4]), "+v"(d[0]), "+v"(d[1]), "+v"(d[2]), "+v"(d[3]), "+v"(d[4]))
#define ACC_TIE5(c) asm volatile("" : "+v"(c[0]), "+v"(c[1]), "+v"(c[2]), "+v"(c[3]), "+v"(c[4]))
#define QP " quad_perm:[1,0,3,2] row_mask:0xf bank_mask:0xf"
struct St { f32x2 a, b; float c; };
struct Lm { f32x2 a, b; float r, s; };
#define PK1 " op_sel_hi:[0,1,1]\n\t"
#define PK2 " op_sel:[1,1,0] op_sel_hi:[1,0,1] neg_lo:[1,0,0]"
#define ST_(t0, t1, t2, h0, h1, h2, b0, b1, b2, l0, l1, l2, l3)                                                                                        \
    "v_pk_fma_f32 %" #t0 ", %" #l0 ", %" #h0 ", %" #b0 PK1 "v_pk_fma_f32 %" #t1 ", %" #l1 ", %" #h1 ", %" #b1 PK1 "v_fma_f32 %" #t2 ", %" #l2 ", %" #h2 ", %" #b2 "\n\t" \
    "v_pk_fma_f32 %" #t0 ", %" #l0 ", %" #h0 ", %" #t0 PK2 "\n\tv_pk_fma_f32 %" #t1 ", %" #l1 ", %" #h1 ", %" #t1 PK2 "\n\tv_fmac_f32_dpp %" #t2 ", %" #h2 ", %" #l3 QP "\n\t"
#define SO(t) "=&v"(t.a), "=&v"(t.b), "=&v"(t.c)
#define SI(t) "v"(t.a), "v"(t.b), "v"(t.c)
#define LI(l) "v"(l.a), "v"(l.b), "v"(l.r), "v"(l.s)
DI void run3(St& t, const St& h, const St* b, const Lm& l) {
    St u;
    asm(ST_(0, 1, 2, 6, 7, 8, 9, 10, 11, 18, 19, 20, 21) ST_(3, 4, 5, 0, 1, 2, 12, 13, 14, 18, 19, 20, 21) ST_(0, 1, 2, 3, 4, 5, 15, 16, 17, 18, 19, 20, 21)
        : SO(t), SO(u) : SI(h), SI(b[0]), SI(b[1]), SI(b[2]), LI(l));
}
DI void run4(St& t, const St& h, const St* b, const Lm& l) {
    St u;
    asm(ST_(3, 4, 5, 6, 7, 8, 9, 10, 11, 21, 22, 23, 24) ST_(0, 1, 2, 3, 4, 5, 12, 13, 14, 21, 22, 23, 24) ST_(3, 4, 5, 0, 1, 2, 15, 16, 17, 21, 22, 23, 24) ST_(0, 1, 2, 3, 4, 5, 18, 19, 20, 21, 22, 23, 24)
        : SO(t), SO(u) : SI(h), SI(b[0]), SI(b[1]), SI(b[2]), SI(b[3]), LI(l));
}
#define AC_(t0, t2, h0, h2, b0, b2, l0, l2, l3)                                                                                   \
    "v_pk_fma_f32 %" #t0 ", %" #l0 ", %" #h0 ", %" #b0 PK1 "v_fma_f32 %" #t2 ", %" #l2 ", %" #h2 ", %" #b2 "\n\t"               \
    "v_pk_fma_f32 %" #t0 ", %" #l0 ", %" #h0 ", %" #t0 PK2 "\n\tv_fmac_f32_dpp %" #t2 ", %" #h2 ", %" #l3 QP "\n\t"
#define AO(t) "=&v"(t.a), "=&v"(t.c)
#define AI(t) "v"(t.a), "v"(t.c)
DI void keep14(St* t, const St& h, const St& x, const St* b, const Lm& lq, const Lm& l) {
    St u;
    asm(AC_(8, 9, 10, 11, 12, 13, 22, 23, 24) AC_(0, 1, 8, 9, 14, 15, 25, 26, 27) AC_(2, 3, 0, 1, 16, 17, 25, 26, 27) AC_(4, 5, 2, 3, 18, 19, 25, 26, 27) AC_(6, 7, 4, 5, 20, 21, 25, 26, 27)
        : AO(t[0]), AO(t[1]), AO(t[2]), AO(t[3]), AO(u) : AI(h), AI(x), AI(b[0]), AI(b[1]), AI(b[2]), AI(b[3]), "v"(lq.a), "v"(lq.r), "v"(lq.s), "v"(l.a), "v"(l.r), "v"(l.s));
}
DI void keep4(St* t, const St& h, const St* b, const Lm& l) {
    asm(AC_(0, 1, 8, 9, 10, 11, 18, 19, 20) AC_(2, 3, 0, 1, 12, 13, 18, 19, 20) AC_(4, 5, 2, 3, 14, 15, 18, 19, 20) AC_(6, 7, 4, 5, 16, 17, 18, 19, 20)
        : AO(t[0]), AO(t[1]), AO(t[2]), AO(t[3]) : AI(h), AI(b[0]), AI(b[1]), AI(b[2]), AI(b[3]), "v"(l.a), "v"(l.r), "v"(l.s));
}
DI void b3cv(f32x2* t, unsigned* o, f32x2 h, f32x2 b0, f32x2 b1, f32x2 b2, f32x2 l0, f32x2 l, const float* e) {
    asm("v_pk_fma_f32 %0, %11, %7, %8" PK1 "v_cvt_pk_bf16_f32 %3, %13, %14\n\tv_pk_fma_f32 %0, %11, %7, %0" PK2 "\n\tv_cvt_pk_bf16_f32 %4, %15, %16\n\t"
        "v_pk_fma_f32 %1, %12, %0, %9" PK1 "v_cvt_pk_bf16_f32 %5, %17, %18\n\tv_pk_fma_f32 %1, %12, %0, %1" PK2 "\n\tv_cvt_pk_bf16_f32 %6, %19, %20\n\t"
        "v_pk_fma_f32 %2, %12, %1, %10" PK1
        : "=&v"(t[0]), "=&v"(t[1]), "=&v"(t[2]), "=&v"(o[0]), "=&v"(o[1]), "=&v"(o[2]), "=&v"(o[3])
        : "v"(h), "v"(b0), "v"(b1), "v"(b2), "v"(l0), "v"(l), "v"(e[0]), "v"(e[1]), "v"(e[2]), "v"(e[3]), "v"(e[4]), "v"(e[5]), "v"(e[6]), "v"(e[7]));
}
DI void bfin(f32x2& t, f32x2 h, f32x2 l) { asm("v_pk_fma_f32 %0, %2, %1, %0" PK2 : "+v"(t) : "v"(h), "v"(l)); }
#define UNDEF8(c) asm volatile("" : "=v"(c[0]), "=v"(c[1]), "=v"(c[2]), "=v"(c[3]), "=v"(c[4]), "=v"(c[5]), "=v"(c[6]), "=v"(c[7]))
#define PIN5(t) asm volatile("s_nop 1" : "+v"(t[0]), "+v"(t[1]), "+v"(t[2]), "+v"(t[3]), "+v"(t[4]))
DI unsigned cvtpk(float lo, float hi) {
    f32x2 v = {lo, hi};
    return __builtin_bit_cast(unsigned, __builtin_convertvector(v, bf16x2v));
}
DI float lo16f(unsigned u) { return __uint_as_float(u << 16); }
DI float hi16f(unsigned u) { return __uint_as_float(u & 0xffff0000u); }
DI float rbf(float v) { return lo16f(cvtpk(v, 0.f)); }
template <int CTRL> DI float dpp(float x) {
    return __int_as_float(__builtin_amdgcn_update_dpp(0, __float_as_int(x), CTRL, 0xF, 0xF, true));
}
DI float rowsum16(float x) {
    x += dpp<0x128>(x); x += dpp<0x124>(x); x += dpp<0x122>(x); x += dpp<0x121>(x);
    return x;
}
DI float xsum32(float x) { auto r = __builtin_amdgcn_permlane32_swap(__float_as_uint(x), __float_as_uint(x), false, false); return __uint_as_float(r[0]) + __uint_as_float(r[1]); }
DI void xsum2(float& a, float& d) {
    auto r = __builtin_amdgcn_permlane16_swap(__float_as_uint(a), __float_as_uint(d), false, false);
    const float s = __uint_as_float(r[0]) + __uint_as_float(r[1]);
    const float t = xsum32(s);
    auto u = __builtin_amdgcn_permlane16_swap(__float_as_uint(t), __float_as_uint(t), false, false);
    a = __uint_as_float(u[0]); d = __uint_as_float(u[1]);
}
DI float xsum2h(float a, float d) {
    auto r = __builtin_amdgcn_permlane16_swap(__float_as_uint(a), __float_as_uint(d), false, false);
    return xsum32(__uint_as_float(r[0]) + __uint_as_float(r[1]));
}
DI float hsum4(f32x4 v) { return (v.x + v.y) + (v.z + v.w); }
typedef __amdgpu_buffer_rsrc_t rsrc_t;
constexpr int SC1 = 16;
DI v4u ld128(rsrc_t rs, unsigned off) { return __builtin_amdgcn_raw_buffer_load_b128(rs, off, 0, SC1); }
DI v4u ld128s(rsrc_t rs, unsigned off, unsigned soff) { return __builtin_amdgcn_raw_buffer_load_b128(rs, off, soff, SC1); }
DI v2u ld64(rsrc_t rs, unsigned off) { return __builtin_amdgcn_raw_buffer_load_b64(rs, off, 0, SC1); }
DI void st128(v4u v, rsrc_t rs, unsigned off) { __builtin_amdgcn_raw_buffer_store_b128(v, rs, off, 0, SC1); }
DI void st64(v2u v, rsrc_t rs, unsigned off) { __builtin_amdgcn_raw_buffer_store_b64(v, rs, off, 0, SC1); }
#define LGKM0() { __builtin_amdgcn_s_waitcnt(0xC07F); asm volatile("" ::: "memory"); }
#define DRAIN() asm volatile("s_waitcnt vmcnt(0)" ::: "memory")
#define ORDER() __builtin_amdgcn_fence(__ATOMIC_ACQUIRE, "wavefront")
#define SCHED() __builtin_amdgcn_sched_barrier(0)
constexpr unsigned kSpinCap = 1u << 17;
#define WAVE_WAIT(...)                                                      \
    {                                                                       \
        unsigned spins_ = dead ? kSpinCap : 0u;                             \
        _Pragma("nounroll") for (;;) {                                      \
            asm volatile("" ::: "memory");                                  \
            bool ok;                                                        \
            __VA_ARGS__;                                                    \
            if (__builtin_amdgcn_ballot_w64(!ok) == 0ull) break;            \
            if (++spins_ > kSpinCap) { dead = true; break; }                \
            __builtin_amdgcn_s_sleep(1);                                    \
        }                                                                   \
        ORDER();                                                            \
    }
#define KPD1 2
#define KPDS 5
#define KPDS0 5
#define KPF0 5
#define KPH0 12
#define KPF1 4
constexpr int PD1 = KPD1, PDS = KPDS, PDS0 = KPDS0;
#define PL(r) "global_load_dwordx2 " r ", %5, %6 sc1\n\t"
#define PS_(r) "s_waitcnt vmcnt(1)\n\tv_cmp_ne_u64 vcc, %7, " r "\n\ts_cbranch_vccz 2f\n\t" PL(r)
template <int D0> DI void pipe_flag(v2u (&t)[4], unsigned off, const void* base, v2u tag, bool& dead) {
    unsigned n = dead ? 0u : kSpinCap;
    asm volatile("s_sleep %8\n\t" PL("%0") "s_sleep %9\n\t" PL("%1")
                 "1:\n\t" PS_("%0") PS_("%1") "s_sub_u32 %4, %4, 1\n\ts_cbranch_scc0 1b\n2:"
                 : "=&v"(t[0]), "=&v"(t[1]), "=&v"(t[2]), "=&v"(t[3]), "+s"(n) : "v"(off), "s"(base), "v"(tag), "i"(D0), "i"(KPF1) : "memory", "vcc", "scc");
    dead = __builtin_amdgcn_readfirstlane(n) == ~0u;
}
#define WAVE_RETRY(OK, ...) { if (__builtin_expect(__builtin_amdgcn_ballot_w64(!(OK)) != 0ull, 0)) WAVE_WAIT({ __VA_ARGS__; ok = OK; }) ORDER(); }
#define STAMP(i)
#define STAMP_DUMP()

#define T1POS 27
#define TW 2
#define TW0 3
#define KLB0 0
#define KLB1 4
#define SKIP0 0
#define SKIP2 0
#define SKIP7 150
#define SKIP5 0
#define SKIP4 0
constexpr int kPads[] = {PADS};
constexpr int pad_next(int id) {
    for (unsigned i = 0; i + 1 < sizeof(kPads) / sizeof(int); ++i) if (kPads[i] == id) return kPads[i + 1];
    return -1;
}
constexpr bool pads_unique() {
    for (unsigned i = 0; i < sizeof(kPads) / sizeof(int); ++i)
        for (unsigned k = 0; k < i; ++k) if (kPads[i] == kPads[k]) return false;
    return true;
}
static_assert(pads_unique(), "");
template <int ID> DI void site() {
    if constexpr (pad_next(ID) >= 0) asm volatile("kp%c0:\n\ts_cbranch_execz kp%c1" ::"i"(ID), "i"(pad_next(ID)));
    else asm volatile("");
}
template <int T> DI void tour() {
    if constexpr (pad_next(9000 + T) >= 0 && pad_next(9000 + T) != 9001 + T) asm volatile("s_mov_b64 exec, 0\n\ts_branch kp%c0\nkp%c1:\n\ts_mov_b64 exec, -1" ::"i"(pad_next(9000 + T)), "i"(9001 + T));
}
template <int N> DI void skip() { if constexpr (N > 0) asm volatile("s_branch 1f\n\t.rept %c0\n\ts_nop 0\n\t.endr\n1:" ::"i"(N)); }
template <int N> DI void room() { asm volatile("s_branch 1f\n\t.p2align 12, , %c0\n1:" ::"i"(N)); }
template <class F, int... I> DI void static_for_impl(std::integer_sequence<int, I...>, F&& f) { (f(std::integral_constant<int, I>{}), ...); }
template <int N, class F> DI void static_for(F&& f) { static_for_impl(std::make_integer_sequence<int, N>{}, f); }
#define LAMBDA(v) [&](auto v) __attribute__((always_inline))
DI f32x2 cmul(f32x2 a, f32x2 b) {
    f32x2 t;
    asm("v_pk_mul_f32 %0, %1, %2 op_sel_hi:[0,1]\n\ts_nop 0\n\tv_pk_fma_f32 %0, %1, %2, %0" PK2 "\n\ts_nop 0" : "=&v"(t) : "v"(a), "v"(b));
    return t;
}
DI f32x2 cfma(f32x2 a, f32x2 b, f32x2 c) {
    f32x2 t;
    asm("v_pk_fma_f32 %0, %1, %2, %3 op_sel_hi:[0,1,1]\n\ts_nop 0\n\tv_pk_fma_f32 %0, %1, %2, %0" PK2 "\n\ts_nop 0" : "=&v"(t) : "v"(a), "v"(b), "v"(c));
    return t;
}
DI void cfma4(f32x2 (&t)[4], f32x2 a, f32x2 b0, f32x2 b1, f32x2 b2, f32x2 b3, f32x2 c0, f32x2 c1, f32x2 c2, f32x2 c3) {
    asm("v_pk_fma_f32 %0, %4, %5, %9 op_sel_hi:[0,1,1]\n\tv_pk_fma_f32 %1, %4, %6, %10 op_sel_hi:[0,1,1]\n\tv_pk_fma_f32 %2, %4, %7, %11 op_sel_hi:[0,1,1]\n\tv_pk_fma_f32 %3, %4, %8, %12 op_sel_hi:[0,1,1]\n\t"
        "v_pk_fma_f32 %0, %4, %5, %0" PK2 "\n\tv_pk_fma_f32 %1, %4, %6, %1" PK2 "\n\tv_pk_fma_f32 %2, %4, %7, %2" PK2 "\n\tv_pk_fma_f32 %3, %4, %8, %3" PK2 "\n\ts_nop 0"
        : "=&v"(t[0]), "=&v"(t[1]), "=&v"(t[2]), "=&v"(t[3]) : "v"(a), "v"(b0), "v"(b1), "v"(b2), "v"(b3), "v"(c0), "v"(c1), "v"(c2), "v"(c3));
}
#define C4(t0, t1, t2, t3, a, b0, b1, b2, b3, c0, c1, c2, c3)                                                                                                         \
    "v_pk_fma_f32 %" #t0 ", %" #a ", %" #b0 ", %" #c0 PK1 "v_pk_fma_f32 %" #t1 ", %" #a ", %" #b1 ", %" #c1 PK1 "v_pk_fma_f32 %" #t2 ", %" #a ", %" #b2 ", %" #c2 PK1 "v_pk_fma_f32 %" #t3 ", %" #a ", %" #b3 ", %" #c3 PK1 \
    "v_pk_fma_f32 %" #t0 ", %" #a ", %" #b0 ", %" #t0 PK2 "\n\tv_pk_fma_f32 %" #t1 ", %" #a ", %" #b1 ", %" #t1 PK2 "\n\tv_pk_fma_f32 %" #t2 ", %" #a ", %" #b2 ", %" #t2 PK2 "\n\tv_pk_fma_f32 %" #t3 ", %" #a ", %" #b3 ", %" #t3 PK2 "\n\t"
DI void cfma4n(f32x2 (&t)[4], f32x2 a, f32x2 b0, f32x2 b1, f32x2 b2, f32x2 b3, f32x2 c0, f32x2 c1, f32x2 c2, f32x2 c3) {
    asm(C4(0, 1, 2, 3, 4, 5, 6, 7, 8, 9, 10, 11, 12) : "=&v"(t[0]), "=&v"(t[1]), "=&v"(t[2]), "=&v"(t[3]) : "v"(a), "v"(b0), "v"(b1), "v"(b2), "v"(b3), "v"(c0), "v"(c1), "v"(c2), "v"(c3));
}
DI void agg(unsigned& er, unsigned& ei, f32x2 (&x)[4], f32x2& a, const f32x2 (&u)[4], const f32x2 (&s)[16], f32x2 l8, f32x2 l32, f32x2 l64) {
    f32x2 v[4], b;
    asm(C4(4, 5, 6, 7, 22, 10, 11, 12, 13, 14, 15, 16, 17) C4(0, 1, 2, 3, 22, 4, 5, 6, 7, 18, 19, 20, 21)
        "v_pk_fma_f32 %8, %23, %0, %1" PK1 "v_pk_fma_f32 %9, %23, %2, %3" PK1 "v_pk_fma_f32 %8, %23, %0, %8" PK2 "\n\tv_pk_fma_f32 %9, %23, %2, %9" PK2
        : "=&v"(x[0]), "=&v"(x[1]), "=&v"(x[2]), "=&v"(x[3]), "=&v"(v[0]), "=&v"(v[1]), "=&v"(v[2]), "=&v"(v[3]), "=&v"(a), "=&v"(b)
        : "v"(u[0]), "v"(u[1]), "v"(u[2]), "v"(u[3]), "v"(s[2]), "v"(s[6]), "v"(s[10]), "v"(s[14]), "v"(s[3]), "v"(s[7]), "v"(s[11]), "v"(s[15]), "v"(l8), "v"(l32));
    asm("s_nop 0\n\tv_fma_f32 %0, %2, %6, %4\n\tv_fma_f32 %1, %2, %7, %5\n\tv_fma_f32 %0, -%3, %7, %0\n\tv_fma_f32 %1, %3, %6, %1"
        : "=&v"(er), "=&v"(ei) : "v"(a.x), "v"(a.y), "v"(b.x), "v"(b.y), "v"(l64.x), "v"(l64.y));
}
DI void gacc2(f32x2& h0, f32x2& h1, f32x2 w0, f32x2 w1, v4u e0, v4u e1) {
    const v2u a0 = {e0.x, e0.y}, b0 = {e0.z, e0.w}, a1 = {e1.x, e1.y}, b1 = {e1.z, e1.w};
    asm("v_pk_fma_f32 %0, %4, %2, %0 op_sel_hi:[0,1,1]\n\tv_pk_fma_f32 %1, %6, %3, %1 op_sel_hi:[0,1,1]\n\t"
        "v_pk_fma_f32 %0, %5, %2, %0 op_sel:[0,1,0] op_sel_hi:[0,0,1] neg_lo:[1,0,0]\n\tv_pk_fma_f32 %1, %7, %3, %1 op_sel:[0,1,0] op_sel_hi:[0,0,1] neg_lo:[1,0,0]"
        : "+v"(h0), "+v"(h1) : "v"(w0), "v"(w1), "v"(a0), "v"(b0), "v"(a1), "v"(b1));
}
DI s16x4 trread(char* smem, int byteoff) {
    return __builtin_amdgcn_ds_read_tr16_b64_v4i16((LAS s16x4*)(smem + byteoff));
}

#define CV_LOAD_A(A, a, rr_, hf) { const unsigned* ap_ = (const unsigned*)(smem + ab[(a) >> 1] + (((a) & 1) ? 0 : 512) + 64 * (7 - (rr_)) + 8 * (hf)); A[a][2 * (hf)] = ap_[0]; A[a][2 * (hf) + 1] = ap_[1]; }
#define CV_LOAD_B(B, e, rr_) { B[e] = *(const v4u*)(smem + bb + 64 * (7 - (rr_)) + 512 * (e)); }
template <int R, bool PF, class F, class G>
DI void conv_round(f32x4 (&ca)[8], v4u (&A)[8], const v4u (&B)[8], v4u (&An)[8], v4u (&Bn)[8], const char* smem, const int (&ab)[4], int bb, F&& fill, G&& last) {
    static_for<36>(LAMBDA(g_) {
        constexpr int g = g_.value;
        constexpr int a = g < 8 ? 0 : g < 15 ? 1 : g < 21 ? 2 : g < 26 ? 3 : g < 30 ? 4 : g < 33 ? 5 : g < 35 ? 6 : 7;
        constexpr int e = g - (a * 8 - a * (a - 1) / 2);
        if constexpr (R == 0 && a == 0) mf0(ca[e], A[0], B[e]); else mf(ca[a + e], A[a], B[e]);
        SCHED();
        if constexpr (R == 0 && g < 16 && (g & 3) != 3) { constexpr int n = g - (g >> 2); CV_LOAD_A(A, 2 + (n >> 1), 0, n & 1) SCHED(); }
        if constexpr (PF && g < 32 && (g & 3) != 3) { if constexpr ((g & 3) == 2) CV_LOAD_B(Bn, g >> 2, R + 1) else CV_LOAD_A(An, g >> 2, R + 1, g & 1) SCHED(); }
        site<3000 + 36 * R + g>();
        if constexpr (R == 7) { last(g_); SCHED(); }
        if constexpr ((g & 3) == 3 && g < 28) { fill(std::integral_constant<int, 7 * R + g / 4>{}); SCHED(); }
    });
}
template <int R0, int R1, bool PFL, class F, class G>
DI void conv_part(f32x4 (&ca)[8], v4u (&A0)[8], v4u (&B0)[8], const char* smem, const int (&ab)[4], int bb, F&& fill, G&& last) {
    v4u A1[8], B1[8];
    static_for<R1 - R0>(LAMBDA(r_) {
        constexpr int r = r_.value;
        if constexpr (r & 1) conv_round<R0 + r, (R0 + r + 1 < R1 || PFL)>(ca, A1, B1, A0, B0, smem, ab, bb, fill, last);
        else conv_round<R0 + r, (R0 + r + 1 < R1 || PFL)>(ca, A0, B0, A1, B1, smem, ab, bb, fill, last);
    });
}
template <int IMM> DI void gld(v4u& dst, unsigned voff, const void* sbase) {
    asm volatile("global_load_dwordx4 %0, %1, %2 offset:%3" : "=v"(dst) : "v"(voff), "s"(sbase), "i"(IMM));
}
#define OOB 3
#define OUTP 16
#define NTM 31
template <int IMM, int BIT> DI void gldn(v4u& dst, unsigned voff, const void* sbase) {
    if constexpr ((NTM >> BIT) & 1) asm volatile("global_load_dwordx4 %0, %1, %2 offset:%3 nt" : "=v"(dst) : "v"(voff), "s"(sbase), "i"(IMM));
    else gld<IMM>(dst, voff, sbase);
}
DI void gldu16(unsigned& dst, unsigned voff, const void* sbase) { asm volatile("global_load_ushort %0, %1, %2" : "=v"(dst) : "v"(voff), "s"(sbase)); }
DI void gld1s(unsigned& dst, unsigned voff, const void* sbase) { asm volatile("global_load_dword %0, %1, %2 sc1" : "=v"(dst) : "v"(voff), "s"(sbase)); }
template <int IMM> DI void gld64(v4u& dst, const void* a) {
    if constexpr ((NTM >> 5) & 1) asm volatile("global_load_dwordx4 %0, %1, off offset:%2 nt" : "=v"(dst) : "v"(a), "i"(IMM));
    else asm volatile("global_load_dwordx4 %0, %1, off offset:%2" : "=v"(dst) : "v"(a), "i"(IMM));
}
template <int V> DI void SETM0(unsigned ldsbase) { asm volatile("s_add_u32 m0, %0, %1" ::"s"(ldsbase), "i"(V)); }
template <int GOFF> DI void GLD0(unsigned voff, const char* sbase) { asm volatile("global_load_lds_dwordx4 %0, %1 offset:%2" ::"v"(voff), "s"(sbase), "i"(GOFF) : "memory"); }
template <int GOFF, int V> DI void GLD1(unsigned voff, const char* sbase, unsigned ldsbase) {
    asm volatile("global_load_lds_dwordx4 %0, %1 offset:%3\n\ts_add_u32 m0, %2, %4" ::"v"(voff), "s"(sbase), "s"(ldsbase), "i"(GOFF), "i"(V) : "memory");
}
template <int L, int G> DI void dma_x(unsigned voff, const void* sbase, unsigned ldsbase) {
    asm volatile("s_add_u32 m0, %2, %4\n\ts_nop 0\n\tglobal_load_lds_dwordx4 %0, %1 offset:%3" ::"v"(voff), "s"(sbase), "s"(ldsbase), "i"(G), "i"(L - G) : "memory", "scc");
}
DI void ld_kv(v4u (&a)[15], int on  , unsigned k0, unsigned k1, unsigned k4, unsigned vo, v4u rk, const void* v, unsigned lds) {
    unsigned t;
    asm volatile("s_cmp_ge_u32 %5, 80\n\ts_cbranch_scc1 1f\n\ts_add_u32 m0, %12, %13\n\ts_movk_i32 %4, 0x2800\n\t"
                 "buffer_load_dwordx4 %6, %10, 0 offen nt lds\n\ts_add_u32 m0, m0, 0x400\n\tglobal_load_dwordx4 %0, %9, %11 nt\n\t"
                 "buffer_load_dwordx4 %7, %10, 0 offen nt lds\n\ts_add_u32 m0, m0, 0x400\n\tglobal_load_dwordx4 %1, %9, %11 offset:1280 nt\n\t"
                 "buffer_load_dwordx4 %7, %10, %4 offen nt lds\n\ts_add_u32 m0, m0, 0x400\n\ts_movk_i32 %4, 0x5000\n\tglobal_load_dwordx4 %2, %9, %11 offset:2560 nt\n\t"
                 "buffer_load_dwordx4 %7, %10, %4 offen nt lds\n\ts_add_u32 m0, m0, 0x400\n\tglobal_load_dwordx4 %3, %9, %11 offset:3840 nt\n\t"
                 "buffer_load_dwordx4 %8, %10, 0 offen nt lds\n1:"
                 : "=&v"(a[7]), "=&v"(a[8]), "=&v"(a[9]), "=&v"(a[10]), "=&s"(t)
                 : "s"(on), "v"(k0), "v"(k1), "v"(k4), "v"(vo), "s"(rk), "s"(v), "s"(lds), "i"(L_XR) : "scc", "memory");
}
DI void ld_wr(v4u (&a)[4], int on, unsigned o, const void* base) {
    asm volatile("s_cmp_ge_u32 %4, 80\n\ts_cbranch_scc0 1f\n\t.rept 4\n\tglobal_load_dword a[32+1], %5, %6\n\t.endr\n\ts_branch 2f\n1:\n\t"
                 "global_load_dwordx4 %0, %5, %6 nt\n\tglobal_load_dwordx4 %1, %5, %6 offset:1024 nt\n\tglobal_load_dwordx4 %2, %5, %6 offset:2048 nt\n\tglobal_load_dwordx4 %3, %5, %6 offset:3072 nt\n2:"
                 : "=&v"(a[0]), "=&v"(a[1]), "=&v"(a[2]), "=&v"(a[3]) : "s"(on), "v"(o), "s"(base) : "scc");
}
template <int L, int G> DI void dma_1(unsigned voff, const void* sbase, unsigned ldsbase) {
    asm volatile("s_add_u32 m0, %2, %4\n\ts_nop 0\n\tglobal_load_lds_dword %0, %1 offset:%3" ::"v"(voff), "s"(sbase), "s"(ldsbase), "i"(G), "i"(L - G) : "memory", "scc");
}
template <int L> DI void dma_n(unsigned voff, const void* sbase, unsigned ldsbase) {
    asm volatile("s_add_u32 m0, %2, %3\n\ts_nop 0\n\tglobal_load_lds_dwordx4 %0, %1 nt" ::"v"(voff), "s"(sbase), "s"(ldsbase), "i"(L) : "memory", "scc");
}
template <int A, int O> DI void lds2a(unsigned a) { asm volatile("ds_read_b64 a[%c1:%c1+1], %0 offset:%2" ::"v"(a), "i"(A), "i"(O) : "memory"); }
constexpr int L_EAR = L_XR + 8192;
constexpr int dma_goff(int it) { return it < 40 ? (it >> 2) * 128 : ((it - 100) / 5) * 256; }
constexpr int dma_loff(int it) { return it < 40 ? L_XR + ((it >> 2) % 3) * 4096 + (it & 3) * 1024 : ((it - 100) % 5) * 20480 + ((it - 100) / 5) * 2048; }
#define KFS 4
#define APOS 0
#define PSA 0
#define CWP 2
#define SAF 2
#define KD0 30
#define KST 59
#define KSL 5
#define KSC 11
#define WSP 1
#define XG0 5
#define XGS 1
#define WOD 1
#define USP 1
#define KSG 0
#define PKF 7
#define XDM 1
#define PSP 3
#define SBL 1
#define SMP 1
#define KW2E 0
#define KW2T 2
#define OTR 1
#define KQ0 0
#define KQ1 18
#define KQ2 4
#define KQ3 0
#define NPD 27
#define IMP 0
#define KSP 1
constexpr int kQ0 = KQ0, kQ1 = KQ1, kQ2 = KQ2, kQ3 = KQ3;
#define KW2S 16
#define KW2M 0x42
#define KFKS 13
#define KFKG 6
#define KUS 17
#define KTS 19
#define KFWP 13
constexpr int kW2S = KW2S, kW2M = KW2M;
#define KSA 4
constexpr int NW2L = (20 - kW2S) * __builtin_popcount(kW2M), NW2P = NW2L + KSA;
constexpr int P1_PRE = NPD + 7;
constexpr int p1_dma(int i) { return i < 5 ? 100 + i : i < 13 ? i - 5 : i < 18 ? 105 + (i - 13) : i < 22 ? 8 + (i - 18) : i < NPD ? 110 + (i - 22) : 400; }
constexpr int kQ1a = KST > KD0 ? kQ1 * (KST - KD0) / (117 - KD0) : 0;
constexpr int p1_pre(int i) { return i < kQ0 + kQ1a ? p1_dma(i) : i == kQ0 + kQ1a ? 400 : p1_dma(i - 1); }
constexpr int p1_item(int s, int g) {
    if (s == KFS && g == (XG0 == 5 ? 4 : 9)) return 324;
    if (WOD ? (s & 1) && s < 10 && (g == 3 || g == 8) : g == 9 && s < 10) return WOD ? 114 + s + (g == 8) : 115 + s;
    if (s == KSL && (g == KSG || g == KSG + 1)) return 202 + g - KSG;
    if (s == KFWP && g == 1) return 208;
    if (s == KFKS && g == KFKG) return 206;
    if (USP && s == KUS && (g == 0 || g == 2 || g == 4 || g == 7)) return 210 + g / 2;
    if (s == KTS && g == 9) return 209;
    if (s >= kW2S && ((kW2M >> g) & 1)) return 500 + (s - kW2S) * __builtin_popcount(kW2M) + __builtin_popcount(kW2M & ((1 << g) - 1));
    if ((s & 1) == 0 && s <= 12 && g >= XG0 && (g - XG0) % XGS == 0 && (g - XG0) / XGS < 4) return 4 * (s / 2 + 3) + (g - XG0) / XGS;
    return -1;
}
constexpr int p1_cnt(int s, int g) {
    int n = P1_PRE;
    for (int t = 0; t <= s; ++t)
        for (int u = 0; u < 10; ++u) {
            if (t == s && u >= g) break;
            if (p1_item(t, u) >= 0) ++n;
            if (!USP && t == KUS && u == 0) n += 4;
        }
    return n;
}
constexpr int p1_pos(int code) {
    for (int i = 0; i < P1_PRE; ++i) if (p1_pre(i) == code) return i;
    for (int t = 0; t < 20; ++t)
        for (int u = 0; u < 10; ++u) if (p1_item(t, u) == code) return p1_cnt(t, u);
    return -1000;
}
constexpr int p1_lds(int s, int g) {
    constexpr int t0 = XG0 == 5 ? 0 : 5;
    if (s >= 2 && s < 10 && !(s & 1) && g >= t0 && g < t0 + 2) return 2 + s + g - t0;
    if (s == KSC + 1 && g == t0 + 2) return 38;
    if (s >= 14 && s < 19 && !(s & 1) && (g == 5 || g == 8)) return 30 + (s - 14) + g / 8;
    if (s == 19 && (g == 0 || g == 4)) return 36 + g / 4;
    if (s == 19 && g == 7) return 39;
    return -1;
}
constexpr int p1_nl(int s, int g0, int g1) {
    int n = 0;
    for (int g = g0; g < g1; ++g) n += (s < 19 && g < 5) + ((s & 1) && s < 19 && g >= 5 && g < 9) + (p1_lds(s, g) >= 0);
    return n;
}
constexpr int p1_cap(int s, int g) {
    int u = p1_nl(s, g, g + 1) + (g == 9 && s < 19) + ((s & 1) && s < 19 && g == 5) + ((s & 1) == 0 && g == XG0 - 1);
    const int it = p1_item(s, g), nt = g < 9 ? p1_item(s, g + 1) : -1;
    u += (it >= 0) + (nt >= 0 && nt < 200) + (it == 324);
    if ((s == 19 && g == 2) || (g == 9 && ((s & 3) == 2 || s == KUS - 1 || s == 18))) u += 2;
    return u < 2 ? 2 - u : 0;
}
constexpr int NFILL = 112;
constexpr int p1_from(int f) { return f < 40 ? 0 : f < 52 ? 9 : f < 72 ? KSC : 14; }
constexpr int p1_fill(int s, int g, int e) {
    int f = 0;
    for (int t = 0; t <= s; ++t)
        for (int u = 0; u < 10; ++u)
            for (int v = 0; v < p1_cap(t, u); ++v) {
                const bool go = f < NFILL && p1_from(f) <= t;
                if (t == s && u == g && v == e) return go ? f : -1;
                f += go;
                if (f == 40) f = 48;
            }
    return f;
}
static_assert(p1_fill(13, 10, 0) >= 52, "");
constexpr bool p1_all() {
    for (int i = 0; i < 40; ++i) if (p1_pos(i) < 0) return false;
    for (int i = 100; i < 125; ++i) if (p1_pos(i) < 0) return false;
    for (int i = 0; i < NW2L; ++i) if (p1_pos(500 + i) < 0) return false;
    return p1_pos(202) >= 0 && p1_pos(203) >= 0 && p1_pos(206) >= 0 && p1_pos(208) >= 0 && p1_pos(209) >= 0 && p1_pos(324) >= 0;
}
static_assert(p1_all() && p1_lds(14, 5) == 30 && p1_lds(14, 8) == 31 && p1_lds(16, 5) == 32, "");
constexpr int ring_req(int G, int I, int R, int X) {
    int n = I, e = 0;
    for (int t = 0; t <= G; ++t) {
        int r = -1;
        if (t % 5 == 0 || t % 5 == 3) {
            if (n < 40 && 5 * (n - R) + 4 < t) r = n++;
            else if (e < X && t % 10 == 3) r = 100 + e++;
        }
        if (t == G) return r;
    }
    return -1;
}
constexpr int g2_adr(int i, int kk) {
    int n = 0;
    for (int t = 0; t <= i; ++t)
        for (int u = 0; u < 5; ++u) {
            const bool ok = u == 4 || (u == 2 && (t < 11 || t > 15)) || (u == 3 && t < 5);
            if (t == i && u == kk) return ok ? n : -1;
            n += ok;
        }
    return -1;
}
constexpr int p1_lim(int n) { return n > 63 ? 63 : n; }
template <int IMM> DI void gld4s(v4u& dst, unsigned voff, const void* sbase) { asm volatile("global_load_dwordx4 %0, %1, %2 offset:%3 sc1" : "=v"(dst) : "v"(voff), "s"(sbase), "i"(IMM)); }
template <int IMM> DI void gld2s(v2u& dst, unsigned voff, const void* sbase) { asm volatile("global_load_dwordx2 %0, %1, %2 offset:%3 sc1" : "=v"(dst) : "v"(voff), "s"(sbase), "i"(IMM)); }
template <class T> DI void pin(T& v) { asm volatile("" : "+v"(v)); }
DI void mul2(float& a, float& b, float x, float y, float s) { asm volatile("v_mul_f32 %0, %2, %4\n\tv_mul_f32 %1, %3, %4" : "=&v"(a), "=&v"(b) : "v"(x), "v"(y), "v"(s)); }
DI void xor2(float& a, float& b, unsigned x, unsigned y, unsigned s) { asm volatile("v_xor_b32 %0, %2, %4\n\tv_xor_b32 %1, %3, %4" : "=&v"(a), "=&v"(b) : "v"(x), "v"(y), "v"(s)); }
DI void rot2(unsigned& a, unsigned& b, unsigned x, unsigned y) { asm volatile("v_alignbit_b32 %0, %2, %2, 16\n\tv_alignbit_b32 %1, %3, %3, 16" : "=&v"(a), "=&v"(b) : "v"(x), "v"(y)); }
DI void mix2(unsigned& a, unsigned& b, unsigned x, unsigned y, unsigned z) { asm volatile("v_bfi_b32 %0, %5, %2, %3\n\tv_bfi_b32 %1, %5, %3, %4" : "=&v"(a), "=&v"(b) : "v"(x), "v"(y), "v"(z), "s"(0xffffu)); }
template <int I> DI void shr1(unsigned& o, unsigned prev, unsigned x) {
    if constexpr (I > 0) asm volatile("v_mov_b32_dpp %0, %1 wave_ror:1 row_mask:0xf bank_mask:0xf\n\tv_mov_b32_dpp %0, %2 wave_shr:1 row_mask:0xf bank_mask:0xf" : "=&v"(o) : "v"(prev), "v"(x));
    else asm volatile("v_mov_b32_dpp %0, %1 wave_shr:1 row_mask:0xf bank_mask:0xf bound_ctrl:0" : "=&v"(o) : "v"(x));
}
template <int G> DI void bc2(float& a, float& b, unsigned x) {
    asm volatile("v_mov_b32_dpp %0, %2 row_newbcast:%3 row_mask:0xf bank_mask:0xf\n\tv_mov_b32_dpp %1, %2 row_newbcast:%4 row_mask:0xf bank_mask:0xf" : "=&v"(a), "=&v"(b) : "v"(x), "i"(G), "i"(G + 8));
}
DI void rot1(unsigned& a, unsigned x) { asm volatile("v_alignbit_b32 %0, %1, %1, 16" : "=v"(a) : "v"(x)); }
DI void mix1(unsigned& a, unsigned x, unsigned y) { asm volatile("v_bfi_b32 %0, %3, %1, %2" : "=v"(a) : "v"(x), "v"(y), "s"(0xffffu)); }
DI void ror1(unsigned& o, unsigned prev) { asm volatile("v_mov_b32_dpp %0, %1 wave_ror:1 row_mask:0xf bank_mask:0xf" : "=v"(o) : "v"(prev)); }
DI void shr1b(unsigned& o, unsigned x) { asm volatile("v_mov_b32_dpp %0, %1 wave_shr:1 row_mask:0xf bank_mask:0xf" : "+v"(o) : "v"(x)); }
DI void xor1(float& a, unsigned x, unsigned s) { asm volatile("v_xor_b32 %0, %1, %2" : "=v"(a) : "v"(x), "v"(s)); }
template <int G> DI void bc1(float& a, unsigned x) { asm volatile("v_mov_b32_dpp %0, %1 row_newbcast:%2 row_mask:0xf bank_mask:0xf" : "=v"(a) : "v"(x), "i"(G)); }
template <int G> DI void mulb(float& o, unsigned x, float y) { asm volatile("v_mul_f32_dpp %0, %1, %2 row_newbcast:%3 row_mask:0xf bank_mask:0xf" : "=v"(o) : "v"(x), "v"(y), "i"(G)); }
template <int N> DI void lgkm() { __builtin_amdgcn_s_waitcnt(0xC07F | (N << 8)); asm volatile("" ::: "memory"); }
DI void unp2(float& a, float& b, unsigned x) { asm volatile("v_lshlrev_b32 %0, 16, %2\n\tv_and_b32 %1, 0xffff0000, %2" : "=&v"(a), "=&v"(b) : "v"(x)); }
#define GL(a) "global_load_dwordx4 a[" #a ":" #a "+3], %7, %8 sc1\n\tglobal_load_dwordx4 a[" #a "+4:" #a "+7], %7, %8 offset:256 sc1\n\t"
#define AR(d, a, o) "v_accvgpr_read_b32 " d ", a[" #a "+" #o "]\n\t"
#define GS_(a) "s_waitcnt vmcnt(4)\n\t" AR("%2", a, 1) AR("%3", a, 3) AR("%4", a, 5) AR("%5", a, 7) AR("%0", a, 0) AR("%1", a, 2)                              \
    "v_xor_b32 %2, %9, %2\n\tv_bitop3_b32 %4, %4, %9, %2 bitop3:0xbe\n\tv_bitop3_b32 %4, %3, %9, %4 bitop3:0xbe\n\tv_bitop3_b32 %4, %5, %9, %4 bitop3:0xbe\n\t" \
    AR("%2", a, 4) AR("%3", a, 6) "v_cmp_ne_u32 vcc, 0, %4\n\ts_cbranch_vccz 2f\n\t" GL(a)
template <int A, int D> DI void gran_req(unsigned off, const void* base) {
    if constexpr (D > 0) __builtin_amdgcn_s_sleep(D);
    asm volatile("global_load_dwordx4 a[%c2:%c2+3], %0, %1 sc1\n\tglobal_load_dwordx4 a[%c2+4:%c2+7], %0, %1 offset:256 sc1" ::"v"(off), "s"(base), "i"(A) : "memory");
}
DI void pipe_gran(float& a, float& d, unsigned off, const void* base, unsigned tag, bool& dead) {
    unsigned n = dead ? 0u : kSpinCap, y, z;
    float a1, d1;
    asm volatile("1:\n\t" GS_(32) GS_(40) GS_(48) "s_sub_u32 %6, %6, 1\n\ts_cbranch_scc0 1b\n2:"
                 : "=&v"(a), "=&v"(d), "=&v"(a1), "=&v"(d1), "=&v"(y), "=&v"(z), "+s"(n) : "v"(off), "s"(base), "s"(tag) : "memory", "vcc", "scc");
    dead = __builtin_amdgcn_readfirstlane(n) == ~0u;
    a += a1; d += d1;
}
DI void fma2(float& a, float& b, float x, float y, float s, float t, float u, float v) {
    asm volatile("v_fma_f32 %0, %2, %4, %6\n\tv_fma_f32 %1, %3, %5, %7" : "=&v"(a), "=&v"(b) : "v"(x), "v"(y), "v"(s), "v"(t), "v"(u), "v"(v));
}
DI void fma2p(f32x2& o, float x, float y, float r, float u, float v) {
    asm("v_fmac_f32 %0, %1, %2" : "+v"(u) : "v"(x), "v"(r));
    asm("v_fmac_f32 %0, %1, %2" : "+v"(v) : "v"(y), "v"(r));
    o = f32x2{u, v};
}
DI void swp2(unsigned& a, unsigned& b, unsigned& c, unsigned& d) { asm volatile("s_nop 1\n\tv_permlane16_swap_b32 %0, %1\n\tv_permlane16_swap_b32 %2, %3" : "+v"(a), "+v"(b), "+v"(c), "+v"(d)); }
DI void swp2n(unsigned& a, unsigned& b, unsigned& c, unsigned& d) { asm volatile("v_permlane16_swap_b32 %0, %1\n\tv_permlane16_swap_b32 %2, %3" : "+v"(a), "+v"(b), "+v"(c), "+v"(d)); }
DI void cp2(unsigned& a, unsigned& b, f32x4 v) { asm volatile("v_cvt_pk_bf16_f32 %0, %2, %3\n\tv_cvt_pk_bf16_f32 %1, %4, %5" : "=&v"(a), "=v"(b) : "v"(v.x), "v"(v.y), "v"(v.z), "v"(v.w)); }
DI void zero2(v2u& a) { asm volatile("v_mov_b64 %0, 0" : "=v"(a)); }
DI void mov1(unsigned& a, unsigned b) { asm volatile("v_mov_b32 %0, %1" : "=v"(a) : "v"(b)); }
DI void sw16(unsigned& a, unsigned& b) { asm volatile("v_permlane16_swap_b32 %0, %1" : "+v"(a), "+v"(b)); }
DI void sw32(unsigned& a, unsigned& b) { asm volatile("v_permlane32_swap_b32 %0, %1" : "+v"(a), "+v"(b)); }
DI void mrg8(float& a, float b) { asm volatile("v_mov_b32_dpp %0, %1 quad_perm:[0,1,2,3] row_mask:0xf bank_mask:0xc" : "+v"(a) : "v"(b)); }
DI void sel1(float& o, unsigned a, unsigned b, u64 m) { asm volatile("v_cndmask_b32 %0, %1, %2, %3" : "=v"(o) : "v"(a), "v"(b), "s"(m)); }
DI void ne1(u64& m, v2u tag, v2u a) { asm volatile("v_cmp_ne_u64 %0, %1, %2" : "=s"(m) : "v"(tag), "v"(a)); }
DI void ne2(u64& m, v2u tag, v2u a, v2u b) { asm volatile("v_cmp_ne_u64 vcc, %1, %2\n\tv_cmp_ne_u64 %0, %1, %3\n\ts_or_b64 %0, %0, vcc" : "=&s"(m) : "v"(tag), "v"(a), "v"(b) : "vcc", "scc"); }
template <int G> DI void bc2b(float& a, float& b, float x, float y) {
    asm volatile("v_mov_b32_dpp %0, %2 row_newbcast:%4 row_mask:0xf bank_mask:0xf\n\tv_mov_b32_dpp %1, %3 row_newbcast:%4 row_mask:0xf bank_mask:0xf" : "=&v"(a), "=&v"(b) : "v"(x), "v"(y), "i"(G));
}
template <class T> DI void pins(T& v) { asm volatile("" : "+s"(v)); }
template <int I> DI void gld_w2(unsigned voff, const void* sbase) {
    asm volatile("global_load_dwordx4 " WA(2) ", %0, %1 offset:%3 sc1" ::"v"(voff), "s"(sbase), "i"(W2R + 4 * I), "i"((I % 5) * 1024 - 2048));
}
DI void gld2t(v2u& dst, unsigned voff, const void* sbase) { asm volatile("global_load_dwordx2 %0, %1, %2 sc1" : "+v"(dst) : "v"(voff), "s"(sbase)); }
DI void wait2(v2u& v, v4u (&x)[3]) { asm volatile("s_waitcnt vmcnt(0)" : "+v"(v), "+v"(x[0]), "+v"(x[1]), "+v"(x[2])::"memory"); }
template <int IMM> DI void gld4p(v4u& dst, unsigned voff, const void* sbase) { asm volatile("global_load_dwordx4 %0, %1, %2 offset:%3" : "+v"(dst) : "v"(voff), "s"(sbase), "i"(IMM)); }
DI void undef4(v4u (&x)[4]) { asm volatile("" : "=v"(x[0]), "=v"(x[1]), "=v"(x[2]), "=v"(x[3])); }
DI void wait1(v2u& v) { asm volatile("s_waitcnt vmcnt(0)" : "+v"(v)::"memory"); }
template <int N> DI void wait_fkn(v2u& v, v2u& u) { asm volatile("s_waitcnt vmcnt(%2)" : "+v"(v), "+v"(u) : "i"(N) : "memory"); }
template <int A, int IMM> DI void lda2(unsigned o, const void* base) { asm volatile("global_load_dwordx2 a[%c2:%c2+1], %0, %1 offset:%3" ::"v"(o), "s"(base), "i"(A), "i"(IMM)); }
DI void wait_fk(v2u& v, v2u& u, unsigned& x) { asm volatile("s_waitcnt vmcnt(0)" : "+v"(v), "+v"(u), "+v"(x)::"memory"); }
DI void ld_ur(unsigned o, const void* base) {
    asm volatile("global_load_dwordx4 a[40:40+3], %0, %1 sc1\n\tglobal_load_dwordx4 a[44:44+3], %0, %1 offset:1024 sc1\n\t"
                 "global_load_dwordx4 a[48:48+3], %0, %1 offset:2048 sc1\n\tglobal_load_dwordx4 a[52:52+3], %0, %1 offset:3072 sc1" ::"v"(o), "s"(base));
}
template <int I> DI void ld_ur1(unsigned o, const void* base) { asm volatile("global_load_dwordx4 a[%c2:%c2+3], %0, %1 offset:%3 sc1" ::"v"(o), "s"(base), "i"(40 + 4 * I), "i"(1024 * I)); }
constexpr int kUY = KSA + (USP ? p1_cnt(20, 0) - 1 - p1_pos(213) : p1_cnt(20, 0) - p1_cnt(KUS, 0) - 4);
DI void st_ur(unsigned a) {
    asm volatile("s_waitcnt vmcnt(%1)\n\tds_write_b128 %0, a[40:40+3] offset:480\n\tds_write_b128 %0, a[44:44+3] offset:1504\n\t"
                 "ds_write_b128 %0, a[48:48+3] offset:2528\n\tds_write_b128 %0, a[52:52+3] offset:3552" ::"v"(a), "i"(kUY < NW2P ? kUY : NW2P) : "memory");
}
template <int I> DI void st_ur1(unsigned a) {
    if constexpr (I == 0) asm volatile("s_waitcnt vmcnt(%0)" ::"i"(kUY) : "memory");
    asm volatile("ds_write_b128 %0, a[%c1:%c1+3] offset:%2" ::"v"(a), "i"(40 + 4 * I), "i"(480 + 1024 * I) : "memory");
}
DI void reload_b(v4u (&B)[8], unsigned a) {
    asm volatile("s_waitcnt lgkmcnt(0)\n\tds_read_b128 %0, %8 offset:448\n\tds_read_b128 %1, %8 offset:960\n\tds_read_b128 %2, %8 offset:1472\n\tds_read_b128 %3, %8 offset:1984\n\t"
                 "ds_read_b128 %4, %8 offset:2496\n\tds_read_b128 %5, %8 offset:3008\n\tds_read_b128 %6, %8 offset:3520\n\tds_read_b128 %7, %8 offset:4032\n\ts_waitcnt lgkmcnt(0)"
                 : "+v"(B[0]), "+v"(B[1]), "+v"(B[2]), "+v"(B[3]), "+v"(B[4]), "+v"(B[5]), "+v"(B[6]), "+v"(B[7]) : "v"(a) : "memory");
}
DI void wait4(v4u (&x)[4]) { asm volatile("s_waitcnt vmcnt(0)" : "+v"(x[0]), "+v"(x[1]), "+v"(x[2]), "+v"(x[3])::"memory"); }
template <int A, int IMM> DI void lda4(unsigned o, const void* base) { asm volatile("global_load_dwordx4 a[%c2:%c2+3], %0, %1 offset:%3" ::"v"(o), "s"(base), "i"(A), "i"(IMM)); }
template <int A> DI void rda(unsigned& v) { asm volatile("v_accvgpr_read_b32 %0, a[%c1+0]" : "=v"(v) : "i"(A)); }
DI void poll1(unsigned a, const void* base) { asm volatile("global_load_dwordx2 a[36:36+1], %0, %1 sc1" ::"v"(a), "s"(base)); }
DI void poll2(unsigned a, unsigned b, const void* base) { asm volatile("global_load_dwordx2 a[36:36+1], %0, %2 sc1\n\tglobal_load_dwordx2 a[36+2:36+3], %1, %2 sc1" ::"v"(a), "v"(b), "s"(base)); }
DI void touch_(unsigned a, const void* base) { asm volatile("global_load_dword a[32+1], %0, %1" ::"v"(a), "s"(base)); }
template <int N> DI void wait_st(v2u& a, v4u& b) { asm volatile("s_waitcnt vmcnt(%2)" : "+v"(a), "+v"(b) : "i"(N) : "memory"); }
DI void undef3(v4u (&x)[3]) { asm volatile("" : "=v"(x[0]), "=v"(x[1]), "=v"(x[2])); }
template <int IMM> DI void gld4t(v4u& dst, unsigned voff, const void* sbase) { asm volatile("global_load_dwordx4 %0, %1, %2 offset:%3 sc1" : "+v"(dst) : "v"(voff), "s"(sbase), "i"(IMM)); }
template <int N> DI void vm_wait() { asm volatile("s_waitcnt vmcnt(%0)" ::"i"(N) : "memory"); }
DI float dot2a(unsigned a, unsigned b, float c) {
    return __builtin_amdgcn_fdot2_f32_bf16(__builtin_bit_cast(bf16x2v, a), __builtin_bit_cast(bf16x2v, b), c, false);
}
DI void disc(unsigned l0, unsigned l1, unsigned l2, f32x2& lam, f32x2& co) {
    const float dt = rbf(__builtin_amdgcn_exp2f(lo16f(l2) * 1.44269504088896f));
    const float lre = lo16f(l0), lim = lo16f(l1);
    const float e_ = __builtin_amdgcn_exp2f(dt * lre * 1.44269504088896f);
    const float rv = __builtin_amdgcn_fractf(dt * lim * 0.159154943091895f);
    lam = f32x2{e_ * __builtin_amdgcn_cosf(rv), e_ * __builtin_amdgcn_sinf(rv)};
    const float nr = lam.x - 1.0f, ni = lam.y, id = __builtin_amdgcn_rcpf(lre * lre + lim * lim);
    co = f32x2{(nr * lre + ni * lim) * id, (ni * lre - nr * lim) * id};
}
DI f32x4 unpk4(v2u u) { return f32x4{lo16f(u.x), hi16f(u.x), lo16f(u.y), hi16f(u.y)}; }
#define D2 "v_dot2_f32_bf16 "
#define CP "v_cvt_pk_bf16_f32 "
DI void d2x4(unsigned* o, unsigned a0, unsigned a1, unsigned a2, unsigned a3, unsigned b0, unsigned b1, unsigned b2, unsigned b3) {
    asm volatile(D2 "%0, %4, %8, 0\n\t" D2 "%1, %5, %9, 0\n\t" D2 "%2, %6, %10, 0\n\t" D2 "%3, %7, %11, 0"
                 : "=&v"(o[0]), "=&v"(o[1]), "=&v"(o[2]), "=&v"(o[3]) : "v"(a0), "v"(a1), "v"(a2), "v"(a3), "v"(b0), "v"(b1), "v"(b2), "v"(b3));
}
DI void cpx4(unsigned* o, unsigned a0, unsigned a1, unsigned a2, unsigned a3, unsigned b0, unsigned b1, unsigned b2, unsigned b3) {
    asm volatile(CP "%0, %4, %8\n\t" CP "%1, %5, %9\n\t" CP "%2, %6, %10\n\t" CP "%3, %7, %11"
                 : "=&v"(o[0]), "=&v"(o[1]), "=&v"(o[2]), "=&v"(o[3]) : "v"(a0), "v"(a1), "v"(a2), "v"(a3), "v"(b0), "v"(b1), "v"(b2), "v"(b3));
}
DI void cpx4i(unsigned* t, const unsigned* ka) {
    asm volatile(CP "%0, %0, %4\n\t" CP "%1, %1, %5\n\t" CP "%2, %2, %6\n\t" CP "%3, %3, %7"
                 : "+v"(t[0]), "+v"(t[1]), "+v"(t[2]), "+v"(t[3]) : "v"(ka[0]), "v"(ka[1]), "v"(ka[2]), "v"(ka[3]));
}
template <int IMM> DI void bld4(v4u& dst, unsigned voff, v4u rsrc) {
    if constexpr ((NTM >> 4) & 1) asm volatile("buffer_load_dwordx4 %0, %1, %2, 0 offen offset:%3 nt" : "=v"(dst) : "v"(voff), "s"(rsrc), "i"(IMM));
    else asm volatile("buffer_load_dwordx4 %0, %1, %2, 0 offen offset:%3" : "=v"(dst) : "v"(voff), "s"(rsrc), "i"(IMM));
}

__global__ __launch_bounds__(256, 1) __attribute__((aligned(4096))) void k_main(Params p) {
    __shared__ __attribute__((aligned(16))) char smem[LDS_BYTES];
    const rsrc_t rs = __builtin_amdgcn_make_buffer_rsrc((void*)p.ws, 0, WS_BYTES, 0x00020000);
    const rsrc_t ro = __builtin_amdgcn_make_buffer_rsrc((void*)p.out, 0, S_ * D_ * 4, 0x00020000);
    const int w0 = __builtin_amdgcn_readfirstlane(threadIdx.x >> 6), b0 = blockIdx.x;
    bool dead = false;
    asm volatile("" ::: "a255");
    float SW[5];
    f32x4 xin[5];
    v2u gt[5];
    f32x4 acc[2][5];
    f32x4 rr[2];
    float tt[2][5][4];
    Lm lam1, lamq;
    f32x2 l8, l32, l128;
    u64 late;
    v4u cA[8], cB[8];
    int sgn_, sg4, ab[4], bb, zpa, uwa, spa, tbw, tbr, tbp, tbq, cwa, cw4, spl, dst[3];
    unsigned w2o, wro, fwo, fko, xso, gso, fgo1, fho1, fho, hso, eo, es, ho[4];
    bool cv0;
    const char* kvtb;
    const char* w2b0[(NW2P + 4) / 5];
    int otw, otr0, otr4;
    int mt1_;
    auto addrs = [&](auto layer_, int lane, int w, int b) __attribute__((always_inline)) {
        constexpr int layer = layer_.value;
        const int c = lane & 15, q = lane >> 4;
        TILES()
        static_assert(l_sg(0, 0) == SC + 0xA900 && l_sg(0, 1) == SC + 0xB400 && l_sg(0, 2) == SC + 0xCE00 && l_sg(0, 3) == SC + 0xDC00, "");
        sgn_ = (layer ? l_sg(1, 0) + w * INBOX : SC + (int)((0xDC00CE00B400A900ull >> (16 * w)) & 0xffff)) + (q >> 1) * 640 + (q & 1) * 8;
        const int c4 = (c >> 1) * 16 + (c & 1) * 4;
        sg4 = sgn_ + c4; sgn_ += c * 16;
        const int pl = lane < 40 ? lane : 39, tbb = l_tb(layer, 0) + w * (layer ? INBOX : 5120);
        spl = SC + pl * 16;
        eo = OFF_EG + (unsigned)(layer * 163840 + nt1 * 640 + pl * 16);
        es = eo + (unsigned)(mt1 * 10240);
        spa = l_rs(layer, 0) + (2 * (mt2 & 1) + (w >> 1)) * (layer ? INBOX : 256) + (2 * (w & 1) + (c >> 3)) * 64 + (c & 7) * 4;
        tbw = tbb + c * 64 + ((q ^ ((c >> 1) & 3)) * 16);
        tbr = tbb + (2 * (c >> 2) + (q & 1)) * 64 + (((2 * (q >> 1) + ((c >> 1) & 1)) ^ (c >> 2)) * 16) + (c & 1) * 8;
        hso = OFF_HF + (unsigned)(layer * 5242880 + ((8 * mt1 + 2 * w + (q >> 1)) * 160 + 10 * nt1 + 5 * (q & 1)) * 256 + c * 16);
        tbp = tbb + (8 * (q & 1) + (c >> 2)) * 64 + (((2 * (q >> 1) + ((c >> 1) & 1)) ^ (c >> 3)) * 16) + (c & 1) * 8;
        tbq = tbp ^ 32;
        cwa = L_CW + c * 16; cw4 = L_CW + c4;
        pin(sg4); pin(tbp); pin(tbq); pin(cw4);
        fho1 = lane ? kNone : OFF_FH + (unsigned)((((layer * 32 + 2 * mt1 + (w >> 1)) * 4 + (nt1 >> 2)) * 8 + (nt1 & 3) * 2 + (w & 1)) * 128);
        fho = OFF_FH + (unsigned)(((layer * 32 + mt2) * 4 + w) * 1024 + (PLM ? lane >> 3 : lane & 7) * 128);
        pin(sgn_); pin(spl); pin(eo); pin(es); pin(spa); pin(tbw); pin(tbr); pin(hso); pin(cwa); pin(fho1); pin(fho);
        const unsigned hb_ = OFF_HF + (unsigned)(layer * 5242880 + (4 * mt2 * 160 + 40 * w) * 256);
#pragma unroll
        for (int m = 0; m < 4; ++m) { ho[m] = hb_ + (unsigned)(((m + 1 + w) & 3) * 40960 + lane * 16); pin(ho[m]); }
#pragma unroll
        for (int m = 0; m < 3; ++m) { dst[m] = SC + ((w + 1 + m) & 3) * INBOX + m * 5120 + lane * 16; pin(dst[m]); }
        mt1_ = mt1;
        if constexpr (layer == 0) {
            const int r0 = SC + (w < 3 ? w : 0) * REGSZ, y0 = 2031 + 8 * q - c, l34 = lane < 33 ? lane : 33;
#pragma unroll
            for (int i = 0; i < 4; ++i) { ab[i] = r0 + ((y0 & 1) ? CV_O : CV_E) + ((y0 + 1) >> 1) * 4 - 448 - 512 - 1024 * i; pin(ab[i]); }
            bb = r0 + 32 * c + 16 * q;
            zpa = w < 3 ? r0 + l34 * 16 + (l34 >= 30 ? CV_E + 4096 - 480 : 0) + (l34 >= 32 ? CV_O + 4100 - 32 - CV_E - 4096 : 0) : SC + 47360 + lane * 16;
            uwa = (w < 3 ? r0 : SC + 47360 - 480) + lane * 16;
            xso = XDM ? (unsigned)((64 * mt2 + 16 * w + (lane >> 2)) * D_ + 80 * nt2 + 8 * ((lane & 3) ^ q)) * 2u : (unsigned)((64 * mt2 + 16 * w + c) * D_ + 80 * nt2 + 16 * (q & 1) + 8 * (q >> 1)) * 2u;
            gso = OFF_GT + (unsigned)((80 * nt2 + i32 + 32 * w) * GSTR + (q & 1) * 512 + (c >> 2) * 128 + (q >> 1) * 64 + (c & 3) * 16);
            if (GSM && i32 + 32 * w >= 80) gso = kNone;
            fgo1 = lane == 0 && i32 + 32 * w < 80 ? OFF_FG + (unsigned)((nt2 * 96 + i32 + 32 * w) * 128) : kNone;
            pin(bb); pin(zpa); pin(uwa); pin(xso); pin(gso); pin(fgo1);
        }
        SCHED();
    };
    unsigned tag0, tag1;
    v2u tagv;
#define FLAG(off) { if (lane == 0) st64(tagv, rs, off); }
#define ISTAG(f) (((f.x ^ tag0) | (f.y ^ tag1)) == 0u)
#define KW_REQ() { ld_ur(wro, kvtb); static_for<NW2P>(LAMBDA(i_) { gld_w2<i_.value>(w2o, p.ws + 2048 + (i_.value / 5) * 5120); }); }
#define FW_OK() { if (__builtin_expect(late != 0ull, 0)) { WAVE_WAIT({ const v2u fk_ = ld64(rs, fko); const v2u fw_ = ld64(rs, fwo); ok = ISTAG(fk_) && ISTAG(fw_); }) KW_REQ() } }
#define WF(g, s) *(const v4u*)(smem + wa[(s) & 1][(g) < 4 ? (g) & 1 : 2] + ((g) & 2) * 20480 + ((s) >> 1) * 1024)
#define TT_GAP(n) { constexpr int mb_ = (n) / 10, nb_ = ((n) % 10) / 2, e_ = ((n) & 1) * 2; \
        mul2(tt[mb_][nb_][e_], tt[mb_][nb_][e_ + 1], rm_[4 * mb_ + e_], rm_[4 * mb_ + e_ + 1], SW[nb_]); }
    {
        const int tid = threadIdx.x, lane = tid & 63, w = w0, b = b0;
        const int c = lane & 15, q = lane >> 4;
        TILES()
        STAMP(0)
        const unsigned lds0 = (unsigned)(size_t)(LAS char*)smem;
        unsigned nonce;
        unsigned cpf0;
        {
            u64 pc;
            int len;
            asm volatile("s_getpc_b64 %0\nkpf_begin:\n\ts_mov_b32 %1, kpf_end-kpf_begin\n\ts_max_i32 %1, %1, kpf_end2-kpf_begin\n\ts_max_i32 %1, %1, kpf_end3-kpf_begin" : "=s"(pc), "=s"(len)::"scc");
            const int o0 = (((j & 15) * 4 + w) + 64 * (lane >> 3)) * 128;
            asm volatile("global_load_dword %0, %1, %2" : "=v"(cpf0) : "v"(o0 < len ? o0 : 0), "s"(pc));
        }
        const bool cv = 32 * w + i32 < 80;
        const int chn = 80 * nt2 + i32 + 32 * (cv ? w : 0);
        const bool kw = 32 * w + 31 - i32 < 80;
        const int kp = w == 2 ? 256 + 8 * nt2 + ((i32 & 15) >> 1) : 32 * nt2 + i32, kh_ = w == 2 ? i32 & 1 : w & 1;
        const int ku = KVU == 3 ? 80 * xq + (w == 2 ? 64 + (j & 7) + 8 * (j >> 4) : 2 * j + (w & 1)) : 80 * nt2 + (w == 2 ? 64 + (i32 & 15) : KVU == 2 ? 2 * i32 + (w & 1) : i32 + 32 * (w & 1));
        const int kv_ = KVM > 1 ? (int)((unsigned)ku * KVM % 640u) : ku;
        const int kc = KVU == 1 ? kp % 10 : KVU == 4 ? kv_ >> 6 : kv_ % 10, kr = KVU == 1 ? 2 * (kp / 10) + kh_ : KVU == 4 ? kv_ & 63 : kv_ / 10;
        const int lc = cv ? lane : 8;
        const unsigned xl = lds0 + (unsigned)(w * REGSZ);
        v4u ax[15], sa[5], cr[2], wr[4];
        unsigned uL[3], uM[3], sdq, kso;
        const int cu_ = (b < 200 ? b : 0) * 4 + w, cdb = cu_ / 20, ccg = cu_ % 20;
        {
            unsigned cpo = (unsigned)(32 * ccg + (lane & 31)) * 2u, cpm = (unsigned)(40 * nt1 + (lane < 39 ? lane : 39)) * 2u;
            unsigned k1 = (unsigned)(32 * kr + 5 + (lane >> 3)) * 1280u + (unsigned)(64 * kc + 8 * (lane & 7)) * 2u, k0 = k1 - 10240u, k4 = lane < 24 ? k1 + 30720u : kNone;
            unsigned vo = k1 - 6400u + (unsigned)(lane >> 3) * 3840u;
            pin(cpo); pin(cpm); pin(k0); pin(k1); pin(k4); pin(vo);
            SCHED();
            gldu16(uL[0], cpo, p.Lre); gldu16(uL[1], cpo, p.Lim); gldu16(uL[2], cpo, p.logdt);
            gldu16(uM[0], cpm, p.Lre); gldu16(uM[1], cpm, p.Lim); gldu16(uM[2], cpm, p.logdt);
            const v4u rk = {(unsigned)(u64)p.k, (unsigned)((u64)p.k >> 32) & 0xffffu, S_ * D_ * 2u, 0x00020000u};
            ld_kv(ax, 32 * w + 31 - i32, k0, k1, k4, vo, rk, p.v, xl);
            SCHED();
        }
        auto small_ = [&]() __attribute__((always_inline)) {
            kso = (unsigned)(512 * kc + lane * 4);
            dma_1<L_EAR + 2048, 0>(kso, p.shk, xl); dma_1<L_EAR + 2304, 256>(kso, p.shk, xl);
            gld1s(nonce, kso & 4u, p.ws + OFF_NONCE);
            SCHED();
        };
        if (SMP == 0) small_();
        const u16* sab = w < 2 ? p.x0 : w == 2 ? p.Bre : p.Bim;
        unsigned sao;
        {
            int r0 = w < 2 ? 128 * mt1 + 8 * nt1 + 4 * w : 40 * nt1 + mt1, r1 = w < 2 ? 1 : 16, r2 = w < 2 ? 2047 : 40 * nt1 + 32 + (mt1 >> 1);
            pins(sab); pins(r0); pins(r1); pins(r2);
            const int r = r0 + q * r1;
            sao = (unsigned)((r < r2 ? r : r2) * 1280 + c * 16);
            pin(sao);
        }
        if (SAF == 0) static_for<5>(LAMBDA(i) { gld<256 * i.value>(sa[i.value], sao, sab); });
        SCHED();
        if (w == TW0 && !kw) tour<0>();
        STAMP(22)
        const int row8 = lane >> 3;
        const unsigned wvo = (unsigned)(40 * nt1 + row8) * 1280u + (unsigned)(((lane & 7) ^ ((w & 1) | ((row8 >> 1) << 1))) * 16 + (w >> 1) * 128);
        const char* const wbase = (const char*)((w & 1) ? p.Bim : p.Bre);
        const unsigned wl = lds0 + (unsigned)(((w & 1) * 10 + (w >> 1)) * 1024);
        const unsigned xvo0 = (unsigned)(8 * (row8 >> 1) + 2 * (row8 & 1)) * 1280u + (unsigned)(((lane & 7) ^ ((row8 >> 1) << 1)) * 16);
        const unsigned xvo1 = (xvo0 ^ 16u) + 1280u;
        const char* const xbase = (const char*)p.x0 + (size_t)(128 * mt1 + 32 * w) * 1280;
        unsigned xv[4] = {xvo0, xvo1, xvo0 + 5120u, xvo1 + 5120u}, wv[5];
#pragma unroll
        for (int i = 0; i < 5; ++i) { wv[i] = wvo + i * 10240u; pin(wv[i]); if (i < 4) pin(xv[i]); }
#define DMA_M0(it) SETM0<dma_loff(it) - dma_goff(it)>((it) < 40 ? xl : wl)
#define DMA_LD1(it, nx_) GLD1<dma_goff(it), dma_loff(nx_) - dma_goff(nx_)>((it) < 40 ? xv[(it) & 3] : wv[(it) < 40 ? 0 : ((it) - 100) % 5], (it) < 40 ? xbase : wbase, (nx_) < 40 ? xl : wl)
#define DMA_LD(it) GLD0<dma_goff(it)>((it) < 40 ? xv[(it) & 3] : wv[(it) < 40 ? 0 : ((it) - 100) % 5], (it) < 40 ? xbase : wbase)
        const int swz = (c & 1) | ((c >> 2) << 1);
        int wa[2][3];
#pragma unroll
        for (int i = 0; i < 2; ++i) {
            wa[i][0] = (c >> 3) * 20480 + (c & 7) * 128 + (((4 * i + q) ^ (c & 6)) * 16); wa[i][1] = (wa[i][0] ^ 16) + 10240;
            wa[i][2] = 81920 + (c & 1) * 10240 + (c >> 1) * 128 + (((4 * i + q) ^ ((c & 1) | ((c >> 2) << 1))) * 16);
            pin(wa[i][0]); pin(wa[i][1]); pin(wa[i][2]);
        }
        int xlane0 = L_XR + w * REGSZ + (c & 1) * 1024 + (c >> 1) * 128 + ((q ^ swz) * 16);
        int xlane1 = L_XR + w * REGSZ + (c & 1) * 1024 + (c >> 1) * 128 + (((4 + q) ^ swz) * 16);
        unsigned cvo = (unsigned)((16 * cdb + (lane >> 2)) * D_ + 32 * ccg + 8 * ((lane & 3) ^ q)) * 2u, for0, for1;
        int ear = L_EAR + w * REGSZ + c * 64 + (q ^ (c >> 2)) * 16, eas = L_EAR + 2048 + w * REGSZ + (lane & 7) * 64;
        int kra = L_XR + w * REGSZ + (lane >> 3) * 512 + (lane & 7) * 16;
        pin(ear); pin(eas); pin(kra);
        {
            const int G = 4 * ccg + q, g5 = G % 5, ch = 2 * (G - g5) + 5 * (g5 & 1) + (g5 & 6), ci = ch + (g5 < 4 ? 1 : 5);
            const unsigned fo = OFF_W2F + (unsigned)(((cdb / 5) * 200 + cdb % 5) * 1024 + c * 16);
            for0 = fo + (unsigned)((ch >> 2) * 5120 + (ch & 3) * 256); for1 = fo + (unsigned)((ci >> 2) * 5120 + (ci & 3) * 256);
        }
        unsigned sto = OFF_ST + (unsigned)((mt1 * 16 + 4 * w + q) * 256 + c * 8), swo = OFF_ST + (unsigned)((PKF & 4) ? ((lane >> 2) * 16 + nt1) * 256 + 128 + (lane & 3) * 16 : (c * 16 + nt1) * 256 + 128 + q * 16);
        unsigned kvo = OFF_KVT + (unsigned)((64 * kc + 8 * (lane >> 3) + ((lane >> 2) & 1)) * KSTR + (8 * kc + (lane >> 3)) * KPS + (32 * kr + 8 * (lane & 3)) * 2);
        int kbw = L_KB + w * 1024 + (lane & 7) * 128 + (((lane >> 3) * 8) ^ ((lane & 7) * 16));
        int kbr = L_KB + w * 1024 + (lane >> 3) * 128 + (((lane & 7) * 16) ^ ((lane >> 3) * 16));
        const unsigned dum = (OOB & 1) ? kNone : OFF_DUM + (unsigned)((b * 4 + w) * 128 + (lane & 7) * 16);
        unsigned fko1 = OFF_FK + (unsigned)((PKF & 1) ? (64 * kc + kr) * 32 : (10 * kr + kc) * 128);
        if (!kw) kvo = dum;
        if (b >= 200) { for0 = dum; for1 = dum; cvo = (unsigned)((b * 4 + w) * 128); }
        wro = (unsigned)(chn * 4096 + lc * 16);
        cv0 = cv;
        pin(kvo); pin(kbw); pin(kbr); pin(fko1);
        pin(xlane0); pin(xlane1); pin(cvo); pin(for0); pin(for1); pin(sto); pin(swo); pin(wro);
        SCHED();
        auto pre = LAMBDA(i_) {
            constexpr int it = p1_dma(i_.value);
            SCHED();
            DMA_M0(it);
            asm volatile("s_nop 0");
            DMA_LD(it);
            SCHED();
        };
        fko = OFF_FK + (unsigned)((PKF & 1) ? ((chn & ~63) + (cv ? lane : 0)) * 32 : ((cv ? lane : 0) * 10 + (chn >> 6)) * 128);
        { const int e_ = (lane & 7) < w ? w : (lane & 7) > w + 4 ? w + 4 : (lane & 7);
          fwo = OFF_FW + (unsigned)((PKF & 2) ? ((5 * nt2 + (lane < 40 ? lane >> 3 : 4)) * 20 + 4 * w + e_) * 32 : ((5 * nt2 + (lane < 25 ? lane / 5 : 0)) * 20 + 5 * w + (lane < 25 ? lane % 5 : 0)) * 128); }
        w2o = OFF_W2F + (unsigned)((nt2 * 40 + 10 * w) * 5120 + lane * 16);
        int ime = SC + (w < 3 ? w * REGSZ + CV_E : 43264) + 1008 - 16 * lane, imo = SC + (w < 3 ? w * REGSZ + CV_O + 4 : 47360) + 1008 - 16 * lane, im3 = w < 3 ? ime + 2048 : imo;
        int tca = L_TAB + c * 136, tcb = tca + 8 + 32 * w + 8 * q, tcc = L_TAB + (32 + (c >> 1)) * 136, tcd = tcc + 8 + 32 * w + 8 * q, tcl = L_TAB + (lane < 40 ? lane : 39) * 136, coa = L_CO + w * REGSZ + q * 64, rsa = l_rs(0, 0) + w * 256 + lane * 4;
        unsigned sgn = (c & 1) ? 0u : 0x80000000u;
        fko1 = kw && lane < 32 ? fko1 : OFF_FW + (unsigned)((b * 4 + w) * ((PKF & 2) ? 32 : 128));
        pin(fko); pin(fwo); pin(w2o); pin(fko1); pin(ime); pin(imo); pin(im3); pin(tca); pin(tcb); pin(tcc); pin(tcd); pin(tcl); pin(coa); pin(rsa); pin(sgn);
        SCHED();
        auto cw_ = [&]() __attribute__((always_inline)) {
            dma_n<L_EAR>(cvo, p.Cre, xl); dma_n<L_EAR + 1024>(cvo, p.Cim, xl);
            dma_1<L_EAR + 2560, 0>((unsigned)(160 * nt2 + (lane < 39 ? lane : 39) * 4), p.Dskip, xl);
            ld_wr(wr, 32 * w + i32, wro, p.ssmk);
            gldu16(sdq, (unsigned)chn * 2u, p.ssmD);
        };
        if (SMP == 1) small_();
        static_assert(SMP == 0 || SAF > 0, "");
        if (SAF == 1) static_for<5>(LAMBDA(i) { gld<256 * i.value>(sa[i.value], sao, sab); });
        if (CWP == 0) cw_();
        STAMP(33)
        asm volatile("s_waitcnt vmcnt(%5)\n\ts_cmp_ge_u32 %4, 80\n\ts_cbranch_scc0 1f\n\ts_waitcnt vmcnt(%6)\n1:" : "+v"(uL[0]), "+v"(uL[1]), "+v"(uL[2]), "+v"(cpf0) : "s"(32 * w + 31 - i32), "i"(12 + (SAF < 2 ? 5 : 0) + (CWP ? 0 : 8)), "i"(3 + (SAF < 2 ? 5 : 0) + (CWP ? 0 : 8)) : "memory", "scc");
        pin(uM[0]); pin(uM[1]); pin(uM[2]);
        {
            f32x2 lam, co;
            disc(uL[0], uL[1], uL[2], lam, co);
            *(f32x2*)(smem + L_CO + w * REGSZ + (lane & 31) * 8) = 2.0f * co;
        }
        SCHED();
        STAMP(35)
        if (SAF == 2) static_for<5>(LAMBDA(i) { gld<256 * i.value>(sa[i.value], sao, sab); });
        if (CWP == 1) cw_();
        if (APOS == 0) addrs(std::integral_constant<int, 0>{}, lane, w, b);
        if (SAF == 3) static_for<5>(LAMBDA(i) { gld<256 * i.value>(sa[i.value], sao, sab); });
        if (CWP == 2) cw_();
        v4u kvt[4];
        unsigned ka[8], kt[8], se[8], vs[8], ko4[4], one2 = 0x3F803F80u;
        pin(one2);
        auto kvop = LAMBDA(n_) {
            constexpr int n = n_.value, h = n ? (n - 1) / 29 : -1, u = n ? (n - 1) % 29 : 26;
            if constexpr (u < 2) d2x4(ka + 4 * u, ax[0][h], ax[1][h], ax[2][h], ax[3][h], se[u], se[u], se[u], se[u]);
            else if constexpr (u == 8) { const v4u sk = ax[11 + h]; se[4] = sk[0] >> 16; se[5] = sk[2] & 0xffff0000u; se[6] = sk[0] & 0xffffu; se[7] = sk[2] << 16; pin(se[4]); pin(se[5]); pin(se[6]); pin(se[7]); }
            else if constexpr (u < 21) {
                constexpr int v = u < 8 ? u - 2 : u - 3, tp = 1 + v / 6, st = v % 6, e = st & 1;
                if constexpr (st < 2) d2x4(kt + 4 * e, ax[tp][h], ax[tp + 1][h], ax[tp + 2][h], ax[tp + 3][h], se[2 * tp + e], se[2 * tp + e], se[2 * tp + e], se[2 * tp + e]);
                else if constexpr (st < 4) cpx4i(kt + 4 * e, ka + 4 * e);
                else d2x4(ka + 4 * e, kt[4 * e], kt[4 * e + 1], kt[4 * e + 2], kt[4 * e + 3], one2, one2, one2, one2);
            }
            else if constexpr (u == 21) { vs[0] = ax[7][h] & 0xffffu; vs[1] = ax[8][h] << 16; vs[2] = ax[9][h] & 0xffffu; vs[3] = ax[10][h] << 16; pin(vs[0]); pin(vs[1]); pin(vs[2]); pin(vs[3]); }
            else if constexpr (u == 22) { vs[4] = ax[7][h] >> 16; vs[5] = ax[8][h] & 0xffff0000u; vs[6] = ax[9][h] >> 16; vs[7] = ax[10][h] & 0xffff0000u; pin(vs[4]); pin(vs[5]); pin(vs[6]); pin(vs[7]); }
            else if constexpr (u == 23) cpx4(ko4, ka[0], ka[2], ka[4], ka[6], ka[1], ka[3], ka[5], ka[7]);
            else if constexpr (u < 26) { constexpr int e = u - 24; d2x4(kt + 4 * e, ko4[2 * e], ko4[2 * e], ko4[2 * e + 1], ko4[2 * e + 1], vs[4 * e], vs[4 * e + 1], vs[4 * e + 2], vs[4 * e + 3]); }
            else if constexpr (u == 26) {
                if constexpr (h < 3) { const v4u sk = ax[12 + h]; se[0] = sk[1] >> 16; se[1] = sk[3] & 0xffff0000u; se[2] = sk[1] & 0xffffu; se[3] = sk[3] << 16; pin(se[0]); pin(se[1]); pin(se[2]); pin(se[3]); }
                else asm volatile("s_nop 2");
            }
            else if constexpr (u == 27) {
                cpx4(ko4, kt[0], kt[2], kt[4], kt[6], kt[1], kt[3], kt[5], kt[7]);
                *(v2u*)(smem + kbw) = v2u{ko4[0], ko4[1]};
                *(v2u*)(smem + (kbw ^ 64)) = v2u{ko4[2], ko4[3]};
            }
            else kvt[h] = *(const v4u*)(smem + kbr);
            SCHED();
        };
        auto deal = [&](auto a_, auto b_, auto n_, auto i_) __attribute__((always_inline)) {
            constexpr int a = a_.value, m = b_.value - a, lo = m * i_.value / n_.value, hi = m * (i_.value + 1) / n_.value;
            static_for<hi - lo>(LAMBDA(e_) { pre(std::integral_constant<int, a + lo + e_.value>{}); });
        };
#define IC(n) std::integral_constant<int, (n)>{}
        static_for<kQ0>(pre);
        static_assert(kQ0 == 0 && KD0 >= 8, "");
        asm volatile("s_waitcnt vmcnt(%4)" : "+v"(ax[7]), "+v"(ax[8]), "+v"(ax[9]), "+v"(ax[10]) : "i"(13 + kQ0) : "memory");
        ax[11] = *(const v4u*)(smem + eas);
        SCHED();
#pragma unroll
        for (int i = 0; i < 7; ++i) { ax[i] = *(const v4u*)(smem + kra + i * 128); SCHED(); }
#pragma unroll
        for (int i = 1; i < 4; ++i) ax[11 + i] = *(const v4u*)(smem + eas + i * 16);
        SCHED();
        pin(nonce);
        STAMP(30)
        unsigned tch = 0u;
        auto kvs = LAMBDA(n_) {
            site<1000 + n_.value>(); SCHED();
            if constexpr (n_.value == KD0) LGKM0()
            kvop(n_);
            if constexpr (n_.value >= KD0) deal(IC(kQ0), IC(kQ0 + kQ1), IC(117 - KD0), IC(n_.value - KD0));
        };
        static_assert((KST - 1) % 29 == 0, "");
        if (kw) static_for<KST>(kvs);
        else {
            if (__builtin_expect(w == 3, !W3O)) {
                f32x2 lam, co, lp[16];
                disc(uM[0], uM[1], uM[2], lam, co);
                lp[0] = cmul(lam, lam); lp[0] = cmul(lp[0], lp[0]); lp[1] = cmul(lp[0], lp[0]);
                lp[0] = f32x2{1.f, 0.f};
                lp[2] = cmul(lp[1], lp[1]); lp[3] = cmul(lp[2], lp[1]); lp[4] = cmul(lp[2], lp[2]);
                const f32x2 z2 = {0.f, 0.f};
                f32x2 t4[4];
                cfma4(t4, lp[4], lp[1], lp[2], lp[3], lp[4], z2, z2, z2, z2);
                lp[5] = t4[0]; lp[6] = t4[1]; lp[7] = t4[2]; lp[8] = t4[3];
                cfma4(t4, lp[8], lp[1], lp[2], lp[3], lp[4], z2, z2, z2, z2);
                lp[9] = t4[0]; lp[10] = t4[1]; lp[11] = t4[2]; lp[12] = t4[3];
                cfma4(t4, lp[8], lp[5], lp[6], lp[7], lp[7], z2, z2, z2, z2);
                lp[13] = t4[0]; lp[14] = t4[1]; lp[15] = t4[2];
                f32x2* tl = (f32x2*)(smem + tcl);
                tl[0] = lam;
#pragma unroll
                for (int i = 0; i < 16; ++i) tl[1 + i] = lp[i];
                SCHED();
                asm volatile("kpf_end3:");
            }
            static_for<kQ1a>(LAMBDA(i_) { pre(IC(kQ0 + i_.value)); });
            undef4(kvt);
            asm volatile("" : "=v"(se[0]), "=v"(se[1]), "=v"(se[2]), "=v"(se[3]));
        }
        tag0 = __builtin_amdgcn_readlane(nonce, 0) + 1u; tag1 = __builtin_amdgcn_readlane(nonce + (tag0 == 0u ? 1u : 0u), 1);
        tagv = v2u{tag0, tag1}; pin(tagv);
        {
            asm volatile("s_waitcnt vmcnt(%5)" : "+v"(sa[0]), "+v"(sa[1]), "+v"(sa[2]), "+v"(sa[3]), "+v"(sa[4]) : "i"(8 + kQ0 + kQ1a) : "memory");
            float s1[2] = {0.f, 0.f}, s2[2] = {0.f, 0.f};
#pragma unroll
            for (int i = 0; i < 5; ++i)
#pragma unroll
                for (int e = 0; e < 4; ++e) { s1[e & 1] = dot2a(sa[i][e], one2, s1[e & 1]); s2[e & 1] = dot2a(sa[i][e], sa[i][e], s2[e & 1]); }
            const float a1 = rowsum16(s1[0] + s1[1]), a2 = rowsum16(s2[0] + s2[1]);
            const float mean = a1 * (1.0f / D_);
            const float var = fmaxf(a2 * (1.0f / D_) - mean * mean, 0.f);
            const float ri = __builtin_amdgcn_rcpf(rbf(__builtin_amdgcn_sqrtf(rbf(rbf(var) + 1e-5f))));
            v2u sv = {__float_as_uint(w >= 2 ? a1 : c ? -ri * rbf(mean) : ri), tag0};
            int o0 = w < 2 ? (mt1 * 16 + nt1) * 256 + 32 * w : ((mt1 >> 2) * 16 + nt1) * 256 + 112 + (mt1 & 3) * 16 + 8 * w, o1 = w < 2 ? 8 : 16384, o2 = w < 2 ? 64 : 0;
            pins(o0); pins(o1); pins(o2);
            unsigned so_ = c < 2 ? OFF_ST + (unsigned)(o0 + q * o1 + c * o2) : kNone;
            pin(sv); pin(so_);
            st64(sv, rs, so_);
            SCHED();
        }
        bool kw2 = kw;
        if (kw2) {
            static_for<117 - KST>(LAMBDA(n_) { kvs(IC(KST + n_.value)); });
        } else {
            if (__builtin_expect(w == 3, !W3O)) {
            const int l = lane, l3 = l & 3;
            unsigned a = OFF_SG + (unsigned)((((((l >> 3) & 1) * 32 + mt2) * 4 + ((l >> 1) & 3)) * 8 + nt2) * 256 + (l & 1) * 128);
            { unsigned t_ = OFF_EG + (unsigned)((l >= 21 ? 163840 - 640 : 0) + (mt1 * 16 + nt1) * 640 + (l < 26 ? l - 16 : 5) * 128); pin(t_); a = l >= 16 ? t_ : a; }
            { unsigned t_ = OFF_PS + (unsigned)(((mt2 * 4 + l3) * 8 + nt2) * 128); pin(t_); a = l >= 28 ? t_ : a; }
            { unsigned t_ = OFF_FH + (unsigned)((((((l >> 2) & 1) * 32 + 2 * mt1 + (l3 >> 1)) * 4 + (nt1 >> 2)) * 8 + (nt1 & 3) * 2 + (l3 & 1)) * 128); pin(t_); a = l >= 32 ? t_ : a; }
            { unsigned t_ = OFF_FX + (unsigned)(((mt1 * 4 + 2 * (mt2 & 1) + (l3 >> 1)) * 16 + nt2 * 2 + (l3 & 1)) * 128); pin(t_); a = l >= 40 ? t_ : a; }
            { unsigned t_ = OFF_FG + (unsigned)((nt2 * 96 + i32 + 16 * ((l3 + 1) & 6)) * 128); pin(t_); a = l >= 48 ? t_ : a; }
            const unsigned big = (l & 7) < 2 ? OFF_HF + (unsigned)((l & 1) * 5242880 + (8 * mt1 * 160 + 10 * nt1) * 256) : (l & 7) == 2 ? OFF_X2F + (unsigned)(2 * mt2 * 40960 + 2560 * nt2)
                               : (l & 7) < 5 ? OFF_KVT + (unsigned)((l & 1) * 640 * 4608 + (80 * nt2 + i32) * GSTR) : OFF_W2F + (unsigned)(nt2 * 204800 + ((l & 7) - 5) * 98304);
            a = l >= 56 ? big : a;
            tch = a;
            pin(tch);
            asm volatile("kpf_end2:");
        }
            static_for<kQ1 - kQ1a>(LAMBDA(i_) { pre(IC(kQ0 + kQ1a + i_.value)); });
            if (w == TW) tour<1>();
            skip<SKIP0>();
            undef4(kvt);
        }
        SCHED();
        STAMP(34)
        if (APOS == 1) addrs(std::integral_constant<int, 0>{}, lane, w, b);
        v4u orr, oi;
        unsigned ie[4][4], io[4][4], iw;
        auto img = LAMBDA(n_) {
            constexpr int i = n_.value / 10, u = n_.value % 10;
            if constexpr (u == 0) { if constexpr (i > 0) ror1(iw, wr[i - 1].w); else { iw = 0u; pin(iw); } }
            else if constexpr (u < 5) rot1(ie[i][u - 1], wr[i][4 - u]);
            else if constexpr (u == 5) shr1b(iw, wr[i].w);
            else if constexpr (u < 9) mix1(io[i][u - 6], wr[i][9 - u], wr[i][8 - u]);
            else mix1(io[i][3], wr[i].x, iw);
        };
        auto tap0 = [&]() __attribute__((always_inline)) {
            wr[0].x = cvtpk(lo16f(wr[0].x) + (lane ? 0.f : lo16f(sdq)), hi16f(wr[0].x));
            pin(wr[0]);
            SCHED();
        };
        {
            static_assert(kQ0 + kQ1 <= 18, "");
            vm_wait<6 + kQ0 + kQ1>();
            cr[0] = *(const v4u*)(smem + ear); cr[1] = *(const v4u*)(smem + ear + 1024);
            static_for<5>(LAMBDA(i) { lds2a<22 + 2 * i.value, 2560 + 32 * i.value>(lds0 + (unsigned)(L_EAR + w * REGSZ + q * 8)); });
            asm volatile("s_waitcnt lgkmcnt(0)" : "+v"(cr[0]), "+v"(cr[1])::"memory");
            static_for<kQ2>(LAMBDA(i_) { pre(IC(kQ0 + kQ1 + i_.value)); });
            static_for<4>(LAMBDA(e_) {
                constexpr int e = e_.value;
                const f32x4 co = *(const f32x4*)(smem + coa + e * 16);
                const f32x2 o0 = lo16f(cr[0][e]) * f32x2{co.x, -co.y} - lo16f(cr[1][e]) * f32x2{co.y, co.x};
                const f32x2 o1 = hi16f(cr[0][e]) * f32x2{co.z, -co.w} - hi16f(cr[1][e]) * f32x2{co.w, co.z};
                orr[e] = cvtpk(o0.x, o1.x); oi[e] = cvtpk(o0.y, o1.y);
            });
            pin(orr); pin(oi);
            SCHED();
            static_for<NPD - kQ0 - kQ1 - kQ2>(LAMBDA(i_) { pre(IC(kQ0 + kQ1 + kQ2 + i_.value)); });
            for (int i = 0; i < 4; ++i) st128(kvt[i], rs, kvo + i * 2 * KSTR);
            st128(orr, rs, for0); st128(oi, rs, for1);
        }
        SCHED();
        STAMP(23)
        if (w == 3) tour<7>();
        asm volatile("s_waitcnt vmcnt(%5) lgkmcnt(0)" : "+v"(wr[0]), "+v"(wr[1]), "+v"(wr[2]), "+v"(wr[3]), "+v"(sdq) : "i"(P1_PRE - 1 - p1_pos(3)) : "memory");
        __builtin_amdgcn_s_barrier();
        asm volatile("" ::: "memory");
        STAMP(24)
        site<10>();
        v4u wf[2][5], af[2][2][2];
#pragma unroll
        for (int nb = 0; nb < 5; ++nb) wf[0][nb] = WF(nb, 0);
#pragma unroll
        for (int i = 0; i < 4; ++i) af[0][i >> 1][i & 1] = *(const v4u*)(smem + ((i >> 1) ? xlane1 : xlane0) + (i & 1) * 2048);
        SCHED();
        tap0();
        site<11>();
        kvtb = p.ws + OFF_KVT + (chn * KPAD + (chn >> 3) * KPS);
        pins(kvtb);
#pragma unroll
        for (int i = 0; i < (NW2P + 4) / 5; ++i) { w2b0[i] = p.ws + 2048 + i * 5120; pins(w2b0[i]); }
        v2u sg, tc[2], fk, fw;
        v4u sw0;
        v2u zq[2];
        float rr_[8];
        unsigned sx[4], sz[4];
        u64 odd = 0xAAAAAAAAAAAAAAAAull;
        pins(odd);
        auto filler = LAMBDA(f_) {
            constexpr int f = f_.value;
            if constexpr (f < 0) {}
            else if constexpr (f < 40) img(IC(f));
            else if constexpr (f == 48) { lam1.r = __uint_as_float(tc[0].x); xor1(lam1.s, tc[0].y, sgn); }
            else if constexpr (f == 49) { lamq.r = __uint_as_float(tc[1].x); xor1(lamq.s, tc[1].y, sgn); }
            else if constexpr (f < 52) zero2(zq[f - 50]);
            else if constexpr (f == 52) { sx[0] = sw0.x; mov1(sx[1], sx[0]); }
            else if constexpr (f == 53) { sz[0] = sw0.z; mov1(sz[1], sz[0]); }
            else if constexpr (f < 56) bc1<f - 54>(rr_[f - 54], sg.x);
            else if constexpr (f == 56) sw16(sx[0], sx[1]);
            else if constexpr (f == 57) sw16(sz[0], sz[1]);
            else if constexpr (f < 60) mov1(sx[f - 56], sx[f - 58]);
            else if constexpr (f < 62) mov1(sz[f - 58], sz[f - 60]);
            else if constexpr (f < 64) sw32(sx[f - 62], sx[f - 60]);
            else if constexpr (f < 66) sw32(sz[f - 64], sz[f - 62]);
            else if constexpr (f < 72) {
                bc1<f - 64>(rr_[f - 64], sg.x);
                if constexpr (f == 66) { SW[0] = __uint_as_float(sx[0]); SW[1] = __uint_as_float(sz[0]); SW[2] = __uint_as_float(sx[1]); SW[3] = __uint_as_float(sz[1]); }
                if constexpr (f == 71) sel1(SW[4], sx[2], sz[2], odd);
            }
            else mulb<8 + (f - 72) % 8>(tt[(f - 72) % 8 / 4][(f - 72) / 8][(f - 72) % 4], sg.x, SW[(f - 72) / 8]);
        };
        auto kstep = LAMBDA(s_) {
            constexpr int s = s_.value, ph = s >> 2, s4 = s & 3, cu = s & 1, nx = cu ^ 1, jj = s >> 1, pp = jj & 1;
            if constexpr (s4 == 3 && ph < 4) {
                constexpr int pw = p1_pos(104 + 5 * (ph + 1)), px = p1_pos(4 * (jj + 1) + 3), pm = pw > px ? pw : px;
                constexpr int vm = p1_lim(p1_cnt(s, 0) - 1 - pm);
                asm volatile("" ::: "memory");
                __builtin_amdgcn_s_waitcnt((vm & 15) | ((vm >> 4) << 14) | 0x70);
                __builtin_amdgcn_s_barrier();
                asm volatile("" ::: "memory");
                STAMP(25 + ph)
                SCHED();
            }
            if constexpr (s == KSC) {
                static_assert(s4 == 3 && p1_pos(203) < p1_pos(4 * (jj + 1) + 3), "");
                pin(sg); pin(sw0);
                if (__builtin_expect(__builtin_amdgcn_ballot_w64(__builtin_amdgcn_bitop3_b32(__builtin_amdgcn_bitop3_b32(sg.y, sw0.y, tag0, 0x7E), sw0.w, tag0, 0xF6) != 0u) != 0ull, 0))
                    WAVE_WAIT({ sg = ld64(rs, sto); sw0 = ld128(rs, swo); ok = ((sg.y ^ tag0) | (sw0.y ^ tag0) | (sw0.w ^ tag0)) == 0u; })
                ORDER();
                STAMP(7)
            }
            if constexpr (s == kW2S && KUS > kW2S) { vm_wait<p1_cnt(s, 0) - 1 - p1_pos(208)>(); SCHED(); }
            if constexpr (s == KUS) {
                static_assert(p1_pos(206) > p1_pos(208), "");
                wait_fkn<p1_cnt(s, 0) - 1 - p1_pos(206)>(fk, fw);
                if (!USP) ld_ur(wro, kvtb);
                SCHED();
            }
            if constexpr (s4 != 3 || ph == 4) lgkm<(s == 0 ? 0 : p1_nl(s - 1, 5 - cu, 10) - 1)>();
            SCHED();
            static_for<10>(LAMBDA(g_) {
                constexpr int g = g_.value, mb = g / 5, k = g % 5, it = p1_item(s, g), nt_ = g < 9 ? p1_item(s, g + 1) : -1;
                constexpr bool d0 = it >= 0 && it < 200, d1 = nt_ >= 0 && nt_ < 200;
                if constexpr (s == 0) mf0(acc[mb][k], af[pp][cu][mb], wf[cu][k]); else mf(acc[mb][k], af[pp][cu][mb], wf[cu][k]);
                SCHED();
                if constexpr (s < 19) {
                    if constexpr (g < 5) wf[nx][g] = WF(g, s + 1);
                    if constexpr (cu == 1 && g == 5) vm_wait<p1_lim(p1_cnt(s, 5) - 1 - p1_pos(4 * (jj + 1) + 3))>();
                    if constexpr (cu == 1 && g >= 5 && g < 9)
                        af[pp ^ 1][(g - 5) >> 1][(g - 5) & 1] = *(const v4u*)(smem + (((g - 5) >> 1) ? xlane1 : xlane0) + ((jj + 1) % 3) * 4096 + ((g - 5) & 1) * 2048);
                }
                if constexpr (cu == 0 && g == XG0 - 1 && s > 0) lgkm<p1_nl(s - 1, 8, 10) + p1_nl(s, 0, XG0 - 1)>();
                constexpr int lq = p1_lds(s, g);
                if constexpr (lq >= 4 && lq < 8) (lq == 4 ? lam1.a : lq == 5 ? lam1.b : lq == 6 ? lamq.a : lamq.b) = *(const f32x2*)(smem + (lq < 6 ? tca : tcb) + (lq & 1) * 2176);
                else if constexpr (lq == 8 || lq == 9) tc[lq - 8] = *(const v2u*)(smem + (lq == 8 ? tcc : tcd));
                else if constexpr (lq == 10) { const f32x2* tp = (const f32x2*)(smem + tcl); l8 = tp[2]; l32 = tp[5]; }
                else if constexpr (lq == 11) l128 = *(const f32x2*)(smem + tcl + 72);
                else if constexpr (lq >= 30 && lq < 33) *(v4u*)(smem + (lq < 32 ? ime + 1024 * (lq - 30) : im3)) = v4u{ie[33 - lq][0], ie[33 - lq][1], ie[33 - lq][2], ie[33 - lq][3]};
                else if constexpr (lq >= 33 && lq < 37) *(v4u*)(smem + imo + 1024 * (36 - lq)) = v4u{io[lq - 33][0], io[lq - 33][1], io[lq - 33][2], io[lq - 33][3]};
                else if constexpr (lq == 37) *(v4u*)(smem + im3 + 1024) = v4u{ie[0][0], ie[0][1], ie[0][2], ie[0][3]};
                else if constexpr (lq == 38) *(unsigned*)(smem + rsa) = sg.x;
                else if constexpr (lq == 39) *(v4u*)(smem + zpa) = v4u{zq[0].x, zq[0].y, zq[1].x, zq[1].y};
                static_for<p1_cap(s, g)>(LAMBDA(e_) { filler(IC(p1_fill(s, g, e_.value))); });
                if constexpr (s == 18 && g == 9) ne2(late, tagv, fk, fw);
                if constexpr (s == 19 && g == 2) { if (__builtin_expect(late != 0ull, 0)) poll2(fko, fwo, p.ws); }
                if constexpr (it >= 0) {
                    if constexpr (d0 && d1) DMA_LD1(it, d1 ? nt_ : 0);
                    else if constexpr (d0) DMA_LD(it);
                    else if constexpr (it == 202) gld2s<0>(sg, sto, p.ws);
                    else if constexpr (it == 203) gld4s<0>(sw0, swo, p.ws);
                    else if constexpr (it == 206) gld2s<0>(fk, fko, p.ws);
                    else if constexpr (it == 208) gld2s<0>(fw, fwo, p.ws);
                    else if constexpr (it == 209) touch_(tch, p.ws);
                    else if constexpr (it >= 210 && it < 214) ld_ur1<it - 210>(wro, kvtb);
                    else if constexpr (it >= 500) gld_w2<it % 50>(w2o, w2b0[(it % 50) / 5]);
                    else { vm_wait<p1_cnt(s, g) - P1_PRE>(); st64(tagv, rs, fko1); }
                }
                if constexpr (d1 && !d0) DMA_M0(d1 ? nt_ : 0);
                site<2000 + 10 * s + g>();
                SCHED();
            });
        };
        static_for<20>(kstep);
        static_for<NFILL - p1_fill(19, 10, 0)>(LAMBDA(i_) { filler(IC(p1_fill(19, 10, 0) + i_.value)); });
        SCHED();
        rr[0] = f32x4{rr_[0], rr_[1], rr_[2], rr_[3]}; rr[1] = f32x4{rr_[4], rr_[5], rr_[6], rr_[7]};
        ACC_FENCE10(acc[0], acc[1]);
        STAMP(1)
    }

    auto layer_body = LAMBDA(layer_) {
        constexpr int layer = layer_.value;
        const int sga = sgn_;
        const bool cv = layer == 0 && cv0;
        v2u fh;
        unsigned ks[10];
        f32x4 ca[8], a2[4][5];
        unsigned gp[4][4];
        v4u gq[4];
        auto gst = LAMBDA(i_) {
            constexpr int i = i_.value >> 2, u = i_.value & 3;
            if constexpr (u < 2) cp2(gp[i][2 * u], gp[i][2 * u + 1], ca[2 * i + u]);
            else if constexpr (u == 2) swp2n(gp[i][0], gp[i][2], gp[i][1], gp[i][3]);
            else if constexpr (GSM) { gq[i] = v4u{gp[i][0], gp[i][1], gp[i][2], gp[i][3]}; pin(gq[i]); }
            else st128(v4u{gp[i][0], gp[i][1], gp[i][2], gp[i][3]}, rs, gso + i * 1024);
        };
        float rsr, rsm;
        St bv[8];
        {
            St a0, a1;
            auto w2s = LAMBDA(n_) {
                constexpr int lo = KSA * n_.value / 4, hi = KSA * (n_.value + 1) / 4;
                if constexpr (layer == 0) { static_for<hi - lo>(LAMBDA(e_) { constexpr int i = NW2L + lo + e_.value; gld_w2<i>(w2o, w2b0[i / 5]); }); SCHED(); }
            };
            static_for<2>(LAMBDA(m_) {
                constexpr int mb = m_.value;
#pragma unroll
                for (int i = 0; i < 4; ++i) fma2p(bv[4 * mb + i].a, acc[mb][0][i], acc[mb][1][i], rr[mb][i], tt[mb][0][i], tt[mb][1][i]);
#pragma unroll
                for (int i = 0; i < 4; ++i) fma2p(bv[4 * mb + i].b, acc[mb][2][i], acc[mb][3][i], rr[mb][i], tt[mb][2][i], tt[mb][3][i]);
                const f32x4 v = __builtin_elementwise_fma(acc[mb][4], rr[mb], f32x4{tt[mb][4][0], tt[mb][4][1], tt[mb][4][2], tt[mb][4][3]});
#pragma unroll
                for (int i = 0; i < 4; ++i) bv[4 * mb + i].c = v[i];
                SCHED();
                w2s(IC(2 * mb));
                if constexpr (mb == 0) run3(a0, bv[0], bv + 1, lam1); else run4(a1, a0, bv + 4, lam1);
                SCHED();
                if constexpr (mb == 0) w2s(IC(1));
            });
            *(f32x2*)(smem + sga) = a1.a; *(f32x2*)(smem + sga + 256) = a1.b; *(float*)(smem + sg4 + 512) = a1.c;
            SCHED();
            w2s(IC(3));
        }
        SCHED();
        const int w_ = w0;
        {
        const int w = w_, mt1 = mt1_;
        if (layer == 0 && __builtin_expect(late != 0ull, 0)) {
            unsigned f0_, f1_, f2_, f3_;
            asm volatile("s_waitcnt vmcnt(0)\n\t" AR("%0", 36, 0) AR("%1", 36, 1) AR("%2", 36, 2) AR("%3", 36, 3) : "=v"(f0_), "=v"(f1_), "=v"(f2_), "=v"(f3_)::"memory");
            late = __builtin_amdgcn_ballot_w64(((f0_ ^ tag0) | (f1_ ^ tag1) | (f2_ ^ tag0) | (f3_ ^ tag1)) != 0u);
            ORDER();
            if (late == 0ull) KW_REQ()
        }
        asm volatile("s_waitcnt lgkmcnt(0)" ::: "memory");
        __builtin_amdgcn_s_barrier();
        asm volatile("" ::: "memory");
        STAMP(layer ? 14 : 2)
#pragma unroll
        for (int s = 0; s < 10; ++s) ks[s] = (unsigned)s * 1024u;
        constexpr int NW2A = NW2P + 7 * kCR1 + kXA, NW3 = NW2P + kN3E + kN3;
        auto w2ld = LAMBDA(i_) {
            constexpr int i = i_.value;
            gld_w2<i>(w2o, p.ws + 2048 + ks[i / 5] * 5u);
        };
        auto w2a = LAMBDA(i_) {
            constexpr int k = i_.value;
            if constexpr (k < kXA) { w2ld(std::integral_constant<int, NW2P + 2 * k>{}); w2ld(std::integral_constant<int, NW2P + 2 * k + 1>{}); }
            else w2ld(std::integral_constant<int, NW2P + kXA + k>{});
        };
        unsigned xtb = (unsigned)(size_t)(LAS char*)smem + (unsigned)(l_tb(0, 0) + w * 5120);
        auto w2x = LAMBDA(i_) {
            constexpr int i = NW2A + i_.value;
            if constexpr (i < 50) w2ld(std::integral_constant<int, i>{});
            else if constexpr (XDM) { if constexpr (i < 53) dma_x<(i - 50) * 1024, (i < 52 ? (i - 50) * 64 : 96)>(xso, p.x0, xtb); }
            else if constexpr (i < 52) lda4<40 + 4 * (i - 50), (i - 50) * 64>(xso, p.x0);
            else if constexpr (i == 52) lda4<48, 128>(xso - 2u * (unsigned)(bb & 16), p.x0);
        };
        auto w2b = LAMBDA(i_) {
            constexpr int k = i_.value - 7 * kCR1;
            constexpr int ne = KW2E < 53 - NW2A ? KW2E : 53 - NW2A;
            if constexpr (k < ne) w2x(std::integral_constant<int, k>{});
            constexpr int k1 = k - kFHK - 1;
            if constexpr (k1 >= 0 && k1 < KW2T) { w2x(std::integral_constant<int, ne + 2 * k1>{}); w2x(std::integral_constant<int, ne + 2 * k1 + 1>{}); }
            else if constexpr (k1 >= 0) w2x(std::integral_constant<int, ne + KW2T + k1>{});
            static_assert(ne <= kFHK + 1 && 53 - NW2A - ne - KW2T + kFHK < 7 * (8 - kCR1) - kFHP + 1, "");
            if constexpr (k == kFHK) { vm_wait<ne>(); st64(tagv, rs, fho1); STAMP(39) }
            if constexpr (k == kFHK - 1) { STAMP(38) }
            if constexpr (k == 7 * (8 - kCR1) - kFHP) poll1(fho, p.ws);
            static_assert(NW2A + 7 * (8 - kCR1) >= 53 && NW2A <= 50 && NW3 <= NW2A, "");
        };
        auto partb = [&]() __attribute__((always_inline)) {
            const float* sp = (const float*)(smem + spa);
            rsr = sp[0]; rsm = sp[8];
            if (layer == 1) { pin(rsr); pin(rsm); }
            if (layer == 1) {
                static_for<5>(LAMBDA(n_) {
                    constexpr int nb = n_.value;
                    unsigned d0, d1;
                    rda<22 + 2 * nb>(d0); rda<23 + 2 * nb>(d1);
                    a2[3][nb] = xin[nb] + (xin[nb] * rsr + rsm) * unpk4(v2u{d0, d1}); pin(a2[3][nb]);
                });
            }
        };
        constexpr int LBW = layer ? 2 : 3;
        if (w == 3) {
            site<3 + 10 * layer>(); SCHED();
            f32x4 s4[8];
#pragma unroll
            for (int i = 0; i < 8; ++i) { s4[i] = *(const f32x4*)(smem + spl + (l_sg(layer, i & 3) - SC + (i >> 2) * 640)); SCHED(); }
            f32x2 sv[16], x[4], P[4][4];
#pragma unroll
            for (int i = 0; i < 16; ++i) { const f32x4 t = s4[(i >> 2) + 2 * (i & 2)]; sv[i] = (i & 1) ? f32x2{t.z, t.w} : f32x2{t.x, t.y}; }
            unsigned er, ei;
            {
                f32x2 u[4];
                cfma4n(u, l8, sv[0], sv[4], sv[8], sv[12], sv[1], sv[5], sv[9], sv[13]);
                agg(er, ei, x, P[0][2], u, sv, l8, l32, l128);
            }
            st128(v4u{er, tag0, ei, tag0}, rs, es);
            SCHED();
            P[0][0] = f32x2{0.f, 0.f}; P[0][1] = x[0]; P[0][3] = cfma(l32, P[0][2], x[2]);
            if (layer == 0) { FW_OK() static_for<kN3E>(LAMBDA(i_) { w2ld(std::integral_constant<int, NW2P + i_.value>{}); }); }
#pragma unroll
            for (int kk = 1; kk < 4; ++kk) cfma4(P[kk], l8, P[kk - 1][0], P[kk - 1][1], P[kk - 1][2], P[kk - 1][3], sv[kk - 1], sv[kk + 3], sv[kk + 7], sv[kk + 11]);
#pragma unroll
            for (int i = 0; i < 8; ++i) { const f32x2 a = P[2 * (i >> 2)][i & 3], b_ = P[2 * (i >> 2) + 1][i & 3]; *(f32x4*)(smem + spl + (l_sg(layer, i & 3) - SC + (i >> 2) * 640)) = f32x4{a.x, a.y, b_.x, b_.y}; }
            partb();
        }
        int w1_ = w, w3_ = w;
        if (layer == 1) { pins(w1_); pins(w3_); }
        if (layer == 1 && w1_ < LBW) { site<4 + 10 * layer>(); SCHED(); partb(); if constexpr (layer == 1) { if (w == 0) tour<6>(); if constexpr (LBR == 1) room<3000>(); } }
        if (w3_ == LBW) {
            site<5 + 10 * layer>(); SCHED();
            f32x2 wg[17], t4[4];
            unsigned ea[15], tg[15];
            v4u eg[15];
            constexpr bool early = (LBE >> layer) & 1;
            const f32x2 z2 = {0.f, 0.f};
#pragma unroll
            for (int i = 0; i < 15; ++i) { ea[i] = (unsigned)(mt1 - 1 - i) * 10240u; pins(ea[i]); ea[i] = ea[i] < kNone ? ea[i] : kNone; pins(ea[i]); }
            if constexpr (early) {
                __builtin_amdgcn_s_sleep(layer ? KLB1 : KLB0);
#pragma unroll
                for (int i = 0; i < 15; ++i) eg[i] = ld128s(rs, eo, ea[i]);
                SCHED();
            }
            wg[1] = cmul(l128, l128); wg[2] = cmul(wg[1], wg[1]); wg[3] = cmul(wg[2], wg[1]); wg[4] = cmul(wg[2], wg[2]);
#pragma unroll
            for (int i = 4; i < 16; i += 4) {
                cfma4(t4, wg[i], wg[1], wg[2], wg[3], wg[4], z2, z2, z2, z2);
#pragma unroll
                for (int e = 0; e < 4; ++e) wg[i + 1 + e] = t4[e];
            }
#pragma unroll
            for (int i = 0; i < 15; ++i) { if (i) pin(wg[i]); tg[i] = mt1 - 1 - i < 0 ? 0u : tag0; pins(tg[i]); }
            if (layer == 1) partb();
            if constexpr (layer == 0) tour<3>();
            f32x2 H0, H1;
            unsigned spins = dead ? kSpinCap : 0u;
            if constexpr (layer == 1 && LBR == 0) { room<1800>(); site<16>(); }
            if constexpr (!early) __builtin_amdgcn_s_sleep(layer ? KLB1 : KLB0);
            SCHED();
            auto eval = [&](const v4u (&e)[15]) __attribute__((always_inline)) {
                unsigned x = __builtin_amdgcn_bitop3_b32(e[0].y, e[0].w, tg[0], 0x7E);
                H0 = f32x2{__uint_as_float(e[0].x), __uint_as_float(e[0].z)}; H1 = f32x2{0.f, 0.f};
#pragma unroll
                for (int i = 1; i < 15; i += 2) {
                    x |= __builtin_amdgcn_bitop3_b32(e[i].y, e[i].w, tg[i], 0x7E) | __builtin_amdgcn_bitop3_b32(e[i + 1].y, e[i + 1].w, tg[i + 1], 0x7E);
                    pin(x);
                    gacc2(H0, H1, wg[i], wg[i + 1], e[i], e[i + 1]);
                    SCHED();
                }
                *(f32x2*)(smem + spl + (L_CW - SC)) = H0 + H1;
                return __builtin_amdgcn_ballot_w64(x != 0u) != 0ull;
            };
            bool again = true;
            if constexpr (early) again = eval(eg);
            if (again) {
                bool miss;
#pragma nounroll
                do {
                    asm volatile("" ::: "memory");
                    v4u e2[15];
#pragma unroll
                    for (int i = 0; i < 15; ++i) e2[i] = ld128s(rs, eo, ea[i]);
                    SCHED();
                    miss = eval(e2);
                    if (__builtin_expect(miss, 0) && ++spins > kSpinCap) { dead = true; miss = false; }
                } while (miss);
            }
            ORDER();
            STAMP(layer ? 36 : 37)
            if (layer == 0) {
                static_for<NW3 - NW2P - kN3E>(LAMBDA(i_) { w2ld(std::integral_constant<int, NW2P + kN3E + i_.value>{}); });
            }
        }
        int w2_ = w;
        pins(w2_);
        if constexpr (layer == 0) site<21>();
        if (layer == 0) UNDEF8(ca);
        if (layer == 0 && w2_ != LBW) {
            if (cv) {
                site<2>(); SCHED();
                partb();
                {
                    FW_OK()
                    st_ur((unsigned)(size_t)(LAS char*)smem + (unsigned)uwa);
                    CV_LOAD_A(cA, 0, 0, 0) CV_LOAD_A(cA, 0, 0, 1)
                    SCHED();
                    static_for<8>(LAMBDA(a_) { constexpr int a = a_.value; CV_LOAD_B(cB, a, 0) SCHED(); });
                    CV_LOAD_A(cA, 1, 0, 0) CV_LOAD_A(cA, 1, 0, 1)
                    SCHED();
                }
                STAMP(32)
                conv_part<0, kCR1, CPF>(ca, cA, cB, smem, ab, bb, w2a, LAMBDA(i_) {});
            } else {
                if constexpr (layer == 0) tour<5>();
                FW_OK()
                static_for<NW2A - NW2P>(LAMBDA(i_) { w2ld(std::integral_constant<int, NW2P + i_.value>{}); __builtin_amdgcn_s_sleep(1); });
                partb();
                if constexpr (layer == 0) room<3900>();
            }
        }
        asm volatile("s_waitcnt lgkmcnt(0)" ::: "memory");
        __builtin_amdgcn_s_barrier();
        asm volatile("" ::: "memory");
        STAMP(layer ? 15 : 4)
        {
            St cw, ex, hv[8];
            cw.a = *(const f32x2*)(smem + cwa); cw.b = *(const f32x2*)(smem + cwa + 256); cw.c = *(const float*)(smem + cw4 + 512);
            ex.a = *(const f32x2*)(smem + sga); ex.b = *(const f32x2*)(smem + sga + 256); ex.c = *(const float*)(smem + sg4 + 512);
            keep14(hv, cw, ex, bv, lamq, lam1);
            keep4(hv + 4, hv[3], bv + 4, lam1);
            SCHED();
            v2u ta[5], td[5];
            f32x2 hb[9];
            static_for<5 + SBL>(LAMBDA(r_) {
                constexpr int r = r_.value, rd = r < 2 ? r : r == 2 ? 4 : r - 1, ro_ = r - SBL < 2 ? r - SBL : r - SBL == 2 ? 4 : r - SBL - 1;
                if constexpr (r < 5) {
                    float e[8];
                    unsigned o[4];
#pragma unroll
                    for (int i = 0; i < 8; ++i) e[i] = rd == 4 ? hv[i].c : rd < 2 ? hv[i].a[rd & 1] : hb[i + 1][rd & 1];
                    if constexpr (r == 0) b3cv(hb, o, cw.b, ex.b, bv[0].b, bv[1].b, lamq.b, lam1.b, e);
                    else if constexpr (r < 3) b3cv(hb + 3 * r, o, hb[3 * r - 1], bv[3 * r - 1].b, bv[3 * r].b, bv[3 * r + 1].b, lam1.b, lam1.b, e);
                    else for (int i = 0; i < 4; ++i) o[i] = cvtpk(e[2 * i], e[2 * i + 1]);
                    *(v4u*)(smem + tbw + rd * 1024) = v4u{o[0], o[1], o[2], o[3]};
                    SCHED();
                    if constexpr (r < 3) { bfin(hb[3 * r + 2], hb[3 * r + 1], lam1.b); SCHED(); }
                    ta[rd] = __builtin_bit_cast(v2u, trread(smem, (rd < 4 ? tbp : tbr) + rd * 1024));
                    td[rd] = __builtin_bit_cast(v2u, trread(smem, (rd < 4 ? tbq + 256 : tbr + 512) + rd * 1024));
                    SCHED();
                }
                if constexpr (r >= SBL) { st128(v4u{ta[ro_].x, ta[ro_].y, td[ro_].x, td[ro_].y}, rs, hso + ro_ * 256); SCHED(); }
            });
        }
#pragma unroll
        for (int s = 0; s < 10; ++s) pins(ks[s]);
        if (layer == 1) { DRAIN(); st64(tagv, rs, fho1); }
        STAMP(layer ? 16 : 5)
        if (cv) {
            if constexpr (layer == 0) site<6>();
            SCHED();
            if constexpr (!CPF) {
                CV_LOAD_A(cA, 0, kCR1, 0) CV_LOAD_A(cA, 0, kCR1, 1)
                SCHED();
                static_for<8>(LAMBDA(a_) { constexpr int a = a_.value; CV_LOAD_B(cB, a, kCR1) SCHED(); });
                static_for<7>(LAMBDA(a_) { constexpr int a = a_.value + 1; CV_LOAD_A(cA, a, kCR1, 0) CV_LOAD_A(cA, a, kCR1, 1) });
                SCHED();
            }
            conv_part<kCR1, 8, false>(ca, cA, cB, smem, ab, bb, w2b, LAMBDA(g_) {
                constexpr int g = g_.value;
                if constexpr (GIN > 0 && (g == 14 || g == 16 || g == 18 || g == 20)) gst(std::integral_constant<int, (g - 14) / 2>{});
                if constexpr (GIN > 1 && (g == 26 || g == 28 || g == 30 || g == 32)) gst(std::integral_constant<int, 4 + (g - 26) / 2>{});
            });
            asm volatile("" : "+v"(ca[0]), "+v"(ca[1]), "+v"(ca[2]), "+v"(ca[3]), "+v"(ca[4]), "+v"(ca[5]), "+v"(ca[6]), "+v"(ca[7]));
            static_for<(GSM ? 0 : 16 - 4 * GIN)>(LAMBDA(i_) {
                constexpr int n = 4 * GIN + i_.value;
                if constexpr (n == 9 && GIN == 2) asm volatile("s_nop 0");
                if constexpr ((n & 3) == 2) asm volatile("s_nop 1");
                gst(std::integral_constant<int, n>{}); SCHED();
            });
        } else if (layer == 0) {
            constexpr int ne = KW2E < 53 - NW2A ? KW2E : 53 - NW2A;
            static_for<54 - NW2A>(LAMBDA(i_) {
                if constexpr (i_.value == ne) { vm_wait<ne>(); st64(tagv, rs, fho1); }
                if constexpr (i_.value < 53 - NW2A) { w2x(i_); __builtin_amdgcn_s_sleep(1); }
            });
            if (w == 3) { static_for<NW2A - NW3>(LAMBDA(i_) { w2ld(std::integral_constant<int, NW3 + i_.value>{}); __builtin_amdgcn_s_sleep(1); }); if constexpr (layer == 0) tour<2>(); }
            poll1(fho, p.ws);
            if constexpr (layer == 0) room<1000>();
            DRAIN();
            asm volatile("" : "=v"(gq[0]), "=v"(gq[1]));
        }
        if (layer == 0) { unsigned f0_, f1_; asm volatile("s_waitcnt vmcnt(%2)\n\t" AR("%0", 36, 0) AR("%1", 36, 1) : "=v"(f0_), "=v"(f1_) : "i"(GSM ? 0 : 4) : "memory"); fh = v2u{f0_, f1_}; }
        STAMP(layer ? 29 : 6)
        }

            v2u pt[4];
            if (layer == 0) {
                u64 hl;
                ne1(hl, tagv, fh);
                if (__builtin_expect(hl != 0ull, 0)) {
                    if constexpr (!GSM) { gld2t(fh, fho, p.ws); wait1(fh); st64(tagv, rs, fgo1); }
                    WAVE_RETRY(ISTAG(fh), fh = ld64(rs, fho))
                }
                ORDER();
            }
            else { room<3400>(); site<18>(); pipe_flag<KPH0>(pt, fho, p.ws, tagv, dead); ORDER(); }
            STAMP(layer ? 17 : 31)
            site<7 + 10 * layer>(); SCHED();
            constexpr int RING2 = layer ? KR2 : KR20;
            v4u ar[RING2];
#pragma unroll
            for (int i = 0; i < KI2; ++i) { ar[i] = ld128s(rs, ho[i / 10], ks[i % 10]); SCHED(); }
        if constexpr (layer == 0 && GSM) { static_assert(GIN == 2, ""); gst(IC(8)); gst(IC(9)); gst(IC(12)); gst(IC(13)); gst(IC(10)); gst(IC(14)); gst(IC(11)); gst(IC(15)); SCHED(); }
        int b_o = b0, w_o = w0;
        asm volatile("" : "+s"(b_o), "+s"(w_o));
        const int b = b_o, w = w_o;
        TILES()
        v4u gl[3];
        v2u f0;
        unsigned fgo, sgo, sgs, glo, gw, gr, srco;
        u64 glate = 0ull;
        const char *gb1 = p.ws + 32 * GSTR, *gb2 = p.ws + 48 * GSTR;
        {
            unsigned xs_[3][4], dq[2];
            float zx[4], zd[4], zt[4], zz[5][4];
            unsigned sA = SC + w * INBOX, s1 = OFF_SG + ((layer * 32 + mt2) * 4 + w) * 2048, s2 = s1 + nt2 * 256, sF = OFF_FG + (nt2 * 96 + 4 * w) * 128, sG = OFF_GT + (80 * nt2 + 4 * w) * GSTR + mt2 * 128,
                     sW = sA + 8192 + (w & 1) * 128, sR = SC + 8192 + w * 32, sX = l_tb(0, 0) + w * 5120, k160 = 160, kIn = INBOX;
            pins(sA); pins(s1); pins(s2);
            if (layer == 0) { pins(sF); pins(sG); pins(sW); pins(sR); pins(sX); pins(k160); pins(kIn); pins(gb1); pins(gb2); }
            unsigned l16, c16, q0, q1, c2, xr0, xr1, ta_, tb_;
#define A1(n_, v_, e_) else if constexpr (n == n_) { v_ = e_; pin(v_); }
            auto adr = LAMBDA(n_) {
                constexpr int n = n_.value + (layer && n_.value >= 4 ? 12 : 0);
                if constexpr (n == 0) { l16 = (unsigned)dst[0] & 1023u; pin(l16); }
                A1(1, c16, l16 & 0xf0u) A1(2, q0, (l16 >> 8) & 1u) A1(3, q1, l16 >> 9)
                A1(4, c2, (l16 >> 6) & 3u) A1(5, xr0, (c16 << 2) + sX) A1(6, ta_, q1 ^ c2) A1(7, xr0, (q0 << 3) + xr0) A1(8, xr0, (ta_ << 4) + xr0) A1(9, xr1, xr0 ^ 32u)
                else if constexpr (n >= 10 && n < 15) { constexpr int nb = n - 10; const v2u t = *(const v2u*)(smem + ((nb & 5) ? xr1 : xr0) + (nb >> 1) * 1024); xs_[nb >> 1][2 * (nb & 1)] = t.x; xs_[nb >> 1][2 * (nb & 1) + 1] = t.y; }
                A1(16, srco, l16 + sA) A1(17, ta_, l16 & 0x300u) A1(18, sgo, s1 + l16 + ta_) A1(19, sgs, (q0 << 3) + c16) A1(20, ta_, (q1 << 30) + s2) A1(21, sgs, sgs + ta_)
                else if constexpr (layer == 1) {}
                A1(22, ta_, l16 < 624u ? l16 : 624u) A1(23, tb_, ta_ & 0x380u) A1(24, ta_, ta_ & 0x60u) A1(25, fgo, (tb_ << 4) + sF) A1(26, fgo, (ta_ << 2) + fgo)
                A1(27, glo, q1 * (16u * GSTR) + sG) A1(28, ta_, l16 & 0x180u) A1(29, tb_, l16 & 0x70u) A1(30, glo, ta_ * (GSTR / 128u) + glo) A1(31, glo, glo + tb_)
                A1(32, ta_, l16 >> 7) A1(33, tb_, l16 & 0x30u) A1(34, gw, (tb_ << 1) + sW) A1(35, gw, __umul24(ta_, k160) + gw) A1(36, ta_, c2 & 1u) A1(37, gw, (ta_ << 4) + gw)
                A1(38, ta_, l16 >> 8) A1(39, gr, (q0 << 7) + sR) A1(40, gr, __umul24(ta_, kIn) + gr) A1(41, gr, __umul24(c2, k160) + gr) A1(42, tb_, tb_ >> 1) A1(43, gr, gr + tb_)
            };
            SCHED();
            if constexpr (STAMPS == 1) { STAMP(layer ? 50 : 40) }
            static_for<40>(LAMBDA(i_) {
                constexpr int i = i_.value, m = i / 10, s = i % 10;
                static_for<5>(LAMBDA(k_) {
                    constexpr int kk = k_.value, pv = ring_req(5 * i + kk, KI2, RING2, 0);
                    if constexpr (s == 0 && m < 3) mfa0<kk>(a2[m][kk], ar[i % RING2]); else mfa<5 * s + kk>(a2[m][kk], ar[i % RING2]);
                    SCHED();
                    if constexpr (i == 0 && kk == 0 && layer == 1) { pin(pt[0]); pin(pt[1]); pin(pt[2]); pin(pt[3]); }
                    if constexpr (g2_adr(i, kk) >= 0) adr(IC(g2_adr(i, kk)));
                    if constexpr (layer == 0 && GSM && kk == 1 && i >= GSI && i < GSI + 4) st128(gq[i - GSI], rs, gso + (i - GSI) * 1024);
                    if constexpr (layer == 0 && GSM && kk == 0 && i == GFI) { vm_wait<GFI - GSI - 4>(); st64(tagv, rs, fgo1); }
                    if constexpr (layer == 0 && i >= 1 && i < 28 && (kk & 1)) {
                        constexpr int n = 2 * (i - 1) + (kk >> 1) - 9, nb = n / 9, u = n % 9 - 1;
                        if constexpr (n < 0 && XDM) {}
                        else if constexpr (n < -3) { rda<40 + 2 * (n + 9)>(xs_[(n + 9) >> 1][(2 * (n + 9)) & 3]); rda<41 + 2 * (n + 9)>(xs_[(n + 9) >> 1][(2 * (n + 9) + 1) & 3]); }
                        else if constexpr (n < 0) swp2n(xs_[n + 3][0], xs_[n + 3][2], xs_[n + 3][1], xs_[n + 3][3]);
                        else if constexpr (u < 0) { rda<22 + 2 * nb>(dq[0]); rda<23 + 2 * nb>(dq[1]); }
                        else if constexpr (u < 2) unp2(zx[2 * u], zx[2 * u + 1], xs_[nb >> 1][2 * (nb & 1) + u]);
                        else if constexpr (u < 4) unp2(zd[2 * u - 4], zd[2 * u - 3], dq[u - 2]);
                        else if constexpr (u < 6) fma2(zt[2 * u - 8], zt[2 * u - 7], zx[2 * u - 8], zx[2 * u - 7], rsr, rsr, rsm, rsm);
                        else fma2(zz[nb][2 * u - 12], zz[nb][2 * u - 11], zt[2 * u - 12], zt[2 * u - 11], zd[2 * u - 12], zd[2 * u - 11], zx[2 * u - 12], zx[2 * u - 11]);
                    }
                    if constexpr (layer == 0 && !GSM && i == 0 && kk == 1) { vm_wait<KI2 - 1 + (ring_req(0, KI2, RING2, 0) >= 0)>(); st64(tagv, rs, fgo1); }
                    if constexpr (layer == 0 && i == kGPI && kk == 2) gld2s<0>(f0, fgo, p.ws);
                    if constexpr (layer == 0 && i == kGCI) {
                        static_assert(kGCI + KR20 > 40 && !GSM, "");
                        if constexpr (kk == 0) { __builtin_amdgcn_s_waitcnt(0x0F70); pin(f0); gld4s<0>(gl[0], glo, p.ws); }
                        if constexpr (kk == 1) { gld4s<0>(gl[1], glo, gb1); gld4s<0>(gl[2], glo, gb2); }
                        if constexpr (kk == 3) ne1(glate, tagv, f0);
                    }
                    if constexpr (pv >= 0) ar[pv % RING2] = ld128s(rs, ho[pv / 10], ks[pv % 10]);
                    if constexpr (kk == 2 && m >= 1 && s >= 1 && s < 6) *(f32x4*)(smem + dst[m - 1] + (s - 1) * 1024) = a2[m - 1][s - 1];
                    site<4000 + 1000 * layer + 5 * i + kk>();
                    if constexpr (i >= 37) a2[i - 37][kk] = *(const f32x4*)(smem + srco + (i - 37) * 5120 + kk * 1024);
                    SCHED();
                });
                if constexpr (i == 10) { asm volatile("s_waitcnt lgkmcnt(0)" ::: "memory"); __builtin_amdgcn_s_barrier(); asm volatile("" ::: "memory"); }
                if constexpr (i == 28 && layer == 0) {
#pragma unroll
                    for (int nb = 0; nb < 5; ++nb) { a2[3][nb] = f32x4{zz[nb][0], zz[nb][1], zz[nb][2], zz[nb][3]}; pin(a2[3][nb]); }
                }
                if constexpr (i == 36) {
                    asm volatile("s_waitcnt lgkmcnt(0)" ::: "memory");
                    __builtin_amdgcn_s_barrier(); asm volatile("" ::: "memory");
                }
                if constexpr (STAMPS == 1 && (i == 0 || i == 9 || i == 10 || i == 15 || i == 25 || i == 35 || i == 36)) { STAMP((layer ? 51 : 41) + (i == 0 ? 0 : i == 9 ? 1 : i == 10 ? 2 : i == 15 ? 3 : i == 25 ? 4 : i == 35 ? 5 : 6)) }
                SCHED();
            });
            STAMP(layer ? 18 : 8)
        }
#pragma unroll
        for (int nb = 0; nb < 5; ++nb) a2[0][nb] = (a2[0][nb] + a2[1][nb]) + a2[2][nb];
        asm volatile("s_nop 1" : "+v"(a2[3][0]), "+v"(a2[3][1]), "+v"(a2[3][2]), "+v"(a2[3][3]), "+v"(a2[3][4]),
                     "+v"(a2[0][0]), "+v"(a2[0][1]), "+v"(a2[0][2]), "+v"(a2[0][3]), "+v"(a2[0][4]));
        float ty;
        {
            f32x4 s1, s2;
#pragma unroll
            for (int nb = 0; nb < 5; ++nb) {
                const f32x4 y = a2[3][nb] + a2[0][nb];
                xin[nb] = y;
                if (nb == 0) { s1 = y; s2 = y * y; } else { s1 += y; s2 += y * y; }
            }
            ty = xsum2h(hsum4(s1), hsum4(s2));
            st64(v2u{__float_as_uint(ty), tag0}, rs, sgs);
        }
        SCHED();
        STAMP(layer ? 19 : 9)
        if (layer == 0 && __builtin_expect(glate != 0ull, 0)) {
            asm volatile("s_waitcnt vmcnt(0)" : "+v"(gl[0]), "+v"(gl[1]), "+v"(gl[2])::"memory");
            WAVE_WAIT({ f0 = ld64(rs, fgo); ok = ISTAG(f0); })
            gld4t<0>(gl[0], glo, p.ws); gld4t<0>(gl[1], glo, gb1); gld4t<0>(gl[2], glo, gb2);
            asm volatile("s_waitcnt vmcnt(0)" : "+v"(gl[0]), "+v"(gl[1]), "+v"(gl[2])::"memory");
        }
        if (layer == 0) {
            asm volatile("s_waitcnt vmcnt(1)" : "+v"(gl[0]), "+v"(gl[1]), "+v"(gl[2])::"memory");
            *(v4u*)(smem + gw) = gl[0]; *(v4u*)(smem + gw + 1280) = gl[1]; *(v4u*)(smem + gw + 1920) = gl[2];
            asm volatile("s_waitcnt lgkmcnt(0)" ::: "memory");
            __builtin_amdgcn_s_barrier(); asm volatile("" ::: "memory");
#pragma unroll
            for (int nb = 0; nb < 5; ++nb) gt[nb] = __builtin_bit_cast(v2u, trread(smem, gr + 640 * nb));
        } else {
            ACC_TIE5(gt);
            if constexpr (OTR) {
#pragma unroll
                for (int nb = 0; nb < 5; ++nb) *(f32x4*)(smem + otw + nb * 64) = xin[nb];
#pragma unroll
                for (int nb = 0; nb < 5; ++nb) xin[nb] = *(const f32x4*)(smem + (nb < 4 ? otr0 + (nb & 1) * 8 * 336 + (nb >> 1) * 128 : otr4));
            }
        }
        SCHED();
        if (PSP >> layer & 1) { gran_req<32, layer ? PDS : PDS0>(sgo, p.ws); SCHED(); }
        f32x4 gf[5], yg[5];
#pragma unroll
        for (int nb = 0; nb < 5; ++nb) { gf[nb] = unpk4(gt[nb]); yg[nb] = xin[nb] * gf[nb]; pin(gf[nb]); pin(yg[nb]); }
        SCHED();
        if (PSP >> layer & 1) { gran_req<40, 0>(sgo, p.ws); SCHED(); }
        int lane_o;
        asm volatile("v_mbcnt_lo_u32_b32 %0, -1, 0\n\tv_mbcnt_hi_u32_b32 %0, -1, %0" : "=v"(lane_o));
        const int lane = lane_o, c = lane & 15, q = lane >> 4;
        unsigned xs = OFF_X2F + (unsigned)((2 * mt2 + (w >> 1)) * 40960 + ((c >> 2) & 1) * 20480 + (10 * nt2 + 2 * (q & 1) + (q >> 1)) * 256 + (8 * (w & 1) + 4 * (c >> 3) + (c & 3)) * 16);
        unsigned pso = q < 2 ? OFF_PS + (unsigned)(((mt2 * 4 + w) * 8 + nt2) * 128 + c * 8 + q * 4) : kNone;
        unsigned fxo1 = OFF_FX + (unsigned)(((mt1 * 4 + 2 * (mt2 & 1) + (w >> 1)) * 16 + nt2 * 2 + (w & 1)) * 128);
        unsigned oo = OTR ? (unsigned)(((64 * mt2 + 16 * w + (lane >> 3)) * D_ + 80 * nt2) * 4 + (nt2 & 1) * 64 + (lane & 7) * 16) : (unsigned)(((64 * mt2 + 16 * w + c) * D_ + 80 * nt2 + 4 * q) * 4);
        unsigned oo1 = oo + 8 * D_ * 4, oo4 = (unsigned)(((64 * mt2 + 16 * w + (lane >> 3) + (lane & 4) * 2) * D_ + 80 * nt2) * 4 + 256 - (nt2 & 1) * 256 + (lane & 3) * 16);
        int bpi = (lane >> 3) * 4;
        u64 up = 0xF0F0F0F0F0F0F0F0ull;
        unsigned xs2 = (q & 1) ? kNone : xs;
        fxo1 = lane ? kNone : fxo1;
        if (layer == 0) { pin(xs); pin(xs2); pin(pso); pin(fxo1); } else { pin(oo); if (OTR) { pin(oo1); pin(oo4); pin(bpi); pins(up); } }
        float c_r, c_m, c_rr, c_rm, c_mm;
        if constexpr (layer == 0 && PSA) {
            f32x4 su = yg[0], sg_ = gf[0], syu = xin[0] * yg[0], suu = yg[0] * yg[0], sug = yg[0] * gf[0], sgg = gf[0] * gf[0];
#pragma unroll
            for (int nb = 1; nb < 5; ++nb) { su += yg[nb]; sg_ += gf[nb]; syu += xin[nb] * yg[nb]; suu += yg[nb] * yg[nb]; sug += yg[nb] * gf[nb]; sgg += gf[nb] * gf[nb]; }
            const float hu = hsum4(su);
            c_r = xsum2h(hu, 2.f * hsum4(syu)); c_m = xsum2h(hsum4(sg_), 2.f * hu);
            c_rr = xsum2h(0.f, hsum4(suu)); c_rm = xsum2h(0.f, 2.f * hsum4(sug)); c_mm = xsum2h(0.f, hsum4(sgg));
            pin(c_r); pin(c_m); pin(c_rr); pin(c_rm); pin(c_mm);
        }
        SCHED();
        {
            float a, d;
                        if (PSP >> layer & 1) gran_req<48, layer ? PD1 : 0>(sgo, p.ws);
            else { gran_req<32, layer ? PDS : PDS0>(sgo, p.ws); gran_req<40, PD1>(sgo, p.ws); gran_req<48, PD1>(sgo, p.ws); }
            room<(layer ? 900 : 1500)>(); site<9 + 10 * layer>();
            pipe_gran(a, d, sgo, p.ws, tag0, dead);
            STAMP(layer ? 20 : 10)
            xsum2(a, d);
            const float me = a * (1.0f / D_);
            const float ri = __builtin_amdgcn_rsqf(fmaxf(d * (1.0f / D_) - me * me, 0.f) + 1e-5f), mr = -me * ri;
            if constexpr (layer == 0 && PSA) {
                const float ps_ = ty + ri * (c_r + ri * c_rr + mr * c_rm) + mr * (c_m + mr * c_mm);
                __builtin_amdgcn_raw_buffer_store_b32(__float_as_uint(ps_), rs, pso, 0, SC1);
                SCHED();
            }
            float rv[3] = {ri, ri, ri}, mv[3] = {mr, mr, mr};
            if constexpr (layer == 1 && OTR) {
#pragma unroll
                for (int i = 0; i < 2; ++i) { rv[i] = __int_as_float(__builtin_amdgcn_ds_bpermute(bpi + 32 * i, __float_as_int(ri))); mv[i] = __int_as_float(__builtin_amdgcn_ds_bpermute(bpi + 32 * i, __float_as_int(mr))); }
                SCHED();
            }
            static_for<5>(LAMBDA(n_) {
                constexpr int nb = layer ? n_.value : (n_.value + 4) % 5;
                if constexpr (layer == 1 && OTR && nb == 4) { sel1(rv[2], __float_as_uint(rv[0]), __float_as_uint(rv[1]), up); sel1(mv[2], __float_as_uint(mv[0]), __float_as_uint(mv[1]), up); }
                xin[nb] = yg[nb] * rv[nb < 4 ? nb & 1 : 2] + (gf[nb] * mv[nb < 4 ? nb & 1 : 2] + xin[nb]);
                if constexpr (layer == 1) { __builtin_amdgcn_raw_buffer_store_b128(__builtin_bit_cast(v4u, xin[nb]), ro, !OTR ? oo + 64 * nb : nb == 4 ? oo4 : ((nb & 1) ? oo1 : oo) + (nb >> 1) * 128, 0, OUTP); if (EST) SCHED(); }
                else if constexpr (nb & 5) {
                    const f32x4 xa = xin[nb & 6], xb = xin[nb];
                    const auto r0 = __builtin_amdgcn_permlane16_swap(cvtpk(xa.x, xa.y), cvtpk(xb.x, xb.y), false, false);
                    const auto r1 = __builtin_amdgcn_permlane16_swap(cvtpk(xa.z, xa.w), cvtpk(xb.z, xb.w), false, false);
                    st128(v4u{r0[0], r1[0], r0[1], r1[1]}, rs, (nb == 4 ? xs2 : xs) + (nb >> 1) * 1024);
                    if (EST) SCHED();
                }
            });
        }
        if constexpr (layer == 0) {
            if constexpr (!PSA) {
                f32x4 s1, s2;
#pragma unroll
                for (int nb = 0; nb < 5; ++nb) { const f32x4 x = xin[nb]; if (nb == 0) { s1 = x; s2 = x * x; } else { s1 += x; s2 += x * x; } }
                __builtin_amdgcn_raw_buffer_store_b32(__float_as_uint(xsum2h(hsum4(s1), hsum4(s2))), rs, pso, 0, SC1);
            }
            SCHED();
            site<12>();
        int wa[2][3];
#pragma unroll
        for (int i = 0; i < 2; ++i) {
            wa[i][0] = (c >> 3) * 20480 + (c & 7) * 128 + (((4 * i + q) ^ (c & 6)) * 16); wa[i][1] = (wa[i][0] ^ 16) + 10240;
            wa[i][2] = 81920 + (c & 1) * 10240 + (c >> 1) * 128 + (((4 * i + q) ^ ((c & 1) | ((c >> 2) << 1))) * 16);
            pin(wa[i][0]); pin(wa[i][1]); pin(wa[i][2]);
        }
            unsigned fxo = OFF_FX + (unsigned)((mt1 * 4 + w) * 2048 + (PLM ? lane >> 2 : c) * 128);
            unsigned xo = OFF_X2F + (unsigned)((4 * mt1 + w) * 40960 + lane * 16), xo1 = xo + 20480u;
            const int row = 8 * q + (c & 7);
            unsigned po = OFF_PS + (unsigned)((((2 * mt1 + (w >> 1)) * 4 + 2 * (w & 1) + (row >> 4)) * 8 + 4 * (c >> 3)) * 128 + (row & 15) * 8);
            int rsw = l_rs(1, 0) + w * INBOX + lane * 4;
            pin(rsw); pin(fxo); pin(xo); pin(xo1); pin(po);
            v4u wf[2][5];
#pragma unroll
            for (int nb = 0; nb < 5; ++nb) wf[0][nb] = WF(nb, 0);
            SCHED();
            DRAIN();
            st64(tagv, rs, fxo1);
            if (b == 255 && w == 0) FLAG(OFF_NONCE)
            STAMP(11)
            if (w == 0) tour<4>();
            if constexpr (OTR) {
                const int sh = (nt2 & 1) * 64, r4 = (lane >> 3) + (lane & 4) * 2;
                const int a0 = c * 336 + q * 16, a1 = (lane >> 3) * 336 + sh + (lane & 7) * 16, a2_ = r4 * 336 + 256 - 4 * sh + (lane & 3) * 16;
                char* tw = smem + SC + w * INBOX + 12288;
#pragma unroll
                for (int nb = 0; nb < 5; ++nb) *(v2u*)(tw + a0 / 2 + nb * 32) = gt[nb];
#pragma unroll
                for (int nb = 0; nb < 5; ++nb) gt[nb] = *(const v2u*)(tw + (nb < 4 ? a1 / 2 + (nb & 1) * 8 * 168 + (nb >> 1) * 64 : a2_ / 2));
                otw = SC + w * INBOX + a0; otr0 = SC + w * INBOX + a1; otr4 = SC + w * INBOX + a2_;
                pin(otw); pin(otr0); pin(otr4);
            }
            addrs(std::integral_constant<int, 1>{}, lane, w, b);
            ACC_TIE5(gt);
            v2u pt[4];
            skip<SKIP5>();
            pipe_flag<KPF0>(pt, fxo, p.ws, tagv, dead);
            ORDER();
            STAMP(12)
            v4u ar[2 * RING1];
#define XF(n) ld128s(rs, (((n) & 1) ? xo1 : xo) + (((n) >> 1) & 3) * 1024, ((n) >> 3) * 4096)
#pragma unroll
            for (int n = 0; n < KI1; ++n) { ar[n] = XF(n); SCHED(); }
            v2u ps[4];
            float t1, t2, rinv, mri, rr_[8], rm_[8];
            static_for<20>(LAMBDA(s_) {
                constexpr int s = s_.value, cu = s & 1, nx = cu ^ 1;
                LGKM0() SCHED();
                static_for<10>(LAMBDA(g_) {
                    constexpr int g = g_.value, mb = g / 5, k = g % 5;
                    if constexpr (s == 0) mf0(acc[mb][k], ar[mb], wf[0][k]); else mf(acc[mb][k], ar[(2 * s + mb) % (2 * RING1)], wf[cu][k]);
                    SCHED();
                    if constexpr (s == 0 && g == 0) { pin(pt[0]); pin(pt[1]); pin(pt[2]); pin(pt[3]); }
                    if constexpr (s < 19 && g < 5) wf[nx][g] = WF(g, s + 1);
                    constexpr int rq = ring_req(10 * s + g, KI1, 2 * RING1, 4);
                    if constexpr (rq >= 100) ps[rq - 100] = ld64(rs, po + (rq - 100) * 128);
                    else if constexpr (rq >= 0) ar[rq % (2 * RING1)] = XF(rq);
                    constexpr int n = g >= 6 ? 4 * (s - KS1) + g - 6 : -1;
                    if constexpr (n == 0) { t1 = __uint_as_float(ps[0].x) + __uint_as_float(ps[1].x); pin(t1); t2 = __uint_as_float(ps[0].y) + __uint_as_float(ps[1].y); pin(t2); }
                    if constexpr (n == 1) { t1 += __uint_as_float(ps[2].x); pin(t1); t2 += __uint_as_float(ps[2].y); pin(t2); }
                    if constexpr (n == 2) { t1 += __uint_as_float(ps[3].x); pin(t1); t2 += __uint_as_float(ps[3].y); pin(t2); }
                    if constexpr (n == 3) { t1 += dpp<0x128>(t1); t2 += dpp<0x128>(t2); pin(t1); pin(t2); }
                    if constexpr (n == 4) { t1 *= (1.0f / D_); pin(t1); t2 *= (1.0f / D_); pin(t2); }
                    if constexpr (n == 5) { t2 = fmaxf(t2 - t1 * t1, 0.f); pin(t2); }
                    if constexpr (n == 6) { rinv = __builtin_amdgcn_rsqf(t2 + 1e-5f); pin(rinv); }
                    if constexpr (n == 8) { mri = -t1 * rinv; pin(mri); }
                    if constexpr (n == 9) { t1 = rinv; mrg8(t1, mri); }
                    if constexpr (n == 10) *(float*)(smem + rsw) = t1;
                    if constexpr (n >= 11 && n < 19) bc2b<n - 11>(rr_[n - 11], rm_[n - 11], rinv, mri);
                    if constexpr (n >= 19 && n < 39) TT_GAP(n - 19)
                    site<6000 + 10 * s + g>();
                    SCHED();
                });
            });
            rr[0] = f32x4{rr_[0], rr_[1], rr_[2], rr_[3]}; rr[1] = f32x4{rr_[4], rr_[5], rr_[6], rr_[7]};
            ACC_FENCE10(acc[0], acc[1]);
            STAMP(13)
        } else {
            STAMP(21)
            STAMP_DUMP()
            asm volatile("kpf_end:");
        }
    };
    layer_body(std::integral_constant<int, 0>{});
    layer_body(std::integral_constant<int, 1>{});
}

extern "C" void kernel_launch(void* const* d_in, const int* in_sizes, int n_in,
                              void* d_out, int out_size, void* d_ws, size_t ws_size,
                              hipStream_t stream) {
    Params p;
    p.x0 = (const u16*)d_in[0];
    p.Lre = (const u16*)d_in[1];
    p.Lim = (const u16*)d_in[2];
    p.Bre = (const u16*)d_in[3];
    p.Bim = (const u16*)d_in[4];
    p.Cre = (const u16*)d_in[5];
    p.Cim = (const u16*)d_in[6];
    p.logdt = (const u16*)d_in[7];
    p.Dskip = (const u16*)d_in[8];
    p.k = (const u16*)d_in[10];
    p.v = (const u16*)d_in[11];
    p.shk = (const u16*)d_in[12];
    p.ssmk = (const u16*)d_in[13];
    p.ssmD = (const u16*)d_in[14];
    p.ws = (char*)d_ws;
    p.out = (float*)d_out;
    k_main<<<256, 256, 0, stream>>>(p);
}
```

```cpp
#include <hip/hip_runtime.h>
#include <stdint.h>
#include <utility>

#define STAMPS 0

typedef unsigned short u16;
typedef unsigned long long u64;
typedef unsigned v4u __attribute__((ext_vector_type(4)));
typedef unsigned v2u __attribute__((ext_vector_type(2)));
typedef float f32x4 __attribute__((ext_vector_type(4)));
typedef float f32x2 __attribute__((ext_vector_type(2)));
typedef short s16x4 __attribute__((ext_vector_type(4)));
typedef __bf16 bf16x2v __attribute__((ext_vector_type(2)));
#define LAS __attribute__((address_space(3)))
#define DI __device__ __forceinline__

constexpr int S_ = 2048, D_ = 640;
#define KR1 5
#define KR2 10
constexpr int RING1 = KR1;
#define KCR1 4
constexpr int kCR1 = KCR1;
#define KXA 0
#define KN3E 16
#define KN3 12
constexpr int kXA = KXA;
constexpr int kN3E = KN3E, kN3 = KN3;
#define KFHK 9
#define KFHP 7
constexpr int kFHK = KFHK, kFHP = KFHP;
#define KGPI 31
#define KGCI 36
#define CPF 0
#define LBE 0
#define LBR 1
#define W3O 1
#define KI2 8
#define KI1 8
#define KS1 8
#define KR20 10
constexpr int kGPI = KGPI, kGCI = KGCI;

#define KPAD 0
#define KPS 0
#define GPAD 0
constexpr int KSTR = 4096 + KPAD, GSTR = 4096 + GPAD;
constexpr unsigned OFF_NONCE = 0;
constexpr unsigned OFF_ST = 4096;
constexpr unsigned OFF_FW = OFF_ST + 65536;
constexpr unsigned OFF_FK = OFF_FW + 131072;
constexpr unsigned OFF_FH = OFF_FK + 81920;
constexpr unsigned OFF_FX = OFF_FH + 262144;
constexpr unsigned OFF_FG = OFF_FX + 131072;
constexpr unsigned OFF_EG = OFF_FG + 98304;
constexpr unsigned OFF_SG = OFF_EG + 327680;
constexpr unsigned OFF_PS = OFF_SG + 524288;
constexpr unsigned OFF_KVT = OFF_PS + 131072;
constexpr unsigned OFF_GT = OFF_KVT + 640 * 4608;
constexpr unsigned OFF_W2F = OFF_GT + 640 * 4608;
constexpr unsigned OFF_X2F = OFF_W2F + 1638400;
constexpr unsigned OFF_DUM = OFF_X2F + 2621440;
constexpr unsigned OFF_HF = OFF_DUM + 163840;
constexpr unsigned WS_BYTES = OFF_HF + 2 * 5242880;
constexpr unsigned kNone = 0x40000000u;
constexpr unsigned OFF_DBG = 0x1800000;
#define TILES() const int xq = b & 7, j = b >> 3, mt1 = 2 * xq + (j >> 4), nt1 = j & 15, mt2 = 4 * xq + (j >> 3), nt2 = j & 7, i32 = 16 * (mt2 & 1) + mt1;

constexpr int SC = 102400;
constexpr int REGSZ = 13056;
constexpr int L_XR = SC;
constexpr int L_CO = SC + 12288;
constexpr int L_KB = SC + 51712;
constexpr int L_TAB = SC + 55808;
constexpr int L_CW = SC + 39168;
constexpr int INBOX = 15360;
constexpr int l_rs(int l, int w) { return l ? SC + w * INBOX : L_KB + w * 256; }
constexpr int l_sg(int l, int w) { return l ? SC + w * INBOX + 256 : SC + (w == 0 ? 43264 : w == 1 ? 46080 : w == 2 ? 52736 : 56320); }
constexpr int l_tb(int l, int w) { return l ? SC + w * INBOX + 2048 : SC + 40960 + w * 5120; }
constexpr int LDS_BYTES = 163840;
constexpr int CV_E = 4608;
constexpr int CV_O = CV_E + (1056 + 15) * 4;

struct Params {
    const u16 *x0, *Lre, *Lim, *Bre, *Bim, *Cre, *Cim, *logdt, *Dskip, *k, *v, *shk, *ssmk, *ssmD;
    char* ws;
    float* out;
};

#define MF "v_mfma_f32_16x16x32_bf16 "
DI void mf(f32x4& c, v4u a, v4u b) { asm(MF "%0, %1, %2, %0" : "+v"(c) : "v"(a), "v"(b)); }
DI void mf0(f32x4& c, v4u a, v4u b) { asm(MF "%0, %1, %2, 0" : "=&v"(c) : "v"(a), "v"(b)); }
constexpr int W2R = 56;
#define WA(n) "a[%c" #n ":%c" #n "+3]"
template <int R> DI void mfa(f32x4& c, v4u a) { asm(MF "%0, " WA(2) ", %1, %0" : "+v"(c) : "v"(a), "i"(W2R + 4 * R)); }
template <int R> DI void mfa0(f32x4& c, v4u a) { asm(MF "%0, " WA(2) ", %1, 0" : "=&v"(c) : "v"(a), "i"(W2R + 4 * R)); }
#define ACC_FENCE10(c, d) asm volatile("" : "+v"(c[0]), "+v"(c[1]), "+v"(c[2]), "+v"(c[3]), "+v"(c[4]), "+v"(d[0]), "+v"(d[1]), "+v"(d[2]), "+v"(d[3]), "+v"(d[4]))
#define ACC_TIE5(c) asm volatile("" : "+v"(c[0]), "+v"(c[1]), "+v"(c[2]), "+v"(c[3]), "+v"(c[4]))
#define QP " quad_perm:[1,0,3,2] row_mask:0xf bank_mask:0xf"
struct St { f32x2 a, b; float c; };
struct Lm { f32x2 a, b; float r, s; };
#define PK1 " op_sel_hi:[0,1,1]\n\t"
#define PK2 " op_sel:[1,1,0] op_sel_hi:[1,0,1] neg_lo:[1,0,0]"
#define ST_(t0, t1, t2, h0, h1, h2, b0, b1, b2, l0, l1, l2, l3)                                                                                        \
    "v_pk_fma_f32 %" #t0 ", %" #l0 ", %" #h0 ", %" #b0 PK1 "v_pk_fma_f32 %" #t1 ", %" #l1 ", %" #h1 ", %" #b1 PK1 "v_fma_f32 %" #t2 ", %" #l2 ", %" #h2 ", %" #b2 "\n\t" \
    "v_pk_fma_f32 %" #t0 ", %" #l0 ", %" #h0 ", %" #t0 PK2 "\n\tv_pk_fma_f32 %" #t1 ", %" #l1 ", %" #h1 ", %" #t1 PK2 "\n\tv_fmac_f32_dpp %" #t2 ", %" #h2 ", %" #l3 QP "\n\t"
#define SO(t) "=&v"(t.a), "=&v"(t.b), "=&v"(t.c)
#define SI(t) "v"(t.a), "v"(t.b), "v"(t.c)
#define LI(l) "v"(l.a), "v"(l.b), "v"(l.r), "v"(l.s)
DI void run3(St& t, const St& h, const St* b, const Lm& l) {
    St u;
    asm(ST_(0, 1, 2, 6, 7, 8, 9, 10, 11, 18, 19, 20, 21) ST_(3, 4, 5, 0, 1, 2, 12, 13, 14, 18, 19, 20, 21) ST_(0, 1, 2, 3, 4, 5, 15, 16, 17, 18, 19, 20, 21)
        : SO(t), SO(u) : SI(h), SI(b[0]), SI(b[1]), SI(b[2]), LI(l));
}
DI void run4(St& t, const St& h, const St* b, const Lm& l) {
    St u;
    asm(ST_(3, 4, 5, 6, 7, 8, 9, 10, 11, 21, 22, 23, 24) ST_(0, 1, 2, 3, 4, 5, 12, 13, 14, 21, 22, 23, 24) ST_(3, 4, 5, 0, 1, 2, 15, 16, 17, 21, 22, 23, 24) ST_(0, 1, 2, 3, 4, 5, 18, 19, 20, 21, 22, 23, 24)
        : SO(t), SO(u) : SI(h), SI(b[0]), SI(b[1]), SI(b[2]), SI(b[3]), LI(l));
}
#define AC_(t0, t2, h0, h2, b0, b2, l0, l2, l3)                                                                                   \
    "v_pk_fma_f32 %" #t0 ", %" #l0 ", %" #h0 ", %" #b0 PK1 "v_fma_f32 %" #t2 ", %" #l2 ", %" #h2 ", %" #b2 "\n\t"               \
    "v_pk_fma_f32 %" #t0 ", %" #l0 ", %" #h0 ", %" #t0 PK2 "\n\tv_fmac_f32_dpp %" #t2 ", %" #h2 ", %" #l3 QP "\n\t"
#define AO(t) "=&v"(t.a), "=&v"(t.c)
#define AI(t) "v"(t.a), "v"(t.c)
DI void keep14(St* t, const St& h, const St& x, const St* b, const Lm& lq, const Lm& l) {
    St u;
    asm(AC_(8, 9, 10, 11, 12, 13, 22, 23, 24) AC_(0, 1, 8, 9, 14, 15, 25, 26, 27) AC_(2, 3, 0, 1, 16, 17, 25, 26, 27) AC_(4, 5, 2, 3, 18, 19, 25, 26, 27) AC_(6, 7, 4, 5, 20, 21, 25, 26, 27)
        : AO(t[0]), AO(t[1]), AO(t[2]), AO(t[3]), AO(u) : AI(h), AI(x), AI(b[0]), AI(b[1]), AI(b[2]), AI(b[3]), "v"(lq.a), "v"(lq.r), "v"(lq.s), "v"(l.a), "v"(l.r), "v"(l.s));
}
DI void keep4(St* t, const St& h, const St* b, const Lm& l) {
    asm(AC_(0, 1, 8, 9, 10, 11, 18, 19, 20) AC_(2, 3, 0, 1, 12, 13, 18, 19, 20) AC_(4, 5, 2, 3, 14, 15, 18, 19, 20) AC_(6, 7, 4, 5, 16, 17, 18, 19, 20)
        : AO(t[0]), AO(t[1]), AO(t[2]), AO(t[3]) : AI(h), AI(b[0]), AI(b[1]), AI(b[2]), AI(b[3]), "v"(l.a), "v"(l.r), "v"(l.s));
}
DI void b3cv(f32x2* t, unsigned* o, f32x2 h, f32x2 b0, f32x2 b1, f32x2 b2, f32x2 l0, f32x2 l, const float* e) {
    asm("v_pk_fma_f32 %0, %11, %7, %8" PK1 "v_cvt_pk_bf16_f32 %3, %13, %14\n\tv_pk_fma_f32 %0, %11, %7, %0" PK2 "\n\tv_cvt_pk_bf16_f32 %4, %15, %16\n\t"
        "v_pk_fma_f32 %1, %12, %0, %9" PK1 "v_cvt_pk_bf16_f32 %5, %17, %18\n\tv_pk_fma_f32 %1, %12, %0, %1" PK2 "\n\tv_cvt_pk_bf16_f32 %6, %19, %20\n\t"
        "v_pk_fma_f32 %2, %12, %1, %10" PK1
        : "=&v"(t[0]), "=&v"(t[1]), "=&v"(t[2]), "=&v"(o[0]), "=&v"(o[1]), "=&v"(o[2]), "=&v"(o[3])
        : "v"(h), "v"(b0), "v"(b1), "v"(b2), "v"(l0), "v"(l), "v"(e[0]), "v"(e[1]), "v"(e[2]), "v"(e[3]), "v"(e[4]), "v"(e[5]), "v"(e[6]), "v"(e[7]));
}
DI void bfin(f32x2& t, f32x2 h, f32x2 l) { asm("v_pk_fma_f32 %0, %2, %1, %0" PK2 : "+v"(t) : "v"(h), "v"(l)); }
#define UNDEF8(c) asm volatile("" : "=v"(c[0]), "=v"(c[1]), "=v"(c[2]), "=v"(c[3]), "=v"(c[4]), "=v"(c[5]), "=v"(c[6]), "=v"(c[7]))
DI unsigned cvtpk(float lo, float hi) {
    f32x2 v = {lo, hi};
    return __builtin_bit_cast(unsigned, __builtin_convertvector(v, bf16x2v));
}
DI float lo16f(unsigned u) { return __uint_as_float(u << 16); }
DI float hi16f(unsigned u) { return __uint_as_float(u & 0xffff0000u); }
DI float rbf(float v) { return lo16f(cvtpk(v, 0.f)); }
template <int CTRL> DI float dpp(float x) {
    return __int_as_float(__builtin_amdgcn_update_dpp(0, __float_as_int(x), CTRL, 0xF, 0xF, true));
}
DI float rowsum16(float x) {
    x += dpp<0x128>(x); x += dpp<0x124>(x); x += dpp<0x122>(x); x += dpp<0x121>(x);
    return x;
}
DI float xsum32(float x) { auto r = __builtin_amdgcn_permlane32_swap(__float_as_uint(x), __float_as_uint(x), false, false); return __uint_as_float(r[0]) + __uint_as_float(r[1]); }
DI void xsum2(float& a, float& d) {
    auto r = __builtin_amdgcn_permlane16_swap(__float_as_uint(a), __float_as_uint(d), false, false);
    const float s = __uint_as_float(r[0]) + __uint_as_float(r[1]);
    const float t = xsum32(s);
    auto u = __builtin_amdgcn_permlane16_swap(__float_as_uint(t), __float_as_uint(t), false, false);
    a = __uint_as_float(u[0]); d = __uint_as_float(u[1]);
}
DI float xsum2h(float a, float d) {
    auto r = __builtin_amdgcn_permlane16_swap(__float_as_uint(a), __float_as_uint(d), false, false);
    return xsum32(__uint_as_float(r[0]) + __uint_as_float(r[1]));
}
DI float hsum4(f32x4 v) { return (v.x + v.y) + (v.z + v.w); }
typedef __amdgpu_buffer_rsrc_t rsrc_t;
constexpr int SC1 = 16;
DI v4u ld128(rsrc_t rs, unsigned off) { return __builtin_amdgcn_raw_buffer_load_b128(rs, off, 0, SC1); }
DI v4u ld128s(rsrc_t rs, unsigned off, unsigned soff) { return __builtin_amdgcn_raw_buffer_load_b128(rs, off, soff, SC1); }
DI v2u ld64(rsrc_t rs, unsigned off) { return __builtin_amdgcn_raw_buffer_load_b64(rs, off, 0, SC1); }
DI void st128(v4u v, rsrc_t rs, unsigned off) { __builtin_amdgcn_raw_buffer_store_b128(v, rs, off, 0, SC1); }
DI void st64(v2u v, rsrc_t rs, unsigned off) { __builtin_amdgcn_raw_buffer_store_b64(v, rs, off, 0, SC1); }
#define LGKM0() { __builtin_amdgcn_s_waitcnt(0xC07F); asm volatile("" ::: "memory"); }
#define DRAIN() asm volatile("s_waitcnt vmcnt(0)" ::: "memory")
#define ORDER() __builtin_amdgcn_fence(__ATOMIC_ACQUIRE, "wavefront")
#define SCHED() __builtin_amdgcn_sched_barrier(0)
constexpr unsigned kSpinCap = 1u << 17;
#define WAVE_WAIT(...)                                                      \
    {                                                                       \
        unsigned spins_ = dead ? kSpinCap : 0u;                             \
        _Pragma("nounroll") for (;;) {                                      \
            asm volatile("" ::: "memory");                                  \
            bool ok;                                                        \
            __VA_ARGS__;                                                    \
            if (__builtin_amdgcn_ballot_w64(!ok) == 0ull) break;            \
            if (++spins_ > kSpinCap) { dead = true; break; }                \
            __builtin_amdgcn_s_sleep(1);                                    \
        }                                                                   \
        ORDER();                                                            \
    }
#define KPD1 2
#define KPDS 5
#define KPDS0 5
#define KPF0 5
#define KPH0 12
#define KPF1 4
constexpr int PD1 = KPD1, PDS = KPDS, PDS0 = KPDS0;
#define PL(r) "global_load_dwordx2 " r ", %5, %6 sc1\n\t"
#define PS_(r) "s_waitcnt vmcnt(1)\n\tv_cmp_ne_u64 vcc, %7, " r "\n\ts_cbranch_vccz 2f\n\t" PL(r)
template <int D0> DI void pipe_flag(v2u (&t)[4], unsigned off, const void* base, v2u tag, bool& dead) {
    unsigned n = dead ? 0u : kSpinCap;
    asm volatile("s_sleep %8\n\t" PL("%0") "s_sleep %9\n\t" PL("%1")
                 "1:\n\t" PS_("%0") PS_("%1") "s_sub_u32 %4, %4, 1\n\ts_cbranch_scc0 1b\n2:"
                 : "=&v"(t[0]), "=&v"(t[1]), "=&v"(t[2]), "=&v"(t[3]), "+s"(n) : "v"(off), "s"(base), "v"(tag), "i"(D0), "i"(KPF1) : "memory", "vcc", "scc");
    dead = __builtin_amdgcn_readfirstlane(n) == ~0u;
}
#define WAVE_RETRY(OK, ...) { if (__builtin_expect(__builtin_amdgcn_ballot_w64(!(OK)) != 0ull, 0)) WAVE_WAIT({ __VA_ARGS__; ok = OK; }) ORDER(); }
#define STAMP(i)
#define STAMP_DUMP()

#define PADS 9000, 1004, 1059, 1095, 2096, 9001, 9002, 4072, 9, 9003, 6, 9004, 9005, 6105, 14, 15, 9006, 18, 19, 9007, 21, 2, 9008
#define TW 2
#define TW0 3
#define KLB0 0
#define KLB1 4
#define SKIP0 0
#define SKIP5 0
constexpr int kPads[] = {PADS};
constexpr int pad_next(int id) {
    for (unsigned i = 0; i + 1 < sizeof(kPads) / sizeof(int); ++i) if (kPads[i] == id) return kPads[i + 1];
    return -1;
}
constexpr bool pads_unique() {
    for (unsigned i = 0; i < sizeof(kPads) / sizeof(int); ++i)
        for (unsigned k = 0; k < i; ++k) if (kPads[i] == kPads[k]) return false;
    return true;
}
static_assert(pads_unique(), "");
template <int ID> DI void site() {
    if constexpr (pad_next(ID) >= 0) asm volatile("kp%c0:\n\ts_cbranch_execz kp%c1" ::"i"(ID), "i"(pad_next(ID)));
    else asm volatile("");
}
template <int T> DI void tour() {
    if constexpr (pad_next(9000 + T) >= 0 && pad_next(9000 + T) != 9001 + T) asm volatile("s_mov_b64 exec, 0\n\ts_branch kp%c0\nkp%c1:\n\ts_mov_b64 exec, -1" ::"i"(pad_next(9000 + T)), "i"(9001 + T));
}
template <int N> DI void skip() { if constexpr (N > 0) asm volatile("s_branch 1f\n\t.rept %c0\n\ts_nop 0\n\t.endr\n1:" ::"i"(N)); }
template <int N> DI void room() { asm volatile("s_branch 1f\n\t.p2align 12, , %c0\n1:" ::"i"(N)); }
template <class F, int... I> DI void static_for_impl(std::integer_sequence<int, I...>, F&& f) { (f(std::integral_constant<int, I>{}), ...); }
template <int N, class F> DI void static_for(F&& f) { static_for_impl(std::make_integer_sequence<int, N>{}, f); }
#define LAMBDA(v) [&](auto v) __attribute__((always_inline))
DI f32x2 cmul(f32x2 a, f32x2 b) {
    f32x2 t;
    asm("v_pk_mul_f32 %0, %1, %2 op_sel_hi:[0,1]\n\ts_nop 0\n\tv_pk_fma_f32 %0, %1, %2, %0" PK2 "\n\ts_nop 0" : "=&v"(t) : "v"(a), "v"(b));
    return t;
}
DI f32x2 cfma(f32x2 a, f32x2 b, f32x2 c) {
    f32x2 t;
    asm("v_pk_fma_f32 %0, %1, %2, %3 op_sel_hi:[0,1,1]\n\ts_nop 0\n\tv_pk_fma_f32 %0, %1, %2, %0" PK2 "\n\ts_nop 0" : "=&v"(t) : "v"(a), "v"(b), "v"(c));
    return t;
}
DI void cfma4(f32x2 (&t)[4], f32x2 a, f32x2 b0, f32x2 b1, f32x2 b2, f32x2 b3, f32x2 c0, f32x2 c1, f32x2 c2, f32x2 c3) {
    asm("v_pk_fma_f32 %0, %4, %5, %9 op_sel_hi:[0,1,1]\n\tv_pk_fma_f32 %1, %4, %6, %10 op_sel_hi:[0,1,1]\n\tv_pk_fma_f32 %2, %4, %7, %11 op_sel_hi:[0,1,1]\n\tv_pk_fma_f32 %3, %4, %8, %12 op_sel_hi:[0,1,1]\n\t"
        "v_pk_fma_f32 %0, %4, %5, %0" PK2 "\n\tv_pk_fma_f32 %1, %4, %6, %1" PK2 "\n\tv_pk_fma_f32 %2, %4, %7, %2" PK2 "\n\tv_pk_fma_f32 %3, %4, %8, %3" PK2 "\n\ts_nop 0"
        : "=&v"(t[0]), "=&v"(t[1]), "=&v"(t[2]), "=&v"(t[3]) : "v"(a), "v"(b0), "v"(b1), "v"(b2), "v"(b3), "v"(c0), "v"(c1), "v"(c2), "v"(c3));
}
#define C4(t0, t1, t2, t3, a, b0, b1, b2, b3, c0, c1, c2, c3)                                                                                                         \
    "v_pk_fma_f32 %" #t0 ", %" #a ", %" #b0 ", %" #c0 PK1 "v_pk_fma_f32 %" #t1 ", %" #a ", %" #b1 ", %" #c1 PK1 "v_pk_fma_f32 %" #t2 ", %" #a ", %" #b2 ", %" #c2 PK1 "v_pk_fma_f32 %" #t3 ", %" #a ", %" #b3 ", %" #c3 PK1 \
    "v_pk_fma_f32 %" #t0 ", %" #a ", %" #b0 ", %" #t0 PK2 "\n\tv_pk_fma_f32 %" #t1 ", %" #a ", %" #b1 ", %" #t1 PK2 "\n\tv_pk_fma_f32 %" #t2 ", %" #a ", %" #b2 ", %" #t2 PK2 "\n\tv_pk_fma_f32 %" #t3 ", %" #a ", %" #b3 ", %" #t3 PK2 "\n\t"
DI void cfma4n(f32x2 (&t)[4], f32x2 a, f32x2 b0, f32x2 b1, f32x2 b2, f32x2 b3, f32x2 c0, f32x2 c1, f32x2 c2, f32x2 c3) {
    asm(C4(0, 1, 2, 3, 4, 5, 6, 7, 8, 9, 10, 11, 12) : "=&v"(t[0]), "=&v"(t[1]), "=&v"(t[2]), "=&v"(t[3]) : "v"(a), "v"(b0), "v"(b1), "v"(b2), "v"(b3), "v"(c0), "v"(c1), "v"(c2), "v"(c3));
}
DI void agg(unsigned& er, unsigned& ei, f32x2 (&x)[4], f32x2& a, const f32x2 (&u)[4], const f32x2 (&s)[16], f32x2 l8, f32x2 l32, f32x2 l64) {
    f32x2 v[4], b;
    asm(C4(4, 5, 6, 7, 22, 10, 11, 12, 13, 14, 15, 16, 17) C4(0, 1, 2, 3, 22, 4, 5, 6, 7, 18, 19, 20, 21)
        "v_pk_fma_f32 %8, %23, %0, %1" PK1 "v_pk_fma_f32 %9, %23, %2, %3" PK1 "v_pk_fma_f32 %8, %23, %0, %8" PK2 "\n\tv_pk_fma_f32 %9, %23, %2, %9" PK2
        : "=&v"(x[0]), "=&v"(x[1]), "=&v"(x[2]), "=&v"(x[3]), "=&v"(v[0]), "=&v"(v[1]), "=&v"(v[2]), "=&v"(v[3]), "=&v"(a), "=&v"(b)
        : "v"(u[0]), "v"(u[1]), "v"(u[2]), "v"(u[3]), "v"(s[2]), "v"(s[6]), "v"(s[10]), "v"(s[14]), "v"(s[3]), "v"(s[7]), "v"(s[11]), "v"(s[15]), "v"(l8), "v"(l32));
    asm("s_nop 0\n\tv_fma_f32 %0, %2, %6, %4\n\tv_fma_f32 %1, %2, %7, %5\n\tv_fma_f32 %0, -%3, %7, %0\n\tv_fma_f32 %1, %3, %6, %1"
        : "=&v"(er), "=&v"(ei) : "v"(a.x), "v"(a.y), "v"(b.x), "v"(b.y), "v"(l64.x), "v"(l64.y));
}
DI void gacc2(f32x2& h0, f32x2& h1, f32x2 w0, f32x2 w1, v4u e0, v4u e1) {
    const v2u a0 = {e0.x, e0.y}, b0 = {e0.z, e0.w}, a1 = {e1.x, e1.y}, b1 = {e1.z, e1.w};
    asm("v_pk_fma_f32 %0, %4, %2, %0 op_sel_hi:[0,1,1]\n\tv_pk_fma_f32 %1, %6, %3, %1 op_sel_hi:[0,1,1]\n\t"
        "v_pk_fma_f32 %0, %5, %2, %0 op_sel:[0,1,0] op_sel_hi:[0,0,1] neg_lo:[1,0,0]\n\tv_pk_fma_f32 %1, %7, %3, %1 op_sel:[0,1,0] op_sel_hi:[0,0,1] neg_lo:[1,0,0]"
        : "+v"(h0), "+v"(h1) : "v"(w0), "v"(w1), "v"(a0), "v"(b0), "v"(a1), "v"(b1));
}
DI s16x4 trread(char* smem, int byteoff) {
    return __builtin_amdgcn_ds_read_tr16_b64_v4i16((LAS s16x4*)(smem + byteoff));
}

#define CV_LOAD_A(A, a, rr_, hf) { const unsigned* ap_ = (const unsigned*)(smem + ab[(a) >> 1] + (((a) & 1) ? 0 : 512) + 64 * (7 - (rr_)) + 8 * (hf)); A[a][2 * (hf)] = ap_[0]; A[a][2 * (hf) + 1] = ap_[1]; }
#define CV_LOAD_B(B, e, rr_) { B[e] = *(const v4u*)(smem + bb + 64 * (7 - (rr_)) + 512 * (e)); }
template <int R, bool PF, class F, class G>
DI void conv_round(f32x4 (&ca)[8], v4u (&A)[8], const v4u (&B)[8], v4u (&An)[8], v4u (&Bn)[8], const char* smem, const int (&ab)[4], int bb, F&& fill, G&& last) {
    static_for<36>(LAMBDA(g_) {
        constexpr int g = g_.value;
        constexpr int a = g < 8 ? 0 : g < 15 ? 1 : g < 21 ? 2 : g < 26 ? 3 : g < 30 ? 4 : g < 33 ? 5 : g < 35 ? 6 : 7;
        constexpr int e = g - (a * 8 - a * (a - 1) / 2);
        if constexpr (R == 0 && a == 0) mf0(ca[e], A[0], B[e]); else mf(ca[a + e], A[a], B[e]);
        SCHED();
        if constexpr (R == 0 && g < 16 && (g & 3) != 3) { constexpr int n = g - (g >> 2); CV_LOAD_A(A, 2 + (n >> 1), 0, n & 1) SCHED(); }
        if constexpr (PF && g < 32 && (g & 3) != 3) { if constexpr ((g & 3) == 2) CV_LOAD_B(Bn, g >> 2, R + 1) else CV_LOAD_A(An, g >> 2, R + 1, g & 1) SCHED(); }
        site<3000 + 36 * R + g>();
        if constexpr (R == 7) { last(g_); SCHED(); }
        if constexpr ((g & 3) == 3 && g < 28) { fill(std::integral_constant<int, 7 * R + g / 4>{}); SCHED(); }
    });
}
template <int R0, int R1, bool PFL, class F, class G>
DI void conv_part(f32x4 (&ca)[8], v4u (&A0)[8], v4u (&B0)[8], const char* smem, const int (&ab)[4], int bb, F&& fill, G&& last) {
    v4u A1[8], B1[8];
    static_for<R1 - R0>(LAMBDA(r_) {
        constexpr int r = r_.value;
        if constexpr (r & 1) conv_round<R0 + r, (R0 + r + 1 < R1 || PFL)>(ca, A1, B1, A0, B0, smem, ab, bb, fill, last);
        else conv_round<R0 + r, (R0 + r + 1 < R1 || PFL)>(ca, A0, B0, A1, B1, smem, ab, bb, fill, last);
    });
}
template <int IMM> DI void gld(v4u& dst, unsigned voff, const void* sbase) {
    asm volatile("global_load_dwordx4 %0, %1, %2 offset:%3" : "=v"(dst) : "v"(voff), "s"(sbase), "i"(IMM));
}
#define OUTP 16
DI void gldu16(unsigned& dst, unsigned voff, const void* sbase) { asm volatile("global_load_ushort %0, %1, %2" : "=v"(dst) : "v"(voff), "s"(sbase)); }
DI void gld1s(unsigned& dst, unsigned voff, const void* sbase) { asm volatile("global_load_dword %0, %1, %2 sc1" : "=v"(dst) : "v"(voff), "s"(sbase)); }
template <int V> DI void SETM0(unsigned ldsbase) { asm volatile("s_add_u32 m0, %0, %1" ::"s"(ldsbase), "i"(V)); }
template <int GOFF> DI void GLD0(unsigned voff, const char* sbase) { asm volatile("global_load_lds_dwordx4 %0, %1 offset:%2" ::"v"(voff), "s"(sbase), "i"(GOFF) : "memory"); }
template <int GOFF, int V> DI void GLD1(unsigned voff, const char* sbase, unsigned ldsbase) {
    asm volatile("global_load_lds_dwordx4 %0, %1 offset:%3\n\ts_add_u32 m0, %2, %4" ::"v"(voff), "s"(sbase), "s"(ldsbase), "i"(GOFF), "i"(V) : "memory");
}
template <int L, int G> DI void dma_x(unsigned voff, const void* sbase, unsigned ldsbase) {
    asm volatile("s_add_u32 m0, %2, %4\n\ts_nop 0\n\tglobal_load_lds_dwordx4 %0, %1 offset:%3" ::"v"(voff), "s"(sbase), "s"(ldsbase), "i"(G), "i"(L - G) : "memory", "scc");
}
DI void ld_kv(v4u (&a)[15], int on  , unsigned k0, unsigned k1, unsigned k4, unsigned vo, v4u rk, const void* v, unsigned lds) {
    unsigned t;
    asm volatile("s_cmp_ge_u32 %5, 80\n\ts_cbranch_scc1 1f\n\ts_add_u32 m0, %12, %13\n\ts_movk_i32 %4, 0x2800\n\t"
                 "buffer_load_dwordx4 %6, %10, 0 offen nt lds\n\ts_add_u32 m0, m0, 0x400\n\tglobal_load_dwordx4 %0, %9, %11 nt\n\t"
                 "buffer_load_dwordx4 %7, %10, 0 offen nt lds\n\ts_add_u32 m0, m0, 0x400\n\tglobal_load_dwordx4 %1, %9, %11 offset:1280 nt\n\t"
                 "buffer_load_dwordx4 %7, %10, %4 offen nt lds\n\ts_add_u32 m0, m0, 0x400\n\ts_movk_i32 %4, 0x5000\n\tglobal_load_dwordx4 %2, %9, %11 offset:2560 nt\n\t"
                 "buffer_load_dwordx4 %7, %10, %4 offen nt lds\n\ts_add_u32 m0, m0, 0x400\n\tglobal_load_dwordx4 %3, %9, %11 offset:3840 nt\n\t"
                 "buffer_load_dwordx4 %8, %10, 0 offen nt lds\n1:"
                 : "=&v"(a[7]), "=&v"(a[8]), "=&v"(a[9]), "=&v"(a[10]), "=&s"(t)
                 : "s"(on), "v"(k0), "v"(k1), "v"(k4), "v"(vo), "s"(rk), "s"(v), "s"(lds), "i"(L_XR) : "scc", "memory");
}
DI void ld_wr(v4u (&a)[4], int on, unsigned o, const void* base) {
    asm volatile("s_cmp_ge_u32 %4, 80\n\ts_cbranch_scc0 1f\n\t.rept 4\n\tglobal_load_dword a[32+1], %5, %6\n\t.endr\n\ts_branch 2f\n1:\n\t"
                 "global_load_dwordx4 %0, %5, %6 nt\n\tglobal_load_dwordx4 %1, %5, %6 offset:1024 nt\n\tglobal_load_dwordx4 %2, %5, %6 offset:2048 nt\n\tglobal_load_dwordx4 %3, %5, %6 offset:3072 nt\n2:"
                 : "=&v"(a[0]), "=&v"(a[1]), "=&v"(a[2]), "=&v"(a[3]) : "s"(on), "v"(o), "s"(base) : "scc");
}
template <int L, int G> DI void dma_1(unsigned voff, const void* sbase, unsigned ldsbase) {
    asm volatile("s_add_u32 m0, %2, %4\n\ts_nop 0\n\tglobal_load_lds_dword %0, %1 offset:%3" ::"v"(voff), "s"(sbase), "s"(ldsbase), "i"(G), "i"(L - G) : "memory", "scc");
}
template <int L> DI void dma_n(unsigned voff, const void* sbase, unsigned ldsbase) {
    asm volatile("s_add_u32 m0, %2, %3\n\ts_nop 0\n\tglobal_load_lds_dwordx4 %0, %1 nt" ::"v"(voff), "s"(sbase), "s"(ldsbase), "i"(L) : "memory", "scc");
}
template <int A, int O> DI void lds2a(unsigned a) { asm volatile("ds_read_b64 a[%c1:%c1+1], %0 offset:%2" ::"v"(a), "i"(A), "i"(O) : "memory"); }
constexpr int L_EAR = L_XR + 8192;
constexpr int dma_goff(int it) { return it < 40 ? (it >> 2) * 128 : ((it - 100) / 5) * 256; }
constexpr int dma_loff(int it) { return it < 40 ? L_XR + ((it >> 2) % 3) * 4096 + (it & 3) * 1024 : ((it - 100) % 5) * 20480 + ((it - 100) / 5) * 2048; }
#define KFS 4
#define APOS 0
#define CWP 2
#define SAF 2
#define KD0 30
#define KST 59
#define KSL 5
#define KSC 11
#define XG0 5
#define XGS 1
#define WOD 1
#define USP 1
#define KSG 0
#define PSP 3
#define SBL 1
#define SMP 1
#define KW2E 0
#define KW2T 2
#define OTR 1
#define KQ0 0
#define KQ1 18
#define KQ2 4
#define KQ3 0
#define NPD 27
constexpr int kQ0 = KQ0, kQ1 = KQ1, kQ2 = KQ2, kQ3 = KQ3;
#define KW2S 16
#define KW2M 0x42
#define KFKS 13
#define KFKG 6
#define KUS 17
#define KTS 19
#define KFWP 13
constexpr int kW2S = KW2S, kW2M = KW2M;
#define KSA 4
constexpr int NW2L = (20 - kW2S) * __builtin_popcount(kW2M), NW2P = NW2L + KSA;
constexpr int P1_PRE = NPD + 7;
constexpr int p1_dma(int i) { return i < 5 ? 100 + i : i < 13 ? i - 5 : i < 18 ? 105 + (i - 13) : i < 22 ? 8 + (i - 18) : i < NPD ? 110 + (i - 22) : 400; }
constexpr int kQ1a = KST > KD0 ? kQ1 * (KST - KD0) / (117 - KD0) : 0;
constexpr int p1_pre(int i) { return i < kQ0 + kQ1a ? p1_dma(i) : i == kQ0 + kQ1a ? 400 : p1_dma(i - 1); }
constexpr int p1_item(int s, int g) {
    if (s == KFS && g == (XG0 == 5 ? 4 : 9)) return 324;
    if (WOD ? (s & 1) && s < 10 && (g == 3 || g == 8) : g == 9 && s < 10) return WOD ? 114 + s + (g == 8) : 115 + s;
    if (s == KSL && (g == KSG || g == KSG + 1)) return 202 + g - KSG;
    if (s == KFWP && g == 1) return 208;
    if (s == KFKS && g == KFKG) return 206;
    if (USP && s == KUS && (g == 0 || g == 2 || g == 4 || g == 7)) return 210 + g / 2;
    if (s == KTS && g == 9) return 209;
    if (s >= kW2S && ((kW2M >> g) & 1)) return 500 + (s - kW2S) * __builtin_popcount(kW2M) + __builtin_popcount(kW2M & ((1 << g) - 1));
    if ((s & 1) == 0 && s <= 12 && g >= XG0 && (g - XG0) % XGS == 0 && (g - XG0) / XGS < 4) return 4 * (s / 2 + 3) + (g - XG0) / XGS;
    return -1;
}
constexpr int p1_cnt(int s, int g) {
    int n = P1_PRE;
    for (int t = 0; t <= s; ++t)
        for (int u = 0; u < 10; ++u) {
            if (t == s && u >= g) break;
            if (p1_item(t, u) >= 0) ++n;
            if (!USP && t == KUS && u == 0) n += 4;
        }
    return n;
}
constexpr int p1_pos(int code) {
    for (int i = 0; i < P1_PRE; ++i) if (p1_pre(i) == code) return i;
    for (int t = 0; t < 20; ++t)
        for (int u = 0; u < 10; ++u) if (p1_item(t, u) == code) return p1_cnt(t, u);
    return -1000;
}
constexpr int p1_lds(int s, int g) {
    constexpr int t0 = XG0 == 5 ? 0 : 5;
    if (s >= 2 && s < 10 && !(s & 1) && g >= t0 && g < t0 + 2) return 2 + s + g - t0;
    if (s == KSC + 1 && g == t0 + 2) return 38;
    if (s >= 14 && s < 19 && !(s & 1) && (g == 5 || g == 8)) return 30 + (s - 14) + g / 8;
    if (s == 19 && (g == 0 || g == 4)) return 36 + g / 4;
    if (s == 19 && g == 7) return 39;
    return -1;
}
constexpr int p1_nl(int s, int g0, int g1) {
    int n = 0;
    for (int g = g0; g < g1; ++g) n += (s < 19 && g < 5) + ((s & 1) && s < 19 && g >= 5 && g < 9) + (p1_lds(s, g) >= 0);
    return n;
}
constexpr int p1_cap(int s, int g) {
    int u = p1_nl(s, g, g + 1) + (g == 9 && s < 19) + ((s & 1) && s < 19 && g == 5) + ((s & 1) == 0 && g == XG0 - 1);
    const int it = p1_item(s, g), nt = g < 9 ? p1_item(s, g + 1) : -1;
    u += (it >= 0) + (nt >= 0 && nt < 200) + (it == 324);
    if ((s == 19 && g == 2) || (g == 9 && ((s & 3) == 2 || s == KUS - 1 || s == 18))) u += 2;
    return u < 2 ? 2 - u : 0;
}
constexpr int NFILL = 112;
constexpr int p1_from(int f) { return f < 40 ? 0 : f < 52 ? 9 : f < 72 ? KSC : 14; }
constexpr int p1_fill(int s, int g, int e) {
    int f = 0;
    for (int t = 0; t <= s; ++t)
        for (int u = 0; u < 10; ++u)
            for (int v = 0; v < p1_cap(t, u); ++v) {
                const bool go = f < NFILL && p1_from(f) <= t;
                if (t == s && u == g && v == e) return go ? f : -1;
                f += go;
                if (f == 40) f = 48;
            }
    return f;
}
static_assert(p1_fill(13, 10, 0) >= 52, "");
constexpr bool p1_all() {
    for (int i = 0; i < 40; ++i) if (p1_pos(i) < 0) return false;
    for (int i = 100; i < 125; ++i) if (p1_pos(i) < 0) return false;
    for (int i = 0; i < NW2L; ++i) if (p1_pos(500 + i) < 0) return false;
    return p1_pos(202) >= 0 && p1_pos(203) >= 0 && p1_pos(206) >= 0 && p1_pos(208) >= 0 && p1_pos(209) >= 0 && p1_pos(324) >= 0;
}
static_assert(p1_all() && p1_lds(14, 5) == 30 && p1_lds(14, 8) == 31 && p1_lds(16, 5) == 32, "");
constexpr int ring_req(int G, int I, int R, int X) {
    int n = I, e = 0;
    for (int t = 0; t <= G; ++t) {
        int r = -1;
        if (t % 5 == 0 || t % 5 == 3) {
            if (n < 40 && 5 * (n - R) + 4 < t) r = n++;
            else if (e < X && t % 10 == 3) r = 100 + e++;
        }
        if (t == G) return r;
    }
    return -1;
}
constexpr int g2_adr(int i, int kk) {
    int n = 0;
    for (int t = 0; t <= i; ++t)
        for (int u = 0; u < 5; ++u) {
            const bool ok = u == 4 || (u == 2 && (t < 11 || t > 15)) || (u == 3 && t < 5);
            if (t == i && u == kk) return ok ? n : -1;
            n += ok;
        }
    return -1;
}
constexpr int p1_lim(int n) { return n > 63 ? 63 : n; }
template <int IMM> DI void gld4s(v4u& dst, unsigned voff, const void* sbase) { asm volatile("global_load_dwordx4 %0, %1, %2 offset:%3 sc1" : "=v"(dst) : "v"(voff), "s"(sbase), "i"(IMM)); }
template <int IMM> DI void gld2s(v2u& dst, unsigned voff, const void* sbase) { asm volatile("global_load_dwordx2 %0, %1, %2 offset:%3 sc1" : "=v"(dst) : "v"(voff), "s"(sbase), "i"(IMM)); }
template <class T> DI void pin(T& v) { asm volatile("" : "+v"(v)); }
DI void mul2(float& a, float& b, float x, float y, float s) { asm volatile("v_mul_f32 %0, %2, %4\n\tv_mul_f32 %1, %3, %4" : "=&v"(a), "=&v"(b) : "v"(x), "v"(y), "v"(s)); }
DI void rot1(unsigned& a, unsigned x) { asm volatile("v_alignbit_b32 %0, %1, %1, 16" : "=v"(a) : "v"(x)); }
DI void mix1(unsigned& a, unsigned x, unsigned y) { asm volatile("v_bfi_b32 %0, %3, %1, %2" : "=v"(a) : "v"(x), "v"(y), "s"(0xffffu)); }
DI void ror1(unsigned& o, unsigned prev) { asm volatile("v_mov_b32_dpp %0, %1 wave_ror:1 row_mask:0xf bank_mask:0xf" : "=v"(o) : "v"(prev)); }
DI void shr1b(unsigned& o, unsigned x) { asm volatile("v_mov_b32_dpp %0, %1 wave_shr:1 row_mask:0xf bank_mask:0xf" : "+v"(o) : "v"(x)); }
DI void xor1(float& a, unsigned x, unsigned s) { asm volatile("v_xor_b32 %0, %1, %2" : "=v"(a) : "v"(x), "v"(s)); }
template <int G> DI void bc1(float& a, unsigned x) { asm volatile("v_mov_b32_dpp %0, %1 row_newbcast:%2 row_mask:0xf bank_mask:0xf" : "=v"(a) : "v"(x), "i"(G)); }
template <int G> DI void mulb(float& o, unsigned x, float y) { asm volatile("v_mul_f32_dpp %0, %1, %2 row_newbcast:%3 row_mask:0xf bank_mask:0xf" : "=v"(o) : "v"(x), "v"(y), "i"(G)); }
template <int N> DI void lgkm() { __builtin_amdgcn_s_waitcnt(0xC07F | (N << 8)); asm volatile("" ::: "memory"); }
DI void unp2(float& a, float& b, unsigned x) { asm volatile("v_lshlrev_b32 %0, 16, %2\n\tv_and_b32 %1, 0xffff0000, %2" : "=&v"(a), "=&v"(b) : "v"(x)); }
#define GL(a) "global_load_dwordx4 a[" #a ":" #a "+3], %7, %8 sc1\n\tglobal_load_dwordx4 a[" #a "+4:" #a "+7], %7, %8 offset:256 sc1\n\t"
#define AR(d, a, o) "v_accvgpr_read_b32 " d ", a[" #a "+" #o "]\n\t"
#define GS_(a) "s_waitcnt vmcnt(4)\n\t" AR("%2", a, 1) AR("%3", a, 3) AR("%4", a, 5) AR("%5", a, 7) AR("%0", a, 0) AR("%1", a, 2)                              \
    "v_xor_b32 %2, %9, %2\n\tv_bitop3_b32 %4, %4, %9, %2 bitop3:0xbe\n\tv_bitop3_b32 %4, %3, %9, %4 bitop3:0xbe\n\tv_bitop3_b32 %4, %5, %9, %4 bitop3:0xbe\n\t" \
    AR("%2", a, 4) AR("%3", a, 6) "v_cmp_ne_u32 vcc, 0, %4\n\ts_cbranch_vccz 2f\n\t" GL(a)
template <int A, int D> DI void gran_req(unsigned off, const void* base) {
    if constexpr (D > 0) __builtin_amdgcn_s_sleep(D);
    asm volatile("global_load_dwordx4 a[%c2:%c2+3], %0, %1 sc1\n\tglobal_load_dwordx4 a[%c2+4:%c2+7], %0, %1 offset:256 sc1" ::"v"(off), "s"(base), "i"(A) : "memory");
}
DI void pipe_gran(float& a, float& d, unsigned off, const void* base, unsigned tag, bool& dead) {
    unsigned n = dead ? 0u : kSpinCap, y, z;
    float a1, d1;
    asm volatile("1:\n\t" GS_(32) GS_(40) GS_(48) "s_sub_u32 %6, %6, 1\n\ts_cbranch_scc0 1b\n2:"
                 : "=&v"(a), "=&v"(d), "=&v"(a1), "=&v"(d1), "=&v"(y), "=&v"(z), "+s"(n) : "v"(off), "s"(base), "s"(tag) : "memory", "vcc", "scc");
    dead = __builtin_amdgcn_readfirstlane(n) == ~0u;
    a += a1; d += d1;
}
DI void fma2(float& a, float& b, float x, float y, float s, float t, float u, float v) {
    asm volatile("v_fma_f32 %0, %2, %4, %6\n\tv_fma_f32 %1, %3, %5, %7" : "=&v"(a), "=&v"(b) : "v"(x), "v"(y), "v"(s), "v"(t), "v"(u), "v"(v));
}
DI void fma2p(f32x2& o, float x, float y, float r, float u, float v) {
    asm("v_fmac_f32 %0, %1, %2" : "+v"(u) : "v"(x), "v"(r));
    asm("v_fmac_f32 %0, %1, %2" : "+v"(v) : "v"(y), "v"(r));
    o = f32x2{u, v};
}
DI void swp2n(unsigned& a, unsigned& b, unsigned& c, unsigned& d) { asm volatile("v_permlane16_swap_b32 %0, %1\n\tv_permlane16_swap_b32 %2, %3" : "+v"(a), "+v"(b), "+v"(c), "+v"(d)); }
DI void cp2(unsigned& a, unsigned& b, f32x4 v) { asm volatile("v_cvt_pk_bf16_f32 %0, %2, %3\n\tv_cvt_pk_bf16_f32 %1, %4, %5" : "=&v"(a), "=v"(b) : "v"(v.x), "v"(v.y), "v"(v.z), "v"(v.w)); }
DI void zero2(v2u& a) { asm volatile("v_mov_b64 %0, 0" : "=v"(a)); }
DI void mov1(unsigned& a, unsigned b) { asm volatile("v_mov_b32 %0, %1" : "=v"(a) : "v"(b)); }
DI void sw16(unsigned& a, unsigned& b) { asm volatile("v_permlane16_swap_b32 %0, %1" : "+v"(a), "+v"(b)); }
DI void sw32(unsigned& a, unsigned& b) { asm volatile("v_permlane32_swap_b32 %0, %1" : "+v"(a), "+v"(b)); }
DI void mrg8(float& a, float b) { asm volatile("v_mov_b32_dpp %0, %1 quad_perm:[0,1,2,3] row_mask:0xf bank_mask:0xc" : "+v"(a) : "v"(b)); }
DI void sel1(float& o, unsigned a, unsigned b, u64 m) { asm volatile("v_cndmask_b32 %0, %1, %2, %3" : "=v"(o) : "v"(a), "v"(b), "s"(m)); }
DI void ne1(u64& m, v2u tag, v2u a) { asm volatile("v_cmp_ne_u64 %0, %1, %2" : "=s"(m) : "v"(tag), "v"(a)); }
DI void ne2(u64& m, v2u tag, v2u a, v2u b) { asm volatile("v_cmp_ne_u64 vcc, %1, %2\n\tv_cmp_ne_u64 %0, %1, %3\n\ts_or_b64 %0, %0, vcc" : "=&s"(m) : "v"(tag), "v"(a), "v"(b) : "vcc", "scc"); }
template <int G> DI void bc2b(float& a, float& b, float x, float y) {
    asm volatile("v_mov_b32_dpp %0, %2 row_newbcast:%4 row_mask:0xf bank_mask:0xf\n\tv_mov_b32_dpp %1, %3 row_newbcast:%4 row_mask:0xf bank_mask:0xf" : "=&v"(a), "=&v"(b) : "v"(x), "v"(y), "i"(G));
}
template <class T> DI void pins(T& v) { asm volatile("" : "+s"(v)); }
template <int I> DI void gld_w2(unsigned voff, const void* sbase) {
    asm volatile("global_load_dwordx4 " WA(2) ", %0, %1 offset:%3 sc1" ::"v"(voff), "s"(sbase), "i"(W2R + 4 * I), "i"((I % 5) * 1024 - 2048));
}
DI void gld2t(v2u& dst, unsigned voff, const void* sbase) { asm volatile("global_load_dwordx2 %0, %1, %2 sc1" : "+v"(dst) : "v"(voff), "s"(sbase)); }
DI void undef4(v4u (&x)[4]) { asm volatile("" : "=v"(x[0]), "=v"(x[1]), "=v"(x[2]), "=v"(x[3])); }
DI void wait1(v2u& v) { asm volatile("s_waitcnt vmcnt(0)" : "+v"(v)::"memory"); }
template <int N> DI void wait_fkn(v2u& v, v2u& u) { asm volatile("s_waitcnt vmcnt(%2)" : "+v"(v), "+v"(u) : "i"(N) : "memory"); }
DI void ld_ur(unsigned o, const void* base) {
    asm volatile("global_load_dwordx4 a[40:40+3], %0, %1 sc1\n\tglobal_load_dwordx4 a[44:44+3], %0, %1 offset:1024 sc1\n\t"
                 "global_load_dwordx4 a[48:48+3], %0, %1 offset:2048 sc1\n\tglobal_load_dwordx4 a[52:52+3], %0, %1 offset:3072 sc1" ::"v"(o), "s"(base));
}
template <int I> DI void ld_ur1(unsigned o, const void* base) { asm volatile("global_load_dwordx4 a[%c2:%c2+3], %0, %1 offset:%3 sc1" ::"v"(o), "s"(base), "i"(40 + 4 * I), "i"(1024 * I)); }
constexpr int kUY = KSA + (USP ? p1_cnt(20, 0) - 1 - p1_pos(213) : p1_cnt(20, 0) - p1_cnt(KUS, 0) - 4);
DI void st_ur(unsigned a) {
    asm volatile("s_waitcnt vmcnt(%1)\n\tds_write_b128 %0, a[40:40+3] offset:480\n\tds_write_b128 %0, a[44:44+3] offset:1504\n\t"
                 "ds_write_b128 %0, a[48:48+3] offset:2528\n\tds_write_b128 %0, a[52:52+3] offset:3552" ::"v"(a), "i"(kUY < NW2P ? kUY : NW2P) : "memory");
}
template <int A> DI void rda(unsigned& v) { asm volatile("v_accvgpr_read_b32 %0, a[%c1+0]" : "=v"(v) : "i"(A)); }
DI void poll1(unsigned a, const void* base) { asm volatile("global_load_dwordx2 a[36:36+1], %0, %1 sc1" ::"v"(a), "s"(base)); }
DI void poll2(unsigned a, unsigned b, const void* base) { asm volatile("global_load_dwordx2 a[36:36+1], %0, %2 sc1\n\tglobal_load_dwordx2 a[36+2:36+3], %1, %2 sc1" ::"v"(a), "v"(b), "s"(base)); }
DI void touch_(unsigned a, const void* base) { asm volatile("global_load_dword a[32+1], %0, %1" ::"v"(a), "s"(base)); }
template <int IMM> DI void gld4t(v4u& dst, unsigned voff, const void* sbase) { asm volatile("global_load_dwordx4 %0, %1, %2 offset:%3 sc1" : "+v"(dst) : "v"(voff), "s"(sbase), "i"(IMM)); }
template <int N> DI void vm_wait() { asm volatile("s_waitcnt vmcnt(%0)" ::"i"(N) : "memory"); }
DI float dot2a(unsigned a, unsigned b, float c) {
    return __builtin_amdgcn_fdot2_f32_bf16(__builtin_bit_cast(bf16x2v, a), __builtin_bit_cast(bf16x2v, b), c, false);
}
DI void disc(unsigned l0, unsigned l1, unsigned l2, f32x2& lam, f32x2& co) {
    const float dt = rbf(__builtin_amdgcn_exp2f(lo16f(l2) * 1.44269504088896f));
    const float lre = lo16f(l0), lim = lo16f(l1);
    const float e_ = __builtin_amdgcn_exp2f(dt * lre * 1.44269504088896f);
    const float rv = __builtin_amdgcn_fractf(dt * lim * 0.159154943091895f);
    lam = f32x2{e_ * __builtin_amdgcn_cosf(rv), e_ * __builtin_amdgcn_sinf(rv)};
    const float nr = lam.x - 1.0f, ni = lam.y, id = __builtin_amdgcn_rcpf(lre * lre + lim * lim);
    co = f32x2{(nr * lre + ni * lim) * id, (ni * lre - nr * lim) * id};
}
DI f32x4 unpk4(v2u u) { return f32x4{lo16f(u.x), hi16f(u.x), lo16f(u.y), hi16f(u.y)}; }
#define D2 "v_dot2_f32_bf16 "
#define CP "v_cvt_pk_bf16_f32 "
DI void d2x4(unsigned* o, unsigned a0, unsigned a1, unsigned a2, unsigned a3, unsigned b0, unsigned b1, unsigned b2, unsigned b3) {
    asm volatile(D2 "%0, %4, %8, 0\n\t" D2 "%1, %5, %9, 0\n\t" D2 "%2, %6, %10, 0\n\t" D2 "%3, %7, %11, 0"
                 : "=&v"(o[0]), "=&v"(o[1]), "=&v"(o[2]), "=&v"(o[3]) : "v"(a0), "v"(a1), "v"(a2), "v"(a3), "v"(b0), "v"(b1), "v"(b2), "v"(b3));
}
DI void cpx4(unsigned* o, unsigned a0, unsigned a1, unsigned a2, unsigned a3, unsigned b0, unsigned b1, unsigned b2, unsigned b3) {
    asm volatile(CP "%0, %4, %8\n\t" CP "%1, %5, %9\n\t" CP "%2, %6, %10\n\t" CP "%3, %7, %11"
                 : "=&v"(o[0]), "=&v"(o[1]), "=&v"(o[2]), "=&v"(o[3]) : "v"(a0), "v"(a1), "v"(a2), "v"(a3), "v"(b0), "v"(b1), "v"(b2), "v"(b3));
}
DI void cpx4i(unsigned* t, const unsigned* ka) {
    asm volatile(CP "%0, %0, %4\n\t" CP "%1, %1, %5\n\t" CP "%2, %2, %6\n\t" CP "%3, %3, %7"
                 : "+v"(t[0]), "+v"(t[1]), "+v"(t[2]), "+v"(t[3]) : "v"(ka[0]), "v"(ka[1]), "v"(ka[2]), "v"(ka[3]));
}

__global__ __launch_bounds__(256, 1) __attribute__((aligned(4096))) void k_main(Params p) {
    __shared__ __attribute__((aligned(16))) char smem[LDS_BYTES];
    const rsrc_t rs = __builtin_amdgcn_make_buffer_rsrc((void*)p.ws, 0, WS_BYTES, 0x00020000);
    const rsrc_t ro = __builtin_amdgcn_make_buffer_rsrc((void*)p.out, 0, S_ * D_ * 4, 0x00020000);
    const int w0 = __builtin_amdgcn_readfirstlane(threadIdx.x >> 6), b0 = blockIdx.x;
    bool dead = false;
    asm volatile("" ::: "a255");
    float SW[5];
    f32x4 xin[5];
    v2u gt[5];
    f32x4 acc[2][5];
    f32x4 rr[2];
    float tt[2][5][4];
    Lm lam1, lamq;
    f32x2 l8, l32, l128;
    u64 late;
    v4u cA[8], cB[8];
    int sgn_, sg4, ab[4], bb, zpa, uwa, spa, tbw, tbr, tbp, tbq, cwa, cw4, spl, dst[3];
    unsigned w2o, wro, fwo, fko, xso, gso, fgo1, fho1, fho, hso, eo, es, ho[4];
    bool cv0;
    const char* kvtb;
    const char* w2b0[(NW2P + 4) / 5];
    int otw, otr0, otr4;
    int mt1_;
    auto addrs = [&](auto layer_, int lane, int w, int b) __attribute__((always_inline)) {
        constexpr int layer = layer_.value;
        const int c = lane & 15, q = lane >> 4;
        TILES()
        static_assert(l_sg(0, 0) == SC + 0xA900 && l_sg(0, 1) == SC + 0xB400 && l_sg(0, 2) == SC + 0xCE00 && l_sg(0, 3) == SC + 0xDC00, "");
        sgn_ = (layer ? l_sg(1, 0) + w * INBOX : SC + (int)((0xDC00CE00B400A900ull >> (16 * w)) & 0xffff)) + (q >> 1) * 640 + (q & 1) * 8;
        const int c4 = (c >> 1) * 16 + (c & 1) * 4;
        sg4 = sgn_ + c4; sgn_ += c * 16;
        const int pl = lane < 40 ? lane : 39, tbb = l_tb(layer, 0) + w * (layer ? INBOX : 5120);
        spl = SC + pl * 16;
        eo = OFF_EG + (unsigned)(layer * 163840 + nt1 * 640 + pl * 16);
        es = eo + (unsigned)(mt1 * 10240);
        spa = l_rs(layer, 0) + (2 * (mt2 & 1) + (w >> 1)) * (layer ? INBOX : 256) + (2 * (w & 1) + (c >> 3)) * 64 + (c & 7) * 4;
        tbw = tbb + c * 64 + ((q ^ ((c >> 1) & 3)) * 16);
        tbr = tbb + (2 * (c >> 2) + (q & 1)) * 64 + (((2 * (q >> 1) + ((c >> 1) & 1)) ^ (c >> 2)) * 16) + (c & 1) * 8;
        hso = OFF_HF + (unsigned)(layer * 5242880 + ((8 * mt1 + 2 * w + (q >> 1)) * 160 + 10 * nt1 + 5 * (q & 1)) * 256 + c * 16);
        tbp = tbb + (8 * (q & 1) + (c >> 2)) * 64 + (((2 * (q >> 1) + ((c >> 1) & 1)) ^ (c >> 3)) * 16) + (c & 1) * 8;
        tbq = tbp ^ 32;
        cwa = L_CW + c * 16; cw4 = L_CW + c4;
        pin(sg4); pin(tbp); pin(tbq); pin(cw4);
        fho1 = lane ? kNone : OFF_FH + (unsigned)((((layer * 32 + 2 * mt1 + (w >> 1)) * 4 + (nt1 >> 2)) * 8 + (nt1 & 3) * 2 + (w & 1)) * 128);
        fho = OFF_FH + (unsigned)(((layer * 32 + mt2) * 4 + w) * 1024 + (lane >> 3) * 128);
        pin(sgn_); pin(spl); pin(eo); pin(es); pin(spa); pin(tbw); pin(tbr); pin(hso); pin(cwa); pin(fho1); pin(fho);
        const unsigned hb_ = OFF_HF + (unsigned)(layer * 5242880 + (4 * mt2 * 160 + 40 * w) * 256);
#pragma unroll
        for (int m = 0; m < 4; ++m) { ho[m] = hb_ + (unsigned)(((m + 1 + w) & 3) * 40960 + lane * 16); pin(ho[m]); }
#pragma unroll
        for (int m = 0; m < 3; ++m) { dst[m] = SC + ((w + 1 + m) & 3) * INBOX + m * 5120 + lane * 16; pin(dst[m]); }
        mt1_ = mt1;
        if constexpr (layer == 0) {
            const int r0 = SC + (w < 3 ? w : 0) * REGSZ, y0 = 2031 + 8 * q - c, l34 = lane < 33 ? lane : 33;
#pragma unroll
            for (int i = 0; i < 4; ++i) { ab[i] = r0 + ((y0 & 1) ? CV_O : CV_E) + ((y0 + 1) >> 1) * 4 - 448 - 512 - 1024 * i; pin(ab[i]); }
            bb = r0 + 32 * c + 16 * q;
            zpa = w < 3 ? r0 + l34 * 16 + (l34 >= 30 ? CV_E + 4096 - 480 : 0) + (l34 >= 32 ? CV_O + 4100 - 32 - CV_E - 4096 : 0) : SC + 47360 + lane * 16;
            uwa = (w < 3 ? r0 : SC + 47360 - 480) + lane * 16;
            xso = (unsigned)((64 * mt2 + 16 * w + (lane >> 2)) * D_ + 80 * nt2 + 8 * ((lane & 3) ^ q)) * 2u;
            gso = OFF_GT + (unsigned)((80 * nt2 + i32 + 32 * w) * GSTR + (q & 1) * 512 + (c >> 2) * 128 + (q >> 1) * 64 + (c & 3) * 16);
            fgo1 = lane == 0 && i32 + 32 * w < 80 ? OFF_FG + (unsigned)((nt2 * 96 + i32 + 32 * w) * 128) : kNone;
            pin(bb); pin(zpa); pin(uwa); pin(xso); pin(gso); pin(fgo1);
        }
        SCHED();
    };
    unsigned tag0, tag1;
    v2u tagv;
#define FLAG(off) { if (lane == 0) st64(tagv, rs, off); }
#define ISTAG(f) (((f.x ^ tag0) | (f.y ^ tag1)) == 0u)
#define KW_REQ() { ld_ur(wro, kvtb); static_for<NW2P>(LAMBDA(i_) { gld_w2<i_.value>(w2o, p.ws + 2048 + (i_.value / 5) * 5120); }); }
#define FW_OK() { if (__builtin_expect(late != 0ull, 0)) { WAVE_WAIT({ const v2u fk_ = ld64(rs, fko); const v2u fw_ = ld64(rs, fwo); ok = ISTAG(fk_) && ISTAG(fw_); }) KW_REQ() } }
#define WF(g, s) *(const v4u*)(smem + wa[(s) & 1][(g) < 4 ? (g) & 1 : 2] + ((g) & 2) * 20480 + ((s) >> 1) * 1024)
#define TT_GAP(n) { constexpr int mb_ = (n) / 10, nb_ = ((n) % 10) / 2, e_ = ((n) & 1) * 2; \
        mul2(tt[mb_][nb_][e_], tt[mb_][nb_][e_ + 1], rm_[4 * mb_ + e_], rm_[4 * mb_ + e_ + 1], SW[nb_]); }
    {
        const int tid = threadIdx.x, lane = tid & 63, w = w0, b = b0;
        const int c = lane & 15, q = lane >> 4;
        TILES()
        STAMP(0)
        const unsigned lds0 = (unsigned)(size_t)(LAS char*)smem;
        unsigned nonce;
        unsigned cpf0;
        {
            u64 pc;
            int len;
            asm volatile("s_getpc_b64 %0\nkpf_begin:\n\ts_mov_b32 %1, kpf_end-kpf_begin\n\ts_max_i32 %1, %1, kpf_end2-kpf_begin\n\ts_max_i32 %1, %1, kpf_end3-kpf_begin" : "=s"(pc), "=s"(len)::"scc");
            const int o0 = (((j & 15) * 4 + w) + 64 * (lane >> 3)) * 128;
            asm volatile("global_load_dword %0, %1, %2" : "=v"(cpf0) : "v"(o0 < len ? o0 : 0), "s"(pc));
        }
        const bool cv = 32 * w + i32 < 80;
        const int chn = 80 * nt2 + i32 + 32 * (cv ? w : 0);
        const bool kw = 32 * w + 31 - i32 < 80;
        const int ku = 80 * nt2 + (w == 2 ? 64 + (i32 & 15) : i32 + 32 * (w & 1)), kc = ku >> 6, kr = ku & 63;
        const int lc = cv ? lane : 8;
        const unsigned xl = lds0 + (unsigned)(w * REGSZ);
        v4u ax[15], sa[5], cr[2], wr[4];
        unsigned uL[3], uM[3], sdq, kso;
        const int cu_ = (b < 200 ? b : 0) * 4 + w, cdb = cu_ / 20, ccg = cu_ % 20;
        {
            unsigned cpo = (unsigned)(32 * ccg + (lane & 31)) * 2u, cpm = (unsigned)(40 * nt1 + (lane < 39 ? lane : 39)) * 2u;
            unsigned k1 = (unsigned)(32 * kr + 5 + (lane >> 3)) * 1280u + (unsigned)(64 * kc + 8 * (lane & 7)) * 2u, k0 = k1 - 10240u, k4 = lane < 24 ? k1 + 30720u : kNone;
            unsigned vo = k1 - 6400u + (unsigned)(lane >> 3) * 3840u;
            pin(cpo); pin(cpm); pin(k0); pin(k1); pin(k4); pin(vo);
            SCHED();
            gldu16(uL[0], cpo, p.Lre); gldu16(uL[1], cpo, p.Lim); gldu16(uL[2], cpo, p.logdt);
            gldu16(uM[0], cpm, p.Lre); gldu16(uM[1], cpm, p.Lim); gldu16(uM[2], cpm, p.logdt);
            const v4u rk = {(unsigned)(u64)p.k, (unsigned)((u64)p.k >> 32) & 0xffffu, S_ * D_ * 2u, 0x00020000u};
            ld_kv(ax, 32 * w + 31 - i32, k0, k1, k4, vo, rk, p.v, xl);
            SCHED();
        }
        auto small_ = [&]() __attribute__((always_inline)) {
            kso = (unsigned)(512 * kc + lane * 4);
            dma_1<L_EAR + 2048, 0>(kso, p.shk, xl); dma_1<L_EAR + 2304, 256>(kso, p.shk, xl);
            gld1s(nonce, kso & 4u, p.ws + OFF_NONCE);
            SCHED();
        };
        if (SMP == 0) small_();
        const u16* sab = w < 2 ? p.x0 : w == 2 ? p.Bre : p.Bim;
        unsigned sao;
        {
            int r0 = w < 2 ? 128 * mt1 + 8 * nt1 + 4 * w : 40 * nt1 + mt1, r1 = w < 2 ? 1 : 16, r2 = w < 2 ? 2047 : 40 * nt1 + 32 + (mt1 >> 1);
            pins(sab); pins(r0); pins(r1); pins(r2);
            const int r = r0 + q * r1;
            sao = (unsigned)((r < r2 ? r : r2) * 1280 + c * 16);
            pin(sao);
        }
        if (SAF == 0) static_for<5>(LAMBDA(i) { gld<256 * i.value>(sa[i.value], sao, sab); });
        SCHED();
        if (w == TW0 && !kw) tour<0>();
        STAMP(22)
        const int row8 = lane >> 3;
        const unsigned wvo = (unsigned)(40 * nt1 + row8) * 1280u + (unsigned)(((lane & 7) ^ ((w & 1) | ((row8 >> 1) << 1))) * 16 + (w >> 1) * 128);
        const char* const wbase = (const char*)((w & 1) ? p.Bim : p.Bre);
        const unsigned wl = lds0 + (unsigned)(((w & 1) * 10 + (w >> 1)) * 1024);
        const unsigned xvo0 = (unsigned)(8 * (row8 >> 1) + 2 * (row8 & 1)) * 1280u + (unsigned)(((lane & 7) ^ ((row8 >> 1) << 1)) * 16);
        const unsigned xvo1 = (xvo0 ^ 16u) + 1280u;
        const char* const xbase = (const char*)p.x0 + (size_t)(128 * mt1 + 32 * w) * 1280;
        unsigned xv[4] = {xvo0, xvo1, xvo0 + 5120u, xvo1 + 5120u}, wv[5];
#pragma unroll
        for (int i = 0; i < 5; ++i) { wv[i] = wvo + i * 10240u; pin(wv[i]); if (i < 4) pin(xv[i]); }
#define DMA_M0(it) SETM0<dma_loff(it) - dma_goff(it)>((it) < 40 ? xl : wl)
#define DMA_LD1(it, nx_) GLD1<dma_goff(it), dma_loff(nx_) - dma_goff(nx_)>((it) < 40 ? xv[(it) & 3] : wv[(it) < 40 ? 0 : ((it) - 100) % 5], (it) < 40 ? xbase : wbase, (nx_) < 40 ? xl : wl)
#define DMA_LD(it) GLD0<dma_goff(it)>((it) < 40 ? xv[(it) & 3] : wv[(it) < 40 ? 0 : ((it) - 100) % 5], (it) < 40 ? xbase : wbase)
        const int swz = (c & 1) | ((c >> 2) << 1);
        int wa[2][3];
#pragma unroll
        for (int i = 0; i < 2; ++i) {
            wa[i][0] = (c >> 3) * 20480 + (c & 7) * 128 + (((4 * i + q) ^ (c & 6)) * 16); wa[i][1] = (wa[i][0] ^ 16) + 10240;
            wa[i][2] = 81920 + (c & 1) * 10240 + (c >> 1) * 128 + (((4 * i + q) ^ ((c & 1) | ((c >> 2) << 1))) * 16);
            pin(wa[i][0]); pin(wa[i][1]); pin(wa[i][2]);
        }
        int xlane0 = L_XR + w * REGSZ + (c & 1) * 1024 + (c >> 1) * 128 + ((q ^ swz) * 16);
        int xlane1 = L_XR + w * REGSZ + (c & 1) * 1024 + (c >> 1) * 128 + (((4 + q) ^ swz) * 16);
        unsigned cvo = (unsigned)((16 * cdb + (lane >> 2)) * D_ + 32 * ccg + 8 * ((lane & 3) ^ q)) * 2u, for0, for1;
        int ear = L_EAR + w * REGSZ + c * 64 + (q ^ (c >> 2)) * 16, eas = L_EAR + 2048 + w * REGSZ + (lane & 7) * 64;
        int kra = L_XR + w * REGSZ + (lane >> 3) * 512 + (lane & 7) * 16;
        pin(ear); pin(eas); pin(kra);
        {
            const int G = 4 * ccg + q, g5 = G % 5, ch = 2 * (G - g5) + 5 * (g5 & 1) + (g5 & 6), ci = ch + (g5 < 4 ? 1 : 5);
            const unsigned fo = OFF_W2F + (unsigned)(((cdb / 5) * 200 + cdb % 5) * 1024 + c * 16);
            for0 = fo + (unsigned)((ch >> 2) * 5120 + (ch & 3) * 256); for1 = fo + (unsigned)((ci >> 2) * 5120 + (ci & 3) * 256);
        }
        unsigned sto = OFF_ST + (unsigned)((mt1 * 16 + 4 * w + q) * 256 + c * 8), swo = OFF_ST + (unsigned)(((lane >> 2) * 16 + nt1) * 256 + 128 + (lane & 3) * 16);
        unsigned kvo = OFF_KVT + (unsigned)((64 * kc + 8 * (lane >> 3) + ((lane >> 2) & 1)) * KSTR + (8 * kc + (lane >> 3)) * KPS + (32 * kr + 8 * (lane & 3)) * 2);
        int kbw = L_KB + w * 1024 + (lane & 7) * 128 + (((lane >> 3) * 8) ^ ((lane & 7) * 16));
        int kbr = L_KB + w * 1024 + (lane >> 3) * 128 + (((lane & 7) * 16) ^ ((lane >> 3) * 16));
        const unsigned dum = kNone;
        unsigned fko1 = OFF_FK + (unsigned)((64 * kc + kr) * 32);
        if (!kw) kvo = dum;
        if (b >= 200) { for0 = dum; for1 = dum; cvo = (unsigned)((b * 4 + w) * 128); }
        wro = (unsigned)(chn * 4096 + lc * 16);
        cv0 = cv;
        pin(kvo); pin(kbw); pin(kbr); pin(fko1);
        pin(xlane0); pin(xlane1); pin(cvo); pin(for0); pin(for1); pin(sto); pin(swo); pin(wro);
        SCHED();
        auto pre = LAMBDA(i_) {
            constexpr int it = p1_dma(i_.value);
            SCHED();
            DMA_M0(it);
            asm volatile("s_nop 0");
            DMA_LD(it);
            SCHED();
        };
        fko = OFF_FK + (unsigned)(((chn & ~63) + (cv ? lane : 0)) * 32);
        { const int e_ = (lane & 7) < w ? w : (lane & 7) > w + 4 ? w + 4 : (lane & 7);
          fwo = OFF_FW + (unsigned)(((5 * nt2 + (lane < 40 ? lane >> 3 : 4)) * 20 + 4 * w + e_) * 32); }
        w2o = OFF_W2F + (unsigned)((nt2 * 40 + 10 * w) * 5120 + lane * 16);
        int ime = SC + (w < 3 ? w * REGSZ + CV_E : 43264) + 1008 - 16 * lane, imo = SC + (w < 3 ? w * REGSZ + CV_O + 4 : 47360) + 1008 - 16 * lane, im3 = w < 3 ? ime + 2048 : imo;
        int tca = L_TAB + c * 136, tcb = tca + 8 + 32 * w + 8 * q, tcc = L_TAB + (32 + (c >> 1)) * 136, tcd = tcc + 8 + 32 * w + 8 * q, tcl = L_TAB + (lane < 40 ? lane : 39) * 136, coa = L_CO + w * REGSZ + q * 64, rsa = l_rs(0, 0) + w * 256 + lane * 4;
        unsigned sgn = (c & 1) ? 0u : 0x80000000u;
        fko1 = kw && lane < 32 ? fko1 : OFF_FW + (unsigned)((b * 4 + w) * 32);
        pin(fko); pin(fwo); pin(w2o); pin(fko1); pin(ime); pin(imo); pin(im3); pin(tca); pin(tcb); pin(tcc); pin(tcd); pin(tcl); pin(coa); pin(rsa); pin(sgn);
        SCHED();
        auto cw_ = [&]() __attribute__((always_inline)) {
            dma_n<L_EAR>(cvo, p.Cre, xl); dma_n<L_EAR + 1024>(cvo, p.Cim, xl);
            dma_1<L_EAR + 2560, 0>((unsigned)(160 * nt2 + (lane < 39 ? lane : 39) * 4), p.Dskip, xl);
            ld_wr(wr, 32 * w + i32, wro, p.ssmk);
            gldu16(sdq, (unsigned)chn * 2u, p.ssmD);
        };
        if (SMP == 1) small_();
        static_assert(SMP == 0 || SAF > 0, "");
        if (SAF == 1) static_for<5>(LAMBDA(i) { gld<256 * i.value>(sa[i.value], sao, sab); });
        if (CWP == 0) cw_();
        STAMP(33)
        asm volatile("s_waitcnt vmcnt(%5)\n\ts_cmp_ge_u32 %4, 80\n\ts_cbranch_scc0 1f\n\ts_waitcnt vmcnt(%6)\n1:" : "+v"(uL[0]), "+v"(uL[1]), "+v"(uL[2]), "+v"(cpf0) : "s"(32 * w + 31 - i32), "i"(12 + (SAF < 2 ? 5 : 0) + (CWP ? 0 : 8)), "i"(3 + (SAF < 2 ? 5 : 0) + (CWP ? 0 : 8)) : "memory", "scc");
        pin(uM[0]); pin(uM[1]); pin(uM[2]);
        {
            f32x2 lam, co;
            disc(uL[0], uL[1], uL[2], lam, co);
            *(f32x2*)(smem + L_CO + w * REGSZ + (lane & 31) * 8) = 2.0f * co;
        }
        SCHED();
        STAMP(35)
        if (SAF == 2) static_for<5>(LAMBDA(i) { gld<256 * i.value>(sa[i.value], sao, sab); });
        if (CWP == 1) cw_();
        if (APOS == 0) addrs(std::integral_constant<int, 0>{}, lane, w, b);
        if (SAF == 3) static_for<5>(LAMBDA(i) { gld<256 * i.value>(sa[i.value], sao, sab); });
        if (CWP == 2) cw_();
        v4u kvt[4];
        unsigned ka[8], kt[8], se[8], vs[8], ko4[4], one2 = 0x3F803F80u;
        pin(one2);
        auto kvop = LAMBDA(n_) {
            constexpr int n = n_.value, h = n ? (n - 1) / 29 : -1, u = n ? (n - 1) % 29 : 26;
            if constexpr (u < 2) d2x4(ka + 4 * u, ax[0][h], ax[1][h], ax[2][h], ax[3][h], se[u], se[u], se[u], se[u]);
            else if constexpr (u == 8) { const v4u sk = ax[11 + h]; se[4] = sk[0] >> 16; se[5] = sk[2] & 0xffff0000u; se[6] = sk[0] & 0xffffu; se[7] = sk[2] << 16; pin(se[4]); pin(se[5]); pin(se[6]); pin(se[7]); }
            else if constexpr (u < 21) {
                constexpr int v = u < 8 ? u - 2 : u - 3, tp = 1 + v / 6, st = v % 6, e = st & 1;
                if constexpr (st < 2) d2x4(kt + 4 * e, ax[tp][h], ax[tp + 1][h], ax[tp + 2][h], ax[tp + 3][h], se[2 * tp + e], se[2 * tp + e], se[2 * tp + e], se[2 * tp + e]);
                else if constexpr (st < 4) cpx4i(kt + 4 * e, ka + 4 * e);
                else d2x4(ka + 4 * e, kt[4 * e], kt[4 * e + 1], kt[4 * e + 2], kt[4 * e + 3], one2, one2, one2, one2);
            }
            else if constexpr (u == 21) { vs[0] = ax[7][h] & 0xffffu; vs[1] = ax[8][h] << 16; vs[2] = ax[9][h] & 0xffffu; vs[3] = ax[10][h] << 16; pin(vs[0]); pin(vs[1]); pin(vs[2]); pin(vs[3]); }
            else if constexpr (u == 22) { vs[4] = ax[7][h] >> 16; vs[5] = ax[8][h] & 0xffff0000u; vs[6] = ax[9][h] >> 16; vs[7] = ax[10][h] & 0xffff0000u; pin(vs[4]); pin(vs[5]); pin(vs[6]); pin(vs[7]); }
            else if constexpr (u == 23) cpx4(ko4, ka[0], ka[2], ka[4], ka[6], ka[1], ka[3], ka[5], ka[7]);
            else if constexpr (u < 26) { constexpr int e = u - 24; d2x4(kt + 4 * e, ko4[2 * e], ko4[2 * e], ko4[2 * e + 1], ko4[2 * e + 1], vs[4 * e], vs[4 * e + 1], vs[4 * e + 2], vs[4 * e + 3]); }
            else if constexpr (u == 26) {
                if constexpr (h < 3) { const v4u sk = ax[12 + h]; se[0] = sk[1] >> 16; se[1] = sk[3] & 0xffff0000u; se[2] = sk[1] & 0xffffu; se[3] = sk[3] << 16; pin(se[0]); pin(se[1]); pin(se[2]); pin(se[3]); }
                else asm volatile("s_nop 2");
            }
            else if constexpr (u == 27) {
                cpx4(ko4, kt[0], kt[2], kt[4], kt[6], kt[1], kt[3], kt[5], kt[7]);
                *(v2u*)(smem + kbw) = v2u{ko4[0], ko4[1]};
                *(v2u*)(smem + (kbw ^ 64)) = v2u{ko4[2], ko4[3]};
            }
            else kvt[h] = *(const v4u*)(smem + kbr);
            SCHED();
        };
        auto deal = [&](auto a_, auto b_, auto n_, auto i_) __attribute__((always_inline)) {
            constexpr int a = a_.value, m = b_.value - a, lo = m * i_.value / n_.value, hi = m * (i_.value + 1) / n_.value;
            static_for<hi - lo>(LAMBDA(e_) { pre(std::integral_constant<int, a + lo + e_.value>{}); });
        };
#define IC(n) std::integral_constant<int, (n)>{}
        static_for<kQ0>(pre);
        static_assert(kQ0 == 0 && KD0 >= 8, "");
        asm volatile("s_waitcnt vmcnt(%4)" : "+v"(ax[7]), "+v"(ax[8]), "+v"(ax[9]), "+v"(ax[10]) : "i"(13 + kQ0) : "memory");
        ax[11] = *(const v4u*)(smem + eas);
        SCHED();
#pragma unroll
        for (int i = 0; i < 7; ++i) { ax[i] = *(const v4u*)(smem + kra + i * 128); SCHED(); }
#pragma unroll
        for (int i = 1; i < 4; ++i) ax[11 + i] = *(const v4u*)(smem + eas + i * 16);
        SCHED();
        pin(nonce);
        STAMP(30)
        unsigned tch = 0u;
        auto kvs = LAMBDA(n_) {
            site<1000 + n_.value>(); SCHED();
            if constexpr (n_.value == KD0) LGKM0()
            kvop(n_);
            if constexpr (n_.value >= KD0) deal(IC(kQ0), IC(kQ0 + kQ1), IC(117 - KD0), IC(n_.value - KD0));
        };
        static_assert((KST - 1) % 29 == 0, "");
        if (kw) static_for<KST>(kvs);
        else {
            if (__builtin_expect(w == 3, !W3O)) {
                f32x2 lam, co, lp[16];
                disc(uM[0], uM[1], uM[2], lam, co);
                lp[0] = cmul(lam, lam); lp[0] = cmul(lp[0], lp[0]); lp[1] = cmul(lp[0], lp[0]);
                lp[0] = f32x2{1.f, 0.f};
                lp[2] = cmul(lp[1], lp[1]); lp[3] = cmul(lp[2], lp[1]); lp[4] = cmul(lp[2], lp[2]);
                const f32x2 z2 = {0.f, 0.f};
                f32x2 t4[4];
                cfma4(t4, lp[4], lp[1], lp[2], lp[3], lp[4], z2, z2, z2, z2);
                lp[5] = t4[0]; lp[6] = t4[1]; lp[7] = t4[2]; lp[8] = t4[3];
                cfma4(t4, lp[8], lp[1], lp[2], lp[3], lp[4], z2, z2, z2, z2);
                lp[9] = t4[0]; lp[10] = t4[1]; lp[11] = t4[2]; lp[12] = t4[3];
                cfma4(t4, lp[8], lp[5], lp[6], lp[7], lp[7], z2, z2, z2, z2);
                lp[13] = t4[0]; lp[14] = t4[1]; lp[15] = t4[2];
                f32x2* tl = (f32x2*)(smem + tcl);
                tl[0] = lam;
#pragma unroll
                for (int i = 0; i < 16; ++i) tl[1 + i] = lp[i];
                SCHED();
                asm volatile("kpf_end3:");
            }
            static_for<kQ1a>(LAMBDA(i_) { pre(IC(kQ0 + i_.value)); });
            undef4(kvt);
            asm volatile("" : "=v"(se[0]), "=v"(se[1]), "=v"(se[2]), "=v"(se[3]));
        }
        tag0 = __builtin_amdgcn_readlane(nonce, 0) + 1u; tag1 = __builtin_amdgcn_readlane(nonce + (tag0 == 0u ? 1u : 0u), 1);
        tagv = v2u{tag0, tag1}; pin(tagv);
        {
            asm volatile("s_waitcnt vmcnt(%5)" : "+v"(sa[0]), "+v"(sa[1]), "+v"(sa[2]), "+v"(sa[3]), "+v"(sa[4]) : "i"(8 + kQ0 + kQ1a) : "memory");
            float s1[2] = {0.f, 0.f}, s2[2] = {0.f, 0.f};
#pragma unroll
            for (int i = 0; i < 5; ++i)
#pragma unroll
                for (int e = 0; e < 4; ++e) { s1[e & 1] = dot2a(sa[i][e], one2, s1[e & 1]); s2[e & 1] = dot2a(sa[i][e], sa[i][e], s2[e & 1]); }
            const float a1 = rowsum16(s1[0] + s1[1]), a2 = rowsum16(s2[0] + s2[1]);
            const float mean = a1 * (1.0f / D_);
            const float var = fmaxf(a2 * (1.0f / D_) - mean * mean, 0.f);
            const float ri = __builtin_amdgcn_rcpf(rbf(__builtin_amdgcn_sqrtf(rbf(rbf(var) + 1e-5f))));
            v2u sv = {__float_as_uint(w >= 2 ? a1 : c ? -ri * rbf(mean) : ri), tag0};
            int o0 = w < 2 ? (mt1 * 16 + nt1) * 256 + 32 * w : ((mt1 >> 2) * 16 + nt1) * 256 + 112 + (mt1 & 3) * 16 + 8 * w, o1 = w < 2 ? 8 : 16384, o2 = w < 2 ? 64 : 0;
            pins(o0); pins(o1); pins(o2);
            unsigned so_ = c < 2 ? OFF_ST + (unsigned)(o0 + q * o1 + c * o2) : kNone;
            pin(sv); pin(so_);
            st64(sv, rs, so_);
            SCHED();
        }
        bool kw2 = kw;
        if (kw2) {
            static_for<117 - KST>(LAMBDA(n_) { kvs(IC(KST + n_.value)); });
        } else {
            if (__builtin_expect(w == 3, !W3O)) {
            const int l = lane, l3 = l & 3;
            unsigned a = OFF_SG + (unsigned)((((((l >> 3) & 1) * 32 + mt2) * 4 + ((l >> 1) & 3)) * 8 + nt2) * 256 + (l & 1) * 128);
            { unsigned t_ = OFF_EG + (unsigned)((l >= 21 ? 163840 - 640 : 0) + (mt1 * 16 + nt1) * 640 + (l < 26 ? l - 16 : 5) * 128); pin(t_); a = l >= 16 ? t_ : a; }
            { unsigned t_ = OFF_PS + (unsigned)(((mt2 * 4 + l3) * 8 + nt2) * 128); pin(t_); a = l >= 28 ? t_ : a; }
            { unsigned t_ = OFF_FH + (unsigned)((((((l >> 2) & 1) * 32 + 2 * mt1 + (l3 >> 1)) * 4 + (nt1 >> 2)) * 8 + (nt1 & 3) * 2 + (l3 & 1)) * 128); pin(t_); a = l >= 32 ? t_ : a; }
            { unsigned t_ = OFF_FX + (unsigned)(((mt1 * 4 + 2 * (mt2 & 1) + (l3 >> 1)) * 16 + nt2 * 2 + (l3 & 1)) * 128); pin(t_); a = l >= 40 ? t_ : a; }
            { unsigned t_ = OFF_FG + (unsigned)((nt2 * 96 + i32 + 16 * ((l3 + 1) & 6)) * 128); pin(t_); a = l >= 48 ? t_ : a; }
            const unsigned big = (l & 7) < 2 ? OFF_HF + (unsigned)((l & 1) * 5242880 + (8 * mt1 * 160 + 10 * nt1) * 256) : (l & 7) == 2 ? OFF_X2F + (unsigned)(2 * mt2 * 40960 + 2560 * nt2)
                               : (l & 7) < 5 ? OFF_KVT + (unsigned)((l & 1) * 640 * 4608 + (80 * nt2 + i32) * GSTR) : OFF_W2F + (unsigned)(nt2 * 204800 + ((l & 7) - 5) * 98304);
            a = l >= 56 ? big : a;
            tch = a;
            pin(tch);
            asm volatile("kpf_end2:");
        }
            static_for<kQ1 - kQ1a>(LAMBDA(i_) { pre(IC(kQ0 + kQ1a + i_.value)); });
            if (w == TW) tour<1>();
            skip<SKIP0>();
            undef4(kvt);
        }
        SCHED();
        STAMP(34)
        if (APOS == 1) addrs(std::integral_constant<int, 0>{}, lane, w, b);
        v4u orr, oi;
        unsigned ie[4][4], io[4][4], iw;
        auto img = LAMBDA(n_) {
            constexpr int i = n_.value / 10, u = n_.value % 10;
            if constexpr (u == 0) { if constexpr (i > 0) ror1(iw, wr[i - 1].w); else { iw = 0u; pin(iw); } }
            else if constexpr (u < 5) rot1(ie[i][u - 1], wr[i][4 - u]);
            else if constexpr (u == 5) shr1b(iw, wr[i].w);
            else if constexpr (u < 9) mix1(io[i][u - 6], wr[i][9 - u], wr[i][8 - u]);
            else mix1(io[i][3], wr[i].x, iw);
        };
        auto tap0 = [&]() __attribute__((always_inline)) {
            wr[0].x = cvtpk(lo16f(wr[0].x) + (lane ? 0.f : lo16f(sdq)), hi16f(wr[0].x));
            pin(wr[0]);
            SCHED();
        };
        {
            static_assert(kQ0 + kQ1 <= 18, "");
            vm_wait<6 + kQ0 + kQ1>();
            cr[0] = *(const v4u*)(smem + ear); cr[1] = *(const v4u*)(smem + ear + 1024);
            static_for<5>(LAMBDA(i) { lds2a<22 + 2 * i.value, 2560 + 32 * i.value>(lds0 + (unsigned)(L_EAR + w * REGSZ + q * 8)); });
            asm volatile("s_waitcnt lgkmcnt(0)" : "+v"(cr[0]), "+v"(cr[1])::"memory");
            static_for<kQ2>(LAMBDA(i_) { pre(IC(kQ0 + kQ1 + i_.value)); });
            static_for<4>(LAMBDA(e_) {
                constexpr int e = e_.value;
                const f32x4 co = *(const f32x4*)(smem + coa + e * 16);
                const f32x2 o0 = lo16f(cr[0][e]) * f32x2{co.x, -co.y} - lo16f(cr[1][e]) * f32x2{co.y, co.x};
                const f32x2 o1 = hi16f(cr[0][e]) * f32x2{co.z, -co.w} - hi16f(cr[1][e]) * f32x2{co.w, co.z};
                orr[e] = cvtpk(o0.x, o1.x); oi[e] = cvtpk(o0.y, o1.y);
            });
            pin(orr); pin(oi);
            SCHED();
            static_for<NPD - kQ0 - kQ1 - kQ2>(LAMBDA(i_) { pre(IC(kQ0 + kQ1 + kQ2 + i_.value)); });
            for (int i = 0; i < 4; ++i) st128(kvt[i], rs, kvo + i * 2 * KSTR);
            st128(orr, rs, for0); st128(oi, rs, for1);
        }
        SCHED();
        STAMP(23)
        if (w == 3) tour<7>();
        asm volatile("s_waitcnt vmcnt(%5) lgkmcnt(0)" : "+v"(wr[0]), "+v"(wr[1]), "+v"(wr[2]), "+v"(wr[3]), "+v"(sdq) : "i"(P1_PRE - 1 - p1_pos(3)) : "memory");
        __builtin_amdgcn_s_barrier();
        asm volatile("" ::: "memory");
        STAMP(24)
        site<10>();
        v4u wf[2][5], af[2][2][2];
#pragma unroll
        for (int nb = 0; nb < 5; ++nb) wf[0][nb] = WF(nb, 0);
#pragma unroll
        for (int i = 0; i < 4; ++i) af[0][i >> 1][i & 1] = *(const v4u*)(smem + ((i >> 1) ? xlane1 : xlane0) + (i & 1) * 2048);
        SCHED();
        tap0();
        site<11>();
        kvtb = p.ws + OFF_KVT + (chn * KPAD + (chn >> 3) * KPS);
        pins(kvtb);
#pragma unroll
        for (int i = 0; i < (NW2P + 4) / 5; ++i) { w2b0[i] = p.ws + 2048 + i * 5120; pins(w2b0[i]); }
        v2u sg, tc[2], fk, fw;
        v4u sw0;
        v2u zq[2];
        float rr_[8];
        unsigned sx[4], sz[4];
        u64 odd = 0xAAAAAAAAAAAAAAAAull;
        pins(odd);
        auto filler = LAMBDA(f_) {
            constexpr int f = f_.value;
            if constexpr (f < 0) {}
            else if constexpr (f < 40) img(IC(f));
            else if constexpr (f == 48) { lam1.r = __uint_as_float(tc[0].x); xor1(lam1.s, tc[0].y, sgn); }
            else if constexpr (f == 49) { lamq.r = __uint_as_float(tc[1].x); xor1(lamq.s, tc[1].y, sgn); }
            else if constexpr (f < 52) zero2(zq[f - 50]);
            else if constexpr (f == 52) { sx[0] = sw0.x; mov1(sx[1], sx[0]); }
            else if constexpr (f == 53) { sz[0] = sw0.z; mov1(sz[1], sz[0]); }
            else if constexpr (f < 56) bc1<f - 54>(rr_[f - 54], sg.x);
            else if constexpr (f == 56) sw16(sx[0], sx[1]);
            else if constexpr (f == 57) sw16(sz[0], sz[1]);
            else if constexpr (f < 60) mov1(sx[f - 56], sx[f - 58]);
            else if constexpr (f < 62) mov1(sz[f - 58], sz[f - 60]);
            else if constexpr (f < 64) sw32(sx[f - 62], sx[f - 60]);
            else if constexpr (f < 66) sw32(sz[f - 64], sz[f - 62]);
            else if constexpr (f < 72) {
                bc1<f - 64>(rr_[f - 64], sg.x);
                if constexpr (f == 66) { SW[0] = __uint_as_float(sx[0]); SW[1] = __uint_as_float(sz[0]); SW[2] = __uint_as_float(sx[1]); SW[3] = __uint_as_float(sz[1]); }
                if constexpr (f == 71) sel1(SW[4], sx[2], sz[2], odd);
            }
            else mulb<8 + (f - 72) % 8>(tt[(f - 72) % 8 / 4][(f - 72) / 8][(f - 72) % 4], sg.x, SW[(f - 72) / 8]);
        };
        auto kstep = LAMBDA(s_) {
            constexpr int s = s_.value, ph = s >> 2, s4 = s & 3, cu = s & 1, nx = cu ^ 1, jj = s >> 1, pp = jj & 1;
            if constexpr (s4 == 3 && ph < 4) {
                constexpr int pw = p1_pos(104 + 5 * (ph + 1)), px = p1_pos(4 * (jj + 1) + 3), pm = pw > px ? pw : px;
                constexpr int vm = p1_lim(p1_cnt(s, 0) - 1 - pm);
                asm volatile("" ::: "memory");
                __builtin_amdgcn_s_waitcnt((vm & 15) | ((vm >> 4) << 14) | 0x70);
                __builtin_amdgcn_s_barrier();
                asm volatile("" ::: "memory");
                STAMP(25 + ph)
                SCHED();
            }
            if constexpr (s == KSC) {
                static_assert(s4 == 3 && p1_pos(203) < p1_pos(4 * (jj + 1) + 3), "");
                pin(sg); pin(sw0);
                if (__builtin_expect(__builtin_amdgcn_ballot_w64(__builtin_amdgcn_bitop3_b32(__builtin_amdgcn_bitop3_b32(sg.y, sw0.y, tag0, 0x7E), sw0.w, tag0, 0xF6) != 0u) != 0ull, 0))
                    WAVE_WAIT({ sg = ld64(rs, sto); sw0 = ld128(rs, swo); ok = ((sg.y ^ tag0) | (sw0.y ^ tag0) | (sw0.w ^ tag0)) == 0u; })
                ORDER();
                STAMP(7)
            }
            if constexpr (s == kW2S && KUS > kW2S) { vm_wait<p1_cnt(s, 0) - 1 - p1_pos(208)>(); SCHED(); }
            if constexpr (s == KUS) {
                static_assert(p1_pos(206) > p1_pos(208), "");
                wait_fkn<p1_cnt(s, 0) - 1 - p1_pos(206)>(fk, fw);
                if (!USP) ld_ur(wro, kvtb);
                SCHED();
            }
            if constexpr (s4 != 3 || ph == 4) lgkm<(s == 0 ? 0 : p1_nl(s - 1, 5 - cu, 10) - 1)>();
            SCHED();
            static_for<10>(LAMBDA(g_) {
                constexpr int g = g_.value, mb = g / 5, k = g % 5, it = p1_item(s, g), nt_ = g < 9 ? p1_item(s, g + 1) : -1;
                constexpr bool d0 = it >= 0 && it < 200, d1 = nt_ >= 0 && nt_ < 200;
                if constexpr (s == 0) mf0(acc[mb][k], af[pp][cu][mb], wf[cu][k]); else mf(acc[mb][k], af[pp][cu][mb], wf[cu][k]);
                SCHED();
                if constexpr (s < 19) {
                    if constexpr (g < 5) wf[nx][g] = WF(g, s + 1);
                    if constexpr (cu == 1 && g == 5) vm_wait<p1_lim(p1_cnt(s, 5) - 1 - p1_pos(4 * (jj + 1) + 3))>();
                    if constexpr (cu == 1 && g >= 5 && g < 9)
                        af[pp ^ 1][(g - 5) >> 1][(g - 5) & 1] = *(const v4u*)(smem + (((g - 5) >> 1) ? xlane1 : xlane0) + ((jj + 1) % 3) * 4096 + ((g - 5) & 1) * 2048);
                }
                if constexpr (cu == 0 && g == XG0 - 1 && s > 0) lgkm<p1_nl(s - 1, 8, 10) + p1_nl(s, 0, XG0 - 1)>();
                constexpr int lq = p1_lds(s, g);
                if constexpr (lq >= 4 && lq < 8) (lq == 4 ? lam1.a : lq == 5 ? lam1.b : lq == 6 ? lamq.a : lamq.b) = *(const f32x2*)(smem + (lq < 6 ? tca : tcb) + (lq & 1) * 2176);
                else if constexpr (lq == 8 || lq == 9) tc[lq - 8] = *(const v2u*)(smem + (lq == 8 ? tcc : tcd));
                else if constexpr (lq == 10) { const f32x2* tp = (const f32x2*)(smem + tcl); l8 = tp[2]; l32 = tp[5]; }
                else if constexpr (lq == 11) l128 = *(const f32x2*)(smem + tcl + 72);
                else if constexpr (lq >= 30 && lq < 33) *(v4u*)(smem + (lq < 32 ? ime + 1024 * (lq - 30) : im3)) = v4u{ie[33 - lq][0], ie[33 - lq][1], ie[33 - lq][2], ie[33 - lq][3]};
                else if constexpr (lq >= 33 && lq < 37) *(v4u*)(smem + imo + 1024 * (36 - lq)) = v4u{io[lq - 33][0], io[lq - 33][1], io[lq - 33][2], io[lq - 33][3]};
                else if constexpr (lq == 37) *(v4u*)(smem + im3 + 1024) = v4u{ie[0][0], ie[0][1], ie[0][2], ie[0][3]};
                else if constexpr (lq == 38) *(unsigned*)(smem + rsa) = sg.x;
                else if constexpr (lq == 39) *(v4u*)(smem + zpa) = v4u{zq[0].x, zq[0].y, zq[1].x, zq[1].y};
                static_for<p1_cap(s, g)>(LAMBDA(e_) { filler(IC(p1_fill(s, g, e_.value))); });
                if constexpr (s == 18 && g == 9) ne2(late, tagv, fk, fw);
                if constexpr (s == 19 && g == 2) { if (__builtin_expect(late != 0ull, 0)) poll2(fko, fwo, p.ws); }
                if constexpr (it >= 0) {
                    if constexpr (d0 && d1) DMA_LD1(it, d1 ? nt_ : 0);
                    else if constexpr (d0) DMA_LD(it);
                    else if constexpr (it == 202) gld2s<0>(sg, sto, p.ws);
                    else if constexpr (it == 203) gld4s<0>(sw0, swo, p.ws);
                    else if constexpr (it == 206) gld2s<0>(fk, fko, p.ws);
                    else if constexpr (it == 208) gld2s<0>(fw, fwo, p.ws);
                    else if constexpr (it == 209) touch_(tch, p.ws);
                    else if constexpr (it >= 210 && it < 214) ld_ur1<it - 210>(wro, kvtb);
                    else if constexpr (it >= 500) gld_w2<it % 50>(w2o, w2b0[(it % 50) / 5]);
                    else { vm_wait<p1_cnt(s, g) - P1_PRE>(); st64(tagv, rs, fko1); }
                }
                if constexpr (d1 && !d0) DMA_M0(d1 ? nt_ : 0);
                site<2000 + 10 * s + g>();
                SCHED();
            });
        };
        static_for<20>(kstep);
        static_for<NFILL - p1_fill(19, 10, 0)>(LAMBDA(i_) { filler(IC(p1_fill(19, 10, 0) + i_.value)); });
        SCHED();
        rr[0] = f32x4{rr_[0], rr_[1], rr_[2], rr_[3]}; rr[1] = f32x4{rr_[4], rr_[5], rr_[6], rr_[7]};
        ACC_FENCE10(acc[0], acc[1]);
        STAMP(1)
    }

    auto layer_body = LAMBDA(layer_) {
        constexpr int layer = layer_.value;
        const int sga = sgn_;
        const bool cv = layer == 0 && cv0;
        v2u fh;
        unsigned ks[10];
        f32x4 ca[8], a2[4][5];
        unsigned gp[4][4];
        auto gst = LAMBDA(i_) {
            constexpr int i = i_.value >> 2, u = i_.value & 3;
            if constexpr (u < 2) cp2(gp[i][2 * u], gp[i][2 * u + 1], ca[2 * i + u]);
            else if constexpr (u == 2) swp2n(gp[i][0], gp[i][2], gp[i][1], gp[i][3]);
            else st128(v4u{gp[i][0], gp[i][1], gp[i][2], gp[i][3]}, rs, gso + i * 1024);
        };
        float rsr, rsm;
        St bv[8];
        {
            St a0, a1;
            auto w2s = LAMBDA(n_) {
                constexpr int lo = KSA * n_.value / 4, hi = KSA * (n_.value + 1) / 4;
                if constexpr (layer == 0) { static_for<hi - lo>(LAMBDA(e_) { constexpr int i = NW2L + lo + e_.value; gld_w2<i>(w2o, w2b0[i / 5]); }); SCHED(); }
            };
            static_for<2>(LAMBDA(m_) {
                constexpr int mb = m_.value;
#pragma unroll
                for (int i = 0; i < 4; ++i) fma2p(bv[4 * mb + i].a, acc[mb][0][i], acc[mb][1][i], rr[mb][i], tt[mb][0][i], tt[mb][1][i]);
#pragma unroll
                for (int i = 0; i < 4; ++i) fma2p(bv[4 * mb + i].b, acc[mb][2][i], acc[mb][3][i], rr[mb][i], tt[mb][2][i], tt[mb][3][i]);
                const f32x4 v = __builtin_elementwise_fma(acc[mb][4], rr[mb], f32x4{tt[mb][4][0], tt[mb][4][1], tt[mb][4][2], tt[mb][4][3]});
#pragma unroll
                for (int i = 0; i < 4; ++i) bv[4 * mb + i].c = v[i];
                SCHED();
                w2s(IC(2 * mb));
                if constexpr (mb == 0) run3(a0, bv[0], bv + 1, lam1); else run4(a1, a0, bv + 4, lam1);
                SCHED();
                if constexpr (mb == 0) w2s(IC(1));
            });
            *(f32x2*)(smem + sga) = a1.a; *(f32x2*)(smem + sga + 256) = a1.b; *(float*)(smem + sg4 + 512) = a1.c;
            SCHED();
            w2s(IC(3));
        }
        SCHED();
        const int w_ = w0;
        {
        const int w = w_, mt1 = mt1_;
        if (layer == 0 && __builtin_expect(late != 0ull, 0)) {
            unsigned f0_, f1_, f2_, f3_;
            asm volatile("s_waitcnt vmcnt(0)\n\t" AR("%0", 36, 0) AR("%1", 36, 1) AR("%2", 36, 2) AR("%3", 36, 3) : "=v"(f0_), "=v"(f1_), "=v"(f2_), "=v"(f3_)::"memory");
            late = __builtin_amdgcn_ballot_w64(((f0_ ^ tag0) | (f1_ ^ tag1) | (f2_ ^ tag0) | (f3_ ^ tag1)) != 0u);
            ORDER();
            if (late == 0ull) KW_REQ()
        }
        asm volatile("s_waitcnt lgkmcnt(0)" ::: "memory");
        __builtin_amdgcn_s_barrier();
        asm volatile("" ::: "memory");
        STAMP(layer ? 14 : 2)
#pragma unroll
        for (int s = 0; s < 10; ++s) ks[s] = (unsigned)s * 1024u;
        constexpr int NW2A = NW2P + 7 * kCR1 + kXA, NW3 = NW2P + kN3E + kN3;
        auto w2ld = LAMBDA(i_) {
            constexpr int i = i_.value;
            gld_w2<i>(w2o, p.ws + 2048 + ks[i / 5] * 5u);
        };
        auto w2a = LAMBDA(i_) {
            constexpr int k = i_.value;
            if constexpr (k < kXA) { w2ld(std::integral_constant<int, NW2P + 2 * k>{}); w2ld(std::integral_constant<int, NW2P + 2 * k + 1>{}); }
            else w2ld(std::integral_constant<int, NW2P + kXA + k>{});
        };
        unsigned xtb = (unsigned)(size_t)(LAS char*)smem + (unsigned)(l_tb(0, 0) + w * 5120);
        auto w2x = LAMBDA(i_) {
            constexpr int i = NW2A + i_.value;
            if constexpr (i < 50) w2ld(std::integral_constant<int, i>{});
            else if constexpr (i < 53) dma_x<(i - 50) * 1024, (i < 52 ? (i - 50) * 64 : 96)>(xso, p.x0, xtb);
        };
        auto w2b = LAMBDA(i_) {
            constexpr int k = i_.value - 7 * kCR1;
            constexpr int ne = KW2E < 53 - NW2A ? KW2E : 53 - NW2A;
            if constexpr (k < ne) w2x(std::integral_constant<int, k>{});
            constexpr int k1 = k - kFHK - 1;
            if constexpr (k1 >= 0 && k1 < KW2T) { w2x(std::integral_constant<int, ne + 2 * k1>{}); w2x(std::integral_constant<int, ne + 2 * k1 + 1>{}); }
            else if constexpr (k1 >= 0) w2x(std::integral_constant<int, ne + KW2T + k1>{});
            static_assert(ne <= kFHK + 1 && 53 - NW2A - ne - KW2T + kFHK < 7 * (8 - kCR1) - kFHP + 1, "");
            if constexpr (k == kFHK) { vm_wait<ne>(); st64(tagv, rs, fho1); STAMP(39) }
            if constexpr (k == kFHK - 1) { STAMP(38) }
            if constexpr (k == 7 * (8 - kCR1) - kFHP) poll1(fho, p.ws);
            static_assert(NW2A + 7 * (8 - kCR1) >= 53 && NW2A <= 50 && NW3 <= NW2A, "");
        };
        auto partb = [&]() __attribute__((always_inline)) {
            const float* sp = (const float*)(smem + spa);
            rsr = sp[0]; rsm = sp[8];
            if (layer == 1) { pin(rsr); pin(rsm); }
            if (layer == 1) {
                static_for<5>(LAMBDA(n_) {
                    constexpr int nb = n_.value;
                    unsigned d0, d1;
                    rda<22 + 2 * nb>(d0); rda<23 + 2 * nb>(d1);
                    a2[3][nb] = xin[nb] + (xin[nb] * rsr + rsm) * unpk4(v2u{d0, d1}); pin(a2[3][nb]);
                });
            }
        };
        constexpr int LBW = layer ? 2 : 3;
        if (w == 3) {
            site<3 + 10 * layer>(); SCHED();
            f32x4 s4[8];
#pragma unroll
            for (int i = 0; i < 8; ++i) { s4[i] = *(const f32x4*)(smem + spl + (l_sg(layer, i & 3) - SC + (i >> 2) * 640)); SCHED(); }
            f32x2 sv[16], x[4], P[4][4];
#pragma unroll
            for (int i = 0; i < 16; ++i) { const f32x4 t = s4[(i >> 2) + 2 * (i & 2)]; sv[i] = (i & 1) ? f32x2{t.z, t.w} : f32x2{t.x, t.y}; }
            unsigned er, ei;
            {
                f32x2 u[4];
                cfma4n(u, l8, sv[0], sv[4], sv[8], sv[12], sv[1], sv[5], sv[9], sv[13]);
                agg(er, ei, x, P[0][2], u, sv, l8, l32, l128);
            }
            st128(v4u{er, tag0, ei, tag0}, rs, es);
            SCHED();
            P[0][0] = f32x2{0.f, 0.f}; P[0][1] = x[0]; P[0][3] = cfma(l32, P[0][2], x[2]);
            if (layer == 0) { FW_OK() static_for<kN3E>(LAMBDA(i_) { w2ld(std::integral_constant<int, NW2P + i_.value>{}); }); }
#pragma unroll
            for (int kk = 1; kk < 4; ++kk) cfma4(P[kk], l8, P[kk - 1][0], P[kk - 1][1], P[kk - 1][2], P[kk - 1][3], sv[kk - 1], sv[kk + 3], sv[kk + 7], sv[kk + 11]);
#pragma unroll
            for (int i = 0; i < 8; ++i) { const f32x2 a = P[2 * (i >> 2)][i & 3], b_ = P[2 * (i >> 2) + 1][i & 3]; *(f32x4*)(smem + spl + (l_sg(layer, i & 3) - SC + (i >> 2) * 640)) = f32x4{a.x, a.y, b_.x, b_.y}; }
            partb();
        }
        int w1_ = w, w3_ = w;
        if (layer == 1) { pins(w1_); pins(w3_); }
        if (layer == 1 && w1_ < LBW) { site<4 + 10 * layer>(); SCHED(); partb(); if constexpr (layer == 1) { if (w == 0) tour<6>(); if constexpr (LBR == 1) room<3000>(); } }
        if (w3_ == LBW) {
            site<5 + 10 * layer>(); SCHED();
            f32x2 wg[17], t4[4];
            unsigned ea[15], tg[15];
            v4u eg[15];
            constexpr bool early = (LBE >> layer) & 1;
            const f32x2 z2 = {0.f, 0.f};
#pragma unroll
            for (int i = 0; i < 15; ++i) { ea[i] = (unsigned)(mt1 - 1 - i) * 10240u; pins(ea[i]); ea[i] = ea[i] < kNone ? ea[i] : kNone; pins(ea[i]); }
            if constexpr (early) {
                __builtin_amdgcn_s_sleep(layer ? KLB1 : KLB0);
#pragma unroll
                for (int i = 0; i < 15; ++i) eg[i] = ld128s(rs, eo, ea[i]);
                SCHED();
            }
            wg[1] = cmul(l128, l128); wg[2] = cmul(wg[1], wg[1]); wg[3] = cmul(wg[2], wg[1]); wg[4] = cmul(wg[2], wg[2]);
#pragma unroll
            for (int i = 4; i < 16; i += 4) {
                cfma4(t4, wg[i], wg[1], wg[2], wg[3], wg[4], z2, z2, z2, z2);
#pragma unroll
                for (int e = 0; e < 4; ++e) wg[i + 1 + e] = t4[e];
            }
#pragma unroll
            for (int i = 0; i < 15; ++i) { if (i) pin(wg[i]); tg[i] = mt1 - 1 - i < 0 ? 0u : tag0; pins(tg[i]); }
            if (layer == 1) partb();
            if constexpr (layer == 0) tour<3>();
            f32x2 H0, H1;
            unsigned spins = dead ? kSpinCap : 0u;
            if constexpr (layer == 1 && LBR == 0) { room<1800>(); site<16>(); }
            if constexpr (!early) __builtin_amdgcn_s_sleep(layer ? KLB1 : KLB0);
            SCHED();
            auto eval = [&](const v4u (&e)[15]) __attribute__((always_inline)) {
                unsigned x = __builtin_amdgcn_bitop3_b32(e[0].y, e[0].w, tg[0], 0x7E);
                H0 = f32x2{__uint_as_float(e[0].x), __uint_as_float(e[0].z)}; H1 = f32x2{0.f, 0.f};
#pragma unroll
                for (int i = 1; i < 15; i += 2) {
                    x |= __builtin_amdgcn_bitop3_b32(e[i].y, e[i].w, tg[i], 0x7E) | __builtin_amdgcn_bitop3_b32(e[i + 1].y, e[i + 1].w, tg[i + 1], 0x7E);
                    pin(x);
                    gacc2(H0, H1, wg[i], wg[i + 1], e[i], e[i + 1]);
                    SCHED();
                }
                *(f32x2*)(smem + spl + (L_CW - SC)) = H0 + H1;
                return __builtin_amdgcn_ballot_w64(x != 0u) != 0ull;
            };
            bool again = true;
            if constexpr (early) again = eval(eg);
            if (again) {
                bool miss;
#pragma nounroll
                do {
                    asm volatile("" ::: "memory");
                    v4u e2[15];
#pragma unroll
                    for (int i = 0; i < 15; ++i) e2[i] = ld128s(rs, eo, ea[i]);
                    SCHED();
                    miss = eval(e2);
                    if (__builtin_expect(miss, 0) && ++spins > kSpinCap) { dead = true; miss = false; }
                } while (miss);
            }
            ORDER();
            STAMP(layer ? 36 : 37)
            if (layer == 0) {
                static_for<NW3 - NW2P - kN3E>(LAMBDA(i_) { w2ld(std::integral_constant<int, NW2P + kN3E + i_.value>{}); });
            }
        }
        int w2_ = w;
        pins(w2_);
        if constexpr (layer == 0) site<21>();
        if (layer == 0) UNDEF8(ca);
        if (layer == 0 && w2_ != LBW) {
            if (cv) {
                site<2>(); SCHED();
                partb();
                {
                    FW_OK()
                    st_ur((unsigned)(size_t)(LAS char*)smem + (unsigned)uwa);
                    CV_LOAD_A(cA, 0, 0, 0) CV_LOAD_A(cA, 0, 0, 1)
                    SCHED();
                    static_for<8>(LAMBDA(a_) { constexpr int a = a_.value; CV_LOAD_B(cB, a, 0) SCHED(); });
                    CV_LOAD_A(cA, 1, 0, 0) CV_LOAD_A(cA, 1, 0, 1)
                    SCHED();
                }
                STAMP(32)
                conv_part<0, kCR1, CPF>(ca, cA, cB, smem, ab, bb, w2a, LAMBDA(i_) {});
            } else {
                if constexpr (layer == 0) tour<5>();
                FW_OK()
                static_for<NW2A - NW2P>(LAMBDA(i_) { w2ld(std::integral_constant<int, NW2P + i_.value>{}); __builtin_amdgcn_s_sleep(1); });
                partb();
                if constexpr (layer == 0) room<3900>();
            }
        }
        asm volatile("s_waitcnt lgkmcnt(0)" ::: "memory");
        __builtin_amdgcn_s_barrier();
        asm volatile("" ::: "memory");
        STAMP(layer ? 15 : 4)
        {
            St cw, ex, hv[8];
            cw.a = *(const f32x2*)(smem + cwa); cw.b = *(const f32x2*)(smem + cwa + 256); cw.c = *(const float*)(smem + cw4 + 512);
            ex.a = *(const f32x2*)(smem + sga); ex.b = *(const f32x2*)(smem + sga + 256); ex.c = *(const float*)(smem + sg4 + 512);
            keep14(hv, cw, ex, bv, lamq, lam1);
            keep4(hv + 4, hv[3], bv + 4, lam1);
            SCHED();
            v2u ta[5], td[5];
            f32x2 hb[9];
            static_for<5 + SBL>(LAMBDA(r_) {
                constexpr int r = r_.value, rd = r < 2 ? r : r == 2 ? 4 : r - 1, ro_ = r - SBL < 2 ? r - SBL : r - SBL == 2 ? 4 : r - SBL - 1;
                if constexpr (r < 5) {
                    float e[8];
                    unsigned o[4];
#pragma unroll
                    for (int i = 0; i < 8; ++i) e[i] = rd == 4 ? hv[i].c : rd < 2 ? hv[i].a[rd & 1] : hb[i + 1][rd & 1];
                    if constexpr (r == 0) b3cv(hb, o, cw.b, ex.b, bv[0].b, bv[1].b, lamq.b, lam1.b, e);
                    else if constexpr (r < 3) b3cv(hb + 3 * r, o, hb[3 * r - 1], bv[3 * r - 1].b, bv[3 * r].b, bv[3 * r + 1].b, lam1.b, lam1.b, e);
                    else for (int i = 0; i < 4; ++i) o[i] = cvtpk(e[2 * i], e[2 * i + 1]);
                    *(v4u*)(smem + tbw + rd * 1024) = v4u{o[0], o[1], o[2], o[3]};
                    SCHED();
                    if constexpr (r < 3) { bfin(hb[3 * r + 2], hb[3 * r + 1], lam1.b); SCHED(); }
                    ta[rd] = __builtin_bit_cast(v2u, trread(smem, (rd < 4 ? tbp : tbr) + rd * 1024));
                    td[rd] = __builtin_bit_cast(v2u, trread(smem, (rd < 4 ? tbq + 256 : tbr + 512) + rd * 1024));
                    SCHED();
                }
                if constexpr (r >= SBL) { st128(v4u{ta[ro_].x, ta[ro_].y, td[ro_].x, td[ro_].y}, rs, hso + ro_ * 256); SCHED(); }
            });
        }
#pragma unroll
        for (int s = 0; s < 10; ++s) pins(ks[s]);
        if (layer == 1) { DRAIN(); st64(tagv, rs, fho1); }
        STAMP(layer ? 16 : 5)
        if (cv) {
            if constexpr (layer == 0) site<6>();
            SCHED();
            if constexpr (!CPF) {
                CV_LOAD_A(cA, 0, kCR1, 0) CV_LOAD_A(cA, 0, kCR1, 1)
                SCHED();
                static_for<8>(LAMBDA(a_) { constexpr int a = a_.value; CV_LOAD_B(cB, a, kCR1) SCHED(); });
                static_for<7>(LAMBDA(a_) { constexpr int a = a_.value + 1; CV_LOAD_A(cA, a, kCR1, 0) CV_LOAD_A(cA, a, kCR1, 1) });
                SCHED();
            }
            conv_part<kCR1, 8, false>(ca, cA, cB, smem, ab, bb, w2b, LAMBDA(g_) {
                constexpr int g = g_.value;
                if constexpr ((g == 14 || g == 16 || g == 18 || g == 20)) gst(std::integral_constant<int, (g - 14) / 2>{});
                if constexpr ((g == 26 || g == 28 || g == 30 || g == 32)) gst(std::integral_constant<int, 4 + (g - 26) / 2>{});
            });
            asm volatile("" : "+v"(ca[0]), "+v"(ca[1]), "+v"(ca[2]), "+v"(ca[3]), "+v"(ca[4]), "+v"(ca[5]), "+v"(ca[6]), "+v"(ca[7]));
            static_for<8>(LAMBDA(i_) {
                constexpr int n = 8 + i_.value;
                if constexpr (n == 9) asm volatile("s_nop 0");
                if constexpr ((n & 3) == 2) asm volatile("s_nop 1");
                gst(std::integral_constant<int, n>{}); SCHED();
            });
        } else if (layer == 0) {
            constexpr int ne = KW2E < 53 - NW2A ? KW2E : 53 - NW2A;
            static_for<54 - NW2A>(LAMBDA(i_) {
                if constexpr (i_.value == ne) { vm_wait<ne>(); st64(tagv, rs, fho1); }
                if constexpr (i_.value < 53 - NW2A) { w2x(i_); __builtin_amdgcn_s_sleep(1); }
            });
            if (w == 3) { static_for<NW2A - NW3>(LAMBDA(i_) { w2ld(std::integral_constant<int, NW3 + i_.value>{}); __builtin_amdgcn_s_sleep(1); }); if constexpr (layer == 0) tour<2>(); }
            poll1(fho, p.ws);
            if constexpr (layer == 0) room<1000>();
            DRAIN();
        }
        if (layer == 0) { unsigned f0_, f1_; asm volatile("s_waitcnt vmcnt(%2)\n\t" AR("%0", 36, 0) AR("%1", 36, 1) : "=v"(f0_), "=v"(f1_) : "i"(4) : "memory"); fh = v2u{f0_, f1_}; }
        STAMP(layer ? 29 : 6)
        }

            v2u pt[4];
            if (layer == 0) {
                u64 hl;
                ne1(hl, tagv, fh);
                if (__builtin_expect(hl != 0ull, 0)) {
                    gld2t(fh, fho, p.ws); wait1(fh); st64(tagv, rs, fgo1);
                    WAVE_RETRY(ISTAG(fh), fh = ld64(rs, fho))
                }
                ORDER();
            }
            else { room<3400>(); site<18>(); pipe_flag<KPH0>(pt, fho, p.ws, tagv, dead); ORDER(); }
            STAMP(layer ? 17 : 31)
            site<7 + 10 * layer>(); SCHED();
            constexpr int RING2 = layer ? KR2 : KR20;
            v4u ar[RING2];
#pragma unroll
            for (int i = 0; i < KI2; ++i) { ar[i] = ld128s(rs, ho[i / 10], ks[i % 10]); SCHED(); }
        int b_o = b0, w_o = w0;
        asm volatile("" : "+s"(b_o), "+s"(w_o));
        const int b = b_o, w = w_o;
        TILES()
        v4u gl[3];
        v2u f0;
        unsigned fgo, sgo, sgs, glo, gw, gr, srco;
        u64 glate = 0ull;
        const char *gb1 = p.ws + 32 * GSTR, *gb2 = p.ws + 48 * GSTR;
        {
            unsigned xs_[3][4], dq[2];
            float zx[4], zd[4], zt[4], zz[5][4];
            unsigned sA = SC + w * INBOX, s1 = OFF_SG + ((layer * 32 + mt2) * 4 + w) * 2048, s2 = s1 + nt2 * 256, sF = OFF_FG + (nt2 * 96 + 4 * w) * 128, sG = OFF_GT + (80 * nt2 + 4 * w) * GSTR + mt2 * 128,
                     sW = sA + 8192 + (w & 1) * 128, sR = SC + 8192 + w * 32, sX = l_tb(0, 0) + w * 5120, k160 = 160, kIn = INBOX;
            pins(sA); pins(s1); pins(s2);
            if (layer == 0) { pins(sF); pins(sG); pins(sW); pins(sR); pins(sX); pins(k160); pins(kIn); pins(gb1); pins(gb2); }
            unsigned l16, c16, q0, q1, c2, xr0, xr1, ta_, tb_;
#define A1(n_, v_, e_) else if constexpr (n == n_) { v_ = e_; pin(v_); }
            auto adr = LAMBDA(n_) {
                constexpr int n = n_.value + (layer && n_.value >= 4 ? 12 : 0);
                if constexpr (n == 0) { l16 = (unsigned)dst[0] & 1023u; pin(l16); }
                A1(1, c16, l16 & 0xf0u) A1(2, q0, (l16 >> 8) & 1u) A1(3, q1, l16 >> 9)
                A1(4, c2, (l16 >> 6) & 3u) A1(5, xr0, (c16 << 2) + sX) A1(6, ta_, q1 ^ c2) A1(7, xr0, (q0 << 3) + xr0) A1(8, xr0, (ta_ << 4) + xr0) A1(9, xr1, xr0 ^ 32u)
                else if constexpr (n >= 10 && n < 15) { constexpr int nb = n - 10; const v2u t = *(const v2u*)(smem + ((nb & 5) ? xr1 : xr0) + (nb >> 1) * 1024); xs_[nb >> 1][2 * (nb & 1)] = t.x; xs_[nb >> 1][2 * (nb & 1) + 1] = t.y; }
                A1(16, srco, l16 + sA) A1(17, ta_, l16 & 0x300u) A1(18, sgo, s1 + l16 + ta_) A1(19, sgs, (q0 << 3) + c16) A1(20, ta_, (q1 << 30) + s2) A1(21, sgs, sgs + ta_)
                else if constexpr (layer == 1) {}
                A1(22, ta_, l16 < 624u ? l16 : 624u) A1(23, tb_, ta_ & 0x380u) A1(24, ta_, ta_ & 0x60u) A1(25, fgo, (tb_ << 4) + sF) A1(26, fgo, (ta_ << 2) + fgo)
                A1(27, glo, q1 * (16u * GSTR) + sG) A1(28, ta_, l16 & 0x180u) A1(29, tb_, l16 & 0x70u) A1(30, glo, ta_ * (GSTR / 128u) + glo) A1(31, glo, glo + tb_)
                A1(32, ta_, l16 >> 7) A1(33, tb_, l16 & 0x30u) A1(34, gw, (tb_ << 1) + sW) A1(35, gw, __umul24(ta_, k160) + gw) A1(36, ta_, c2 & 1u) A1(37, gw, (ta_ << 4) + gw)
                A1(38, ta_, l16 >> 8) A1(39, gr, (q0 << 7) + sR) A1(40, gr, __umul24(ta_, kIn) + gr) A1(41, gr, __umul24(c2, k160) + gr) A1(42, tb_, tb_ >> 1) A1(43, gr, gr + tb_)
            };
            SCHED();
            if constexpr (STAMPS == 1) { STAMP(layer ? 50 : 40) }
            static_for<40>(LAMBDA(i_) {
                constexpr int i = i_.value, m = i / 10, s = i % 10;
                static_for<5>(LAMBDA(k_) {
                    constexpr int kk = k_.value, pv = ring_req(5 * i + kk, KI2, RING2, 0);
                    if constexpr (s == 0 && m < 3) mfa0<kk>(a2[m][kk], ar[i % RING2]); else mfa<5 * s + kk>(a2[m][kk], ar[i % RING2]);
                    SCHED();
                    if constexpr (i == 0 && kk == 0 && layer == 1) { pin(pt[0]); pin(pt[1]); pin(pt[2]); pin(pt[3]); }
                    if constexpr (g2_adr(i, kk) >= 0) adr(IC(g2_adr(i, kk)));
                    if constexpr (layer == 0 && i >= 1 && i < 28 && (kk & 1)) {
                        constexpr int n = 2 * (i - 1) + (kk >> 1) - 9, nb = n / 9, u = n % 9 - 1;
                        if constexpr (n < 0) {}
                        else if constexpr (u < 0) { rda<22 + 2 * nb>(dq[0]); rda<23 + 2 * nb>(dq[1]); }
                        else if constexpr (u < 2) unp2(zx[2 * u], zx[2 * u + 1], xs_[nb >> 1][2 * (nb & 1) + u]);
                        else if constexpr (u < 4) unp2(zd[2 * u - 4], zd[2 * u - 3], dq[u - 2]);
                        else if constexpr (u < 6) fma2(zt[2 * u - 8], zt[2 * u - 7], zx[2 * u - 8], zx[2 * u - 7], rsr, rsr, rsm, rsm);
                        else fma2(zz[nb][2 * u - 12], zz[nb][2 * u - 11], zt[2 * u - 12], zt[2 * u - 11], zd[2 * u - 12], zd[2 * u - 11], zx[2 * u - 12], zx[2 * u - 11]);
                    }
                    if constexpr (layer == 0 && i == 0 && kk == 1) { vm_wait<KI2 - 1 + (ring_req(0, KI2, RING2, 0) >= 0)>(); st64(tagv, rs, fgo1); }
                    if constexpr (layer == 0 && i == kGPI && kk == 2) gld2s<0>(f0, fgo, p.ws);
                    if constexpr (layer == 0 && i == kGCI) {
                        static_assert(kGCI + KR20 > 40, "");
                        if constexpr (kk == 0) { __builtin_amdgcn_s_waitcnt(0x0F70); pin(f0); gld4s<0>(gl[0], glo, p.ws); }
                        if constexpr (kk == 1) { gld4s<0>(gl[1], glo, gb1); gld4s<0>(gl[2], glo, gb2); }
                        if constexpr (kk == 3) ne1(glate, tagv, f0);
                    }
                    if constexpr (pv >= 0) ar[pv % RING2] = ld128s(rs, ho[pv / 10], ks[pv % 10]);
                    if constexpr (kk == 2 && m >= 1 && s >= 1 && s < 6) *(f32x4*)(smem + dst[m - 1] + (s - 1) * 1024) = a2[m - 1][s - 1];
                    site<4000 + 1000 * layer + 5 * i + kk>();
                    if constexpr (i >= 37) a2[i - 37][kk] = *(const f32x4*)(smem + srco + (i - 37) * 5120 + kk * 1024);
                    SCHED();
                });
                if constexpr (i == 10) { asm volatile("s_waitcnt lgkmcnt(0)" ::: "memory"); __builtin_amdgcn_s_barrier(); asm volatile("" ::: "memory"); }
                if constexpr (i == 28 && layer == 0) {
#pragma unroll
                    for (int nb = 0; nb < 5; ++nb) { a2[3][nb] = f32x4{zz[nb][0], zz[nb][1], zz[nb][2], zz[nb][3]}; pin(a2[3][nb]); }
                }
                if constexpr (i == 36) {
                    asm volatile("s_waitcnt lgkmcnt(0)" ::: "memory");
                    __builtin_amdgcn_s_barrier(); asm volatile("" ::: "memory");
                }
                if constexpr (STAMPS == 1 && (i == 0 || i == 9 || i == 10 || i == 15 || i == 25 || i == 35 || i == 36)) { STAMP((layer ? 51 : 41) + (i == 0 ? 0 : i == 9 ? 1 : i == 10 ? 2 : i == 15 ? 3 : i == 25 ? 4 : i == 35 ? 5 : 6)) }
                SCHED();
            });
            STAMP(layer ? 18 : 8)
        }
#pragma unroll
        for (int nb = 0; nb < 5; ++nb) a2[0][nb] = (a2[0][nb] + a2[1][nb]) + a2[2][nb];
        asm volatile("s_nop 1" : "+v"(a2[3][0]), "+v"(a2[3][1]), "+v"(a2[3][2]), "+v"(a2[3][3]), "+v"(a2[3][4]),
                     "+v"(a2[0][0]), "+v"(a2[0][1]), "+v"(a2[0][2]), "+v"(a2[0][3]), "+v"(a2[0][4]));
        float ty;
        {
            f32x4 s1, s2;
#pragma unroll
            for (int nb = 0; nb < 5; ++nb) {
                const f32x4 y = a2[3][nb] + a2[0][nb];
                xin[nb] = y;
                if (nb == 0) { s1 = y; s2 = y * y; } else { s1 += y; s2 += y * y; }
            }
            ty = xsum2h(hsum4(s1), hsum4(s2));
            st64(v2u{__float_as_uint(ty), tag0}, rs, sgs);
        }
        SCHED();
        STAMP(layer ? 19 : 9)
        if (layer == 0 && __builtin_expect(glate != 0ull, 0)) {
            asm volatile("s_waitcnt vmcnt(0)" : "+v"(gl[0]), "+v"(gl[1]), "+v"(gl[2])::"memory");
            WAVE_WAIT({ f0 = ld64(rs, fgo); ok = ISTAG(f0); })
            gld4t<0>(gl[0], glo, p.ws); gld4t<0>(gl[1], glo, gb1); gld4t<0>(gl[2], glo, gb2);
            asm volatile("s_waitcnt vmcnt(0)" : "+v"(gl[0]), "+v"(gl[1]), "+v"(gl[2])::"memory");
        }
        if (layer == 0) {
            asm volatile("s_waitcnt vmcnt(1)" : "+v"(gl[0]), "+v"(gl[1]), "+v"(gl[2])::"memory");
            *(v4u*)(smem + gw) = gl[0]; *(v4u*)(smem + gw + 1280) = gl[1]; *(v4u*)(smem + gw + 1920) = gl[2];
            asm volatile("s_waitcnt lgkmcnt(0)" ::: "memory");
            __builtin_amdgcn_s_barrier(); asm volatile("" ::: "memory");
#pragma unroll
            for (int nb = 0; nb < 5; ++nb) gt[nb] = __builtin_bit_cast(v2u, trread(smem, gr + 640 * nb));
        } else {
            ACC_TIE5(gt);
            if constexpr (OTR) {
#pragma unroll
                for (int nb = 0; nb < 5; ++nb) *(f32x4*)(smem + otw + nb * 64) = xin[nb];
#pragma unroll
                for (int nb = 0; nb < 5; ++nb) xin[nb] = *(const f32x4*)(smem + (nb < 4 ? otr0 + (nb & 1) * 8 * 336 + (nb >> 1) * 128 : otr4));
            }
        }
        SCHED();
        if (PSP >> layer & 1) { gran_req<32, layer ? PDS : PDS0>(sgo, p.ws); SCHED(); }
        f32x4 gf[5], yg[5];
#pragma unroll
        for (int nb = 0; nb < 5; ++nb) { gf[nb] = unpk4(gt[nb]); yg[nb] = xin[nb] * gf[nb]; pin(gf[nb]); pin(yg[nb]); }
        SCHED();
        if (PSP >> layer & 1) { gran_req<40, 0>(sgo, p.ws); SCHED(); }
        int lane_o;
        asm volatile("v_mbcnt_lo_u32_b32 %0, -1, 0\n\tv_mbcnt_hi_u32_b32 %0, -1, %0" : "=v"(lane_o));
        const int lane = lane_o, c = lane & 15, q = lane >> 4;
        unsigned xs = OFF_X2F + (unsigned)((2 * mt2 + (w >> 1)) * 40960 + ((c >> 2) & 1) * 20480 + (10 * nt2 + 2 * (q & 1) + (q >> 1)) * 256 + (8 * (w & 1) + 4 * (c >> 3) + (c & 3)) * 16);
        unsigned pso = q < 2 ? OFF_PS + (unsigned)(((mt2 * 4 + w) * 8 + nt2) * 128 + c * 8 + q * 4) : kNone;
        unsigned fxo1 = OFF_FX + (unsigned)(((mt1 * 4 + 2 * (mt2 & 1) + (w >> 1)) * 16 + nt2 * 2 + (w & 1)) * 128);
        unsigned oo = OTR ? (unsigned)(((64 * mt2 + 16 * w + (lane >> 3)) * D_ + 80 * nt2) * 4 + (nt2 & 1) * 64 + (lane & 7) * 16) : (unsigned)(((64 * mt2 + 16 * w + c) * D_ + 80 * nt2 + 4 * q) * 4);
        unsigned oo1 = oo + 8 * D_ * 4, oo4 = (unsigned)(((64 * mt2 + 16 * w + (lane >> 3) + (lane & 4) * 2) * D_ + 80 * nt2) * 4 + 256 - (nt2 & 1) * 256 + (lane & 3) * 16);
        int bpi = (lane >> 3) * 4;
        u64 up = 0xF0F0F0F0F0F0F0F0ull;
        unsigned xs2 = (q & 1) ? kNone : xs;
        fxo1 = lane ? kNone : fxo1;
        if (layer == 0) { pin(xs); pin(xs2); pin(pso); pin(fxo1); } else { pin(oo); if (OTR) { pin(oo1); pin(oo4); pin(bpi); pins(up); } }
        SCHED();
        {
            float a, d;
                        if (PSP >> layer & 1) gran_req<48, layer ? PD1 : 0>(sgo, p.ws);
            else { gran_req<32, layer ? PDS : PDS0>(sgo, p.ws); gran_req<40, PD1>(sgo, p.ws); gran_req<48, PD1>(sgo, p.ws); }
            room<(layer ? 900 : 1500)>(); site<9 + 10 * layer>();
            pipe_gran(a, d, sgo, p.ws, tag0, dead);
            STAMP(layer ? 20 : 10)
            xsum2(a, d);
            const float me = a * (1.0f / D_);
            const float ri = __builtin_amdgcn_rsqf(fmaxf(d * (1.0f / D_) - me * me, 0.f) + 1e-5f), mr = -me * ri;
            float rv[3] = {ri, ri, ri}, mv[3] = {mr, mr, mr};
            if constexpr (layer == 1 && OTR) {
#pragma unroll
                for (int i = 0; i < 2; ++i) { rv[i] = __int_as_float(__builtin_amdgcn_ds_bpermute(bpi + 32 * i, __float_as_int(ri))); mv[i] = __int_as_float(__builtin_amdgcn_ds_bpermute(bpi + 32 * i, __float_as_int(mr))); }
                SCHED();
            }
            static_for<5>(LAMBDA(n_) {
                constexpr int nb = layer ? n_.value : (n_.value + 4) % 5;
                if constexpr (layer == 1 && OTR && nb == 4) { sel1(rv[2], __float_as_uint(rv[0]), __float_as_uint(rv[1]), up); sel1(mv[2], __float_as_uint(mv[0]), __float_as_uint(mv[1]), up); }
                xin[nb] = yg[nb] * rv[nb < 4 ? nb & 1 : 2] + (gf[nb] * mv[nb < 4 ? nb & 1 : 2] + xin[nb]);
                if constexpr (layer == 1) { __builtin_amdgcn_raw_buffer_store_b128(__builtin_bit_cast(v4u, xin[nb]), ro, !OTR ? oo + 64 * nb : nb == 4 ? oo4 : ((nb & 1) ? oo1 : oo) + (nb >> 1) * 128, 0, OUTP); SCHED(); }
                else if constexpr (nb & 5) {
                    const f32x4 xa = xin[nb & 6], xb = xin[nb];
                    const auto r0 = __builtin_amdgcn_permlane16_swap(cvtpk(xa.x, xa.y), cvtpk(xb.x, xb.y), false, false);
                    const auto r1 = __builtin_amdgcn_permlane16_swap(cvtpk(xa.z, xa.w), cvtpk(xb.z, xb.w), false, false);
                    st128(v4u{r0[0], r1[0], r0[1], r1[1]}, rs, (nb == 4 ? xs2 : xs) + (nb >> 1) * 1024);
                    SCHED();
                }
            });
        }
        if constexpr (layer == 0) {
            {
                f32x4 s1, s2;
#pragma unroll
                for (int nb = 0; nb < 5; ++nb) { const f32x4 x = xin[nb]; if (nb == 0) { s1 = x; s2 = x * x; } else { s1 += x; s2 += x * x; } }
                __builtin_amdgcn_raw_buffer_store_b32(__float_as_uint(xsum2h(hsum4(s1), hsum4(s2))), rs, pso, 0, SC1);
            }
            SCHED();
            site<12>();
        int wa[2][3];
#pragma unroll
        for (int i = 0; i < 2; ++i) {
            wa[i][0] = (c >> 3) * 20480 + (c & 7) * 128 + (((4 * i + q) ^ (c & 6)) * 16); wa[i][1] = (wa[i][0] ^ 16) + 10240;
            wa[i][2] = 81920 + (c & 1) * 10240 + (c >> 1) * 128 + (((4 * i + q) ^ ((c & 1) | ((c >> 2) << 1))) * 16);
            pin(wa[i][0]); pin(wa[i][1]); pin(wa[i][2]);
        }
            unsigned fxo = OFF_FX + (unsigned)((mt1 * 4 + w) * 2048 + (lane >> 2) * 128);
            unsigned xo = OFF_X2F + (unsigned)((4 * mt1 + w) * 40960 + lane * 16), xo1 = xo + 20480u;
            const int row = 8 * q + (c & 7);
            unsigned po = OFF_PS + (unsigned)((((2 * mt1 + (w >> 1)) * 4 + 2 * (w & 1) + (row >> 4)) * 8 + 4 * (c >> 3)) * 128 + (row & 15) * 8);
            int rsw = l_rs(1, 0) + w * INBOX + lane * 4;
            pin(rsw); pin(fxo); pin(xo); pin(xo1); pin(po);
            v4u wf[2][5];
#pragma unroll
            for (int nb = 0; nb < 5; ++nb) wf[0][nb] = WF(nb, 0);
            SCHED();
            DRAIN();
            st64(tagv, rs, fxo1);
            if (b == 255 && w == 0) FLAG(OFF_NONCE)
            STAMP(11)
            if (w == 0) tour<4>();
            if constexpr (OTR) {
                const int sh = (nt2 & 1) * 64, r4 = (lane >> 3) + (lane & 4) * 2;
                const int a0 = c * 336 + q * 16, a1 = (lane >> 3) * 336 + sh + (lane & 7) * 16, a2_ = r4 * 336 + 256 - 4 * sh + (lane & 3) * 16;
                char* tw = smem + SC + w * INBOX + 12288;
#pragma unroll
                for (int nb = 0; nb < 5; ++nb) *(v2u*)(tw + a0 / 2 + nb * 32) = gt[nb];
#pragma unroll
                for (int nb = 0; nb < 5; ++nb) gt[nb] = *(const v2u*)(tw + (nb < 4 ? a1 / 2 + (nb & 1) * 8 * 168 + (nb >> 1) * 64 : a2_ / 2));
                otw = SC + w * INBOX + a0; otr0 = SC + w * INBOX + a1; otr4 = SC + w * INBOX + a2_;
                pin(otw); pin(otr0); pin(otr4);
            }
            addrs(std::integral_constant<int, 1>{}, lane, w, b);
            ACC_TIE5(gt);
            v2u pt[4];
            skip<SKIP5>();
            pipe_flag<KPF0>(pt, fxo, p.ws, tagv, dead);
            ORDER();
            STAMP(12)
            v4u ar[2 * RING1];
#define XF(n) ld128s(rs, (((n) & 1) ? xo1 : xo) + (((n) >> 1) & 3) * 1024, ((n) >> 3) * 4096)
#pragma unroll
            for (int n = 0; n < KI1; ++n) { ar[n] = XF(n); SCHED(); }
            v2u ps[4];
            float t1, t2, rinv, mri, rr_[8], rm_[8];
            static_for<20>(LAMBDA(s_) {
                constexpr int s = s_.value, cu = s & 1, nx = cu ^ 1;
                LGKM0() SCHED();
                static_for<10>(LAMBDA(g_) {
                    constexpr int g = g_.value, mb = g / 5, k = g % 5;
                    if constexpr (s == 0) mf0(acc[mb][k], ar[mb], wf[0][k]); else mf(acc[mb][k], ar[(2 * s + mb) % (2 * RING1)], wf[cu][k]);
                    SCHED();
                    if constexpr (s == 0 && g == 0) { pin(pt[0]); pin(pt[1]); pin(pt[2]); pin(pt[3]); }
                    if constexpr (s < 19 && g < 5) wf[nx][g] = WF(g, s + 1);
                    constexpr int rq = ring_req(10 * s + g, KI1, 2 * RING1, 4);
                    if constexpr (rq >= 100) ps[rq - 100] = ld64(rs, po + (rq - 100) * 128);
                    else if constexpr (rq >= 0) ar[rq % (2 * RING1)] = XF(rq);
                    constexpr int n = g >= 6 ? 4 * (s - KS1) + g - 6 : -1;
                    if constexpr (n == 0) { t1 = __uint_as_float(ps[0].x) + __uint_as_float(ps[1].x); pin(t1); t2 = __uint_as_float(ps[0].y) + __uint_as_float(ps[1].y); pin(t2); }
                    if constexpr (n == 1) { t1 += __uint_as_float(ps[2].x); pin(t1); t2 += __uint_as_float(ps[2].y); pin(t2); }
                    if constexpr (n == 2) { t1 += __uint_as_float(ps[3].x); pin(t1); t2 += __uint_as_float(ps[3].y); pin(t2); }
                    if constexpr (n == 3) { t1 += dpp<0x128>(t1); t2 += dpp<0x128>(t2); pin(t1); pin(t2); }
                    if constexpr (n == 4) { t1 *= (1.0f / D_); pin(t1); t2 *= (1.0f / D_); pin(t2); }
                    if constexpr (n == 5) { t2 = fmaxf(t2 - t1 * t1, 0.f); pin(t2); }
                    if constexpr (n == 6) { rinv = __builtin_amdgcn_rsqf(t2 + 1e-5f); pin(rinv); }
                    if constexpr (n == 8) { mri = -t1 * rinv; pin(mri); }
                    if constexpr (n == 9) { t1 = rinv; mrg8(t1, mri); }
                    if constexpr (n == 10) *(float*)(smem + rsw) = t1;
                    if constexpr (n >= 11 && n < 19) bc2b<n - 11>(rr_[n - 11], rm_[n - 11], rinv, mri);
                    if constexpr (n >= 19 && n < 39) TT_GAP(n - 19)
                    site<6000 + 10 * s + g>();
                    SCHED();
                });
            });
            rr[0] = f32x4{rr_[0], rr_[1], rr_[2], rr_[3]}; rr[1] = f32x4{rr_[4], rr_[5], rr_[6], rr_[7]};
            ACC_FENCE10(acc[0], acc[1]);
            STAMP(13)
        } else {
            STAMP(21)
            STAMP_DUMP()
            asm volatile("kpf_end:");
        }
    };
    layer_body(std::integral_constant<int, 0>{});
    layer_body(std::integral_constant<int, 1>{});
}

extern "C" void kernel_launch(void* const* d_in, const int* in_sizes, int n_in,
                              void* d_out, int out_size, void* d_ws, size_t ws_size,
                              hipStream_t stream) {
    Params p;
    p.x0 = (const u16*)d_in[0];
    p.Lre = (const u16*)d_in[1];
    p.Lim = (const u16*)d_in[2];
    p.Bre = (const u16*)d_in[3];
    p.Bim = (const u16*)d_in[4];
    p.Cre = (const u16*)d_in[5];
    p.Cim = (const u16*)d_in[6];
    p.logdt = (const u16*)d_in[7];
    p.Dskip = (const u16*)d_in[8];
    p.k = (const u16*)d_in[10];
    p.v = (const u16*)d_in[11];
    p.shk = (const u16*)d_in[12];
    p.ssmk = (const u16*)d_in[13];
    p.ssmD = (const u16*)d_in[14];
    p.ws = (char*)d_ws;
    p.out = (float*)d_out;
    k_main<<<256, 256, 0, stream>>>(p);
}
```

```cpp
#include <hip/hip_runtime.h>
#include <stdint.h>
#include <utility>

#define STAMPS 0

typedef unsigned short u16;
typedef unsigned long long u64;
typedef unsigned v4u __attribute__((ext_vector_type(4)));
typedef unsigned v2u __attribute__((ext_vector_type(2)));
typedef float f32x4 __attribute__((ext_vector_type(4)));
typedef float f32x2 __attribute__((ext_vector_type(2)));
typedef short s16x4 __attribute__((ext_vector_type(4)));
typedef __bf16 bf16x2v __attribute__((ext_vector_type(2)));
#define LAS __attribute__((address_space(3)))
#define DI __device__ __forceinline__

constexpr int S_ = 2048, D_ = 640;
#define KR1 5
#define KR2 10
constexpr int RING1 = KR1;
#define KCR1 4
constexpr int kCR1 = KCR1;
#define KXA 0
#define KN3E 16
#define KN3 12
constexpr int kXA = KXA;
constexpr int kN3E = KN3E, kN3 = KN3;
#define KFHK 9
#define KFHP 7
constexpr int kFHK = KFHK, kFHP = KFHP;
#define KGPI 31
#define KGCI 36
#define CPF 0
#define LBE 0
#define LBR 1
#define W3O 1
#define KI2 8
#define KI1 8
#define KS1 8
#define RSL 2
#define KR20 10
constexpr int kGPI = KGPI, kGCI = KGCI;

#define KPAD 0
#define KPS 0
#define GPAD 0
constexpr int KSTR = 4096 + KPAD, GSTR = 4096 + GPAD;
constexpr unsigned OFF_NONCE = 0;
constexpr unsigned OFF_ST = 4096;
constexpr unsigned OFF_FW = OFF_ST + 65536;
constexpr unsigned OFF_FK = OFF_FW + 131072;
constexpr unsigned OFF_VT = OFF_FK + 65536;
constexpr unsigned OFF_FG = OFF_FK + 81920;
constexpr unsigned OFF_EG = OFF_FG + 98304;
constexpr unsigned OFF_KVT = OFF_EG + 327680;
constexpr unsigned OFF_GT = OFF_KVT + 640 * 4608;
constexpr unsigned OFF_W2F = OFF_GT + 640 * 4608;
constexpr unsigned WS_BYTES = OFF_W2F + 1638400;
constexpr unsigned OFF_GRP = 0x2000000, NB_ = 16384;
constexpr unsigned N_HF = 0;
constexpr unsigned N_X2 = 80 * NB_;
constexpr unsigned N_SG = 100 * NB_;
constexpr unsigned N_PS = 104 * NB_;
constexpr unsigned N_FH = 105 * NB_;
constexpr unsigned N_FX = 107 * NB_;
constexpr unsigned N_END = 108 * NB_;
constexpr unsigned kNone = 0x40000000u;
constexpr unsigned OFF_DBG = 0x1800000;
#define TILES() const int xq = b & 7, j = b >> 3, mt1 = 2 * xq + (j >> 4), nt1 = j & 15, mt2 = 4 * xq + (j >> 3), nt2 = j & 7, i32 = 16 * (mt2 & 1) + mt1;

constexpr int SC = 102400;
constexpr int REGSZ = 13056;
constexpr int L_XR = SC;
constexpr int L_CO = SC + 12288;
constexpr int L_KB = SC + 51712;
constexpr int L_TAB = SC + 55808;
constexpr int L_CW = SC + 39168;
constexpr int INBOX = 15360;
constexpr int l_rs(int l, int w) { return l ? SC + w * INBOX : L_KB + w * 256; }
constexpr int l_sg(int l, int w) { return l ? SC + w * INBOX + 256 : SC + (w == 0 ? 43264 : w == 1 ? 46080 : w == 2 ? 52736 : 56320); }
constexpr int l_tb(int l, int w) { return l ? SC + w * INBOX + 2048 : SC + 40960 + w * 5120; }
constexpr int LDS_BYTES = 163840;
constexpr int CV_E = 4608;
constexpr int CV_O = CV_E + (1056 + 15) * 4;

struct Params {
    const u16 *x0, *Lre, *Lim, *Bre, *Bim, *Cre, *Cim, *logdt, *Dskip, *k, *v, *shk, *ssmk, *ssmD;
    char* ws;
    float* out;
};

#define MF "v_mfma_f32_16x16x32_bf16 "
DI void mf(f32x4& c, v4u a, v4u b) { asm(MF "%0, %1, %2, %0" : "+v"(c) : "v"(a), "v"(b)); }
DI void mf0(f32x4& c, v4u a, v4u b) { asm(MF "%0, %1, %2, 0" : "=&v"(c) : "v"(a), "v"(b)); }
constexpr int W2R = 56;
#define WA(n) "a[%c" #n ":%c" #n "+3]"
template <int R> DI void mfa(f32x4& c, v4u a) { asm(MF "%0, " WA(2) ", %1, %0" : "+v"(c) : "v"(a), "i"(W2R + 4 * R)); }
template <int R> DI void mfa0(f32x4& c, v4u a) { asm(MF "%0, " WA(2) ", %1, 0" : "=&v"(c) : "v"(a), "i"(W2R + 4 * R)); }
#define ACC_FENCE10(c, d) asm volatile("" : "+v"(c[0]), "+v"(c[1]), "+v"(c[2]), "+v"(c[3]), "+v"(c[4]), "+v"(d[0]), "+v"(d[1]), "+v"(d[2]), "+v"(d[3]), "+v"(d[4]))
#define ACC_TIE5(c) asm volatile("" : "+v"(c[0]), "+v"(c[1]), "+v"(c[2]), "+v"(c[3]), "+v"(c[4]))
#define QP " quad_perm:[1,0,3,2] row_mask:0xf bank_mask:0xf"
struct St { f32x2 a, b; float c; };
struct Lm { f32x2 a, b; float r, s; };
#define PK1 " op_sel_hi:[0,1,1]\n\t"
#define PK2 " op_sel:[1,1,0] op_sel_hi:[1,0,1] neg_lo:[1,0,0]"
#define ST_(t0, t1, t2, h0, h1, h2, b0, b1, b2, l0, l1, l2, l3)                                                                                        \
    "v_pk_fma_f32 %" #t0 ", %" #l0 ", %" #h0 ", %" #b0 PK1 "v_pk_fma_f32 %" #t1 ", %" #l1 ", %" #h1 ", %" #b1 PK1 "v_fma_f32 %" #t2 ", %" #l2 ", %" #h2 ", %" #b2 "\n\t" \
    "v_pk_fma_f32 %" #t0 ", %" #l0 ", %" #h0 ", %" #t0 PK2 "\n\tv_pk_fma_f32 %" #t1 ", %" #l1 ", %" #h1 ", %" #t1 PK2 "\n\tv_fmac_f32_dpp %" #t2 ", %" #h2 ", %" #l3 QP "\n\t"
#define SO(t) "=&v"(t.a), "=&v"(t.b), "=&v"(t.c)
#define SI(t) "v"(t.a), "v"(t.b), "v"(t.c)
#define LI(l) "v"(l.a), "v"(l.b), "v"(l.r), "v"(l.s)
DI void run3(St& t, const St& h, const St* b, const Lm& l) {
    St u;
    asm(ST_(0, 1, 2, 6, 7, 8, 9, 10, 11, 18, 19, 20, 21) ST_(3, 4, 5, 0, 1, 2, 12, 13, 14, 18, 19, 20, 21) ST_(0, 1, 2, 3, 4, 5, 15, 16, 17, 18, 19, 20, 21)
        : SO(t), SO(u) : SI(h), SI(b[0]), SI(b[1]), SI(b[2]), LI(l));
}
DI void run4(St& t, const St& h, const St* b, const Lm& l) {
    St u;
    asm(ST_(3, 4, 5, 6, 7, 8, 9, 10, 11, 21, 22, 23, 24) ST_(0, 1, 2, 3, 4, 5, 12, 13, 14, 21, 22, 23, 24) ST_(3, 4, 5, 0, 1, 2, 15, 16, 17, 21, 22, 23, 24) ST_(0, 1, 2, 3, 4, 5, 18, 19, 20, 21, 22, 23, 24)
        : SO(t), SO(u) : SI(h), SI(b[0]), SI(b[1]), SI(b[2]), SI(b[3]), LI(l));
}
#define AC_(t0, t2, h0, h2, b0, b2, l0, l2, l3)                                                                                   \
    "v_pk_fma_f32 %" #t0 ", %" #l0 ", %" #h0 ", %" #b0 PK1 "v_fma_f32 %" #t2 ", %" #l2 ", %" #h2 ", %" #b2 "\n\t"               \
    "v_pk_fma_f32 %" #t0 ", %" #l0 ", %" #h0 ", %" #t0 PK2 "\n\tv_fmac_f32_dpp %" #t2 ", %" #h2 ", %" #l3 QP "\n\t"
#define AO(t) "=&v"(t.a), "=&v"(t.c)
#define AI(t) "v"(t.a), "v"(t.c)
DI void keep14(St* t, const St& h, const St& x, const St* b, const Lm& lq, const Lm& l) {
    St u;
    asm(AC_(8, 9, 10, 11, 12, 13, 22, 23, 24) AC_(0, 1, 8, 9, 14, 15, 25, 26, 27) AC_(2, 3, 0, 1, 16, 17, 25, 26, 27) AC_(4, 5, 2, 3, 18, 19, 25, 26, 27) AC_(6, 7, 4, 5, 20, 21, 25, 26, 27)
        : AO(t[0]), AO(t[1]), AO(t[2]), AO(t[3]), AO(u) : AI(h), AI(x), AI(b[0]), AI(b[1]), AI(b[2]), AI(b[3]), "v"(lq.a), "v"(lq.r), "v"(lq.s), "v"(l.a), "v"(l.r), "v"(l.s));
}
DI void keep4(St* t, const St& h, const St* b, const Lm& l) {
    asm(AC_(0, 1, 8, 9, 10, 11, 18, 19, 20) AC_(2, 3, 0, 1, 12, 13, 18, 19, 20) AC_(4, 5, 2, 3, 14, 15, 18, 19, 20) AC_(6, 7, 4, 5, 16, 17, 18, 19, 20)
        : AO(t[0]), AO(t[1]), AO(t[2]), AO(t[3]) : AI(h), AI(b[0]), AI(b[1]), AI(b[2]), AI(b[3]), "v"(l.a), "v"(l.r), "v"(l.s));
}
DI void b3cv(f32x2* t, unsigned* o, f32x2 h, f32x2 b0, f32x2 b1, f32x2 b2, f32x2 l0, f32x2 l, const float* e) {
    asm("v_pk_fma_f32 %0, %11, %7, %8" PK1 "v_cvt_pk_bf16_f32 %3, %13, %14\n\tv_pk_fma_f32 %0, %11, %7, %0" PK2 "\n\tv_cvt_pk_bf16_f32 %4, %15, %16\n\t"
        "v_pk_fma_f32 %1, %12, %0, %9" PK1 "v_cvt_pk_bf16_f32 %5, %17, %18\n\tv_pk_fma_f32 %1, %12, %0, %1" PK2 "\n\tv_cvt_pk_bf16_f32 %6, %19, %20\n\t"
        "v_pk_fma_f32 %2, %12, %1, %10" PK1
        : "=&v"(t[0]), "=&v"(t[1]), "=&v"(t[2]), "=&v"(o[0]), "=&v"(o[1]), "=&v"(o[2]), "=&v"(o[3])
        : "v"(h), "v"(b0), "v"(b1), "v"(b2), "v"(l0), "v"(l), "v"(e[0]), "v"(e[1]), "v"(e[2]), "v"(e[3]), "v"(e[4]), "v"(e[5]), "v"(e[6]), "v"(e[7]));
}
DI void bfin(f32x2& t, f32x2 h, f32x2 l) { asm("v_pk_fma_f32 %0, %2, %1, %0" PK2 : "+v"(t) : "v"(h), "v"(l)); }
#define UNDEF8(c) asm volatile("" : "=v"(c[0]), "=v"(c[1]), "=v"(c[2]), "=v"(c[3]), "=v"(c[4]), "=v"(c[5]), "=v"(c[6]), "=v"(c[7]))
DI unsigned cvtpk(float lo, float hi) {
    f32x2 v = {lo, hi};
    return __builtin_bit_cast(unsigned, __builtin_convertvector(v, bf16x2v));
}
DI float lo16f(unsigned u) { return __uint_as_float(u << 16); }
DI float hi16f(unsigned u) { return __uint_as_float(u & 0xffff0000u); }
DI float rbf(float v) { return lo16f(cvtpk(v, 0.f)); }
template <int CTRL> DI float dpp(float x) {
    return __int_as_float(__builtin_amdgcn_update_dpp(0, __float_as_int(x), CTRL, 0xF, 0xF, true));
}
DI float rowsum16(float x) {
    x += dpp<0x128>(x); x += dpp<0x124>(x); x += dpp<0x122>(x); x += dpp<0x121>(x);
    return x;
}
DI float xsum32(float x) { auto r = __builtin_amdgcn_permlane32_swap(__float_as_uint(x), __float_as_uint(x), false, false); return __uint_as_float(r[0]) + __uint_as_float(r[1]); }
DI void xsum2(float& a, float& d) {
    auto r = __builtin_amdgcn_permlane16_swap(__float_as_uint(a), __float_as_uint(d), false, false);
    const float s = __uint_as_float(r[0]) + __uint_as_float(r[1]);
    const float t = xsum32(s);
    auto u = __builtin_amdgcn_permlane16_swap(__float_as_uint(t), __float_as_uint(t), false, false);
    a = __uint_as_float(u[0]); d = __uint_as_float(u[1]);
}
DI float xsum2h(float a, float d) {
    auto r = __builtin_amdgcn_permlane16_swap(__float_as_uint(a), __float_as_uint(d), false, false);
    return xsum32(__uint_as_float(r[0]) + __uint_as_float(r[1]));
}
DI float hsum4(f32x4 v) { return (v.x + v.y) + (v.z + v.w); }
typedef __amdgpu_buffer_rsrc_t rsrc_t;
constexpr int SC1 = 16;
DI v4u ld128(rsrc_t rs, unsigned off) { return __builtin_amdgcn_raw_buffer_load_b128(rs, off, 0, SC1); }
DI v4u ld128s(rsrc_t rs, unsigned off, unsigned soff) { return __builtin_amdgcn_raw_buffer_load_b128(rs, off, soff, SC1); }
DI v2u ld64(rsrc_t rs, unsigned off) { return __builtin_amdgcn_raw_buffer_load_b64(rs, off, 0, SC1); }
DI void st128(v4u v, rsrc_t rs, unsigned off) { __builtin_amdgcn_raw_buffer_store_b128(v, rs, off, 0, SC1); }
DI void st64(v2u v, rsrc_t rs, unsigned off) { __builtin_amdgcn_raw_buffer_store_b64(v, rs, off, 0, SC1); }
#define LGKM0() { __builtin_amdgcn_s_waitcnt(0xC07F); asm volatile("" ::: "memory"); }
#define DRAIN() asm volatile("s_waitcnt vmcnt(0)" ::: "memory")
#define ORDER() __builtin_amdgcn_fence(__ATOMIC_ACQUIRE, "wavefront")
#define SCHED() __builtin_amdgcn_sched_barrier(0)
constexpr unsigned kSpinCap = 1u << 17;
#define WAVE_WAIT(...)                                                      \
    {                                                                       \
        unsigned spins_ = dead ? kSpinCap : 0u;                             \
        _Pragma("nounroll") for (;;) {                                      \
            asm volatile("" ::: "memory");                                  \
            bool ok;                                                        \
            __VA_ARGS__;                                                    \
            if (__builtin_amdgcn_ballot_w64(!ok) == 0ull) break;            \
            if (++spins_ > kSpinCap) { dead = true; break; }                \
            __builtin_amdgcn_s_sleep(1);                                    \
        }                                                                   \
        ORDER();                                                            \
    }
#define KPD1 1
#define KPDS 5
#define KPDS0 5
#define KPF0 1
#define KPH0 12
#define KPF1 4
constexpr int PD1 = KPD1, PDS = KPDS, PDS0 = KPDS0;
#define PL(r) "global_load_dwordx2 " r ", %5, %6 sc1\n\t"
#define PS_(r) "s_waitcnt vmcnt(1)\n\tv_cmp_ne_u64 vcc, %7, " r "\n\ts_cbranch_vccz 2f\n\t" PL(r)
template <int D0> DI void pipe_flag(v2u (&t)[4], unsigned off, const void* base, v2u tag, bool& dead) {
    unsigned n = dead ? 0u : kSpinCap;
    asm volatile("s_sleep %8\n\t" PL("%0") "s_sleep %9\n\t" PL("%1")
                 "1:\n\t" PS_("%0") PS_("%1") "s_sub_u32 %4, %4, 1\n\ts_cbranch_scc0 1b\n2:"
                 : "=&v"(t[0]), "=&v"(t[1]), "=&v"(t[2]), "=&v"(t[3]), "+s"(n) : "v"(off), "s"(base), "v"(tag), "i"(D0), "i"(KPF1) : "memory", "vcc", "scc");
    dead = __builtin_amdgcn_readfirstlane(n) == ~0u;
}
#define WAVE_RETRY(OK, ...) { if (__builtin_expect(__builtin_amdgcn_ballot_w64(!(OK)) != 0ull, 0)) WAVE_WAIT({ __VA_ARGS__; ok = OK; }) ORDER(); }
#define STAMP(i)
#define STAMP_DUMP()

#define PADS 9000, 1000, 1059, 1088, 2085, 9001, 9002, 4071, 9, 9003, 6, 9004, 9005, 6105, 14, 15, 9006, 18, 19, 9007, 21, 2, 9008
#define TW 2
#define TW0 2
#define KLB0 0
#define KLB1 4
#define SKIP0 0
#define SKIP5 0
constexpr int kPads[] = {PADS};
constexpr int pad_next(int id) {
    for (unsigned i = 0; i + 1 < sizeof(kPads) / sizeof(int); ++i) if (kPads[i] == id) return kPads[i + 1];
    return -1;
}
constexpr bool pads_unique() {
    for (unsigned i = 0; i < sizeof(kPads) / sizeof(int); ++i)
        for (unsigned k = 0; k < i; ++k) if (kPads[i] == kPads[k]) return false;
    return true;
}
static_assert(pads_unique(), "");
template <int ID> DI void site() {
    if constexpr (pad_next(ID) >= 0) asm volatile("kp%c0:\n\ts_cbranch_execz kp%c1" ::"i"(ID), "i"(pad_next(ID)));
    else asm volatile("");
}
template <int T> DI void tour() {
    if constexpr (pad_next(9000 + T) >= 0 && pad_next(9000 + T) != 9001 + T) asm volatile("s_mov_b64 exec, 0\n\ts_branch kp%c0\nkp%c1:\n\ts_mov_b64 exec, -1" ::"i"(pad_next(9000 + T)), "i"(9001 + T));
}
template <int N> DI void skip() { if constexpr (N > 0) asm volatile("s_branch 1f\n\t.rept %c0\n\ts_nop 0\n\t.endr\n1:" ::"i"(N)); }
template <int N> DI void room() { asm volatile("s_branch 1f\n\t.p2align 12, , %c0\n1:" ::"i"(N)); }
template <class F, int... I> DI void static_for_impl(std::integer_sequence<int, I...>, F&& f) { (f(std::integral_constant<int, I>{}), ...); }
template <int N, class F> DI void static_for(F&& f) { static_for_impl(std::make_integer_sequence<int, N>{}, f); }
#define LAMBDA(v) [&](auto v) __attribute__((always_inline))
DI f32x2 cmul(f32x2 a, f32x2 b) {
    f32x2 t;
    asm("v_pk_mul_f32 %0, %1, %2 op_sel_hi:[0,1]\n\ts_nop 0\n\tv_pk_fma_f32 %0, %1, %2, %0" PK2 "\n\ts_nop 0" : "=&v"(t) : "v"(a), "v"(b));
    return t;
}
DI f32x2 cfma(f32x2 a, f32x2 b, f32x2 c) {
    f32x2 t;
    asm("v_pk_fma_f32 %0, %1, %2, %3 op_sel_hi:[0,1,1]\n\ts_nop 0\n\tv_pk_fma_f32 %0, %1, %2, %0" PK2 "\n\ts_nop 0" : "=&v"(t) : "v"(a), "v"(b), "v"(c));
    return t;
}
DI void cfma4(f32x2 (&t)[4], f32x2 a, f32x2 b0, f32x2 b1, f32x2 b2, f32x2 b3, f32x2 c0, f32x2 c1, f32x2 c2, f32x2 c3) {
    asm("v_pk_fma_f32 %0, %4, %5, %9 op_sel_hi:[0,1,1]\n\tv_pk_fma_f32 %1, %4, %6, %10 op_sel_hi:[0,1,1]\n\tv_pk_fma_f32 %2, %4, %7, %11 op_sel_hi:[0,1,1]\n\tv_pk_fma_f32 %3, %4, %8, %12 op_sel_hi:[0,1,1]\n\t"
        "v_pk_fma_f32 %0, %4, %5, %0" PK2 "\n\tv_pk_fma_f32 %1, %4, %6, %1" PK2 "\n\tv_pk_fma_f32 %2, %4, %7, %2" PK2 "\n\tv_pk_fma_f32 %3, %4, %8, %3" PK2 "\n\ts_nop 0"
        : "=&v"(t[0]), "=&v"(t[1]), "=&v"(t[2]), "=&v"(t[3]) : "v"(a), "v"(b0), "v"(b1), "v"(b2), "v"(b3), "v"(c0), "v"(c1), "v"(c2), "v"(c3));
}
#define C4(t0, t1, t2, t3, a, b0, b1, b2, b3, c0, c1, c2, c3)                                                                                                         \
    "v_pk_fma_f32 %" #t0 ", %" #a ", %" #b0 ", %" #c0 PK1 "v_pk_fma_f32 %" #t1 ", %" #a ", %" #b1 ", %" #c1 PK1 "v_pk_fma_f32 %" #t2 ", %" #a ", %" #b2 ", %" #c2 PK1 "v_pk_fma_f32 %" #t3 ", %" #a ", %" #b3 ", %" #c3 PK1 \
    "v_pk_fma_f32 %" #t0 ", %" #a ", %" #b0 ", %" #t0 PK2 "\n\tv_pk_fma_f32 %" #t1 ", %" #a ", %" #b1 ", %" #t1 PK2 "\n\tv_pk_fma_f32 %" #t2 ", %" #a ", %" #b2 ", %" #t2 PK2 "\n\tv_pk_fma_f32 %" #t3 ", %" #a ", %" #b3 ", %" #t3 PK2 "\n\t"
DI void cfma4n(f32x2 (&t)[4], f32x2 a, f32x2 b0, f32x2 b1, f32x2 b2, f32x2 b3, f32x2 c0, f32x2 c1, f32x2 c2, f32x2 c3) {
    asm(C4(0, 1, 2, 3, 4, 5, 6, 7, 8, 9, 10, 11, 12) : "=&v"(t[0]), "=&v"(t[1]), "=&v"(t[2]), "=&v"(t[3]) : "v"(a), "v"(b0), "v"(b1), "v"(b2), "v"(b3), "v"(c0), "v"(c1), "v"(c2), "v"(c3));
}
DI void agg(unsigned& er, unsigned& ei, f32x2 (&x)[4], f32x2& a, const f32x2 (&u)[4], const f32x2 (&s)[16], f32x2 l8, f32x2 l32, f32x2 l64) {
    f32x2 v[4], b;
    asm(C4(4, 5, 6, 7, 22, 10, 11, 12, 13, 14, 15, 16, 17) C4(0, 1, 2, 3, 22, 4, 5, 6, 7, 18, 19, 20, 21)
        "v_pk_fma_f32 %8, %23, %0, %1" PK1 "v_pk_fma_f32 %9, %23, %2, %3" PK1 "v_pk_fma_f32 %8, %23, %0, %8" PK2 "\n\tv_pk_fma_f32 %9, %23, %2, %9" PK2
        : "=&v"(x[0]), "=&v"(x[1]), "=&v"(x[2]), "=&v"(x[3]), "=&v"(v[0]), "=&v"(v[1]), "=&v"(v[2]), "=&v"(v[3]), "=&v"(a), "=&v"(b)
        : "v"(u[0]), "v"(u[1]), "v"(u[2]), "v"(u[3]), "v"(s[2]), "v"(s[6]), "v"(s[10]), "v"(s[14]), "v"(s[3]), "v"(s[7]), "v"(s[11]), "v"(s[15]), "v"(l8), "v"(l32));
    asm("s_nop 0\n\tv_fma_f32 %0, %2, %6, %4\n\tv_fma_f32 %1, %2, %7, %5\n\tv_fma_f32 %0, -%3, %7, %0\n\tv_fma_f32 %1, %3, %6, %1"
        : "=&v"(er), "=&v"(ei) : "v"(a.x), "v"(a.y), "v"(b.x), "v"(b.y), "v"(l64.x), "v"(l64.y));
}
DI void gacc2(f32x2& h0, f32x2& h1, f32x2 w0, f32x2 w1, v4u e0, v4u e1) {
    const v2u a0 = {e0.x, e0.y}, b0 = {e0.z, e0.w}, a1 = {e1.x, e1.y}, b1 = {e1.z, e1.w};
    asm("v_pk_fma_f32 %0, %4, %2, %0 op_sel_hi:[0,1,1]\n\tv_pk_fma_f32 %1, %6, %3, %1 op_sel_hi:[0,1,1]\n\t"
        "v_pk_fma_f32 %0, %5, %2, %0 op_sel:[0,1,0] op_sel_hi:[0,0,1] neg_lo:[1,0,0]\n\tv_pk_fma_f32 %1, %7, %3, %1 op_sel:[0,1,0] op_sel_hi:[0,0,1] neg_lo:[1,0,0]"
        : "+v"(h0), "+v"(h1) : "v"(w0), "v"(w1), "v"(a0), "v"(b0), "v"(a1), "v"(b1));
}
DI s16x4 trread(char* smem, int byteoff) {
    return __builtin_amdgcn_ds_read_tr16_b64_v4i16((LAS s16x4*)(smem + byteoff));
}

#define CV_LOAD_A(A, a, rr_, hf) { const unsigned* ap_ = (const unsigned*)(smem + ab[(a) >> 1] + (((a) & 1) ? 0 : 512) + 64 * (7 - (rr_)) + 8 * (hf)); A[a][2 * (hf)] = ap_[0]; A[a][2 * (hf) + 1] = ap_[1]; }
#define CV_LOAD_B(B, e, rr_) { B[e] = *(const v4u*)(smem + bb + 64 * (7 - (rr_)) + 512 * (e)); }
template <int R, bool PF, class F, class G>
DI void conv_round(f32x4 (&ca)[8], v4u (&A)[8], const v4u (&B)[8], v4u (&An)[8], v4u (&Bn)[8], const char* smem, const int (&ab)[4], int bb, F&& fill, G&& last) {
    static_for<36>(LAMBDA(g_) {
        constexpr int g = g_.value;
        constexpr int a = g < 8 ? 0 : g < 15 ? 1 : g < 21 ? 2 : g < 26 ? 3 : g < 30 ? 4 : g < 33 ? 5 : g < 35 ? 6 : 7;
        constexpr int e = g - (a * 8 - a * (a - 1) / 2);
        if constexpr (R == 0 && a == 0) mf0(ca[e], A[0], B[e]); else mf(ca[a + e], A[a], B[e]);
        SCHED();
        if constexpr (R == 0 && g < 16 && (g & 3) != 3) { constexpr int n = g - (g >> 2); CV_LOAD_A(A, 2 + (n >> 1), 0, n & 1) SCHED(); }
        if constexpr (PF && g < 32 && (g & 3) != 3) { if constexpr ((g & 3) == 2) CV_LOAD_B(Bn, g >> 2, R + 1) else CV_LOAD_A(An, g >> 2, R + 1, g & 1) SCHED(); }
        site<3000 + 36 * R + g>();
        if constexpr (R == 7) { last(g_); SCHED(); }
        if constexpr ((g & 3) == 3 && g < 28) { fill(std::integral_constant<int, 7 * R + g / 4>{}); SCHED(); }
    });
}
template <int R0, int R1, bool PFL, class F, class G>
DI void conv_part(f32x4 (&ca)[8], v4u (&A0)[8], v4u (&B0)[8], const char* smem, const int (&ab)[4], int bb, F&& fill, G&& last) {
    v4u A1[8], B1[8];
    static_for<R1 - R0>(LAMBDA(r_) {
        constexpr int r = r_.value;
        if constexpr (r & 1) conv_round<R0 + r, (R0 + r + 1 < R1 || PFL)>(ca, A1, B1, A0, B0, smem, ab, bb, fill, last);
        else conv_round<R0 + r, (R0 + r + 1 < R1 || PFL)>(ca, A0, B0, A1, B1, smem, ab, bb, fill, last);
    });
}
template <int IMM> DI void gld(v4u& dst, unsigned voff, const void* sbase) {
    asm volatile("global_load_dwordx4 %0, %1, %2 offset:%3" : "=v"(dst) : "v"(voff), "s"(sbase), "i"(IMM));
}
#define OUTP 16
DI void gldu16(unsigned& dst, unsigned voff, const void* sbase) { asm volatile("global_load_ushort %0, %1, %2" : "=v"(dst) : "v"(voff), "s"(sbase)); }
DI void gld1s(unsigned& dst, unsigned voff, const void* sbase) { asm volatile("global_load_dword %0, %1, %2 sc1" : "=v"(dst) : "v"(voff), "s"(sbase)); }
template <int V> DI void SETM0(unsigned ldsbase) { asm volatile("s_add_u32 m0, %0, %1" ::"s"(ldsbase), "i"(V)); }
template <int GOFF> DI void GLD0(unsigned voff, const char* sbase) { asm volatile("global_load_lds_dwordx4 %0, %1 offset:%2" ::"v"(voff), "s"(sbase), "i"(GOFF) : "memory"); }
template <int GOFF, int V> DI void GLD1(unsigned voff, const char* sbase, unsigned ldsbase) {
    asm volatile("global_load_lds_dwordx4 %0, %1 offset:%3\n\ts_add_u32 m0, %2, %4" ::"v"(voff), "s"(sbase), "s"(ldsbase), "i"(GOFF), "i"(V) : "memory");
}
template <int L, int G> DI void dma_x(unsigned voff, const void* sbase, unsigned ldsbase) {
    asm volatile("s_add_u32 m0, %2, %4\n\ts_nop 0\n\tglobal_load_lds_dwordx4 %0, %1 offset:%3" ::"v"(voff), "s"(sbase), "s"(ldsbase), "i"(G), "i"(L - G) : "memory", "scc");
}
DI void ld_kv(v4u (&a)[15], int on  , unsigned k0, unsigned k1, unsigned k4, unsigned vo, v4u rk, const void* v, unsigned lds) {
    unsigned t;
    asm volatile("s_cmp_ge_u32 %5, 80\n\ts_cbranch_scc1 1f\n\ts_add_u32 m0, %12, %13\n\ts_movk_i32 %4, 0x2800\n\t"
                 "buffer_load_dwordx4 %6, %10, 0 offen nt lds\n\ts_add_u32 m0, m0, 0x400\n\tglobal_load_dwordx4 %0, %9, %11 nt\n\t"
                 "buffer_load_dwordx4 %7, %10, 0 offen nt lds\n\ts_add_u32 m0, m0, 0x400\n\tglobal_load_dwordx4 %1, %9, %11 offset:1280 nt\n\t"
                 "buffer_load_dwordx4 %7, %10, %4 offen nt lds\n\ts_add_u32 m0, m0, 0x400\n\ts_movk_i32 %4, 0x5000\n\tglobal_load_dwordx4 %2, %9, %11 offset:2560 nt\n\t"
                 "buffer_load_dwordx4 %7, %10, %4 offen nt lds\n\ts_add_u32 m0, m0, 0x400\n\tglobal_load_dwordx4 %3, %9, %11 offset:3840 nt\n\t"
                 "buffer_load_dwordx4 %8, %10, 0 offen nt lds\n1:"
                 : "=&v"(a[7]), "=&v"(a[8]), "=&v"(a[9]), "=&v"(a[10]), "=&s"(t)
                 : "s"(on), "v"(k0), "v"(k1), "v"(k4), "v"(vo), "s"(rk), "s"(v), "s"(lds), "i"(L_XR) : "scc", "memory");
}
DI int race(const char* a, const char* b) {
    unsigned u, v;
    int r;
    asm volatile("s_waitcnt lgkmcnt(0)\n\ts_load_dword %1, %3, 0x0\n\ts_load_dword %2, %4, 0x0\n\ts_waitcnt lgkmcnt(1)\n\ts_sleep %5\n\t"
                 "s_getreg_b32 %0, hwreg(HW_REG_IB_STS, 8, 4)\n\ts_waitcnt lgkmcnt(0)"
                 : "=&s"(r), "=&s"(u), "=&s"(v) : "s"(a), "s"(b), "i"(RSL) : "memory");
    return r;
}
DI void ld_wr(v4u (&a)[4], int on, unsigned o, const void* base) {
    asm volatile("s_cmp_ge_u32 %4, 80\n\ts_cbranch_scc0 1f\n\t.rept 4\n\tglobal_load_dword a[32+1], %5, %6\n\t.endr\n\ts_branch 2f\n1:\n\t"
                 "global_load_dwordx4 %0, %5, %6 nt\n\tglobal_load_dwordx4 %1, %5, %6 offset:1024 nt\n\tglobal_load_dwordx4 %2, %5, %6 offset:2048 nt\n\tglobal_load_dwordx4 %3, %5, %6 offset:3072 nt\n2:"
                 : "=&v"(a[0]), "=&v"(a[1]), "=&v"(a[2]), "=&v"(a[3]) : "s"(on), "v"(o), "s"(base) : "scc");
}
template <int L, int G> DI void dma_1(unsigned voff, const void* sbase, unsigned ldsbase) {
    asm volatile("s_add_u32 m0, %2, %4\n\ts_nop 0\n\tglobal_load_lds_dword %0, %1 offset:%3" ::"v"(voff), "s"(sbase), "s"(ldsbase), "i"(G), "i"(L - G) : "memory", "scc");
}
template <int L> DI void dma_n(unsigned voff, const void* sbase, unsigned ldsbase) {
    asm volatile("s_add_u32 m0, %2, %3\n\ts_nop 0\n\tglobal_load_lds_dwordx4 %0, %1 nt" ::"v"(voff), "s"(sbase), "s"(ldsbase), "i"(L) : "memory", "scc");
}
template <int A, int O> DI void lds2a(unsigned a) { asm volatile("ds_read_b64 a[%c1:%c1+1], %0 offset:%2" ::"v"(a), "i"(A), "i"(O) : "memory"); }
constexpr int L_EAR = L_XR + 8192;
constexpr int dma_goff(int it) { return it < 40 ? (it >> 2) * 128 : ((it - 100) / 5) * 256; }
constexpr int dma_loff(int it) { return it < 40 ? L_XR + ((it >> 2) % 3) * 4096 + (it & 3) * 1024 : ((it - 100) % 5) * 20480 + ((it - 100) / 5) * 2048; }
#define KFS 4
#define APOS 0
#define CWP 2
#define SAF 2
#define KD0 30
#define KST 59
#define KSL 5
#define KSC 11
#define XG0 5
#define XGS 1
#define WOD 1
#define USP 1
#define KSG 0
#define SGP0 0x122021
#define SGP1 0x202121
#define SBL 1
#define SMP 1
#define KW2E 0
#define KW2T 2
#define OTR 1
#define KQ0 0
#define KQ1 18
#define KQ2 4
#define KQ3 0
#define NPD 27
constexpr int kQ0 = KQ0, kQ1 = KQ1, kQ2 = KQ2, kQ3 = KQ3;
#define KW2S 16
#define KW2M 0x42
#define KFKS 13
#define KFKG 6
#define KUS 17
#define KTS 19
#define KFWP 13
constexpr int kW2S = KW2S, kW2M = KW2M;
#define KSA 4
constexpr int NW2L = (20 - kW2S) * __builtin_popcount(kW2M), NW2P = NW2L + KSA;
constexpr int P1_PRE = NPD + 7;
constexpr int p1_dma(int i) { return i < 5 ? 100 + i : i < 13 ? i - 5 : i < 18 ? 105 + (i - 13) : i < 22 ? 8 + (i - 18) : i < NPD ? 110 + (i - 22) : 400; }
constexpr int kQ1a = KST > KD0 ? kQ1 * (KST - KD0) / (117 - KD0) : 0;
constexpr int p1_pre(int i) { return i < kQ0 + kQ1a ? p1_dma(i) : i == kQ0 + kQ1a ? 400 : p1_dma(i - 1); }
constexpr int p1_item(int s, int g) {
    if (s == KFS && g == (XG0 == 5 ? 4 : 9)) return 324;
    if (WOD ? (s & 1) && s < 10 && (g == 3 || g == 8) : g == 9 && s < 10) return WOD ? 114 + s + (g == 8) : 115 + s;
    if (s == KSL && (g == KSG || g == KSG + 1)) return 202 + g - KSG;
    if (s == KSL && g == 6) return 204;
    if (s == KFWP && g == 1) return 208;
    if (s == KFKS && g == KFKG) return 206;
    if (USP && s == KUS && (g == 0 || g == 2 || g == 4 || g == 7)) return 210 + g / 2;
    if (s == KTS && g == 9) return 209;
    if (s >= kW2S && ((kW2M >> g) & 1)) return 500 + (s - kW2S) * __builtin_popcount(kW2M) + __builtin_popcount(kW2M & ((1 << g) - 1));
    if ((s & 1) == 0 && s <= 12 && g >= XG0 && (g - XG0) % XGS == 0 && (g - XG0) / XGS < 4) return 4 * (s / 2 + 3) + (g - XG0) / XGS;
    return -1;
}
constexpr int p1_cnt(int s, int g) {
    int n = P1_PRE;
    for (int t = 0; t <= s; ++t)
        for (int u = 0; u < 10; ++u) {
            if (t == s && u >= g) break;
            if (p1_item(t, u) >= 0) ++n;
            if (!USP && t == KUS && u == 0) n += 4;
        }
    return n;
}
constexpr int p1_pos(int code) {
    for (int i = 0; i < P1_PRE; ++i) if (p1_pre(i) == code) return i;
    for (int t = 0; t < 20; ++t)
        for (int u = 0; u < 10; ++u) if (p1_item(t, u) == code) return p1_cnt(t, u);
    return -1000;
}
constexpr int p1_lds(int s, int g) {
    constexpr int t0 = XG0 == 5 ? 0 : 5;
    if (s >= 2 && s < 10 && !(s & 1) && g >= t0 && g < t0 + 2) return 2 + s + g - t0;
    if (s == KSC + 1 && g == t0 + 2) return 38;
    if (s >= 14 && s < 19 && !(s & 1) && (g == 5 || g == 8)) return 30 + (s - 14) + g / 8;
    if (s == 19 && (g == 0 || g == 4)) return 36 + g / 4;
    if (s == 19 && g == 7) return 39;
    return -1;
}
constexpr int p1_nl(int s, int g0, int g1) {
    int n = 0;
    for (int g = g0; g < g1; ++g) n += (s < 19 && g < 5) + ((s & 1) && s < 19 && g >= 5 && g < 9) + (p1_lds(s, g) >= 0);
    return n;
}
constexpr int p1_cap(int s, int g) {
    int u = p1_nl(s, g, g + 1) + (g == 9 && s < 19) + ((s & 1) && s < 19 && g == 5) + ((s & 1) == 0 && g == XG0 - 1);
    const int it = p1_item(s, g), nt = g < 9 ? p1_item(s, g + 1) : -1;
    u += (it >= 0) + (nt >= 0 && nt < 200) + (it == 324);
    if ((s == 19 && g == 2) || (g == 9 && ((s & 3) == 2 || s == KUS - 1 || s == 18))) u += 2;
    return u < 2 ? 2 - u : 0;
}
constexpr int NFILL = 112;
constexpr int p1_from(int f) { return f < 40 ? 0 : f < 52 ? 9 : f < 72 ? KSC : 14; }
constexpr int p1_fill(int s, int g, int e) {
    int f = 0;
    for (int t = 0; t <= s; ++t)
        for (int u = 0; u < 10; ++u)
            for (int v = 0; v < p1_cap(t, u); ++v) {
                const bool go = f < NFILL && p1_from(f) <= t;
                if (t == s && u == g && v == e) return go ? f : -1;
                f += go;
                if (f == 40) f = 48;
            }
    return f;
}
static_assert(p1_fill(13, 10, 0) >= 52, "");
constexpr bool p1_all() {
    for (int i = 0; i < 40; ++i) if (p1_pos(i) < 0) return false;
    for (int i = 100; i < 125; ++i) if (p1_pos(i) < 0) return false;
    for (int i = 0; i < NW2L; ++i) if (p1_pos(500 + i) < 0) return false;
    return p1_pos(202) >= 0 && p1_pos(203) >= 0 && p1_pos(206) >= 0 && p1_pos(208) >= 0 && p1_pos(209) >= 0 && p1_pos(324) >= 0;
}
static_assert(p1_all() && p1_lds(14, 5) == 30 && p1_lds(14, 8) == 31 && p1_lds(16, 5) == 32, "");
constexpr int ring_req(int G, int I, int R, int X) {
    int n = I, e = 0;
    for (int t = 0; t <= G; ++t) {
        int r = -1;
        if (t % 5 == 0 || t % 5 == 3) {
            if (n < 40 && 5 * (n - R) + 4 < t) r = n++;
            else if (e < X && t % 10 == 3) r = 100 + e++;
        }
        if (t == G) return r;
    }
    return -1;
}
constexpr int g2_adr(int i, int kk) {
    int n = 0;
    for (int t = 0; t <= i; ++t)
        for (int u = 0; u < 5; ++u) {
            const bool ok = u == 4 || (u == 2 && (t < 11 || t > 15)) || (u == 3 && t < 5);
            if (t == i && u == kk) return ok ? n : -1;
            n += ok;
        }
    return -1;
}
constexpr int p1_lim(int n) { return n > 63 ? 63 : n; }
template <int IMM> DI void gld4s(v4u& dst, unsigned voff, const void* sbase) { asm volatile("global_load_dwordx4 %0, %1, %2 offset:%3 sc1" : "=v"(dst) : "v"(voff), "s"(sbase), "i"(IMM)); }
template <int IMM> DI void gld2s(v2u& dst, unsigned voff, const void* sbase) { asm volatile("global_load_dwordx2 %0, %1, %2 offset:%3 sc1" : "=v"(dst) : "v"(voff), "s"(sbase), "i"(IMM)); }
template <class T> DI void pin(T& v) { asm volatile("" : "+v"(v)); }
DI void mul2(float& a, float& b, float x, float y, float s) { asm volatile("v_mul_f32 %0, %2, %4\n\tv_mul_f32 %1, %3, %4" : "=&v"(a), "=&v"(b) : "v"(x), "v"(y), "v"(s)); }
DI void rot1(unsigned& a, unsigned x) { asm volatile("v_alignbit_b32 %0, %1, %1, 16" : "=v"(a) : "v"(x)); }
DI void mix1(unsigned& a, unsigned x, unsigned y) { asm volatile("v_bfi_b32 %0, %3, %1, %2" : "=v"(a) : "v"(x), "v"(y), "s"(0xffffu)); }
DI void ror1(unsigned& o, unsigned prev) { asm volatile("v_mov_b32_dpp %0, %1 wave_ror:1 row_mask:0xf bank_mask:0xf" : "=v"(o) : "v"(prev)); }
DI void shr1b(unsigned& o, unsigned x) { asm volatile("v_mov_b32_dpp %0, %1 wave_shr:1 row_mask:0xf bank_mask:0xf" : "+v"(o) : "v"(x)); }
DI void xor1(float& a, unsigned x, unsigned s) { asm volatile("v_xor_b32 %0, %1, %2" : "=v"(a) : "v"(x), "v"(s)); }
template <int G> DI void bc1(float& a, unsigned x) { asm volatile("v_mov_b32_dpp %0, %1 row_newbcast:%2 row_mask:0xf bank_mask:0xf" : "=v"(a) : "v"(x), "i"(G)); }
template <int G> DI void mulb(float& o, unsigned x, float y) { asm volatile("v_mul_f32_dpp %0, %1, %2 row_newbcast:%3 row_mask:0xf bank_mask:0xf" : "=v"(o) : "v"(x), "v"(y), "i"(G)); }
template <int N> DI void lgkm() { __builtin_amdgcn_s_waitcnt(0xC07F | (N << 8)); asm volatile("" ::: "memory"); }
DI void unp2(float& a, float& b, unsigned x) { asm volatile("v_lshlrev_b32 %0, 16, %2\n\tv_and_b32 %1, 0xffff0000, %2" : "=&v"(a), "=&v"(b) : "v"(x)); }
#define GL(a) "global_load_dwordx4 a[" #a ":" #a "+3], %7, %8 sc1\n\tglobal_load_dwordx4 a[" #a "+4:" #a "+7], %7, %8 offset:256 sc1\n\t"
#define AR(d, a, o) "v_accvgpr_read_b32 " d ", a[" #a "+" #o "]\n\t"
#define GS_(a) "s_waitcnt vmcnt(4)\n\t" AR("%2", a, 1) AR("%3", a, 3) AR("%4", a, 5) AR("%5", a, 7) AR("%0", a, 0) AR("%1", a, 2)                              \
    "v_xor_b32 %2, %9, %2\n\tv_bitop3_b32 %4, %4, %9, %2 bitop3:0xbe\n\tv_bitop3_b32 %4, %3, %9, %4 bitop3:0xbe\n\tv_bitop3_b32 %4, %5, %9, %4 bitop3:0xbe\n\t" \
    AR("%2", a, 4) AR("%3", a, 6) "v_cmp_ne_u32 vcc, 0, %4\n\ts_cbranch_vccz 2f\n\t" GL(a)
template <int A, int D> DI void gran_req(unsigned off, const void* base) {
    if constexpr (D > 0) __builtin_amdgcn_s_sleep(D);
    asm volatile("global_load_dwordx4 a[%c2:%c2+3], %0, %1 sc1\n\tglobal_load_dwordx4 a[%c2+4:%c2+7], %0, %1 offset:256 sc1" ::"v"(off), "s"(base), "i"(A) : "memory");
}
DI void pipe_gran(float& a, float& d, unsigned off, const void* base, unsigned tag, bool& dead) {
    unsigned n = dead ? 0u : kSpinCap, y, z;
    float a1, d1;
    asm volatile("1:\n\t" GS_(32) GS_(40) GS_(48) "s_sub_u32 %6, %6, 1\n\ts_cbranch_scc0 1b\n2:"
                 : "=&v"(a), "=&v"(d), "=&v"(a1), "=&v"(d1), "=&v"(y), "=&v"(z), "+s"(n) : "v"(off), "s"(base), "s"(tag) : "memory", "vcc", "scc");
    dead = __builtin_amdgcn_readfirstlane(n) == ~0u;
    a += a1; d += d1;
}
DI void fma2(float& a, float& b, float x, float y, float s, float t, float u, float v) {
    asm volatile("v_fma_f32 %0, %2, %4, %6\n\tv_fma_f32 %1, %3, %5, %7" : "=&v"(a), "=&v"(b) : "v"(x), "v"(y), "v"(s), "v"(t), "v"(u), "v"(v));
}
DI void fma2p(f32x2& o, float x, float y, float r, float u, float v) {
    asm("v_fmac_f32 %0, %1, %2" : "+v"(u) : "v"(x), "v"(r));
    asm("v_fmac_f32 %0, %1, %2" : "+v"(v) : "v"(y), "v"(r));
    o = f32x2{u, v};
}
DI void swp2n(unsigned& a, unsigned& b, unsigned& c, unsigned& d) { asm volatile("v_permlane16_swap_b32 %0, %1\n\tv_permlane16_swap_b32 %2, %3" : "+v"(a), "+v"(b), "+v"(c), "+v"(d)); }
DI void cp2(unsigned& a, unsigned& b, f32x4 v) { asm volatile("v_cvt_pk_bf16_f32 %0, %2, %3\n\tv_cvt_pk_bf16_f32 %1, %4, %5" : "=&v"(a), "=v"(b) : "v"(v.x), "v"(v.y), "v"(v.z), "v"(v.w)); }
DI void zero2(v2u& a) { asm volatile("v_mov_b64 %0, 0" : "=v"(a)); }
DI void mov1(unsigned& a, unsigned b) { asm volatile("v_mov_b32 %0, %1" : "=v"(a) : "v"(b)); }
DI void sw16(unsigned& a, unsigned& b) { asm volatile("v_permlane16_swap_b32 %0, %1" : "+v"(a), "+v"(b)); }
DI void sw32(unsigned& a, unsigned& b) { asm volatile("v_permlane32_swap_b32 %0, %1" : "+v"(a), "+v"(b)); }
DI void mrg8(float& a, float b) { asm volatile("v_mov_b32_dpp %0, %1 quad_perm:[0,1,2,3] row_mask:0xf bank_mask:0xc" : "+v"(a) : "v"(b)); }
DI void sel1(float& o, unsigned a, unsigned b, u64 m) { asm volatile("v_cndmask_b32 %0, %1, %2, %3" : "=v"(o) : "v"(a), "v"(b), "s"(m)); }
DI void count_(unsigned& side, unsigned v) {
    u64 m;
    unsigned n;
    asm volatile("v_cmp_lt_u32 vcc, 4, %3\n\tv_cmp_gt_u32 %1, 4, %3\n\ts_bcnt1_i32_b64 %0, vcc\n\ts_bcnt1_i32_b64 %2, %1\n\ts_cmp_gt_u32 %0, %2\n\ts_cselect_b32 %0, 0x2000, 0"
                 : "=&s"(side), "=&s"(m), "=&s"(n) : "v"(v) : "vcc", "scc");
}
DI void ne1(u64& m, v2u tag, v2u a) { asm volatile("v_cmp_ne_u64 %0, %1, %2" : "=s"(m) : "v"(tag), "v"(a)); }
DI void ne2(u64& m, v2u tag, v2u a, v2u b) { asm volatile("v_cmp_ne_u64 vcc, %1, %2\n\tv_cmp_ne_u64 %0, %1, %3\n\ts_or_b64 %0, %0, vcc" : "=&s"(m) : "v"(tag), "v"(a), "v"(b) : "vcc", "scc"); }
template <int G> DI void bc2b(float& a, float& b, float x, float y) {
    asm volatile("v_mov_b32_dpp %0, %2 row_newbcast:%4 row_mask:0xf bank_mask:0xf\n\tv_mov_b32_dpp %1, %3 row_newbcast:%4 row_mask:0xf bank_mask:0xf" : "=&v"(a), "=&v"(b) : "v"(x), "v"(y), "i"(G));
}
template <class T> DI void pins(T& v) { asm volatile("" : "+s"(v)); }
template <int I> DI void gld_w2(unsigned voff, const void* sbase) {
    asm volatile("global_load_dwordx4 " WA(2) ", %0, %1 offset:%3 sc1" ::"v"(voff), "s"(sbase), "i"(W2R + 4 * I), "i"((I % 5) * 1024 - 2048));
}
DI void gld2t(v2u& dst, unsigned voff, const void* sbase) { asm volatile("global_load_dwordx2 %0, %1, %2 sc1" : "+v"(dst) : "v"(voff), "s"(sbase)); }
DI void undef4(v4u (&x)[4]) { asm volatile("" : "=v"(x[0]), "=v"(x[1]), "=v"(x[2]), "=v"(x[3])); }
DI void wait1(v2u& v) { asm volatile("s_waitcnt vmcnt(0)" : "+v"(v)::"memory"); }
template <int N> DI void wait_fkn(v2u& v, v2u& u) { asm volatile("s_waitcnt vmcnt(%2)" : "+v"(v), "+v"(u) : "i"(N) : "memory"); }
DI void ld_ur(unsigned o, const void* base) {
    asm volatile("global_load_dwordx4 a[40:40+3], %0, %1 sc1\n\tglobal_load_dwordx4 a[44:44+3], %0, %1 offset:1024 sc1\n\t"
                 "global_load_dwordx4 a[48:48+3], %0, %1 offset:2048 sc1\n\tglobal_load_dwordx4 a[52:52+3], %0, %1 offset:3072 sc1" ::"v"(o), "s"(base));
}
template <int I> DI void ld_ur1(unsigned o, const void* base) { asm volatile("global_load_dwordx4 a[%c2:%c2+3], %0, %1 offset:%3 sc1" ::"v"(o), "s"(base), "i"(40 + 4 * I), "i"(1024 * I)); }
constexpr int kUY = KSA + (USP ? p1_cnt(20, 0) - 1 - p1_pos(213) : p1_cnt(20, 0) - p1_cnt(KUS, 0) - 4);
DI void st_ur(unsigned a) {
    asm volatile("s_waitcnt vmcnt(%1)\n\tds_write_b128 %0, a[40:40+3] offset:480\n\tds_write_b128 %0, a[44:44+3] offset:1504\n\t"
                 "ds_write_b128 %0, a[48:48+3] offset:2528\n\tds_write_b128 %0, a[52:52+3] offset:3552" ::"v"(a), "i"(kUY < NW2P ? kUY : NW2P) : "memory");
}
template <int A> DI void rda(unsigned& v) { asm volatile("v_accvgpr_read_b32 %0, a[%c1+0]" : "=v"(v) : "i"(A)); }
DI void poll1(unsigned a, const void* base) { asm volatile("global_load_dwordx2 a[36:36+1], %0, %1 sc1" ::"v"(a), "s"(base)); }
DI void poll2(unsigned a, unsigned b, const void* base) { asm volatile("global_load_dwordx2 a[36:36+1], %0, %2 sc1\n\tglobal_load_dwordx2 a[36+2:36+3], %1, %2 sc1" ::"v"(a), "v"(b), "s"(base)); }
DI void touch_(unsigned a, const void* base) { asm volatile("global_load_dword a[32+1], %0, %1" ::"v"(a), "s"(base)); }
template <int IMM> DI void gld4t(v4u& dst, unsigned voff, const void* sbase) { asm volatile("global_load_dwordx4 %0, %1, %2 offset:%3 sc1" : "+v"(dst) : "v"(voff), "s"(sbase), "i"(IMM)); }
template <int N> DI void vm_wait() { asm volatile("s_waitcnt vmcnt(%0)" ::"i"(N) : "memory"); }
DI float dot2a(unsigned a, unsigned b, float c) {
    return __builtin_amdgcn_fdot2_f32_bf16(__builtin_bit_cast(bf16x2v, a), __builtin_bit_cast(bf16x2v, b), c, false);
}
DI void disc(unsigned l0, unsigned l1, unsigned l2, f32x2& lam, f32x2& co) {
    const float dt = rbf(__builtin_amdgcn_exp2f(lo16f(l2) * 1.44269504088896f));
    const float lre = lo16f(l0), lim = lo16f(l1);
    const float e_ = __builtin_amdgcn_exp2f(dt * lre * 1.44269504088896f);
    const float rv = __builtin_amdgcn_fractf(dt * lim * 0.159154943091895f);
    lam = f32x2{e_ * __builtin_amdgcn_cosf(rv), e_ * __builtin_amdgcn_sinf(rv)};
    const float nr = lam.x - 1.0f, ni = lam.y, id = __builtin_amdgcn_rcpf(lre * lre + lim * lim);
    co = f32x2{(nr * lre + ni * lim) * id, (ni * lre - nr * lim) * id};
}
DI f32x4 unpk4(v2u u) { return f32x4{lo16f(u.x), hi16f(u.x), lo16f(u.y), hi16f(u.y)}; }
#define D2 "v_dot2_f32_bf16 "
#define CP "v_cvt_pk_bf16_f32 "
DI void d2x4(unsigned* o, unsigned a0, unsigned a1, unsigned a2, unsigned a3, unsigned b0, unsigned b1, unsigned b2, unsigned b3) {
    asm volatile(D2 "%0, %4, %8, 0\n\t" D2 "%1, %5, %9, 0\n\t" D2 "%2, %6, %10, 0\n\t" D2 "%3, %7, %11, 0"
                 : "=&v"(o[0]), "=&v"(o[1]), "=&v"(o[2]), "=&v"(o[3]) : "v"(a0), "v"(a1), "v"(a2), "v"(a3), "v"(b0), "v"(b1), "v"(b2), "v"(b3));
}
DI void cpx4(unsigned* o, unsigned a0, unsigned a1, unsigned a2, unsigned a3, unsigned b0, unsigned b1, unsigned b2, unsigned b3) {
    asm volatile(CP "%0, %4, %8\n\t" CP "%1, %5, %9\n\t" CP "%2, %6, %10\n\t" CP "%3, %7, %11"
                 : "=&v"(o[0]), "=&v"(o[1]), "=&v"(o[2]), "=&v"(o[3]) : "v"(a0), "v"(a1), "v"(a2), "v"(a3), "v"(b0), "v"(b1), "v"(b2), "v"(b3));
}
DI void cpx4i(unsigned* t, const unsigned* ka) {
    asm volatile(CP "%0, %0, %4\n\t" CP "%1, %1, %5\n\t" CP "%2, %2, %6\n\t" CP "%3, %3, %7"
                 : "+v"(t[0]), "+v"(t[1]), "+v"(t[2]), "+v"(t[3]) : "v"(ka[0]), "v"(ka[1]), "v"(ka[2]), "v"(ka[3]));
}

__global__ __launch_bounds__(256, 1) __attribute__((aligned(4096))) void k_main(Params p) {
    __shared__ __attribute__((aligned(16))) char smem[LDS_BYTES];
    const rsrc_t rs = __builtin_amdgcn_make_buffer_rsrc((void*)p.ws, 0, WS_BYTES, 0x00020000);
    const rsrc_t ro = __builtin_amdgcn_make_buffer_rsrc((void*)p.out, 0, S_ * D_ * 4, 0x00020000);
    const int w0 = __builtin_amdgcn_readfirstlane(threadIdx.x >> 6), b0 = blockIdx.x;
    bool dead = false;
    asm volatile("" ::: "a255");
    float SW[5];
    f32x4 xin[5];
    v2u gt[5];
    f32x4 acc[2][5];
    f32x4 rr[2];
    float tt[2][5][4];
    Lm lam1, lamq;
    f32x2 l8, l32, l128;
    u64 late;
    v4u cA[8], cB[8];
    int sgn_, sg4, ab[4], bb, zpa, uwa, spa, tbw, tbr, tbp, tbq, cwa, cw4, spl, dst[3];
    unsigned w2o, wro, fwo, fko, xso, gso, fgo1, fho1, fho, hso, eo, es, ho[4];
    bool cv0;
    const char* gn;
    rsrc_t rn;
    const char* kvtb;
    const char* w2b0[(NW2P + 4) / 5];
    int otw, otr0, otr4;
    int mt1_;
    auto addrs = [&](auto layer_, int lane, int w, int b) __attribute__((always_inline)) {
        constexpr int layer = layer_.value;
        const int c = lane & 15, q = lane >> 4;
        TILES()
        static_assert(l_sg(0, 0) == SC + 0xA900 && l_sg(0, 1) == SC + 0xB400 && l_sg(0, 2) == SC + 0xCE00 && l_sg(0, 3) == SC + 0xDC00, "");
        sgn_ = (layer ? l_sg(1, 0) + w * INBOX : SC + (int)((0xDC00CE00B400A900ull >> (16 * w)) & 0xffff)) + (q >> 1) * 640 + (q & 1) * 8;
        const int c4 = (c >> 1) * 16 + (c & 1) * 4;
        sg4 = sgn_ + c4; sgn_ += c * 16;
        const int pl = lane < 40 ? lane : 39, tbb = l_tb(layer, 0) + w * (layer ? INBOX : 5120);
        spl = SC + pl * 16;
        eo = OFF_EG + (unsigned)(layer * 163840 + nt1 * 640 + pl * 16);
        es = eo + (unsigned)(mt1 * 10240);
        spa = l_rs(layer, 0) + (2 * (mt2 & 1) + (w >> 1)) * (layer ? INBOX : 256) + (2 * (w & 1) + (c >> 3)) * 64 + (c & 7) * 4;
        tbw = tbb + c * 64 + ((q ^ ((c >> 1) & 3)) * 16);
        tbr = tbb + (2 * (c >> 2) + (q & 1)) * 64 + (((2 * (q >> 1) + ((c >> 1) & 1)) ^ (c >> 2)) * 16) + (c & 1) * 8;
        hso = N_HF + (unsigned)((layer * 8 + 2 * w + (q >> 1)) * 5 * NB_ + (2 * nt1 + (q & 1)) * 256 + c * 16);
        tbp = tbb + (8 * (q & 1) + (c >> 2)) * 64 + (((2 * (q >> 1) + ((c >> 1) & 1)) ^ (c >> 3)) * 16) + (c & 1) * 8;
        tbq = tbp ^ 32;
        cwa = L_CW + c * 16; cw4 = L_CW + c4;
        pin(sg4); pin(tbp); pin(tbq); pin(cw4);
        fho1 = lane ? kNone : N_FH + (unsigned)(layer * NB_ + (w >> 1) * 4096 + (nt1 * 2 + (w & 1)) * 128);
        fho = N_FH + (unsigned)(layer * NB_ + ((mt2 & 1) * 4 + w) * 1024 + (lane >> 3) * 128);
        pin(sgn_); pin(spl); pin(eo); pin(es); pin(spa); pin(tbw); pin(tbr); pin(hso); pin(cwa); pin(fho1); pin(fho);
        const unsigned hb_ = N_HF + (unsigned)((layer * 8 + 4 * (mt2 & 1)) * 5 * NB_ + w * 2048);
#pragma unroll
        for (int m = 0; m < 4; ++m) { ho[m] = hb_ + (unsigned)(((m + 1 + w) & 3) * 5 * NB_ + lane * 16); pin(ho[m]); }
#pragma unroll
        for (int m = 0; m < 3; ++m) { dst[m] = SC + ((w + 1 + m) & 3) * INBOX + m * 5120 + lane * 16; pin(dst[m]); }
        mt1_ = mt1;
        if constexpr (layer == 0) {
            const int r0 = SC + (w < 3 ? w : 0) * REGSZ, y0 = 2031 + 8 * q - c, l34 = lane < 33 ? lane : 33;
#pragma unroll
            for (int i = 0; i < 4; ++i) { ab[i] = r0 + ((y0 & 1) ? CV_O : CV_E) + ((y0 + 1) >> 1) * 4 - 448 - 512 - 1024 * i; pin(ab[i]); }
            bb = r0 + 32 * c + 16 * q;
            zpa = w < 3 ? r0 + l34 * 16 + (l34 >= 30 ? CV_E + 4096 - 480 : 0) + (l34 >= 32 ? CV_O + 4100 - 32 - CV_E - 4096 : 0) : SC + 47360 + lane * 16;
            uwa = (w < 3 ? r0 : SC + 47360 - 480) + lane * 16;
            xso = (unsigned)((64 * mt2 + 16 * w + (lane >> 2)) * D_ + 80 * nt2 + 8 * ((lane & 3) ^ q)) * 2u;
            gso = OFF_GT + (unsigned)((80 * nt2 + i32 + 32 * w) * GSTR + (q & 1) * 512 + (c >> 2) * 128 + (q >> 1) * 64 + (c & 3) * 16);
            fgo1 = lane == 0 && i32 + 32 * w < 80 ? OFF_FG + (unsigned)((nt2 * 96 + i32 + 32 * w) * 128) : kNone;
            pin(bb); pin(zpa); pin(uwa); pin(xso); pin(gso); pin(fgo1);
        }
        SCHED();
    };
    unsigned tag0, tag1;
    v2u tagv;
#define FLAG(off) { if (lane == 0) st64(tagv, rs, off); }
#define ISTAG(f) (((f.x ^ tag0) | (f.y ^ tag1)) == 0u)
#define KW_REQ() { ld_ur(wro, kvtb); static_for<NW2P>(LAMBDA(i_) { gld_w2<i_.value>(w2o, p.ws + 2048 + (i_.value / 5) * 5120); }); }
#define FW_OK() { if (__builtin_expect(late != 0ull, 0)) { WAVE_WAIT({ const v2u fk_ = ld64(rs, fko); const v2u fw_ = ld64(rs, fwo); ok = ISTAG(fk_) && ISTAG(fw_); }) KW_REQ() } }
#define WF(g, s) *(const v4u*)(smem + wa[(s) & 1][(g) < 4 ? (g) & 1 : 2] + ((g) & 2) * 20480 + ((s) >> 1) * 1024)
#define TT_GAP(n) { constexpr int mb_ = (n) / 10, nb_ = ((n) % 10) / 2, e_ = ((n) & 1) * 2; \
        mul2(tt[mb_][nb_][e_], tt[mb_][nb_][e_ + 1], rm_[4 * mb_ + e_], rm_[4 * mb_ + e_ + 1], SW[nb_]); }
    {
        const int tid = threadIdx.x, lane = tid & 63, w = w0, b = b0;
        const int c = lane & 15, q = lane >> 4;
        TILES()
        STAMP(0)
        const unsigned lds0 = (unsigned)(size_t)(LAS char*)smem;
        unsigned nonce;
        unsigned cpf0;
        {
            u64 pc;
            int len;
            asm volatile("s_getpc_b64 %0\nkpf_begin:\n\ts_mov_b32 %1, kpf_end-kpf_begin\n\ts_max_i32 %1, %1, kpf_end2-kpf_begin\n\ts_max_i32 %1, %1, kpf_end3-kpf_begin" : "=s"(pc), "=s"(len)::"scc");
            const int o0 = (((j & 15) * 4 + w) + 64 * (lane >> 3)) * 128;
            asm volatile("global_load_dword %0, %1, %2" : "=v"(cpf0) : "v"(o0 < len ? o0 : 0), "s"(pc));
        }
        const bool cv = 32 * w + i32 < 80;
        const int chn = 80 * nt2 + i32 + 32 * (cv ? w : 0);
        const bool kw = 32 * w + 31 - i32 < 80;
        const int ku = 80 * nt2 + (w == 2 ? 64 + (i32 & 15) : i32 + 32 * (w & 1)), kc = ku >> 6, kr = ku & 63;
        const int lc = cv ? lane : 8;
        const unsigned xl = lds0 + (unsigned)(w * REGSZ);
        v4u ax[15], sa[5], cr[2], wr[4];
        unsigned uL[3], uM[3], sdq, kso;
        const int cu_ = (b < 200 ? b : 0) * 4 + w, cdb = cu_ / 20, ccg = cu_ % 20;
        {
            unsigned cpo = (unsigned)(32 * ccg + (lane & 31)) * 2u, cpm = (unsigned)(40 * nt1 + (lane < 39 ? lane : 39)) * 2u;
            unsigned k1 = (unsigned)(32 * kr + 5 + (lane >> 3)) * 1280u + (unsigned)(64 * kc + 8 * (lane & 7)) * 2u, k0 = k1 - 10240u, k4 = lane < 24 ? k1 + 30720u : kNone;
            unsigned vo = k1 - 6400u + (unsigned)(lane >> 3) * 3840u;
            pin(cpo); pin(cpm); pin(k0); pin(k1); pin(k4); pin(vo);
            SCHED();
            gldu16(uL[0], cpo, p.Lre); gldu16(uL[1], cpo, p.Lim); gldu16(uL[2], cpo, p.logdt);
            gldu16(uM[0], cpm, p.Lre); gldu16(uM[1], cpm, p.Lim); gldu16(uM[2], cpm, p.logdt);
            const v4u rk = {(unsigned)(u64)p.k, (unsigned)((u64)p.k >> 32) & 0xffffu, S_ * D_ * 2u, 0x00020000u};
            ld_kv(ax, 32 * w + 31 - i32, k0, k1, k4, vo, rk, p.v, xl);
            SCHED();
        }
        const unsigned gof = OFF_GRP + (unsigned)mt1 * 0x200000u - ((unsigned)(u64)p.ws & 0x1fffffu);
        unsigned vote = 4u, side;
        unsigned wu;
        asm volatile("s_load_dword %0, %1, 0x0" : "=s"(wu) : "s"(p.ws + (gof + 0x1ff000u)));
        SCHED();
        auto small_ = [&]() __attribute__((always_inline)) {
            kso = (unsigned)(512 * kc + lane * 4);
            dma_1<L_EAR + 2048, 0>(kso, p.shk, xl); dma_1<L_EAR + 2304, 256>(kso, p.shk, xl);
            gld1s(nonce, kso & 4u, p.ws + OFF_NONCE);
            SCHED();
        };
        if (SMP == 0) small_();
        const u16* sab = w < 2 ? p.x0 : w == 2 ? p.Bre : p.Bim;
        unsigned sao;
        {
            int r0 = w < 2 ? 128 * mt1 + 8 * nt1 + 4 * w : 40 * nt1 + mt1, r1 = w < 2 ? 1 : 16, r2 = w < 2 ? 2047 : 40 * nt1 + 32 + (mt1 >> 1);
            pins(sab); pins(r0); pins(r1); pins(r2);
            const int r = r0 + q * r1;
            sao = (unsigned)((r < r2 ? r : r2) * 1280 + c * 16);
            pin(sao);
        }
        if (SAF == 0) static_for<5>(LAMBDA(i) { gld<256 * i.value>(sa[i.value], sao, sab); });
        SCHED();
        if (w == TW0 && !kw) tour<0>();
        STAMP(22)
        const int row8 = lane >> 3;
        const unsigned wvo = (unsigned)(40 * nt1 + row8) * 1280u + (unsigned)(((lane & 7) ^ ((w & 1) | ((row8 >> 1) << 1))) * 16 + (w >> 1) * 128);
        const char* const wbase = (const char*)((w & 1) ? p.Bim : p.Bre);
        const unsigned wl = lds0 + (unsigned)(((w & 1) * 10 + (w >> 1)) * 1024);
        const unsigned xvo0 = (unsigned)(8 * (row8 >> 1) + 2 * (row8 & 1)) * 1280u + (unsigned)(((lane & 7) ^ ((row8 >> 1) << 1)) * 16);
        const unsigned xvo1 = (xvo0 ^ 16u) + 1280u;
        const char* const xbase = (const char*)p.x0 + (size_t)(128 * mt1 + 32 * w) * 1280;
        unsigned xv[4] = {xvo0, xvo1, xvo0 + 5120u, xvo1 + 5120u}, wv[5];
#pragma unroll
        for (int i = 0; i < 5; ++i) { wv[i] = wvo + i * 10240u; pin(wv[i]); if (i < 4) pin(xv[i]); }
#define DMA_M0(it) SETM0<dma_loff(it) - dma_goff(it)>((it) < 40 ? xl : wl)
#define DMA_LD1(it, nx_) GLD1<dma_goff(it), dma_loff(nx_) - dma_goff(nx_)>((it) < 40 ? xv[(it) & 3] : wv[(it) < 40 ? 0 : ((it) - 100) % 5], (it) < 40 ? xbase : wbase, (nx_) < 40 ? xl : wl)
#define DMA_LD(it) GLD0<dma_goff(it)>((it) < 40 ? xv[(it) & 3] : wv[(it) < 40 ? 0 : ((it) - 100) % 5], (it) < 40 ? xbase : wbase)
        const int swz = (c & 1) | ((c >> 2) << 1);
        int wa[2][3];
#pragma unroll
        for (int i = 0; i < 2; ++i) {
            wa[i][0] = (c >> 3) * 20480 + (c & 7) * 128 + (((4 * i + q) ^ (c & 6)) * 16); wa[i][1] = (wa[i][0] ^ 16) + 10240;
            wa[i][2] = 81920 + (c & 1) * 10240 + (c >> 1) * 128 + (((4 * i + q) ^ ((c & 1) | ((c >> 2) << 1))) * 16);
            pin(wa[i][0]); pin(wa[i][1]); pin(wa[i][2]);
        }
        int xlane0 = L_XR + w * REGSZ + (c & 1) * 1024 + (c >> 1) * 128 + ((q ^ swz) * 16);
        int xlane1 = L_XR + w * REGSZ + (c & 1) * 1024 + (c >> 1) * 128 + (((4 + q) ^ swz) * 16);
        unsigned cvo = (unsigned)((16 * cdb + (lane >> 2)) * D_ + 32 * ccg + 8 * ((lane & 3) ^ q)) * 2u, for0, for1;
        int ear = L_EAR + w * REGSZ + c * 64 + (q ^ (c >> 2)) * 16, eas = L_EAR + 2048 + w * REGSZ + (lane & 7) * 64;
        int kra = L_XR + w * REGSZ + (lane >> 3) * 512 + (lane & 7) * 16;
        pin(ear); pin(eas); pin(kra);
        {
            const int G = 4 * ccg + q, nt = G / 5, g5 = G - 5 * nt;
            for0 = OFF_W2F + (unsigned)(((cdb / 5) * 200 + cdb % 5) * 1024 + c * 16 + (10 * (nt >> 2) + 2 * (g5 & 6) + ((nt >> 1) & 1)) * 5120 + (2 * (nt & 1) + (g5 & 1)) * 256);
            for1 = for0 + (g5 < 4 ? 10240u : 256u);
        }
        unsigned sto = OFF_ST + (unsigned)((mt1 * 16 + 4 * w + q) * 256 + c * 8), swo = OFF_ST + (unsigned)(((lane >> 2) * 16 + nt1) * 256 + 128 + (lane & 3) * 16);
        unsigned vto = OFF_VT + (unsigned)(mt1 * 512 + c * 32 + (q & 1) * 8);
        unsigned kvo = OFF_KVT + (unsigned)((64 * kc + 8 * (lane >> 3) + ((lane >> 2) & 1)) * KSTR + (8 * kc + (lane >> 3)) * KPS + (32 * kr + 8 * (lane & 3)) * 2);
        int kbw = L_KB + w * 1024 + (lane & 7) * 128 + (((lane >> 3) * 8) ^ ((lane & 7) * 16));
        int kbr = L_KB + w * 1024 + (lane >> 3) * 128 + (((lane & 7) * 16) ^ ((lane >> 3) * 16));
        const unsigned dum = kNone;
        unsigned fko1 = OFF_FK + (unsigned)((64 * kc + kr) * 32);
        if (!kw) kvo = dum;
        if (b >= 200) { for0 = dum; for1 = dum; cvo = (unsigned)((b * 4 + w) * 128); }
        wro = (unsigned)(chn * 4096 + lc * 16);
        cv0 = cv;
        pin(kvo); pin(kbw); pin(kbr); pin(fko1);
        pin(xlane0); pin(xlane1); pin(cvo); pin(for0); pin(for1); pin(sto); pin(swo); pin(vto); pin(wro);
        SCHED();
        auto pre = LAMBDA(i_) {
            constexpr int it = p1_dma(i_.value);
            SCHED();
            DMA_M0(it);
            asm volatile("s_nop 0");
            DMA_LD(it);
            SCHED();
        };
        fko = OFF_FK + (unsigned)(((chn & ~63) + (cv ? lane : 0)) * 32);
        { const int e_ = (lane & 7) < w ? w : (lane & 7) > w + 4 ? w + 4 : (lane & 7);
          fwo = OFF_FW + (unsigned)(((5 * nt2 + (lane < 40 ? lane >> 3 : 4)) * 20 + 4 * w + e_) * 32); }
        w2o = OFF_W2F + (unsigned)((nt2 * 40 + 10 * w) * 5120 + lane * 16);
        int ime = SC + (w < 3 ? w * REGSZ + CV_E : 43264) + 1008 - 16 * lane, imo = SC + (w < 3 ? w * REGSZ + CV_O + 4 : 47360) + 1008 - 16 * lane, im3 = w < 3 ? ime + 2048 : imo;
        int tca = L_TAB + c * 136, tcb = tca + 8 + 32 * w + 8 * q, tcc = L_TAB + (32 + (c >> 1)) * 136, tcd = tcc + 8 + 32 * w + 8 * q, tcl = L_TAB + (lane < 40 ? lane : 39) * 136, coa = L_CO + w * REGSZ + q * 64, rsa = l_rs(0, 0) + w * 256 + lane * 4;
        unsigned sgn = (c & 1) ? 0u : 0x80000000u;
        fko1 = kw && lane < 32 ? fko1 : OFF_FW + (unsigned)((b * 4 + w) * 32);
        pin(fko); pin(fwo); pin(w2o); pin(fko1); pin(ime); pin(imo); pin(im3); pin(tca); pin(tcb); pin(tcc); pin(tcd); pin(tcl); pin(coa); pin(rsa); pin(sgn);
        SCHED();
        auto cw_ = [&]() __attribute__((always_inline)) {
            dma_n<L_EAR>(cvo, p.Cre, xl); dma_n<L_EAR + 1024>(cvo, p.Cim, xl);
            dma_1<L_EAR + 2560, 0>((unsigned)(160 * nt2 + (lane < 39 ? lane : 39) * 4), p.Dskip, xl);
            ld_wr(wr, 32 * w + i32, wro, p.ssmk);
            gldu16(sdq, (unsigned)chn * 2u, p.ssmD);
        };
        if (SMP == 1) small_();
        static_assert(SMP == 0 || SAF > 0, "");
        if (SAF == 1) static_for<5>(LAMBDA(i) { gld<256 * i.value>(sa[i.value], sao, sab); });
        if (CWP == 0) cw_();
        STAMP(33)
        asm volatile("s_waitcnt vmcnt(%5)\n\ts_cmp_ge_u32 %4, 80\n\ts_cbranch_scc0 1f\n\ts_waitcnt vmcnt(%6)\n1:" : "+v"(uL[0]), "+v"(uL[1]), "+v"(uL[2]), "+v"(cpf0) : "s"(32 * w + 31 - i32), "i"(12 + (SAF < 2 ? 5 : 0) + (CWP ? 0 : 8)), "i"(3 + (SAF < 2 ? 5 : 0) + (CWP ? 0 : 8)) : "memory", "scc");
        pin(uM[0]); pin(uM[1]); pin(uM[2]);
        {
            f32x2 lam, co;
            disc(uL[0], uL[1], uL[2], lam, co);
            *(f32x2*)(smem + L_CO + w * REGSZ + (lane & 31) * 8) = 2.0f * co;
        }
        SCHED();
        STAMP(35)
        if (SAF == 2) static_for<5>(LAMBDA(i) { gld<256 * i.value>(sa[i.value], sao, sab); });
        asm volatile("s_waitcnt lgkmcnt(0)" : "+s"(wu)::"memory");
        if (CWP == 1) cw_();
        if (APOS == 0) addrs(std::integral_constant<int, 0>{}, lane, w, b);
        if (SAF == 3) static_for<5>(LAMBDA(i) { gld<256 * i.value>(sa[i.value], sao, sab); });
        if (CWP == 2) cw_();
        v4u kvt[4];
        unsigned ka[8], kt[8], se[8], vs[8], ko4[4], one2 = 0x3F803F80u;
        pin(one2);
        auto kvop = LAMBDA(n_) {
            constexpr int n = n_.value, h = n ? (n - 1) / 29 : -1, u = n ? (n - 1) % 29 : 26;
            if constexpr (u < 2) d2x4(ka + 4 * u, ax[0][h], ax[1][h], ax[2][h], ax[3][h], se[u], se[u], se[u], se[u]);
            else if constexpr (u == 8) { const v4u sk = ax[11 + h]; se[4] = sk[0] >> 16; se[5] = sk[2] & 0xffff0000u; se[6] = sk[0] & 0xffffu; se[7] = sk[2] << 16; pin(se[4]); pin(se[5]); pin(se[6]); pin(se[7]); }
            else if constexpr (u < 21) {
                constexpr int v = u < 8 ? u - 2 : u - 3, tp = 1 + v / 6, st = v % 6, e = st & 1;
                if constexpr (st < 2) d2x4(kt + 4 * e, ax[tp][h], ax[tp + 1][h], ax[tp + 2][h], ax[tp + 3][h], se[2 * tp + e], se[2 * tp + e], se[2 * tp + e], se[2 * tp + e]);
                else if constexpr (st < 4) cpx4i(kt + 4 * e, ka + 4 * e);
                else d2x4(ka + 4 * e, kt[4 * e], kt[4 * e + 1], kt[4 * e + 2], kt[4 * e + 3], one2, one2, one2, one2);
            }
            else if constexpr (u == 21) { vs[0] = ax[7][h] & 0xffffu; vs[1] = ax[8][h] << 16; vs[2] = ax[9][h] & 0xffffu; vs[3] = ax[10][h] << 16; pin(vs[0]); pin(vs[1]); pin(vs[2]); pin(vs[3]); }
            else if constexpr (u == 22) { vs[4] = ax[7][h] >> 16; vs[5] = ax[8][h] & 0xffff0000u; vs[6] = ax[9][h] >> 16; vs[7] = ax[10][h] & 0xffff0000u; pin(vs[4]); pin(vs[5]); pin(vs[6]); pin(vs[7]); }
            else if constexpr (u == 23) cpx4(ko4, ka[0], ka[2], ka[4], ka[6], ka[1], ka[3], ka[5], ka[7]);
            else if constexpr (u < 26) { constexpr int e = u - 24; d2x4(kt + 4 * e, ko4[2 * e], ko4[2 * e], ko4[2 * e + 1], ko4[2 * e + 1], vs[4 * e], vs[4 * e + 1], vs[4 * e + 2], vs[4 * e + 3]); }
            else if constexpr (u == 26) {
                if constexpr (h < 3) { const v4u sk = ax[12 + h]; se[0] = sk[1] >> 16; se[1] = sk[3] & 0xffff0000u; se[2] = sk[1] & 0xffffu; se[3] = sk[3] << 16; pin(se[0]); pin(se[1]); pin(se[2]); pin(se[3]); }
                else asm volatile("s_nop 2");
            }
            else if constexpr (u == 27) {
                cpx4(ko4, kt[0], kt[2], kt[4], kt[6], kt[1], kt[3], kt[5], kt[7]);
                *(v2u*)(smem + kbw) = v2u{ko4[0], ko4[1]};
                *(v2u*)(smem + (kbw ^ 64)) = v2u{ko4[2], ko4[3]};
            }
            else kvt[h] = *(const v4u*)(smem + kbr);
            SCHED();
        };
        auto deal = [&](auto a_, auto b_, auto n_, auto i_) __attribute__((always_inline)) {
            constexpr int a = a_.value, m = b_.value - a, lo = m * i_.value / n_.value, hi = m * (i_.value + 1) / n_.value;
            static_for<hi - lo>(LAMBDA(e_) { pre(std::integral_constant<int, a + lo + e_.value>{}); });
        };
#define IC(n) std::integral_constant<int, (n)>{}
        static_for<kQ0>(pre);
        static_assert(kQ0 == 0 && KD0 >= 8, "");
        asm volatile("s_waitcnt vmcnt(%4)" : "+v"(ax[7]), "+v"(ax[8]), "+v"(ax[9]), "+v"(ax[10]) : "i"(13 + kQ0) : "memory");
        ax[11] = *(const v4u*)(smem + eas);
        SCHED();
#pragma unroll
        for (int i = 0; i < 7; ++i) { ax[i] = *(const v4u*)(smem + kra + i * 128); SCHED(); }
#pragma unroll
        for (int i = 1; i < 4; ++i) ax[11 + i] = *(const v4u*)(smem + eas + i * 16);
        SCHED();
        pin(nonce);
        STAMP(30)
        unsigned tch = 0u;
        auto kvs = LAMBDA(n_) {
            site<1000 + n_.value>(); SCHED();
            if constexpr (n_.value == KD0) LGKM0()
            kvop(n_);
            if constexpr (n_.value >= KD0) deal(IC(kQ0), IC(kQ0 + kQ1), IC(117 - KD0), IC(n_.value - KD0));
        };
        static_assert((KST - 1) % 29 == 0, "");
        if (kw) static_for<KST>(kvs);
        else {
            {
                const unsigned t_ = (unsigned)__builtin_amdgcn_s_memtime(), f_ = ((nt1 ^ w) & 1) << 13;
                const unsigned o_ = gof + N_END + (t_ & 0x1e0u) * 512u + (unsigned)(nt1 * 512 + (w & 1) * 256) + (t_ & 0x10u) * 8u;
                const int r_ = race(p.ws + (o_ + f_), p.ws + (o_ + (f_ ^ 0x2000u)));
                vote += f_ ? r_ : -r_;
            }
            if (__builtin_expect(w == 3, !W3O)) {
                f32x2 lam, co, lp[16];
                disc(uM[0], uM[1], uM[2], lam, co);
                lp[0] = cmul(lam, lam); lp[0] = cmul(lp[0], lp[0]); lp[1] = cmul(lp[0], lp[0]);
                lp[0] = f32x2{1.f, 0.f};
                lp[2] = cmul(lp[1], lp[1]); lp[3] = cmul(lp[2], lp[1]); lp[4] = cmul(lp[2], lp[2]);
                const f32x2 z2 = {0.f, 0.f};
                f32x2 t4[4];
                cfma4(t4, lp[4], lp[1], lp[2], lp[3], lp[4], z2, z2, z2, z2);
                lp[5] = t4[0]; lp[6] = t4[1]; lp[7] = t4[2]; lp[8] = t4[3];
                cfma4(t4, lp[8], lp[1], lp[2], lp[3], lp[4], z2, z2, z2, z2);
                lp[9] = t4[0]; lp[10] = t4[1]; lp[11] = t4[2]; lp[12] = t4[3];
                cfma4(t4, lp[8], lp[5], lp[6], lp[7], lp[7], z2, z2, z2, z2);
                lp[13] = t4[0]; lp[14] = t4[1]; lp[15] = t4[2];
                f32x2* tl = (f32x2*)(smem + tcl);
                tl[0] = lam;
#pragma unroll
                for (int i = 0; i < 16; ++i) tl[1 + i] = lp[i];
                SCHED();
                asm volatile("kpf_end3:");
            }
            static_for<kQ1a>(LAMBDA(i_) { pre(IC(kQ0 + i_.value)); });
            undef4(kvt);
            asm volatile("" : "=v"(se[0]), "=v"(se[1]), "=v"(se[2]), "=v"(se[3]));
        }
        tag0 = __builtin_amdgcn_readlane(nonce, 0) + 1u; tag1 = __builtin_amdgcn_readlane(nonce + (tag0 == 0u ? 1u : 0u), 1);
        tagv = v2u{tag0, tag1}; pin(tagv);
        {
            asm volatile("s_waitcnt vmcnt(%5)" : "+v"(sa[0]), "+v"(sa[1]), "+v"(sa[2]), "+v"(sa[3]), "+v"(sa[4]) : "i"(8 + kQ0 + kQ1a) : "memory");
            float s1[2] = {0.f, 0.f}, s2[2] = {0.f, 0.f};
#pragma unroll
            for (int i = 0; i < 5; ++i)
#pragma unroll
                for (int e = 0; e < 4; ++e) { s1[e & 1] = dot2a(sa[i][e], one2, s1[e & 1]); s2[e & 1] = dot2a(sa[i][e], sa[i][e], s2[e & 1]); }
            const float a1 = rowsum16(s1[0] + s1[1]), a2 = rowsum16(s2[0] + s2[1]);
            const float mean = a1 * (1.0f / D_);
            const float var = fmaxf(a2 * (1.0f / D_) - mean * mean, 0.f);
            const float ri = __builtin_amdgcn_rcpf(rbf(__builtin_amdgcn_sqrtf(rbf(rbf(var) + 1e-5f))));
            v2u sv = {__float_as_uint(w >= 2 ? a1 : c ? -ri * rbf(mean) : ri), tag0};
            int o0 = w < 2 ? (mt1 * 16 + nt1) * 256 + 32 * w : ((mt1 >> 2) * 16 + nt1) * 256 + 112 + (mt1 & 3) * 16 + 8 * w, o1 = w < 2 ? 8 : 16384, o2 = w < 2 ? 64 : 0;
            pins(o0); pins(o1); pins(o2);
            unsigned so_ = c < 2 ? OFF_ST + (unsigned)(o0 + q * o1 + c * o2) : kNone;
            if (w >= 2 && lane == 2) { sv.x = vote; so_ = OFF_VT + (unsigned)((mt1 * 16 + nt1) * 32 + (w & 1) * 8); }
            pin(sv); pin(so_);
            st64(sv, rs, so_);
            SCHED();
        }
        bool kw2 = kw;
        if (kw2) {
            static_for<117 - KST>(LAMBDA(n_) { kvs(IC(KST + n_.value)); });
        } else {
            if (__builtin_expect(w == 3, !W3O)) {
            const int l = lane, l3 = l & 3;
            unsigned a = gof + N_SG + (unsigned)((((l >> 3) & 1) * 2 + (mt2 & 1)) * NB_ + ((l >> 1) & 3) * 2048 + nt2 * 256 + (l & 1) * 128);
            { unsigned t_ = OFF_EG + (unsigned)((l >= 21 ? 163840 - 640 : 0) + (mt1 * 16 + nt1) * 640 + (l < 26 ? l - 16 : 5) * 128); pin(t_); a = l >= 16 ? t_ : a; }
            { unsigned t_ = gof + N_PS + (unsigned)((((mt2 & 1) * 4 + l3) * 8 + nt2) * 128); pin(t_); a = l >= 28 ? t_ : a; }
            { unsigned t_ = gof + N_FH + (unsigned)(((l >> 2) & 1) * NB_ + (l3 >> 1) * 4096 + (nt1 * 2 + (l3 & 1)) * 128); pin(t_); a = l >= 32 ? t_ : a; }
            { unsigned t_ = gof + N_FX + (unsigned)(((2 * (mt2 & 1) + (l3 >> 1)) * 16 + nt2 * 2 + (l3 & 1)) * 128); pin(t_); a = l >= 40 ? t_ : a; }
            { unsigned t_ = OFF_FG + (unsigned)((nt2 * 96 + i32 + 16 * ((l3 + 1) & 6)) * 128); pin(t_); a = l >= 48 ? t_ : a; }
            const unsigned big = (l & 7) < 5 ? OFF_KVT + (unsigned)((l & 1) * 640 * 4608 + (80 * nt2 + i32) * GSTR) : OFF_W2F + (unsigned)(nt2 * 204800 + ((l & 7) - 5) * 98304);
            a = l >= 56 ? big : a;
            tch = a;
            pin(tch);
            asm volatile("kpf_end2:");
        }
            static_for<kQ1 - kQ1a>(LAMBDA(i_) { pre(IC(kQ0 + kQ1a + i_.value)); });
            if (w == TW) tour<1>();
            skip<SKIP0>();
            undef4(kvt);
        }
        SCHED();
        STAMP(34)
        if (APOS == 1) addrs(std::integral_constant<int, 0>{}, lane, w, b);
        v4u orr, oi;
        unsigned ie[4][4], io[4][4], iw;
        auto img = LAMBDA(n_) {
            constexpr int i = n_.value / 10, u = n_.value % 10;
            if constexpr (u == 0) { if constexpr (i > 0) ror1(iw, wr[i - 1].w); else { iw = 0u; pin(iw); } }
            else if constexpr (u < 5) rot1(ie[i][u - 1], wr[i][4 - u]);
            else if constexpr (u == 5) shr1b(iw, wr[i].w);
            else if constexpr (u < 9) mix1(io[i][u - 6], wr[i][9 - u], wr[i][8 - u]);
            else mix1(io[i][3], wr[i].x, iw);
        };
        auto tap0 = [&]() __attribute__((always_inline)) {
            wr[0].x = cvtpk(lo16f(wr[0].x) + (lane ? 0.f : lo16f(sdq)), hi16f(wr[0].x));
            pin(wr[0]);
            SCHED();
        };
        {
            static_assert(kQ0 + kQ1 <= 18, "");
            vm_wait<6 + kQ0 + kQ1>();
            cr[0] = *(const v4u*)(smem + ear); cr[1] = *(const v4u*)(smem + ear + 1024);
            static_for<5>(LAMBDA(i) { lds2a<22 + 2 * i.value, 2560 + 32 * i.value>(lds0 + (unsigned)(L_EAR + w * REGSZ + q * 8)); });
            asm volatile("s_waitcnt lgkmcnt(0)" : "+v"(cr[0]), "+v"(cr[1])::"memory");
            static_for<kQ2>(LAMBDA(i_) { pre(IC(kQ0 + kQ1 + i_.value)); });
            static_for<4>(LAMBDA(e_) {
                constexpr int e = e_.value;
                const f32x4 co = *(const f32x4*)(smem + coa + e * 16);
                const f32x2 o0 = lo16f(cr[0][e]) * f32x2{co.x, -co.y} - lo16f(cr[1][e]) * f32x2{co.y, co.x};
                const f32x2 o1 = hi16f(cr[0][e]) * f32x2{co.z, -co.w} - hi16f(cr[1][e]) * f32x2{co.w, co.z};
                orr[e] = cvtpk(o0.x, o1.x); oi[e] = cvtpk(o0.y, o1.y);
            });
            pin(orr); pin(oi);
            SCHED();
            static_for<NPD - kQ0 - kQ1 - kQ2>(LAMBDA(i_) { pre(IC(kQ0 + kQ1 + kQ2 + i_.value)); });
            for (int i = 0; i < 4; ++i) st128(kvt[i], rs, kvo + i * 2 * KSTR);
            st128(orr, rs, for0); st128(oi, rs, for1);
        }
        SCHED();
        STAMP(23)
        if (w == 3) tour<7>();
        asm volatile("s_waitcnt vmcnt(%5) lgkmcnt(0)" : "+v"(wr[0]), "+v"(wr[1]), "+v"(wr[2]), "+v"(wr[3]), "+v"(sdq) : "i"(P1_PRE - 1 - p1_pos(3)) : "memory");
        __builtin_amdgcn_s_barrier();
        asm volatile("" ::: "memory");
        STAMP(24)
        site<10>();
        v4u wf[2][5], af[2][2][2];
#pragma unroll
        for (int nb = 0; nb < 5; ++nb) wf[0][nb] = WF(nb, 0);
#pragma unroll
        for (int i = 0; i < 4; ++i) af[0][i >> 1][i & 1] = *(const v4u*)(smem + ((i >> 1) ? xlane1 : xlane0) + (i & 1) * 2048);
        SCHED();
        tap0();
        site<11>();
        kvtb = p.ws + OFF_KVT + (chn * KPAD + (chn >> 3) * KPS);
        pins(kvtb);
#pragma unroll
        for (int i = 0; i < (NW2P + 4) / 5; ++i) { w2b0[i] = p.ws + 2048 + i * 5120; pins(w2b0[i]); }
        v2u sg, tc[2], fk, fw, vt;
        v4u sw0;
        v2u zq[2];
        float rr_[8];
        unsigned sx[4], sz[4];
        u64 odd = 0xAAAAAAAAAAAAAAAAull;
        pins(odd);
        auto filler = LAMBDA(f_) {
            constexpr int f = f_.value;
            if constexpr (f < 0) {}
            else if constexpr (f < 40) img(IC(f));
            else if constexpr (f == 48) { lam1.r = __uint_as_float(tc[0].x); xor1(lam1.s, tc[0].y, sgn); }
            else if constexpr (f == 49) { lamq.r = __uint_as_float(tc[1].x); xor1(lamq.s, tc[1].y, sgn); }
            else if constexpr (f < 52) zero2(zq[f - 50]);
            else if constexpr (f == 52) { sx[0] = sw0.x; mov1(sx[1], sx[0]); }
            else if constexpr (f == 53) { sz[0] = sw0.z; mov1(sz[1], sz[0]); }
            else if constexpr (f < 56) bc1<f - 54>(rr_[f - 54], sg.x);
            else if constexpr (f == 56) sw16(sx[0], sx[1]);
            else if constexpr (f == 57) sw16(sz[0], sz[1]);
            else if constexpr (f < 60) mov1(sx[f - 56], sx[f - 58]);
            else if constexpr (f < 62) mov1(sz[f - 58], sz[f - 60]);
            else if constexpr (f < 64) sw32(sx[f - 62], sx[f - 60]);
            else if constexpr (f < 66) sw32(sz[f - 64], sz[f - 62]);
            else if constexpr (f < 72) {
                bc1<f - 64>(rr_[f - 64], sg.x);
                if constexpr (f == 66) { SW[0] = __uint_as_float(sx[0]); SW[1] = __uint_as_float(sz[0]); SW[2] = __uint_as_float(sx[1]); SW[3] = __uint_as_float(sz[1]); }
                if constexpr (f == 71) sel1(SW[4], sx[2], sz[2], odd);
            }
            else mulb<8 + (f - 72) % 8>(tt[(f - 72) % 8 / 4][(f - 72) / 8][(f - 72) % 4], sg.x, SW[(f - 72) / 8]);
        };
        auto kstep = LAMBDA(s_) {
            constexpr int s = s_.value, ph = s >> 2, s4 = s & 3, cu = s & 1, nx = cu ^ 1, jj = s >> 1, pp = jj & 1;
            if constexpr (s4 == 3 && ph < 4) {
                constexpr int pw = p1_pos(104 + 5 * (ph + 1)), px = p1_pos(4 * (jj + 1) + 3), pm = pw > px ? pw : px;
                constexpr int vm = p1_lim(p1_cnt(s, 0) - 1 - pm);
                asm volatile("" ::: "memory");
                __builtin_amdgcn_s_waitcnt((vm & 15) | ((vm >> 4) << 14) | 0x70);
                __builtin_amdgcn_s_barrier();
                asm volatile("" ::: "memory");
                STAMP(25 + ph)
                SCHED();
            }
            if constexpr (s == KSC) {
                static_assert(s4 == 3 && p1_pos(204) < p1_pos(4 * (jj + 1) + 3), "");
                pin(sg); pin(sw0); pin(vt);
                if (__builtin_expect(__builtin_amdgcn_ballot_w64((__builtin_amdgcn_bitop3_b32(sg.y, sw0.y, tag0, 0x7E) | __builtin_amdgcn_bitop3_b32(vt.y, sw0.w, tag0, 0x7E)) != 0u) != 0ull, 0))
                    WAVE_WAIT({ sg = ld64(rs, sto); sw0 = ld128(rs, swo); vt = ld64(rs, vto); ok = ((sg.y ^ tag0) | (sw0.y ^ tag0) | (sw0.w ^ tag0) | (vt.y ^ tag0)) == 0u; })
                ORDER();
                STAMP(7)
            }
            if constexpr (s == kW2S && KUS > kW2S) { vm_wait<p1_cnt(s, 0) - 1 - p1_pos(208)>(); SCHED(); }
            if constexpr (s == KUS) {
                static_assert(p1_pos(206) > p1_pos(208), "");
                wait_fkn<p1_cnt(s, 0) - 1 - p1_pos(206)>(fk, fw);
                if (!USP) ld_ur(wro, kvtb);
                SCHED();
            }
            if constexpr (s4 != 3 || ph == 4) lgkm<(s == 0 ? 0 : p1_nl(s - 1, 5 - cu, 10) - 1)>();
            SCHED();
            static_for<10>(LAMBDA(g_) {
                constexpr int g = g_.value, mb = g / 5, k = g % 5, it = p1_item(s, g), nt_ = g < 9 ? p1_item(s, g + 1) : -1;
                constexpr bool d0 = it >= 0 && it < 200, d1 = nt_ >= 0 && nt_ < 200;
                if constexpr (s == 0) mf0(acc[mb][k], af[pp][cu][mb], wf[cu][k]); else mf(acc[mb][k], af[pp][cu][mb], wf[cu][k]);
                SCHED();
                if constexpr (s < 19) {
                    if constexpr (g < 5) wf[nx][g] = WF(g, s + 1);
                    if constexpr (cu == 1 && g == 5) vm_wait<p1_lim(p1_cnt(s, 5) - 1 - p1_pos(4 * (jj + 1) + 3))>();
                    if constexpr (cu == 1 && g >= 5 && g < 9)
                        af[pp ^ 1][(g - 5) >> 1][(g - 5) & 1] = *(const v4u*)(smem + (((g - 5) >> 1) ? xlane1 : xlane0) + ((jj + 1) % 3) * 4096 + ((g - 5) & 1) * 2048);
                }
                if constexpr (cu == 0 && g == XG0 - 1 && s > 0) lgkm<p1_nl(s - 1, 8, 10) + p1_nl(s, 0, XG0 - 1)>();
                constexpr int lq = p1_lds(s, g);
                if constexpr (lq >= 4 && lq < 8) (lq == 4 ? lam1.a : lq == 5 ? lam1.b : lq == 6 ? lamq.a : lamq.b) = *(const f32x2*)(smem + (lq < 6 ? tca : tcb) + (lq & 1) * 2176);
                else if constexpr (lq == 8 || lq == 9) tc[lq - 8] = *(const v2u*)(smem + (lq == 8 ? tcc : tcd));
                else if constexpr (lq == 10) { const f32x2* tp = (const f32x2*)(smem + tcl); l8 = tp[2]; l32 = tp[5]; }
                else if constexpr (lq == 11) l128 = *(const f32x2*)(smem + tcl + 72);
                else if constexpr (lq >= 30 && lq < 33) *(v4u*)(smem + (lq < 32 ? ime + 1024 * (lq - 30) : im3)) = v4u{ie[33 - lq][0], ie[33 - lq][1], ie[33 - lq][2], ie[33 - lq][3]};
                else if constexpr (lq >= 33 && lq < 37) *(v4u*)(smem + imo + 1024 * (36 - lq)) = v4u{io[lq - 33][0], io[lq - 33][1], io[lq - 33][2], io[lq - 33][3]};
                else if constexpr (lq == 37) *(v4u*)(smem + im3 + 1024) = v4u{ie[0][0], ie[0][1], ie[0][2], ie[0][3]};
                else if constexpr (lq == 38) *(unsigned*)(smem + rsa) = sg.x;
                else if constexpr (lq == 39) *(v4u*)(smem + zpa) = v4u{zq[0].x, zq[0].y, zq[1].x, zq[1].y};
                static_for<p1_cap(s, g)>(LAMBDA(e_) { filler(IC(p1_fill(s, g, e_.value))); });
                if constexpr (s == 18 && g == 9) ne2(late, tagv, fk, fw);
                if constexpr (s == 19 && g == 5) count_(side, vt.x);
                if constexpr (s == 19 && g == 2) { if (__builtin_expect(late != 0ull, 0)) poll2(fko, fwo, p.ws); }
                if constexpr (it >= 0) {
                    if constexpr (d0 && d1) DMA_LD1(it, d1 ? nt_ : 0);
                    else if constexpr (d0) DMA_LD(it);
                    else if constexpr (it == 202) gld2s<0>(sg, sto, p.ws);
                    else if constexpr (it == 203) gld4s<0>(sw0, swo, p.ws);
                    else if constexpr (it == 204) gld2s<0>(vt, vto, p.ws);
                    else if constexpr (it == 206) gld2s<0>(fk, fko, p.ws);
                    else if constexpr (it == 208) gld2s<0>(fw, fwo, p.ws);
                    else if constexpr (it == 209) { unsigned t2 = tch + side; asm volatile("v_cndmask_b32 %0, %1, %0, %2" : "+v"(t2) : "v"(tch), "s"(0x0000FFFFF000FFFFull)); touch_(t2, p.ws); }
                    else if constexpr (it >= 210 && it < 214) ld_ur1<it - 210>(wro, kvtb);
                    else if constexpr (it >= 500) gld_w2<it % 50>(w2o, w2b0[(it % 50) / 5]);
                    else { vm_wait<p1_cnt(s, g) - P1_PRE>(); st64(tagv, rs, fko1); }
                }
                if constexpr (d1 && !d0) DMA_M0(d1 ? nt_ : 0);
                site<2000 + 10 * s + g>();
                SCHED();
            });
        };
        static_for<20>(kstep);
        static_for<NFILL - p1_fill(19, 10, 0)>(LAMBDA(i_) { filler(IC(p1_fill(19, 10, 0) + i_.value)); });
        SCHED();
        rr[0] = f32x4{rr_[0], rr_[1], rr_[2], rr_[3]}; rr[1] = f32x4{rr_[4], rr_[5], rr_[6], rr_[7]};
        ACC_FENCE10(acc[0], acc[1]);
        gn = p.ws + (gof + side);
        rn = __builtin_amdgcn_make_buffer_rsrc((void*)gn, 0, N_END, 0x00020000);
        STAMP(1)
    }

    auto layer_body = LAMBDA(layer_) {
        constexpr int layer = layer_.value;
        const int sga = sgn_;
        const bool cv = layer == 0 && cv0;
        v2u fh;
        unsigned ks[5];
        f32x4 ca[8], a2[4][5];
        unsigned gp[4][4];
        auto gst = LAMBDA(i_) {
            constexpr int i = i_.value >> 2, u = i_.value & 3;
            if constexpr (u < 2) cp2(gp[i][2 * u], gp[i][2 * u + 1], ca[2 * i + u]);
            else if constexpr (u == 2) swp2n(gp[i][0], gp[i][2], gp[i][1], gp[i][3]);
            else st128(v4u{gp[i][0], gp[i][1], gp[i][2], gp[i][3]}, rs, gso + i * 1024);
        };
        float rsr, rsm;
        St bv[8];
        {
            St a0, a1;
            auto w2s = LAMBDA(n_) {
                constexpr int lo = KSA * n_.value / 4, hi = KSA * (n_.value + 1) / 4;
                if constexpr (layer == 0) { static_for<hi - lo>(LAMBDA(e_) { constexpr int i = NW2L + lo + e_.value; gld_w2<i>(w2o, w2b0[i / 5]); }); SCHED(); }
            };
            static_for<2>(LAMBDA(m_) {
                constexpr int mb = m_.value;
#pragma unroll
                for (int i = 0; i < 4; ++i) fma2p(bv[4 * mb + i].a, acc[mb][0][i], acc[mb][1][i], rr[mb][i], tt[mb][0][i], tt[mb][1][i]);
#pragma unroll
                for (int i = 0; i < 4; ++i) fma2p(bv[4 * mb + i].b, acc[mb][2][i], acc[mb][3][i], rr[mb][i], tt[mb][2][i], tt[mb][3][i]);
                const f32x4 v = __builtin_elementwise_fma(acc[mb][4], rr[mb], f32x4{tt[mb][4][0], tt[mb][4][1], tt[mb][4][2], tt[mb][4][3]});
#pragma unroll
                for (int i = 0; i < 4; ++i) bv[4 * mb + i].c = v[i];
                SCHED();
                w2s(IC(2 * mb));
                if constexpr (mb == 0) run3(a0, bv[0], bv + 1, lam1); else run4(a1, a0, bv + 4, lam1);
                SCHED();
                if constexpr (mb == 0) w2s(IC(1));
            });
            *(f32x2*)(smem + sga) = a1.a; *(f32x2*)(smem + sga + 256) = a1.b; *(float*)(smem + sg4 + 512) = a1.c;
            SCHED();
            w2s(IC(3));
        }
        SCHED();
        const int w_ = w0;
        {
        const int w = w_, mt1 = mt1_;
        if (layer == 0 && __builtin_expect(late != 0ull, 0)) {
            unsigned f0_, f1_, f2_, f3_;
            asm volatile("s_waitcnt vmcnt(0)\n\t" AR("%0", 36, 0) AR("%1", 36, 1) AR("%2", 36, 2) AR("%3", 36, 3) : "=v"(f0_), "=v"(f1_), "=v"(f2_), "=v"(f3_)::"memory");
            late = __builtin_amdgcn_ballot_w64(((f0_ ^ tag0) | (f1_ ^ tag1) | (f2_ ^ tag0) | (f3_ ^ tag1)) != 0u);
            ORDER();
            if (late == 0ull) KW_REQ()
        }
        asm volatile("s_waitcnt lgkmcnt(0)" ::: "memory");
        __builtin_amdgcn_s_barrier();
        asm volatile("" ::: "memory");
        STAMP(layer ? 14 : 2)
#pragma unroll
        for (int s = 0; s < 5; ++s) ks[s] = (unsigned)s * NB_;
        constexpr int NW2A = NW2P + 7 * kCR1 + kXA, NW3 = NW2P + kN3E + kN3;
        auto w2ld = LAMBDA(i_) {
            constexpr int i = i_.value;
            gld_w2<i>(w2o, p.ws + 2048 + (i / 5) * 5120);
        };
        auto w2a = LAMBDA(i_) {
            constexpr int k = i_.value;
            if constexpr (k < kXA) { w2ld(std::integral_constant<int, NW2P + 2 * k>{}); w2ld(std::integral_constant<int, NW2P + 2 * k + 1>{}); }
            else w2ld(std::integral_constant<int, NW2P + kXA + k>{});
        };
        unsigned xtb = (unsigned)(size_t)(LAS char*)smem + (unsigned)(l_tb(0, 0) + w * 5120);
        auto w2x = LAMBDA(i_) {
            constexpr int i = NW2A + i_.value;
            if constexpr (i < 50) w2ld(std::integral_constant<int, i>{});
            else if constexpr (i < 53) dma_x<(i - 50) * 1024, (i < 52 ? (i - 50) * 64 : 96)>(xso, p.x0, xtb);
        };
        auto w2b = LAMBDA(i_) {
            constexpr int k = i_.value - 7 * kCR1;
            constexpr int ne = KW2E < 53 - NW2A ? KW2E : 53 - NW2A;
            if constexpr (k < ne) w2x(std::integral_constant<int, k>{});
            constexpr int k1 = k - kFHK - 1;
            if constexpr (k1 >= 0 && k1 < KW2T) { w2x(std::integral_constant<int, ne + 2 * k1>{}); w2x(std::integral_constant<int, ne + 2 * k1 + 1>{}); }
            else if constexpr (k1 >= 0) w2x(std::integral_constant<int, ne + KW2T + k1>{});
            static_assert(ne <= kFHK + 1 && 53 - NW2A - ne - KW2T + kFHK < 7 * (8 - kCR1) - kFHP + 1, "");
            if constexpr (k == kFHK) { vm_wait<ne>(); st64(tagv, rn, fho1); STAMP(39) }
            if constexpr (k == kFHK - 1) { STAMP(38) }
            if constexpr (k == 7 * (8 - kCR1) - kFHP) poll1(fho, gn);
            static_assert(NW2A + 7 * (8 - kCR1) >= 53 && NW2A <= 50 && NW3 <= NW2A, "");
        };
        auto partb = [&]() __attribute__((always_inline)) {
            const float* sp = (const float*)(smem + spa);
            rsr = sp[0]; rsm = sp[8];
            if (layer == 1) { pin(rsr); pin(rsm); }
            if (layer == 1) {
                static_for<5>(LAMBDA(n_) {
                    constexpr int nb = n_.value;
                    unsigned d0, d1;
                    rda<22 + 2 * nb>(d0); rda<23 + 2 * nb>(d1);
                    a2[3][nb] = xin[nb] + (xin[nb] * rsr + rsm) * unpk4(v2u{d0, d1}); pin(a2[3][nb]);
                });
            }
        };
        constexpr int LBW = layer ? 2 : 3;
        if (w == 3) {
            site<3 + 10 * layer>(); SCHED();
            f32x4 s4[8];
#pragma unroll
            for (int i = 0; i < 8; ++i) { s4[i] = *(const f32x4*)(smem + spl + (l_sg(layer, i & 3) - SC + (i >> 2) * 640)); SCHED(); }
            f32x2 sv[16], x[4], P[4][4];
#pragma unroll
            for (int i = 0; i < 16; ++i) { const f32x4 t = s4[(i >> 2) + 2 * (i & 2)]; sv[i] = (i & 1) ? f32x2{t.z, t.w} : f32x2{t.x, t.y}; }
            unsigned er, ei;
            {
                f32x2 u[4];
                cfma4n(u, l8, sv[0], sv[4], sv[8], sv[12], sv[1], sv[5], sv[9], sv[13]);
                agg(er, ei, x, P[0][2], u, sv, l8, l32, l128);
            }
            st128(v4u{er, tag0, ei, tag0}, rs, es);
            SCHED();
            P[0][0] = f32x2{0.f, 0.f}; P[0][1] = x[0]; P[0][3] = cfma(l32, P[0][2], x[2]);
            if (layer == 0) { FW_OK() static_for<kN3E>(LAMBDA(i_) { w2ld(std::integral_constant<int, NW2P + i_.value>{}); }); }
#pragma unroll
            for (int kk = 1; kk < 4; ++kk) cfma4(P[kk], l8, P[kk - 1][0], P[kk - 1][1], P[kk - 1][2], P[kk - 1][3], sv[kk - 1], sv[kk + 3], sv[kk + 7], sv[kk + 11]);
#pragma unroll
            for (int i = 0; i < 8; ++i) { const f32x2 a = P[2 * (i >> 2)][i & 3], b_ = P[2 * (i >> 2) + 1][i & 3]; *(f32x4*)(smem + spl + (l_sg(layer, i & 3) - SC + (i >> 2) * 640)) = f32x4{a.x, a.y, b_.x, b_.y}; }
            partb();
        }
        int w1_ = w, w3_ = w;
        if (layer == 1) { pins(w1_); pins(w3_); }
        if (layer == 1 && w1_ < LBW) { site<4 + 10 * layer>(); SCHED(); partb(); if constexpr (layer == 1) { if (w == 0) tour<6>(); if constexpr (LBR == 1) room<3000>(); } }
        if (w3_ == LBW) {
            site<5 + 10 * layer>(); SCHED();
            f32x2 wg[17], t4[4];
            unsigned ea[15], tg[15];
            v4u eg[15];
            constexpr bool early = (LBE >> layer) & 1;
            const f32x2 z2 = {0.f, 0.f};
#pragma unroll
            for (int i = 0; i < 15; ++i) { ea[i] = (unsigned)(mt1 - 1 - i) * 10240u; pins(ea[i]); ea[i] = ea[i] < kNone ? ea[i] : kNone; pins(ea[i]); }
            if constexpr (early) {
                __builtin_amdgcn_s_sleep(layer ? KLB1 : KLB0);
#pragma unroll
                for (int i = 0; i < 15; ++i) eg[i] = ld128s(rs, eo, ea[i]);
                SCHED();
            }
            wg[1] = cmul(l128, l128); wg[2] = cmul(wg[1], wg[1]); wg[3] = cmul(wg[2], wg[1]); wg[4] = cmul(wg[2], wg[2]);
#pragma unroll
            for (int i = 4; i < 16; i += 4) {
                cfma4(t4, wg[i], wg[1], wg[2], wg[3], wg[4], z2, z2, z2, z2);
#pragma unroll
                for (int e = 0; e < 4; ++e) wg[i + 1 + e] = t4[e];
            }
#pragma unroll
            for (int i = 0; i < 15; ++i) { if (i) pin(wg[i]); tg[i] = mt1 - 1 - i < 0 ? 0u : tag0; pins(tg[i]); }
            if (layer == 1) partb();
            if constexpr (layer == 0) tour<3>();
            f32x2 H0, H1;
            unsigned spins = dead ? kSpinCap : 0u;
            if constexpr (layer == 1 && LBR == 0) { room<1800>(); site<16>(); }
            if constexpr (!early) __builtin_amdgcn_s_sleep(layer ? KLB1 : KLB0);
            SCHED();
            auto eval = [&](const v4u (&e)[15]) __attribute__((always_inline)) {
                unsigned x = __builtin_amdgcn_bitop3_b32(e[0].y, e[0].w, tg[0], 0x7E);
                H0 = f32x2{__uint_as_float(e[0].x), __uint_as_float(e[0].z)}; H1 = f32x2{0.f, 0.f};
#pragma unroll
                for (int i = 1; i < 15; i += 2) {
                    x |= __builtin_amdgcn_bitop3_b32(e[i].y, e[i].w, tg[i], 0x7E) | __builtin_amdgcn_bitop3_b32(e[i + 1].y, e[i + 1].w, tg[i + 1], 0x7E);
                    pin(x);
                    gacc2(H0, H1, wg[i], wg[i + 1], e[i], e[i + 1]);
                    SCHED();
                }
                *(f32x2*)(smem + spl + (L_CW - SC)) = H0 + H1;
                return __builtin_amdgcn_ballot_w64(x != 0u) != 0ull;
            };
            bool again = true;
            if constexpr (early) again = eval(eg);
            if (again) {
                bool miss;
#pragma nounroll
                do {
                    asm volatile("" ::: "memory");
                    v4u e2[15];
#pragma unroll
                    for (int i = 0; i < 15; ++i) e2[i] = ld128s(rs, eo, ea[i]);
                    SCHED();
                    miss = eval(e2);
                    if (__builtin_expect(miss, 0) && ++spins > kSpinCap) { dead = true; miss = false; }
                } while (miss);
            }
            ORDER();
            STAMP(layer ? 36 : 37)
            if (layer == 0) {
                static_for<NW3 - NW2P - kN3E>(LAMBDA(i_) { w2ld(std::integral_constant<int, NW2P + kN3E + i_.value>{}); });
            }
        }
        int w2_ = w;
        pins(w2_);
        if constexpr (layer == 0) site<21>();
        if (layer == 0) UNDEF8(ca);
        if (layer == 0 && w2_ != LBW) {
            if (cv) {
                site<2>(); SCHED();
                partb();
                {
                    FW_OK()
                    st_ur((unsigned)(size_t)(LAS char*)smem + (unsigned)uwa);
                    CV_LOAD_A(cA, 0, 0, 0) CV_LOAD_A(cA, 0, 0, 1)
                    SCHED();
                    static_for<8>(LAMBDA(a_) { constexpr int a = a_.value; CV_LOAD_B(cB, a, 0) SCHED(); });
                    CV_LOAD_A(cA, 1, 0, 0) CV_LOAD_A(cA, 1, 0, 1)
                    SCHED();
                }
                STAMP(32)
                conv_part<0, kCR1, CPF>(ca, cA, cB, smem, ab, bb, w2a, LAMBDA(i_) {});
            } else {
                if constexpr (layer == 0) tour<5>();
                FW_OK()
                static_for<NW2A - NW2P>(LAMBDA(i_) { w2ld(std::integral_constant<int, NW2P + i_.value>{}); __builtin_amdgcn_s_sleep(1); });
                partb();
                if constexpr (layer == 0) room<3900>();
            }
        }
        asm volatile("s_waitcnt lgkmcnt(0)" ::: "memory");
        __builtin_amdgcn_s_barrier();
        asm volatile("" ::: "memory");
        STAMP(layer ? 15 : 4)
        {
            St cw, ex, hv[8];
            cw.a = *(const f32x2*)(smem + cwa); cw.b = *(const f32x2*)(smem + cwa + 256); cw.c = *(const float*)(smem + cw4 + 512);
            ex.a = *(const f32x2*)(smem + sga); ex.b = *(const f32x2*)(smem + sga + 256); ex.c = *(const float*)(smem + sg4 + 512);
            SCHED();
#pragma unroll
            for (int s = 1; s < 5; ++s) pins(ks[s]);
            SCHED();
            keep14(hv, cw, ex, bv, lamq, lam1);
            keep4(hv + 4, hv[3], bv + 4, lam1);
            SCHED();
            v2u ta[5], td[5];
            f32x2 hb[9];
            static_for<5 + SBL>(LAMBDA(r_) {
                constexpr int r = r_.value, rd = r < 2 ? r : r == 2 ? 4 : r - 1, ro_ = r - SBL < 2 ? r - SBL : r - SBL == 2 ? 4 : r - SBL - 1;
                if constexpr (r < 5) {
                    float e[8];
                    unsigned o[4];
#pragma unroll
                    for (int i = 0; i < 8; ++i) e[i] = rd == 4 ? hv[i].c : rd < 2 ? hv[i].a[rd & 1] : hb[i + 1][rd & 1];
                    if constexpr (r == 0) b3cv(hb, o, cw.b, ex.b, bv[0].b, bv[1].b, lamq.b, lam1.b, e);
                    else if constexpr (r < 3) b3cv(hb + 3 * r, o, hb[3 * r - 1], bv[3 * r - 1].b, bv[3 * r].b, bv[3 * r + 1].b, lam1.b, lam1.b, e);
                    else for (int i = 0; i < 4; ++i) o[i] = cvtpk(e[2 * i], e[2 * i + 1]);
                    *(v4u*)(smem + tbw + rd * 1024) = v4u{o[0], o[1], o[2], o[3]};
                    SCHED();
                    if constexpr (r < 3) { bfin(hb[3 * r + 2], hb[3 * r + 1], lam1.b); SCHED(); }
                    ta[rd] = __builtin_bit_cast(v2u, trread(smem, (rd < 4 ? tbp : tbr) + rd * 1024));
                    td[rd] = __builtin_bit_cast(v2u, trread(smem, (rd < 4 ? tbq + 256 : tbr + 512) + rd * 1024));
                    SCHED();
                }
                if constexpr (r >= SBL) { __builtin_amdgcn_raw_buffer_store_b128(v4u{ta[ro_].x, ta[ro_].y, td[ro_].x, td[ro_].y}, rn, hso, ks[ro_], SC1); SCHED(); }
            });
        }
        if (layer == 1) { DRAIN(); st64(tagv, rn, fho1); }
        STAMP(layer ? 16 : 5)
        if (cv) {
            if constexpr (layer == 0) site<6>();
            SCHED();
            if constexpr (!CPF) {
                CV_LOAD_A(cA, 0, kCR1, 0) CV_LOAD_A(cA, 0, kCR1, 1)
                SCHED();
                static_for<8>(LAMBDA(a_) { constexpr int a = a_.value; CV_LOAD_B(cB, a, kCR1) SCHED(); });
                static_for<7>(LAMBDA(a_) { constexpr int a = a_.value + 1; CV_LOAD_A(cA, a, kCR1, 0) CV_LOAD_A(cA, a, kCR1, 1) });
                SCHED();
            }
            conv_part<kCR1, 8, false>(ca, cA, cB, smem, ab, bb, w2b, LAMBDA(g_) {
                constexpr int g = g_.value;
                if constexpr ((g == 14 || g == 16 || g == 18 || g == 20)) gst(std::integral_constant<int, (g - 14) / 2>{});
                if constexpr ((g == 26 || g == 28 || g == 30 || g == 32)) gst(std::integral_constant<int, 4 + (g - 26) / 2>{});
            });
            asm volatile("" : "+v"(ca[0]), "+v"(ca[1]), "+v"(ca[2]), "+v"(ca[3]), "+v"(ca[4]), "+v"(ca[5]), "+v"(ca[6]), "+v"(ca[7]));
            static_for<8>(LAMBDA(i_) {
                constexpr int n = 8 + i_.value;
                if constexpr (n == 9) asm volatile("s_nop 0");
                if constexpr ((n & 3) == 2) asm volatile("s_nop 1");
                gst(std::integral_constant<int, n>{}); SCHED();
            });
        } else if (layer == 0) {
            constexpr int ne = KW2E < 53 - NW2A ? KW2E : 53 - NW2A;
            static_for<54 - NW2A>(LAMBDA(i_) {
                if constexpr (i_.value == ne) { vm_wait<ne>(); st64(tagv, rn, fho1); }
                if constexpr (i_.value < 53 - NW2A) { w2x(i_); __builtin_amdgcn_s_sleep(1); }
            });
            if (w == 3) { static_for<NW2A - NW3>(LAMBDA(i_) { w2ld(std::integral_constant<int, NW3 + i_.value>{}); __builtin_amdgcn_s_sleep(1); }); if constexpr (layer == 0) tour<2>(); }
            poll1(fho, gn);
            if constexpr (layer == 0) room<1000>();
            DRAIN();
        }
        if (layer == 0) { unsigned f0_, f1_; asm volatile("s_waitcnt vmcnt(%2)\n\t" AR("%0", 36, 0) AR("%1", 36, 1) : "=v"(f0_), "=v"(f1_) : "i"(4) : "memory"); fh = v2u{f0_, f1_}; }
        STAMP(layer ? 29 : 6)
        }

            v2u pt[4];
            if (layer == 0) {
                u64 hl;
                ne1(hl, tagv, fh);
                if (__builtin_expect(hl != 0ull, 0)) {
                    gld2t(fh, fho, gn); wait1(fh); st64(tagv, rs, fgo1);
                    WAVE_RETRY(ISTAG(fh), fh = ld64(rn, fho))
                }
                ORDER();
            }
            else { room<3400>(); site<18>(); pipe_flag<KPH0>(pt, fho, gn, tagv, dead); ORDER(); }
            STAMP(layer ? 17 : 31)
            site<7 + 10 * layer>(); SCHED();
            constexpr int RING2 = layer ? KR2 : KR20;
            v4u ar[RING2];
#pragma unroll
            for (int i = 0; i < KI2; ++i) { ar[i] = ld128s(rn, ho[i / 10] + (i & 1) * 1024, ks[i % 10 >> 1]); SCHED(); }
        int b_o = b0, w_o = w0;
        asm volatile("" : "+s"(b_o), "+s"(w_o));
        const int b = b_o, w = w_o;
        TILES()
        v4u gl[3];
        v2u f0;
        unsigned fgo, sgo, sgs, glo, gw, gr, srco;
        u64 glate = 0ull;
        const char *gb1 = p.ws + 32 * GSTR, *gb2 = p.ws + 48 * GSTR;
        {
            unsigned xs_[3][4], dq[2];
            float zx[4], zd[4], zt[4], zz[5][4];
            unsigned sA = SC + w * INBOX, s1 = N_SG + (layer * 2 + (mt2 & 1)) * NB_ + w * 2048, s2 = s1 + nt2 * 256, sF = OFF_FG + (nt2 * 96 + 4 * w) * 128, sG = OFF_GT + (80 * nt2 + 4 * w) * GSTR + mt2 * 128,
                     sW = sA + 8192 + (w & 1) * 128, sR = SC + 8192 + w * 32, sX = l_tb(0, 0) + w * 5120, k160 = 160, kIn = INBOX;
            pins(sA); pins(s1); pins(s2);
            if (layer == 0) { pins(sF); pins(sG); pins(sW); pins(sR); pins(sX); pins(k160); pins(kIn); pins(gb1); pins(gb2); }
            unsigned l16, c16, q0, q1, c2, xr0, xr1, ta_, tb_;
#define A1(n_, v_, e_) else if constexpr (n == n_) { v_ = e_; pin(v_); }
            auto adr = LAMBDA(n_) {
                constexpr int n = n_.value + (layer && n_.value >= 4 ? 12 : 0);
                if constexpr (n == 0) { l16 = (unsigned)dst[0] & 1023u; pin(l16); }
                A1(1, c16, l16 & 0xf0u) A1(2, q0, (l16 >> 8) & 1u) A1(3, q1, l16 >> 9)
                A1(4, c2, (l16 >> 6) & 3u) A1(5, xr0, (c16 << 2) + sX) A1(6, ta_, q1 ^ c2) A1(7, xr0, (q0 << 3) + xr0) A1(8, xr0, (ta_ << 4) + xr0) A1(9, xr1, xr0 ^ 32u)
                else if constexpr (n >= 10 && n < 15) { constexpr int nb = n - 10; const v2u t = *(const v2u*)(smem + ((nb & 5) ? xr1 : xr0) + (nb >> 1) * 1024); xs_[nb >> 1][2 * (nb & 1)] = t.x; xs_[nb >> 1][2 * (nb & 1) + 1] = t.y; }
                A1(16, srco, l16 + sA) A1(17, ta_, l16 & 0x300u) A1(18, sgo, s1 + l16 + ta_) A1(19, sgs, (q0 << 3) + c16) A1(20, ta_, (q1 << 30) + s2) A1(21, sgs, sgs + ta_)
                else if constexpr (layer == 1) {}
                A1(22, ta_, l16 < 624u ? l16 : 624u) A1(23, tb_, ta_ & 0x380u) A1(24, ta_, ta_ & 0x60u) A1(25, fgo, (tb_ << 4) + sF) A1(26, fgo, (ta_ << 2) + fgo)
                A1(27, glo, q1 * (16u * GSTR) + sG) A1(28, ta_, l16 & 0x180u) A1(29, tb_, l16 & 0x70u) A1(30, glo, ta_ * (GSTR / 128u) + glo) A1(31, glo, glo + tb_)
                A1(32, ta_, l16 >> 7) A1(33, tb_, l16 & 0x30u) A1(34, gw, (tb_ << 1) + sW) A1(35, gw, __umul24(ta_, k160) + gw) A1(36, ta_, c2 & 1u) A1(37, gw, (ta_ << 4) + gw)
                A1(38, ta_, l16 >> 8) A1(39, gr, (q0 << 7) + sR) A1(40, gr, __umul24(ta_, kIn) + gr) A1(41, gr, __umul24(c2, k160) + gr) A1(42, tb_, tb_ >> 1) A1(43, gr, gr + tb_)
            };
            SCHED();
            if constexpr (STAMPS == 1) { STAMP(layer ? 50 : 40) }
            static_for<40>(LAMBDA(i_) {
                constexpr int i = i_.value, m = i / 10, s = i % 10;
                static_for<5>(LAMBDA(k_) {
                    constexpr int kk = k_.value, pv = ring_req(5 * i + kk, KI2, RING2, 0);
                    if constexpr (s == 0 && m < 3) mfa0<kk>(a2[m][kk], ar[i % RING2]); else mfa<5 * s + kk>(a2[m][kk], ar[i % RING2]);
                    SCHED();
                    if constexpr (i == 0 && kk == 0 && layer == 1) { pin(pt[0]); pin(pt[1]); pin(pt[2]); pin(pt[3]); }
                    if constexpr (g2_adr(i, kk) >= 0) adr(IC(g2_adr(i, kk)));
                    if constexpr (layer == 0 && i >= 1 && i < 28 && (kk & 1)) {
                        constexpr int n = 2 * (i - 1) + (kk >> 1) - 9, nb = n / 9, u = n % 9 - 1;
                        if constexpr (n < 0) {}
                        else if constexpr (u < 0) { rda<22 + 2 * nb>(dq[0]); rda<23 + 2 * nb>(dq[1]); }
                        else if constexpr (u < 2) unp2(zx[2 * u], zx[2 * u + 1], xs_[nb >> 1][2 * (nb & 1) + u]);
                        else if constexpr (u < 4) unp2(zd[2 * u - 4], zd[2 * u - 3], dq[u - 2]);
                        else if constexpr (u < 6) fma2(zt[2 * u - 8], zt[2 * u - 7], zx[2 * u - 8], zx[2 * u - 7], rsr, rsr, rsm, rsm);
                        else fma2(zz[nb][2 * u - 12], zz[nb][2 * u - 11], zt[2 * u - 12], zt[2 * u - 11], zd[2 * u - 12], zd[2 * u - 11], zx[2 * u - 12], zx[2 * u - 11]);
                    }
                    if constexpr (layer == 0 && i == 0 && kk == 1) { vm_wait<KI2 - 1 + (ring_req(0, KI2, RING2, 0) >= 0)>(); st64(tagv, rs, fgo1); }
                    if constexpr (layer == 0 && i == kGPI && kk == 2) gld2s<0>(f0, fgo, p.ws);
                    if constexpr (layer == 0 && i == kGCI) {
                        static_assert(kGCI + KR20 > 40, "");
                        if constexpr (kk == 0) { __builtin_amdgcn_s_waitcnt(0x0F70); pin(f0); gld4s<0>(gl[0], glo, p.ws); }
                        if constexpr (kk == 1) { gld4s<0>(gl[1], glo, gb1); gld4s<0>(gl[2], glo, gb2); }
                        if constexpr (kk == 3) ne1(glate, tagv, f0);
                    }
                    if constexpr (pv >= 0) ar[pv % RING2] = ld128s(rn, ho[pv / 10] + (pv & 1) * 1024, ks[pv % 10 >> 1]);
                    if constexpr (kk == 2 && m >= 1 && s >= 1 && s < 6) *(f32x4*)(smem + dst[m - 1] + (s - 1) * 1024) = a2[m - 1][s - 1];
                    site<4000 + 1000 * layer + 5 * i + kk>();
                    if constexpr (i >= 37) a2[i - 37][kk] = *(const f32x4*)(smem + srco + (i - 37) * 5120 + kk * 1024);
                    SCHED();
                });
                if constexpr (i == 10) { asm volatile("s_waitcnt lgkmcnt(0)" ::: "memory"); __builtin_amdgcn_s_barrier(); asm volatile("" ::: "memory"); }
                if constexpr (i == 28 && layer == 0) {
#pragma unroll
                    for (int nb = 0; nb < 5; ++nb) { a2[3][nb] = f32x4{zz[nb][0], zz[nb][1], zz[nb][2], zz[nb][3]}; pin(a2[3][nb]); }
                }
                if constexpr (i == 36) {
                    asm volatile("s_waitcnt lgkmcnt(0)" ::: "memory");
                    __builtin_amdgcn_s_barrier(); asm volatile("" ::: "memory");
                }
                if constexpr (STAMPS == 1 && (i == 0 || i == 9 || i == 10 || i == 15 || i == 25 || i == 35 || i == 36)) { STAMP((layer ? 51 : 41) + (i == 0 ? 0 : i == 9 ? 1 : i == 10 ? 2 : i == 15 ? 3 : i == 25 ? 4 : i == 35 ? 5 : 6)) }
                SCHED();
            });
            STAMP(layer ? 18 : 8)
        }
#pragma unroll
        for (int nb = 0; nb < 5; ++nb) a2[0][nb] = (a2[0][nb] + a2[1][nb]) + a2[2][nb];
        asm volatile("s_nop 1" : "+v"(a2[3][0]), "+v"(a2[3][1]), "+v"(a2[3][2]), "+v"(a2[3][3]), "+v"(a2[3][4]),
                     "+v"(a2[0][0]), "+v"(a2[0][1]), "+v"(a2[0][2]), "+v"(a2[0][3]), "+v"(a2[0][4]));
        float ty;
        {
            f32x4 s1, s2;
#pragma unroll
            for (int nb = 0; nb < 5; ++nb) {
                const f32x4 y = a2[3][nb] + a2[0][nb];
                xin[nb] = y;
                if (nb == 0) { s1 = y; s2 = y * y; } else { s1 += y; s2 += y * y; }
            }
            ty = xsum2h(hsum4(s1), hsum4(s2));
            st64(v2u{__float_as_uint(ty), tag0}, rn, sgs);
        }
        SCHED();
        STAMP(layer ? 19 : 9)
        if (layer == 0 && __builtin_expect(glate != 0ull, 0)) {
            asm volatile("s_waitcnt vmcnt(0)" : "+v"(gl[0]), "+v"(gl[1]), "+v"(gl[2])::"memory");
            WAVE_WAIT({ f0 = ld64(rs, fgo); ok = ISTAG(f0); })
            gld4t<0>(gl[0], glo, p.ws); gld4t<0>(gl[1], glo, gb1); gld4t<0>(gl[2], glo, gb2);
            asm volatile("s_waitcnt vmcnt(0)" : "+v"(gl[0]), "+v"(gl[1]), "+v"(gl[2])::"memory");
        }
        if (layer == 0) {
            asm volatile("s_waitcnt vmcnt(1)" : "+v"(gl[0]), "+v"(gl[1]), "+v"(gl[2])::"memory");
            *(v4u*)(smem + gw) = gl[0]; *(v4u*)(smem + gw + 1280) = gl[1]; *(v4u*)(smem + gw + 1920) = gl[2];
            asm volatile("s_waitcnt lgkmcnt(0)" ::: "memory");
            __builtin_amdgcn_s_barrier(); asm volatile("" ::: "memory");
#pragma unroll
            for (int nb = 0; nb < 5; ++nb) gt[nb] = __builtin_bit_cast(v2u, trread(smem, gr + 640 * nb));
        } else {
            ACC_TIE5(gt);
            if constexpr (OTR) {
#pragma unroll
                for (int nb = 0; nb < 5; ++nb) *(f32x4*)(smem + otw + nb * 64) = xin[nb];
#pragma unroll
                for (int nb = 0; nb < 5; ++nb) xin[nb] = *(const f32x4*)(smem + (nb < 4 ? otr0 + (nb & 1) * 8 * 336 + (nb >> 1) * 128 : otr4));
            }
        }
        SCHED();
        auto sgp = LAMBDA(p_) {
            constexpr int P = layer ? SGP1 : SGP0;
            static_for<3>(LAMBDA(k_) { constexpr int d = P >> (16 - 8 * k_.value); if constexpr (((d >> 4) & 15) == p_.value) { gran_req<32 + 8 * k_.value, d & 15>(sgo, gn); SCHED(); } });
        };
        sgp(IC(0));
        f32x4 gf[5], yg[5];
#pragma unroll
        for (int nb = 0; nb < 5; ++nb) { gf[nb] = unpk4(gt[nb]); yg[nb] = xin[nb] * gf[nb]; pin(gf[nb]); pin(yg[nb]); }
        SCHED();
        sgp(IC(1));
        int lane_o;
        asm volatile("v_mbcnt_lo_u32_b32 %0, -1, 0\n\tv_mbcnt_hi_u32_b32 %0, -1, %0" : "=v"(lane_o));
        const int lane = lane_o, c = lane & 15, q = lane >> 4;
        unsigned xs[3];
#pragma unroll
        for (int i = 0; i < 3; ++i) {
            const int ch = 10 * nt2 + 4 * i + 2 * (q & 1) + (q >> 1);
            xs[i] = N_X2 + (unsigned)((2 * (mt2 & 1) + (w >> 1)) * 5 * NB_ + (ch >> 4) * NB_ + ((c >> 2) & 1) * 4096 + (ch & 15) * 256 + (8 * (w & 1) + 4 * (c >> 3) + (c & 3)) * 16);
        }
        unsigned pso = q < 2 ? N_PS + (unsigned)((((mt2 & 1) * 4 + w) * 8 + nt2) * 128 + c * 8 + q * 4) : kNone;
        unsigned fxo1 = N_FX + (unsigned)(((2 * (mt2 & 1) + (w >> 1)) * 16 + nt2 * 2 + (w & 1)) * 128);
        unsigned oo = OTR ? (unsigned)(((64 * mt2 + 16 * w + (lane >> 3)) * D_ + 80 * nt2) * 4 + (nt2 & 1) * 64 + (lane & 7) * 16) : (unsigned)(((64 * mt2 + 16 * w + c) * D_ + 80 * nt2 + 4 * q) * 4);
        unsigned oo1 = oo + 8 * D_ * 4, oo4 = (unsigned)(((64 * mt2 + 16 * w + (lane >> 3) + (lane & 4) * 2) * D_ + 80 * nt2) * 4 + 256 - (nt2 & 1) * 256 + (lane & 3) * 16);
        int bpi = (lane >> 3) * 4;
        u64 up = 0xF0F0F0F0F0F0F0F0ull;
        xs[2] = (q & 1) ? kNone : xs[2];
        fxo1 = lane ? kNone : fxo1;
        if (layer == 0) { pin(xs[0]); pin(xs[1]); pin(xs[2]); pin(pso); pin(fxo1); } else { pin(oo); if (OTR) { pin(oo1); pin(oo4); pin(bpi); pins(up); } }
        SCHED();
        {
            float a, d;
            sgp(IC(2));
            room<(layer ? 900 : 1500)>(); site<9 + 10 * layer>();
            pipe_gran(a, d, sgo, gn, tag0, dead);
            STAMP(layer ? 20 : 10)
            xsum2(a, d);
            const float me = a * (1.0f / D_);
            const float ri = __builtin_amdgcn_rsqf(fmaxf(d * (1.0f / D_) - me * me, 0.f) + 1e-5f), mr = -me * ri;
            float rv[3] = {ri, ri, ri}, mv[3] = {mr, mr, mr};
            if constexpr (layer == 1 && OTR) {
#pragma unroll
                for (int i = 0; i < 2; ++i) { rv[i] = __int_as_float(__builtin_amdgcn_ds_bpermute(bpi + 32 * i, __float_as_int(ri))); mv[i] = __int_as_float(__builtin_amdgcn_ds_bpermute(bpi + 32 * i, __float_as_int(mr))); }
                SCHED();
            }
            static_for<5>(LAMBDA(n_) {
                constexpr int nb = layer ? n_.value : (n_.value + 4) % 5;
                if constexpr (layer == 1 && OTR && nb == 4) { sel1(rv[2], __float_as_uint(rv[0]), __float_as_uint(rv[1]), up); sel1(mv[2], __float_as_uint(mv[0]), __float_as_uint(mv[1]), up); }
                xin[nb] = yg[nb] * rv[nb < 4 ? nb & 1 : 2] + (gf[nb] * mv[nb < 4 ? nb & 1 : 2] + xin[nb]);
                if constexpr (layer == 1) { __builtin_amdgcn_raw_buffer_store_b128(__builtin_bit_cast(v4u, xin[nb]), ro, !OTR ? oo + 64 * nb : nb == 4 ? oo4 : ((nb & 1) ? oo1 : oo) + (nb >> 1) * 128, 0, OUTP); SCHED(); }
                else if constexpr (nb & 5) {
                    const f32x4 xa = xin[nb & 6], xb = xin[nb];
                    const auto r0 = __builtin_amdgcn_permlane16_swap(cvtpk(xa.x, xa.y), cvtpk(xb.x, xb.y), false, false);
                    const auto r1 = __builtin_amdgcn_permlane16_swap(cvtpk(xa.z, xa.w), cvtpk(xb.z, xb.w), false, false);
                    st128(v4u{r0[0], r1[0], r0[1], r1[1]}, rn, xs[nb >> 1]);
                    SCHED();
                }
            });
        }
        if constexpr (layer == 0) {
            {
                f32x4 s1, s2;
#pragma unroll
                for (int nb = 0; nb < 5; ++nb) { const f32x4 x = xin[nb]; if (nb == 0) { s1 = x; s2 = x * x; } else { s1 += x; s2 += x * x; } }
                __builtin_amdgcn_raw_buffer_store_b32(__float_as_uint(xsum2h(hsum4(s1), hsum4(s2))), rn, pso, 0, SC1);
            }
            SCHED();
            site<12>();
        int wa[2][3];
#pragma unroll
        for (int i = 0; i < 2; ++i) {
            wa[i][0] = (c >> 3) * 20480 + (c & 7) * 128 + (((4 * i + q) ^ (c & 6)) * 16); wa[i][1] = (wa[i][0] ^ 16) + 10240;
            wa[i][2] = 81920 + (c & 1) * 10240 + (c >> 1) * 128 + (((4 * i + q) ^ ((c & 1) | ((c >> 2) << 1))) * 16);
            pin(wa[i][0]); pin(wa[i][1]); pin(wa[i][2]);
        }
            unsigned fxo = N_FX + (unsigned)(w * 2048 + (lane >> 2) * 128);
            unsigned xo = N_X2 + (unsigned)(w * 5 * NB_ + lane * 16), xo1 = xo + 4096u;
            const int row = 8 * q + (c & 7);
            unsigned po = N_PS + (unsigned)(((2 * w + (row >> 4)) * 8 + 4 * (c >> 3)) * 128 + (row & 15) * 8);
            int rsw = l_rs(1, 0) + w * INBOX + lane * 4;
            pin(rsw); pin(fxo); pin(xo); pin(xo1); pin(po);
            v4u wf[2][5];
#pragma unroll
            for (int nb = 0; nb < 5; ++nb) wf[0][nb] = WF(nb, 0);
            SCHED();
            DRAIN();
            st64(tagv, rn, fxo1);
            if (b == 255 && w == 0) FLAG(OFF_NONCE)
            STAMP(11)
            if (w == 0) tour<4>();
            if constexpr (OTR) {
                const int sh = (nt2 & 1) * 64, r4 = (lane >> 3) + (lane & 4) * 2;
                const int a0 = c * 336 + q * 16, a1 = (lane >> 3) * 336 + sh + (lane & 7) * 16, a2_ = r4 * 336 + 256 - 4 * sh + (lane & 3) * 16;
                char* tw = smem + SC + w * INBOX + 12288;
#pragma unroll
                for (int nb = 0; nb < 5; ++nb) *(v2u*)(tw + a0 / 2 + nb * 32) = gt[nb];
#pragma unroll
                for (int nb = 0; nb < 5; ++nb) gt[nb] = *(const v2u*)(tw + (nb < 4 ? a1 / 2 + (nb & 1) * 8 * 168 + (nb >> 1) * 64 : a2_ / 2));
                otw = SC + w * INBOX + a0; otr0 = SC + w * INBOX + a1; otr4 = SC + w * INBOX + a2_;
                pin(otw); pin(otr0); pin(otr4);
            }
            addrs(std::integral_constant<int, 1>{}, lane, w, b);
            ACC_TIE5(gt);
            v2u pt[4];
            skip<SKIP5>();
            pipe_flag<KPF0>(pt, fxo, gn, tagv, dead);
            ORDER();
            STAMP(12)
            v4u ar[2 * RING1];
#define XF(n) ld128s(rn, (((n) & 1) ? xo1 : xo) + (((n) >> 1) & 3) * 1024, ((n) >> 3) * NB_)
#pragma unroll
            for (int n = 0; n < KI1; ++n) { ar[n] = XF(n); SCHED(); }
            v2u ps[4];
            float t1, t2, rinv, mri, rr_[8], rm_[8];
            static_for<20>(LAMBDA(s_) {
                constexpr int s = s_.value, cu = s & 1, nx = cu ^ 1;
                LGKM0() SCHED();
                static_for<10>(LAMBDA(g_) {
                    constexpr int g = g_.value, mb = g / 5, k = g % 5;
                    if constexpr (s == 0) mf0(acc[mb][k], ar[mb], wf[0][k]); else mf(acc[mb][k], ar[(2 * s + mb) % (2 * RING1)], wf[cu][k]);
                    SCHED();
                    if constexpr (s == 0 && g == 0) { pin(pt[0]); pin(pt[1]); pin(pt[2]); pin(pt[3]); }
                    if constexpr (s < 19 && g < 5) wf[nx][g] = WF(g, s + 1);
                    constexpr int rq = ring_req(10 * s + g, KI1, 2 * RING1, 4);
                    if constexpr (rq >= 100) ps[rq - 100] = ld64(rn, po + (rq - 100) * 128);
                    else if constexpr (rq >= 0) ar[rq % (2 * RING1)] = XF(rq);
                    constexpr int n = g >= 6 ? 4 * (s - KS1) + g - 6 : -1;
                    if constexpr (n == 0) { t1 = __uint_as_float(ps[0].x) + __uint_as_float(ps[1].x); pin(t1); t2 = __uint_as_float(ps[0].y) + __uint_as_float(ps[1].y); pin(t2); }
                    if constexpr (n == 1) { t1 += __uint_as_float(ps[2].x); pin(t1); t2 += __uint_as_float(ps[2].y); pin(t2); }
                    if constexpr (n == 2) { t1 += __uint_as_float(ps[3].x); pin(t1); t2 += __uint_as_float(ps[3].y); pin(t2); }
                    if constexpr (n == 3) { t1 += dpp<0x128>(t1); t2 += dpp<0x128>(t2); pin(t1); pin(t2); }
                    if constexpr (n == 4) { t1 *= (1.0f / D_); pin(t1); t2 *= (1.0f / D_); pin(t2); }
                    if constexpr (n == 5) { t2 = fmaxf(t2 - t1 * t1, 0.f); pin(t2); }
                    if constexpr (n == 6) { rinv = __builtin_amdgcn_rsqf(t2 + 1e-5f); pin(rinv); }
                    if constexpr (n == 8) { mri = -t1 * rinv; pin(mri); }
                    if constexpr (n == 9) { t1 = rinv; mrg8(t1, mri); }
                    if constexpr (n == 10) *(float*)(smem + rsw) = t1;
                    if constexpr (n >= 11 && n < 19) bc2b<n - 11>(rr_[n - 11], rm_[n - 11], rinv, mri);
                    if constexpr (n >= 19 && n < 39) TT_GAP(n - 19)
                    site<6000 + 10 * s + g>();
                    SCHED();
                });
            });
            rr[0] = f32x4{rr_[0], rr_[1], rr_[2], rr_[3]}; rr[1] = f32x4{rr_[4], rr_[5], rr_[6], rr_[7]};
            ACC_FENCE10(acc[0], acc[1]);
            STAMP(13)
        } else {
            STAMP(21)
            STAMP_DUMP()
            asm volatile("kpf_end:");
        }
    };
    layer_body(std::integral_constant<int, 0>{});
    layer_body(std::integral_constant<int, 1>{});
}

extern "C" void kernel_launch(void* const* d_in, const int* in_sizes, int n_in,
                              void* d_out, int out_size, void* d_ws, size_t ws_size,
                              hipStream_t stream) {
    Params p;
    p.x0 = (const u16*)d_in[0];
    p.Lre = (const u16*)d_in[1];
    p.Lim = (const u16*)d_in[2];
    p.Bre = (const u16*)d_in[3];
    p.Bim = (const u16*)d_in[4];
    p.Cre = (const u16*)d_in[5];
    p.Cim = (const u16*)d_in[6];
    p.logdt = (const u16*)d_in[7];
    p.Dskip = (const u16*)d_in[8];
    p.k = (const u16*)d_in[10];
    p.v = (const u16*)d_in[11];
    p.shk = (const u16*)d_in[12];
    p.ssmk = (const u16*)d_in[13];
    p.ssmD = (const u16*)d_in[14];
    p.ws = (char*)d_ws;
    p.out = (float*)d_out;
    k_main<<<256, 256, 0, stream>>>(p);
}
```

```cpp
#include <hip/hip_runtime.h>
#include <stdint.h>
#include <utility>

#define STAMPS 0

typedef unsigned short u16;
typedef unsigned long long u64;
typedef unsigned v4u __attribute__((ext_vector_type(4)));
typedef unsigned v2u __attribute__((ext_vector_type(2)));
typedef float f32x4 __attribute__((ext_vector_type(4)));
typedef float f32x2 __attribute__((ext_vector_type(2)));
typedef short s16x4 __attribute__((ext_vector_type(4)));
typedef __bf16 bf16x2v __attribute__((ext_vector_type(2)));
#define LAS __attribute__((address_space(3)))
#define DI __device__ __forceinline__

constexpr int S_ = 2048, D_ = 640;
#define KR1 4
#define KR2 8
constexpr int RING1 = KR1;
#define KCR1 4
constexpr int kCR1 = KCR1;
#define KXA 0
#define KN3E 16
#define KN3 12
constexpr int kXA = KXA;
constexpr int kN3E = KN3E, kN3 = KN3;
#define KFHK 9
#define KFHP 7
constexpr int kFHK = KFHK, kFHP = KFHP;
#define KGPI 31
#define KGCI 36
#define CPF 0
#define LBE 0
#define LBR 1
#define W3O 1
#define KI2 6
#define KI1 6
#define KS1 8
#define RSL 2
#define KR20 10
constexpr int kGPI = KGPI, kGCI = KGCI;

#define KPAD 0
#define KPS 0
#define GPAD 0
constexpr int KSTR = 4096 + KPAD, GSTR = 4096 + GPAD;
constexpr unsigned OFF_NONCE = 0;
constexpr unsigned OFF_ST = 4096;
constexpr unsigned OFF_FW = OFF_ST + 65536;
constexpr unsigned OFF_FK = OFF_FW + 131072;
constexpr unsigned OFF_VT = OFF_FK + 65536;
constexpr unsigned OFF_FG = OFF_FK + 81920;
constexpr unsigned OFF_EG = OFF_FG + 98304;
constexpr unsigned OFF_KVT = OFF_EG + 327680;
constexpr unsigned OFF_GT = OFF_KVT + 640 * 4608;
constexpr unsigned OFF_W2F = OFF_GT + 640 * 4608;
constexpr unsigned WS_BYTES = OFF_W2F + 1638400;
constexpr unsigned OFF_GRP = 0x2000000, NB_ = 16384;
constexpr unsigned N_HF = 0;
constexpr unsigned N_X2 = 80 * NB_;
constexpr unsigned N_SG = 100 * NB_;
constexpr unsigned N_PS = 104 * NB_;
constexpr unsigned N_FH = 105 * NB_;
constexpr unsigned N_FX = 107 * NB_;
constexpr unsigned N_END = 108 * NB_;
constexpr unsigned kNone = 0x40000000u;
constexpr unsigned OFF_DBG = 0x1800000;
#define TILES() const int xq = b & 7, j = b >> 3, mt1 = 2 * xq + (j >> 4), nt1 = j & 15, mt2 = 4 * xq + (j >> 3), nt2 = j & 7, i32 = 16 * (mt2 & 1) + mt1;

constexpr int SC = 102400;
constexpr int REGSZ = 13056;
constexpr int L_XR = SC;
constexpr int L_CO = SC + 12288;
constexpr int L_KB = SC + 51712;
constexpr int L_TAB = SC + 55808;
constexpr int L_CW = SC + 39168;
constexpr int INBOX = 15360;
constexpr int l_rs(int l, int w) { return l ? SC + w * INBOX : L_KB + w * 256; }
constexpr int l_sg(int l, int w) { return l ? SC + w * INBOX + 256 : SC + (w == 0 ? 43264 : w == 1 ? 46080 : w == 2 ? 52736 : 56320); }
constexpr int l_tb(int l, int w) { return l ? SC + w * INBOX + 2048 : SC + 40960 + w * 5120; }
constexpr int LDS_BYTES = 163840;
constexpr int CV_E = 4608;
constexpr int CV_O = CV_E + (1056 + 15) * 4;

struct Params {
    const u16 *x0, *Lre, *Lim, *Bre, *Bim, *Cre, *Cim, *logdt, *Dskip, *k, *v, *shk, *ssmk, *ssmD;
    char* ws;
    float* out;
};

#define MF "v_mfma_f32_16x16x32_bf16 "
DI void mf(f32x4& c, v4u a, v4u b) { asm(MF "%0, %1, %2, %0" : "+v"(c) : "v"(a), "v"(b)); }
DI void mf0(f32x4& c, v4u a, v4u b) { asm(MF "%0, %1, %2, 0" : "=&v"(c) : "v"(a), "v"(b)); }
constexpr int W2R = 56;
#define WA(n) "a[%c" #n ":%c" #n "+3]"
template <int R> DI void mfa(f32x4& c, v4u a) { asm(MF "%0, " WA(2) ", %1, %0" : "+v"(c) : "v"(a), "i"(W2R + 4 * R)); }
template <int R> DI void mfa0(f32x4& c, v4u a) { asm(MF "%0, " WA(2) ", %1, 0" : "=&v"(c) : "v"(a), "i"(W2R + 4 * R)); }
#define ACC_FENCE10(c, d) asm volatile("" : "+v"(c[0]), "+v"(c[1]), "+v"(c[2]), "+v"(c[3]), "+v"(c[4]), "+v"(d[0]), "+v"(d[1]), "+v"(d[2]), "+v"(d[3]), "+v"(d[4]))
#define ACC_TIE5(c) asm volatile("" : "+v"(c[0]), "+v"(c[1]), "+v"(c[2]), "+v"(c[3]), "+v"(c[4]))
#define QP " quad_perm:[1,0,3,2] row_mask:0xf bank_mask:0xf"
struct St { f32x2 a, b; float c; };
struct Lm { f32x2 a, b; float r, s; };
#define PK1 " op_sel_hi:[0,1,1]\n\t"
#define PK2 " op_sel:[1,1,0] op_sel_hi:[1,0,1] neg_lo:[1,0,0]"
#define ST_(t0, t1, t2, h0, h1, h2, b0, b1, b2, l0, l1, l2, l3)                                                                                        \
    "v_pk_fma_f32 %" #t0 ", %" #l0 ", %" #h0 ", %" #b0 PK1 "v_pk_fma_f32 %" #t1 ", %" #l1 ", %" #h1 ", %" #b1 PK1 "v_fma_f32 %" #t2 ", %" #l2 ", %" #h2 ", %" #b2 "\n\t" \
    "v_pk_fma_f32 %" #t0 ", %" #l0 ", %" #h0 ", %" #t0 PK2 "\n\tv_pk_fma_f32 %" #t1 ", %" #l1 ", %" #h1 ", %" #t1 PK2 "\n\tv_fmac_f32_dpp %" #t2 ", %" #h2 ", %" #l3 QP "\n\t"
#define SO(t) "=&v"(t.a), "=&v"(t.b), "=&v"(t.c)
#define SI(t) "v"(t.a), "v"(t.b), "v"(t.c)
#define LI(l) "v"(l.a), "v"(l.b), "v"(l.r), "v"(l.s)
DI void run3(St& t, const St& h, const St* b, const Lm& l) {
    St u;
    asm(ST_(0, 1, 2, 6, 7, 8, 9, 10, 11, 18, 19, 20, 21) ST_(3, 4, 5, 0, 1, 2, 12, 13, 14, 18, 19, 20, 21) ST_(0, 1, 2, 3, 4, 5, 15, 16, 17, 18, 19, 20, 21)
        : SO(t), SO(u) : SI(h), SI(b[0]), SI(b[1]), SI(b[2]), LI(l));
}
DI void run4(St& t, const St& h, const St* b, const Lm& l) {
    St u;
    asm(ST_(3, 4, 5, 6, 7, 8, 9, 10, 11, 21, 22, 23, 24) ST_(0, 1, 2, 3, 4, 5, 12, 13, 14, 21, 22, 23, 24) ST_(3, 4, 5, 0, 1, 2, 15, 16, 17, 21, 22, 23, 24) ST_(0, 1, 2, 3, 4, 5, 18, 19, 20, 21, 22, 23, 24)
        : SO(t), SO(u) : SI(h), SI(b[0]), SI(b[1]), SI(b[2]), SI(b[3]), LI(l));
}
#define AC_(t0, t2, h0, h2, b0, b2, l0, l2, l3)                                                                                   \
    "v_pk_fma_f32 %" #t0 ", %" #l0 ", %" #h0 ", %" #b0 PK1 "v_fma_f32 %" #t2 ", %" #l2 ", %" #h2 ", %" #b2 "\n\t"               \
    "v_pk_fma_f32 %" #t0 ", %" #l0 ", %" #h0 ", %" #t0 PK2 "\n\tv_fmac_f32_dpp %" #t2 ", %" #h2 ", %" #l3 QP "\n\t"
#define AO(t) "=&v"(t.a), "=&v"(t.c)
#define AI(t) "v"(t.a), "v"(t.c)
DI void keep14(St* t, const St& h, const St& x, const St* b, const Lm& lq, const Lm& l) {
    St u;
    asm(AC_(8, 9, 10, 11, 12, 13, 22, 23, 24) AC_(0, 1, 8, 9, 14, 15, 25, 26, 27) AC_(2, 3, 0, 1, 16, 17, 25, 26, 27) AC_(4, 5, 2, 3, 18, 19, 25, 26, 27) AC_(6, 7, 4, 5, 20, 21, 25, 26, 27)
        : AO(t[0]), AO(t[1]), AO(t[2]), AO(t[3]), AO(u) : AI(h), AI(x), AI(b[0]), AI(b[1]), AI(b[2]), AI(b[3]), "v"(lq.a), "v"(lq.r), "v"(lq.s), "v"(l.a), "v"(l.r), "v"(l.s));
}
DI void keep4(St* t, const St& h, const St* b, const Lm& l) {
    asm(AC_(0, 1, 8, 9, 10, 11, 18, 19, 20) AC_(2, 3, 0, 1, 12, 13, 18, 19, 20) AC_(4, 5, 2, 3, 14, 15, 18, 19, 20) AC_(6, 7, 4, 5, 16, 17, 18, 19, 20)
        : AO(t[0]), AO(t[1]), AO(t[2]), AO(t[3]) : AI(h), AI(b[0]), AI(b[1]), AI(b[2]), AI(b[3]), "v"(l.a), "v"(l.r), "v"(l.s));
}
DI void b3cv(f32x2* t, unsigned* o, f32x2 h, f32x2 b0, f32x2 b1, f32x2 b2, f32x2 l0, f32x2 l, const float* e) {
    asm("v_pk_fma_f32 %0, %11, %7, %8" PK1 "v_cvt_pk_bf16_f32 %3, %13, %14\n\tv_pk_fma_f32 %0, %11, %7, %0" PK2 "\n\tv_cvt_pk_bf16_f32 %4, %15, %16\n\t"
        "v_pk_fma_f32 %1, %12, %0, %9" PK1 "v_cvt_pk_bf16_f32 %5, %17, %18\n\tv_pk_fma_f32 %1, %12, %0, %1" PK2 "\n\tv_cvt_pk_bf16_f32 %6, %19, %20\n\t"
        "v_pk_fma_f32 %2, %12, %1, %10" PK1
        : "=&v"(t[0]), "=&v"(t[1]), "=&v"(t[2]), "=&v"(o[0]), "=&v"(o[1]), "=&v"(o[2]), "=&v"(o[3])
        : "v"(h), "v"(b0), "v"(b1), "v"(b2), "v"(l0), "v"(l), "v"(e[0]), "v"(e[1]), "v"(e[2]), "v"(e[3]), "v"(e[4]), "v"(e[5]), "v"(e[6]), "v"(e[7]));
}
DI void bfin(f32x2& t, f32x2 h, f32x2 l) { asm("v_pk_fma_f32 %0, %2, %1, %0" PK2 : "+v"(t) : "v"(h), "v"(l)); }
#define UNDEF8(c) asm volatile("" : "=v"(c[0]), "=v"(c[1]), "=v"(c[2]), "=v"(c[3]), "=v"(c[4]), "=v"(c[5]), "=v"(c[6]), "=v"(c[7]))
DI unsigned cvtpk(float lo, float hi) {
    f32x2 v = {lo, hi};
    return __builtin_bit_cast(unsigned, __builtin_convertvector(v, bf16x2v));
}
DI float lo16f(unsigned u) { return __uint_as_float(u << 16); }
DI float hi16f(unsigned u) { return __uint_as_float(u & 0xffff0000u); }
DI float rbf(float v) { return lo16f(cvtpk(v, 0.f)); }
template <int CTRL> DI float dpp(float x) {
    return __int_as_float(__builtin_amdgcn_update_dpp(0, __float_as_int(x), CTRL, 0xF, 0xF, true));
}
DI float rowsum16(float x) {
    x += dpp<0x128>(x); x += dpp<0x124>(x); x += dpp<0x122>(x); x += dpp<0x121>(x);
    return x;
}
DI float xsum32(float x) { auto r = __builtin_amdgcn_permlane32_swap(__float_as_uint(x), __float_as_uint(x), false, false); return __uint_as_float(r[0]) + __uint_as_float(r[1]); }
DI void xsum2(float& a, float& d) {
    auto r = __builtin_amdgcn_permlane16_swap(__float_as_uint(a), __float_as_uint(d), false, false);
    const float s = __uint_as_float(r[0]) + __uint_as_float(r[1]);
    const float t = xsum32(s);
    auto u = __builtin_amdgcn_permlane16_swap(__float_as_uint(t), __float_as_uint(t), false, false);
    a = __uint_as_float(u[0]); d = __uint_as_float(u[1]);
}
DI float xsum2h(float a, float d) {
    auto r = __builtin_amdgcn_permlane16_swap(__float_as_uint(a), __float_as_uint(d), false, false);
    return xsum32(__uint_as_float(r[0]) + __uint_as_float(r[1]));
}
DI float hsum4(f32x4 v) { return (v.x + v.y) + (v.z + v.w); }
typedef __amdgpu_buffer_rsrc_t rsrc_t;
constexpr int SC1 = 16;
DI v4u ld128(rsrc_t rs, unsigned off) { return __builtin_amdgcn_raw_buffer_load_b128(rs, off, 0, SC1); }
DI v4u ld128s(rsrc_t rs, unsigned off, unsigned soff) { return __builtin_amdgcn_raw_buffer_load_b128(rs, off, soff, SC1); }
DI v2u ld64(rsrc_t rs, unsigned off) { return __builtin_amdgcn_raw_buffer_load_b64(rs, off, 0, SC1); }
DI void st128(v4u v, rsrc_t rs, unsigned off) { __builtin_amdgcn_raw_buffer_store_b128(v, rs, off, 0, SC1); }
DI void st64(v2u v, rsrc_t rs, unsigned off) { __builtin_amdgcn_raw_buffer_store_b64(v, rs, off, 0, SC1); }
#define LGKM0() { __builtin_amdgcn_s_waitcnt(0xC07F); asm volatile("" ::: "memory"); }
#define DRAIN() asm volatile("s_waitcnt vmcnt(0)" ::: "memory")
#define ORDER() __builtin_amdgcn_fence(__ATOMIC_ACQUIRE, "wavefront")
#define SCHED() __builtin_amdgcn_sched_barrier(0)
constexpr unsigned kSpinCap = 1u << 17;
#define WAVE_WAIT(...)                                                      \
    {                                                                       \
        unsigned spins_ = dead ? kSpinCap : 0u;                             \
        _Pragma("nounroll") for (;;) {                                      \
            asm volatile("" ::: "memory");                                  \
            bool ok;                                                        \
            __VA_ARGS__;                                                    \
            if (__builtin_amdgcn_ballot_w64(!ok) == 0ull) break;            \
            if (++spins_ > kSpinCap) { dead = true; break; }                \
            __builtin_amdgcn_s_sleep(1);                                    \
        }                                                                   \
        ORDER();                                                            \
    }
#define KPD1 1
#define KPDS 5
#define KPDS0 5
#define KPF0 1
#define KPH0 12
#define KPF1 4
constexpr int PD1 = KPD1, PDS = KPDS, PDS0 = KPDS0;
#define PL(r) "global_load_dwordx2 " r ", %5, %6 sc1\n\t"
#define PS_(r) "s_waitcnt vmcnt(1)\n\tv_cmp_ne_u64 vcc, %7, " r "\n\ts_cbranch_vccz 2f\n\t" PL(r)
template <int D0> DI void pipe_flag(v2u (&t)[4], unsigned off, const void* base, v2u tag, bool& dead) {
    unsigned n = dead ? 0u : kSpinCap;
    asm volatile("s_sleep %8\n\t" PL("%0") "s_sleep %9\n\t" PL("%1")
                 "1:\n\t" PS_("%0") PS_("%1") "s_sub_u32 %4, %4, 1\n\ts_cbranch_scc0 1b\n2:"
                 : "=&v"(t[0]), "=&v"(t[1]), "=&v"(t[2]), "=&v"(t[3]), "+s"(n) : "v"(off), "s"(base), "v"(tag), "i"(D0), "i"(KPF1) : "memory", "vcc", "scc");
    dead = __builtin_amdgcn_readfirstlane(n) == ~0u;
}
#define WAVE_RETRY(OK, ...) { if (__builtin_expect(__builtin_amdgcn_ballot_w64(!(OK)) != 0ull, 0)) WAVE_WAIT({ __VA_ARGS__; ok = OK; }) ORDER(); }
#define STAMP(i)
#define STAMP_DUMP()

#define PADS 9000, 1000, 1059, 1088, 2085, 9001, 9002, 4071, 9, 9003, 6, 9004, 9005, 6106, 14, 15, 9006, 18, 19, 9007, 21, 2, 9008
#define TW 2
#define TW0 2
#define KLB0 0
#define KLB1 4
#define SKIP0 0
#define SKIP5 0
constexpr int kPads[] = {PADS};
constexpr int pad_next(int id) {
    for (unsigned i = 0; i + 1 < sizeof(kPads) / sizeof(int); ++i) if (kPads[i] == id) return kPads[i + 1];
    return -1;
}
constexpr bool pads_unique() {
    for (unsigned i = 0; i < sizeof(kPads) / sizeof(int); ++i)
        for (unsigned k = 0; k < i; ++k) if (kPads[i] == kPads[k]) return false;
    return true;
}
static_assert(pads_unique(), "");
template <int ID> DI void site() {
    if constexpr (pad_next(ID) >= 0) asm volatile("kp%c0:\n\ts_cbranch_execz kp%c1" ::"i"(ID), "i"(pad_next(ID)));
    else asm volatile("");
}
template <int T> DI void tour() {
    if constexpr (pad_next(9000 + T) >= 0 && pad_next(9000 + T) != 9001 + T) asm volatile("s_mov_b64 exec, 0\n\ts_branch kp%c0\nkp%c1:\n\ts_mov_b64 exec, -1" ::"i"(pad_next(9000 + T)), "i"(9001 + T));
}
template <int N> DI void skip() { if constexpr (N > 0) asm volatile("s_branch 1f\n\t.rept %c0\n\ts_nop 0\n\t.endr\n1:" ::"i"(N)); }
template <int N> DI void room() { asm volatile("s_branch 1f\n\t.p2align 12, , %c0\n1:" ::"i"(N)); }
template <class F, int... I> DI void static_for_impl(std::integer_sequence<int, I...>, F&& f) { (f(std::integral_constant<int, I>{}), ...); }
template <int N, class F> DI void static_for(F&& f) { static_for_impl(std::make_integer_sequence<int, N>{}, f); }
#define LAMBDA(v) [&](auto v) __attribute__((always_inline))
DI f32x2 cmul(f32x2 a, f32x2 b) {
    f32x2 t;
    asm("v_pk_mul_f32 %0, %1, %2 op_sel_hi:[0,1]\n\ts_nop 0\n\tv_pk_fma_f32 %0, %1, %2, %0" PK2 "\n\ts_nop 0" : "=&v"(t) : "v"(a), "v"(b));
    return t;
}
DI f32x2 cfma(f32x2 a, f32x2 b, f32x2 c) {
    f32x2 t;
    asm("v_pk_fma_f32 %0, %1, %2, %3 op_sel_hi:[0,1,1]\n\ts_nop 0\n\tv_pk_fma_f32 %0, %1, %2, %0" PK2 "\n\ts_nop 0" : "=&v"(t) : "v"(a), "v"(b), "v"(c));
    return t;
}
DI void cfma4(f32x2 (&t)[4], f32x2 a, f32x2 b0, f32x2 b1, f32x2 b2, f32x2 b3, f32x2 c0, f32x2 c1, f32x2 c2, f32x2 c3) {
    asm("v_pk_fma_f32 %0, %4, %5, %9 op_sel_hi:[0,1,1]\n\tv_pk_fma_f32 %1, %4, %6, %10 op_sel_hi:[0,1,1]\n\tv_pk_fma_f32 %2, %4, %7, %11 op_sel_hi:[0,1,1]\n\tv_pk_fma_f32 %3, %4, %8, %12 op_sel_hi:[0,1,1]\n\t"
        "v_pk_fma_f32 %0, %4, %5, %0" PK2 "\n\tv_pk_fma_f32 %1, %4, %6, %1" PK2 "\n\tv_pk_fma_f32 %2, %4, %7, %2" PK2 "\n\tv_pk_fma_f32 %3, %4, %8, %3" PK2 "\n\ts_nop 0"
        : "=&v"(t[0]), "=&v"(t[1]), "=&v"(t[2]), "=&v"(t[3]) : "v"(a), "v"(b0), "v"(b1), "v"(b2), "v"(b3), "v"(c0), "v"(c1), "v"(c2), "v"(c3));
}
#define C4(t0, t1, t2, t3, a, b0, b1, b2, b3, c0, c1, c2, c3)                                                                                                         \
    "v_pk_fma_f32 %" #t0 ", %" #a ", %" #b0 ", %" #c0 PK1 "v_pk_fma_f32 %" #t1 ", %" #a ", %" #b1 ", %" #c1 PK1 "v_pk_fma_f32 %" #t2 ", %" #a ", %" #b2 ", %" #c2 PK1 "v_pk_fma_f32 %" #t3 ", %" #a ", %" #b3 ", %" #c3 PK1 \
    "v_pk_fma_f32 %" #t0 ", %" #a ", %" #b0 ", %" #t0 PK2 "\n\tv_pk_fma_f32 %" #t1 ", %" #a ", %" #b1 ", %" #t1 PK2 "\n\tv_pk_fma_f32 %" #t2 ", %" #a ", %" #b2 ", %" #t2 PK2 "\n\tv_pk_fma_f32 %" #t3 ", %" #a ", %" #b3 ", %" #t3 PK2 "\n\t"
DI void cfma4n(f32x2 (&t)[4], f32x2 a, f32x2 b0, f32x2 b1, f32x2 b2, f32x2 b3, f32x2 c0, f32x2 c1, f32x2 c2, f32x2 c3) {
    asm(C4(0, 1, 2, 3, 4, 5, 6, 7, 8, 9, 10, 11, 12) : "=&v"(t[0]), "=&v"(t[1]), "=&v"(t[2]), "=&v"(t[3]) : "v"(a), "v"(b0), "v"(b1), "v"(b2), "v"(b3), "v"(c0), "v"(c1), "v"(c2), "v"(c3));
}
DI void agg(unsigned& er, unsigned& ei, f32x2 (&x)[4], f32x2& a, const f32x2 (&u)[4], const f32x2 (&s)[16], f32x2 l8, f32x2 l32, f32x2 l64) {
    f32x2 v[4], b;
    asm(C4(4, 5, 6, 7, 22, 10, 11, 12, 13, 14, 15, 16, 17) C4(0, 1, 2, 3, 22, 4, 5, 6, 7, 18, 19, 20, 21)
        "v_pk_fma_f32 %8, %23, %0, %1" PK1 "v_pk_fma_f32 %9, %23, %2, %3" PK1 "v_pk_fma_f32 %8, %23, %0, %8" PK2 "\n\tv_pk_fma_f32 %9, %23, %2, %9" PK2
        : "=&v"(x[0]), "=&v"(x[1]), "=&v"(x[2]), "=&v"(x[3]), "=&v"(v[0]), "=&v"(v[1]), "=&v"(v[2]), "=&v"(v[3]), "=&v"(a), "=&v"(b)
        : "v"(u[0]), "v"(u[1]), "v"(u[2]), "v"(u[3]), "v"(s[2]), "v"(s[6]), "v"(s[10]), "v"(s[14]), "v"(s[3]), "v"(s[7]), "v"(s[11]), "v"(s[15]), "v"(l8), "v"(l32));
    asm("s_nop 0\n\tv_fma_f32 %0, %2, %6, %4\n\tv_fma_f32 %1, %2, %7, %5\n\tv_fma_f32 %0, -%3, %7, %0\n\tv_fma_f32 %1, %3, %6, %1"
        : "=&v"(er), "=&v"(ei) : "v"(a.x), "v"(a.y), "v"(b.x), "v"(b.y), "v"(l64.x), "v"(l64.y));
}
DI void gacc2(f32x2& h0, f32x2& h1, f32x2 w0, f32x2 w1, v4u e0, v4u e1) {
    const v2u a0 = {e0.x, e0.y}, b0 = {e0.z, e0.w}, a1 = {e1.x, e1.y}, b1 = {e1.z, e1.w};
    asm("v_pk_fma_f32 %0, %4, %2, %0 op_sel_hi:[0,1,1]\n\tv_pk_fma_f32 %1, %6, %3, %1 op_sel_hi:[0,1,1]\n\t"
        "v_pk_fma_f32 %0, %5, %2, %0 op_sel:[0,1,0] op_sel_hi:[0,0,1] neg_lo:[1,0,0]\n\tv_pk_fma_f32 %1, %7, %3, %1 op_sel:[0,1,0] op_sel_hi:[0,0,1] neg_lo:[1,0,0]"
        : "+v"(h0), "+v"(h1) : "v"(w0), "v"(w1), "v"(a0), "v"(b0), "v"(a1), "v"(b1));
}
DI s16x4 trread(char* smem, int byteoff) {
    return __builtin_amdgcn_ds_read_tr16_b64_v4i16((LAS s16x4*)(smem + byteoff));
}

#define CV_LOAD_A(A, a, rr_, hf) { const unsigned* ap_ = (const unsigned*)(smem + ab[(a) >> 1] + (((a) & 1) ? 0 : 512) + 64 * (7 - (rr_)) + 8 * (hf)); A[a][2 * (hf)] = ap_[0]; A[a][2 * (hf) + 1] = ap_[1]; }
#define CV_LOAD_B(B, e, rr_) { B[e] = *(const v4u*)(smem + bb + 64 * (7 - (rr_)) + 512 * (e)); }
template <int R, bool PF, class F, class G>
DI void conv_round(f32x4 (&ca)[8], v4u (&A)[8], const v4u (&B)[8], v4u (&An)[8], v4u (&Bn)[8], const char* smem, const int (&ab)[4], int bb, F&& fill, G&& last) {
    static_for<36>(LAMBDA(g_) {
        constexpr int g = g_.value;
        constexpr int a = g < 8 ? 0 : g < 15 ? 1 : g < 21 ? 2 : g < 26 ? 3 : g < 30 ? 4 : g < 33 ? 5 : g < 35 ? 6 : 7;
        constexpr int e = g - (a * 8 - a * (a - 1) / 2);
        if constexpr (R == 0 && a == 0) mf0(ca[e], A[0], B[e]); else mf(ca[a + e], A[a], B[e]);
        SCHED();
        if constexpr (R == 0 && g < 16 && (g & 3) != 3) { constexpr int n = g - (g >> 2); CV_LOAD_A(A, 2 + (n >> 1), 0, n & 1) SCHED(); }
        if constexpr (PF && g < 32 && (g & 3) != 3) { if constexpr ((g & 3) == 2) CV_LOAD_B(Bn, g >> 2, R + 1) else CV_LOAD_A(An, g >> 2, R + 1, g & 1) SCHED(); }
        site<3000 + 36 * R + g>();
        if constexpr (R == 7) { last(g_); SCHED(); }
        if constexpr ((g & 3) == 3 && g < 28) { fill(std::integral_constant<int, 7 * R + g / 4>{}); SCHED(); }
    });
}
template <int R0, int R1, bool PFL, class F, class G>
DI void conv_part(f32x4 (&ca)[8], v4u (&A0)[8], v4u (&B0)[8], const char* smem, const int (&ab)[4], int bb, F&& fill, G&& last) {
    v4u A1[8], B1[8];
    static_for<R1 - R0>(LAMBDA(r_) {
        constexpr int r = r_.value;
        if constexpr (r & 1) conv_round<R0 + r, (R0 + r + 1 < R1 || PFL)>(ca, A1, B1, A0, B0, smem, ab, bb, fill, last);
        else conv_round<R0 + r, (R0 + r + 1 < R1 || PFL)>(ca, A0, B0, A1, B1, smem, ab, bb, fill, last);
    });
}
template <int IMM> DI void gld(v4u& dst, unsigned voff, const void* sbase) {
    asm volatile("global_load_dwordx4 %0, %1, %2 offset:%3" : "=v"(dst) : "v"(voff), "s"(sbase), "i"(IMM));
}
#define OUTP 16
DI void gldu16(unsigned& dst, unsigned voff, const void* sbase) { asm volatile("global_load_ushort %0, %1, %2" : "=v"(dst) : "v"(voff), "s"(sbase)); }
DI void gld1s(unsigned& dst, unsigned voff, const void* sbase) { asm volatile("global_load_dword %0, %1, %2 sc1" : "=v"(dst) : "v"(voff), "s"(sbase)); }
template <int V> DI void SETM0(unsigned ldsbase) { asm volatile("s_add_u32 m0, %0, %1" ::"s"(ldsbase), "i"(V)); }
template <int GOFF> DI void GLD0(unsigned voff, const char* sbase) { asm volatile("global_load_lds_dwordx4 %0, %1 offset:%2" ::"v"(voff), "s"(sbase), "i"(GOFF) : "memory"); }
template <int GOFF, int V> DI void GLD1(unsigned voff, const char* sbase, unsigned ldsbase) {
    asm volatile("global_load_lds_dwordx4 %0, %1 offset:%3\n\ts_add_u32 m0, %2, %4" ::"v"(voff), "s"(sbase), "s"(ldsbase), "i"(GOFF), "i"(V) : "memory");
}
template <int L, int G> DI void dma_x(unsigned voff, const void* sbase, unsigned ldsbase) {
    asm volatile("s_add_u32 m0, %2, %4\n\ts_nop 0\n\tglobal_load_lds_dwordx4 %0, %1 offset:%3" ::"v"(voff), "s"(sbase), "s"(ldsbase), "i"(G), "i"(L - G) : "memory", "scc");
}
DI void ld_kv(v4u (&a)[15], int on  , unsigned k0, unsigned k1, unsigned k4, unsigned vo, v4u rk, const void* v, unsigned lds) {
    unsigned t;
    asm volatile("s_cmp_ge_u32 %5, 80\n\ts_cbranch_scc1 1f\n\ts_add_u32 m0, %12, %13\n\ts_movk_i32 %4, 0x2800\n\t"
                 "buffer_load_dwordx4 %6, %10, 0 offen nt lds\n\ts_add_u32 m0, m0, 0x400\n\tglobal_load_dwordx4 %0, %9, %11 nt\n\t"
                 "buffer_load_dwordx4 %7, %10, 0 offen nt lds\n\ts_add_u32 m0, m0, 0x400\n\tglobal_load_dwordx4 %1, %9, %11 offset:1280 nt\n\t"
                 "buffer_load_dwordx4 %7, %10, %4 offen nt lds\n\ts_add_u32 m0, m0, 0x400\n\ts_movk_i32 %4, 0x5000\n\tglobal_load_dwordx4 %2, %9, %11 offset:2560 nt\n\t"
                 "buffer_load_dwordx4 %7, %10, %4 offen nt lds\n\ts_add_u32 m0, m0, 0x400\n\tglobal_load_dwordx4 %3, %9, %11 offset:3840 nt\n\t"
                 "buffer_load_dwordx4 %8, %10, 0 offen nt lds\n1:"
                 : "=&v"(a[7]), "=&v"(a[8]), "=&v"(a[9]), "=&v"(a[10]), "=&s"(t)
                 : "s"(on), "v"(k0), "v"(k1), "v"(k4), "v"(vo), "s"(rk), "s"(v), "s"(lds), "i"(L_XR) : "scc", "memory");
}
DI int race(const char* a, const char* b) {
    unsigned u, v;
    int r;
    asm volatile("s_waitcnt lgkmcnt(0)\n\ts_load_dword %1, %3, 0x0\n\ts_load_dword %2, %4, 0x0\n\ts_waitcnt lgkmcnt(1)\n\ts_sleep %5\n\t"
                 "s_getreg_b32 %0, hwreg(HW_REG_IB_STS, 8, 4)\n\ts_waitcnt lgkmcnt(0)"
                 : "=&s"(r), "=&s"(u), "=&s"(v) : "s"(a), "s"(b), "i"(RSL) : "memory");
    return r;
}
DI void ld_wr(v4u (&a)[4], int on, unsigned o, const void* base) {
    asm volatile("s_cmp_ge_u32 %4, 80\n\ts_cbranch_scc0 1f\n\t.rept 4\n\tglobal_load_dword a[32+1], %5, %6\n\t.endr\n\ts_branch 2f\n1:\n\t"
                 "global_load_dwordx4 %0, %5, %6 nt\n\tglobal_load_dwordx4 %1, %5, %6 offset:1024 nt\n\tglobal_load_dwordx4 %2, %5, %6 offset:2048 nt\n\tglobal_load_dwordx4 %3, %5, %6 offset:3072 nt\n2:"
                 : "=&v"(a[0]), "=&v"(a[1]), "=&v"(a[2]), "=&v"(a[3]) : "s"(on), "v"(o), "s"(base) : "scc");
}
template <int L, int G> DI void dma_1(unsigned voff, const void* sbase, unsigned ldsbase) {
    asm volatile("s_add_u32 m0, %2, %4\n\ts_nop 0\n\tglobal_load_lds_dword %0, %1 offset:%3" ::"v"(voff), "s"(sbase), "s"(ldsbase), "i"(G), "i"(L - G) : "memory", "scc");
}
template <int L> DI void dma_n(unsigned voff, const void* sbase, unsigned ldsbase) {
    asm volatile("s_add_u32 m0, %2, %3\n\ts_nop 0\n\tglobal_load_lds_dwordx4 %0, %1 nt" ::"v"(voff), "s"(sbase), "s"(ldsbase), "i"(L) : "memory", "scc");
}
template <int A, int O> DI void lds2a(unsigned a) { asm volatile("ds_read_b64 a[%c1:%c1+1], %0 offset:%2" ::"v"(a), "i"(A), "i"(O) : "memory"); }
constexpr int L_EAR = L_XR + 8192;
constexpr int dma_goff(int it) { return it < 40 ? (it >> 2) * 128 : ((it - 100) / 5) * 256; }
constexpr int dma_loff(int it) { return it < 40 ? L_XR + ((it >> 2) % 3) * 4096 + (it & 3) * 1024 : ((it - 100) % 5) * 20480 + ((it - 100) / 5) * 2048; }
#define KFS 4
#define APOS 0
#define CWP 2
#define SAF 2
#define KD0 30
#define KST 59
#define KSL 5
#define KSC 11
#define XG0 5
#define XGS 1
#define WOD 1
#define USP 1
#define KSG 0
#define SGP0 0x122021
#define SGP1 0x202121
#define SBL 1
#define SMP 1
#define KW2E 0
#define KW2T 2
#define OTR 1
#define KQ0 0
#define KQ1 18
#define KQ2 4
#define KQ3 0
#define NPD 27
constexpr int kQ0 = KQ0, kQ1 = KQ1, kQ2 = KQ2, kQ3 = KQ3;
#define KW2S 16
#define KW2M 0x42
#define KFKS 13
#define KFKG 6
#define KUS 17
#define KTS 19
#define KFWP 13
constexpr int kW2S = KW2S, kW2M = KW2M;
#define KSA 4
constexpr int NW2L = (20 - kW2S) * __builtin_popcount(kW2M), NW2P = NW2L + KSA;
constexpr int P1_PRE = NPD + 7;
constexpr int p1_dma(int i) { return i < 5 ? 100 + i : i < 13 ? i - 5 : i < 18 ? 105 + (i - 13) : i < 22 ? 8 + (i - 18) : i < NPD ? 110 + (i - 22) : 400; }
constexpr int kQ1a = KST > KD0 ? kQ1 * (KST - KD0) / (117 - KD0) : 0;
constexpr int p1_pre(int i) { return i < kQ0 + kQ1a ? p1_dma(i) : i == kQ0 + kQ1a ? 400 : p1_dma(i - 1); }
constexpr int p1_item(int s, int g) {
    if (s == KFS && g == (XG0 == 5 ? 4 : 9)) return 324;
    if (WOD ? (s & 1) && s < 10 && (g == 3 || g == 8) : g == 9 && s < 10) return WOD ? 114 + s + (g == 8) : 115 + s;
    if (s == KSL && (g == KSG || g == KSG + 1)) return 202 + g - KSG;
    if (s == KSL && g == 6) return 204;
    if (s == KFWP && g == 1) return 208;
    if (s == KFKS && g == KFKG) return 206;
    if (USP && s == KUS && (g == 0 || g == 2 || g == 4 || g == 7)) return 210 + g / 2;
    if (s == KTS && g == 9) return 209;
    if (s >= kW2S && ((kW2M >> g) & 1)) return 500 + (s - kW2S) * __builtin_popcount(kW2M) + __builtin_popcount(kW2M & ((1 << g) - 1));
    if ((s & 1) == 0 && s <= 12 && g >= XG0 && (g - XG0) % XGS == 0 && (g - XG0) / XGS < 4) return 4 * (s / 2 + 3) + (g - XG0) / XGS;
    return -1;
}
constexpr int p1_cnt(int s, int g) {
    int n = P1_PRE;
    for (int t = 0; t <= s; ++t)
        for (int u = 0; u < 10; ++u) {
            if (t == s && u >= g) break;
            if (p1_item(t, u) >= 0) ++n;
            if (!USP && t == KUS && u == 0) n += 4;
        }
    return n;
}
constexpr int p1_pos(int code) {
    for (int i = 0; i < P1_PRE; ++i) if (p1_pre(i) == code) return i;
    for (int t = 0; t < 20; ++t)
        for (int u = 0; u < 10; ++u) if (p1_item(t, u) == code) return p1_cnt(t, u);
    return -1000;
}
constexpr int p1_lds(int s, int g) {
    constexpr int t0 = XG0 == 5 ? 0 : 5;
    if (s >= 2 && s < 10 && !(s & 1) && g >= t0 && g < t0 + 2) return 2 + s + g - t0;
    if (s == KSC + 1 && g == t0 + 2) return 38;
    if (s >= 14 && s < 19 && !(s & 1) && (g == 5 || g == 8)) return 30 + (s - 14) + g / 8;
    if (s == 19 && (g == 0 || g == 4)) return 36 + g / 4;
    if (s == 19 && g == 7) return 39;
    return -1;
}
constexpr int p1_nl(int s, int g0, int g1) {
    int n = 0;
    for (int g = g0; g < g1; ++g) n += (s < 19 && g < 5) + ((s & 1) && s < 19 && g >= 5 && g < 9) + (p1_lds(s, g) >= 0);
    return n;
}
constexpr int p1_cap(int s, int g) {
    int u = p1_nl(s, g, g + 1) + (g == 9 && s < 19) + ((s & 1) && s < 19 && g == 5) + ((s & 1) == 0 && g == XG0 - 1);
    const int it = p1_item(s, g), nt = g < 9 ? p1_item(s, g + 1) : -1;
    u += (it >= 0) + (nt >= 0 && nt < 200) + (it == 324);
    if ((s == 19 && g == 2) || (g == 9 && ((s & 3) == 2 || s == KUS - 1 || s == 18))) u += 2;
    return u < 2 ? 2 - u : 0;
}
constexpr int NFILL = 112;
constexpr int p1_from(int f) { return f < 40 ? 0 : f < 52 ? 9 : f < 72 ? KSC : 14; }
constexpr int p1_fill(int s, int g, int e) {
    int f = 0;
    for (int t = 0; t <= s; ++t)
        for (int u = 0; u < 10; ++u)
            for (int v = 0; v < p1_cap(t, u); ++v) {
                const bool go = f < NFILL && p1_from(f) <= t;
                if (t == s && u == g && v == e) return go ? f : -1;
                f += go;
                if (f == 40) f = 48;
            }
    return f;
}
static_assert(p1_fill(13, 10, 0) >= 52, "");
constexpr bool p1_all() {
    for (int i = 0; i < 40; ++i) if (p1_pos(i) < 0) return false;
    for (int i = 100; i < 125; ++i) if (p1_pos(i) < 0) return false;
    for (int i = 0; i < NW2L; ++i) if (p1_pos(500 + i) < 0) return false;
    return p1_pos(202) >= 0 && p1_pos(203) >= 0 && p1_pos(206) >= 0 && p1_pos(208) >= 0 && p1_pos(209) >= 0 && p1_pos(324) >= 0;
}
static_assert(p1_all() && p1_lds(14, 5) == 30 && p1_lds(14, 8) == 31 && p1_lds(16, 5) == 32, "");
constexpr int ring_req(int G, int I, int R, int X) {
    int n = I, e = 0;
    for (int t = 0; t <= G; ++t) {
        int r = -1;
        if (t % 5 == 0 || t % 5 == 3) {
            if (n < 40 && 5 * (n - R) + 4 < t) r = n++;
            else if (e < X && t % 10 == 3) r = 100 + e++;
        }
        if (t == G) return r;
    }
    return -1;
}
constexpr int g2_adr(int i, int kk) {
    int n = 0;
    for (int t = 0; t <= i; ++t)
        for (int u = 0; u < 5; ++u) {
            const bool ok = u == 4 || (u == 2 && (t < 11 || t > 15)) || (u == 3 && t < 5);
            if (t == i && u == kk) return ok ? n : -1;
            n += ok;
        }
    return -1;
}
constexpr int p1_lim(int n) { return n > 63 ? 63 : n; }
template <int IMM> DI void gld4s(v4u& dst, unsigned voff, const void* sbase) { asm volatile("global_load_dwordx4 %0, %1, %2 offset:%3 sc1" : "=v"(dst) : "v"(voff), "s"(sbase), "i"(IMM)); }
template <int IMM> DI void gld2s(v2u& dst, unsigned voff, const void* sbase) { asm volatile("global_load_dwordx2 %0, %1, %2 offset:%3 sc1" : "=v"(dst) : "v"(voff), "s"(sbase), "i"(IMM)); }
template <class T> DI void pin(T& v) { asm volatile("" : "+v"(v)); }
DI void mul2(float& a, float& b, float x, float y, float s) { asm volatile("v_mul_f32 %0, %2, %4\n\tv_mul_f32 %1, %3, %4" : "=&v"(a), "=&v"(b) : "v"(x), "v"(y), "v"(s)); }
DI void rot1(unsigned& a, unsigned x) { asm volatile("v_alignbit_b32 %0, %1, %1, 16" : "=v"(a) : "v"(x)); }
DI void mix1(unsigned& a, unsigned x, unsigned y) { asm volatile("v_bfi_b32 %0, %3, %1, %2" : "=v"(a) : "v"(x), "v"(y), "s"(0xffffu)); }
DI void ror1(unsigned& o, unsigned prev) { asm volatile("v_mov_b32_dpp %0, %1 wave_ror:1 row_mask:0xf bank_mask:0xf" : "=v"(o) : "v"(prev)); }
DI void shr1b(unsigned& o, unsigned x) { asm volatile("v_mov_b32_dpp %0, %1 wave_shr:1 row_mask:0xf bank_mask:0xf" : "+v"(o) : "v"(x)); }
DI void xor1(float& a, unsigned x, unsigned s) { asm volatile("v_xor_b32 %0, %1, %2" : "=v"(a) : "v"(x), "v"(s)); }
template <int G> DI void bc1(float& a, unsigned x) { asm volatile("v_mov_b32_dpp %0, %1 row_newbcast:%2 row_mask:0xf bank_mask:0xf" : "=v"(a) : "v"(x), "i"(G)); }
template <int G> DI void mulb(float& o, unsigned x, float y) { asm volatile("v_mul_f32_dpp %0, %1, %2 row_newbcast:%3 row_mask:0xf bank_mask:0xf" : "=v"(o) : "v"(x), "v"(y), "i"(G)); }
template <int N> DI void lgkm() { __builtin_amdgcn_s_waitcnt(0xC07F | (N << 8)); asm volatile("" ::: "memory"); }
DI void unp2(float& a, float& b, unsigned x) { asm volatile("v_lshlrev_b32 %0, 16, %2\n\tv_and_b32 %1, 0xffff0000, %2" : "=&v"(a), "=&v"(b) : "v"(x)); }
#define GL(a) "global_load_dwordx4 a[" #a ":" #a "+3], %7, %8 sc1\n\tglobal_load_dwordx4 a[" #a "+4:" #a "+7], %7, %8 offset:256 sc1\n\t"
#define AR(d, a, o) "v_accvgpr_read_b32 " d ", a[" #a "+" #o "]\n\t"
#define GS_(a) "s_waitcnt vmcnt(4)\n\t" AR("%2", a, 1) AR("%3", a, 3) AR("%4", a, 5) AR("%5", a, 7) AR("%0", a, 0) AR("%1", a, 2)                              \
    "v_xor_b32 %2, %9, %2\n\tv_bitop3_b32 %4, %4, %9, %2 bitop3:0xbe\n\tv_bitop3_b32 %4, %3, %9, %4 bitop3:0xbe\n\tv_bitop3_b32 %4, %5, %9, %4 bitop3:0xbe\n\t" \
    AR("%2", a, 4) AR("%3", a, 6) "v_cmp_ne_u32 vcc, 0, %4\n\ts_cbranch_vccz 2f\n\t" GL(a)
template <int A, int D> DI void gran_req(unsigned off, const void* base) {
    if constexpr (D > 0) __builtin_amdgcn_s_sleep(D);
    asm volatile("global_load_dwordx4 a[%c2:%c2+3], %0, %1 sc1\n\tglobal_load_dwordx4 a[%c2+4:%c2+7], %0, %1 offset:256 sc1" ::"v"(off), "s"(base), "i"(A) : "memory");
}
DI void pipe_gran(float& a, float& d, unsigned off, const void* base, unsigned tag, bool& dead) {
    unsigned n = dead ? 0u : kSpinCap, y, z;
    float a1, d1;
    asm volatile("1:\n\t" GS_(32) GS_(40) GS_(48) "s_sub_u32 %6, %6, 1\n\ts_cbranch_scc0 1b\n2:"
                 : "=&v"(a), "=&v"(d), "=&v"(a1), "=&v"(d1), "=&v"(y), "=&v"(z), "+s"(n) : "v"(off), "s"(base), "s"(tag) : "memory", "vcc", "scc");
    dead = __builtin_amdgcn_readfirstlane(n) == ~0u;
    a += a1; d += d1;
}
DI void fma2(float& a, float& b, float x, float y, float s, float t, float u, float v) {
    asm volatile("v_fma_f32 %0, %2, %4, %6\n\tv_fma_f32 %1, %3, %5, %7" : "=&v"(a), "=&v"(b) : "v"(x), "v"(y), "v"(s), "v"(t), "v"(u), "v"(v));
}
DI void fma2p(f32x2& o, float x, float y, float r, float u, float v) {
    asm("v_fmac_f32 %0, %1, %2" : "+v"(u) : "v"(x), "v"(r));
    asm("v_fmac_f32 %0, %1, %2" : "+v"(v) : "v"(y), "v"(r));
    o = f32x2{u, v};
}
DI void swp2n(unsigned& a, unsigned& b, unsigned& c, unsigned& d) { asm volatile("v_permlane16_swap_b32 %0, %1\n\tv_permlane16_swap_b32 %2, %3" : "+v"(a), "+v"(b), "+v"(c), "+v"(d)); }
DI void cp2(unsigned& a, unsigned& b, f32x4 v) { asm volatile("v_cvt_pk_bf16_f32 %0, %2, %3\n\tv_cvt_pk_bf16_f32 %1, %4, %5" : "=&v"(a), "=v"(b) : "v"(v.x), "v"(v.y), "v"(v.z), "v"(v.w)); }
DI void zero2(v2u& a) { asm volatile("v_mov_b64 %0, 0" : "=v"(a)); }
DI void mov1(unsigned& a, unsigned b) { asm volatile("v_mov_b32 %0, %1" : "=v"(a) : "v"(b)); }
DI void sw16(unsigned& a, unsigned& b) { asm volatile("v_permlane16_swap_b32 %0, %1" : "+v"(a), "+v"(b)); }
DI void sw32(unsigned& a, unsigned& b) { asm volatile("v_permlane32_swap_b32 %0, %1" : "+v"(a), "+v"(b)); }
DI void mrg8(float& a, float b) { asm volatile("v_mov_b32_dpp %0, %1 quad_perm:[0,1,2,3] row_mask:0xf bank_mask:0xc" : "+v"(a) : "v"(b)); }
DI void sel1(float& o, unsigned a, unsigned b, u64 m) { asm volatile("v_cndmask_b32 %0, %1, %2, %3" : "=v"(o) : "v"(a), "v"(b), "s"(m)); }
DI void count_(unsigned& side, unsigned v) {
    u64 m;
    unsigned n;
    asm volatile("v_cmp_lt_u32 vcc, 4, %3\n\tv_cmp_gt_u32 %1, 4, %3\n\ts_bcnt1_i32_b64 %0, vcc\n\ts_bcnt1_i32_b64 %2, %1\n\ts_cmp_gt_u32 %0, %2\n\ts_cselect_b32 %0, 0x2000, 0"
                 : "=&s"(side), "=&s"(m), "=&s"(n) : "v"(v) : "vcc", "scc");
}
DI void ne1(u64& m, v2u tag, v2u a) { asm volatile("v_cmp_ne_u64 %0, %1, %2" : "=s"(m) : "v"(tag), "v"(a)); }
DI void ne2(u64& m, v2u tag, v2u a, v2u b) { asm volatile("v_cmp_ne_u64 vcc, %1, %2\n\tv_cmp_ne_u64 %0, %1, %3\n\ts_or_b64 %0, %0, vcc" : "=&s"(m) : "v"(tag), "v"(a), "v"(b) : "vcc", "scc"); }
template <int G> DI void bc2b(float& a, float& b, float x, float y) {
    asm volatile("v_mov_b32_dpp %0, %2 row_newbcast:%4 row_mask:0xf bank_mask:0xf\n\tv_mov_b32_dpp %1, %3 row_newbcast:%4 row_mask:0xf bank_mask:0xf" : "=&v"(a), "=&v"(b) : "v"(x), "v"(y), "i"(G));
}
template <class T> DI void pins(T& v) { asm volatile("" : "+s"(v)); }
template <int I> DI void gld_w2(unsigned voff, const void* sbase) {
    asm volatile("global_load_dwordx4 " WA(2) ", %0, %1 offset:%3 sc1" ::"v"(voff), "s"(sbase), "i"(W2R + 4 * I), "i"((I % 5) * 1024 - 2048));
}
DI void gld2t(v2u& dst, unsigned voff, const void* sbase) { asm volatile("global_load_dwordx2 %0, %1, %2 sc1" : "+v"(dst) : "v"(voff), "s"(sbase)); }
DI void undef4(v4u (&x)[4]) { asm volatile("" : "=v"(x[0]), "=v"(x[1]), "=v"(x[2]), "=v"(x[3])); }
DI void wait1(v2u& v) { asm volatile("s_waitcnt vmcnt(0)" : "+v"(v)::"memory"); }
template <int N> DI void wait_fkn(v2u& v, v2u& u) { asm volatile("s_waitcnt vmcnt(%2)" : "+v"(v), "+v"(u) : "i"(N) : "memory"); }
DI void ld_ur(unsigned o, const void* base) {
    asm volatile("global_load_dwordx4 a[40:40+3], %0, %1 sc1\n\tglobal_load_dwordx4 a[44:44+3], %0, %1 offset:1024 sc1\n\t"
                 "global_load_dwordx4 a[48:48+3], %0, %1 offset:2048 sc1\n\tglobal_load_dwordx4 a[52:52+3], %0, %1 offset:3072 sc1" ::"v"(o), "s"(base));
}
template <int I> DI void ld_ur1(unsigned o, const void* base) { asm volatile("global_load_dwordx4 a[%c2:%c2+3], %0, %1 offset:%3 sc1" ::"v"(o), "s"(base), "i"(40 + 4 * I), "i"(1024 * I)); }
constexpr int kUY = KSA + (USP ? p1_cnt(20, 0) - 1 - p1_pos(213) : p1_cnt(20, 0) - p1_cnt(KUS, 0) - 4);
DI void st_ur(unsigned a) {
    asm volatile("s_waitcnt vmcnt(%1)\n\tds_write_b128 %0, a[40:40+3] offset:480\n\tds_write_b128 %0, a[44:44+3] offset:1504\n\t"
                 "ds_write_b128 %0, a[48:48+3] offset:2528\n\tds_write_b128 %0, a[52:52+3] offset:3552" ::"v"(a), "i"(kUY < NW2P ? kUY : NW2P) : "memory");
}
template <int A> DI void rda(unsigned& v) { asm volatile("v_accvgpr_read_b32 %0, a[%c1+0]" : "=v"(v) : "i"(A)); }
DI void poll1(unsigned a, const void* base) { asm volatile("global_load_dwordx2 a[36:36+1], %0, %1 sc1" ::"v"(a), "s"(base)); }
DI void poll2(unsigned a, unsigned b, const void* base) { asm volatile("global_load_dwordx2 a[36:36+1], %0, %2 sc1\n\tglobal_load_dwordx2 a[36+2:36+3], %1, %2 sc1" ::"v"(a), "v"(b), "s"(base)); }
DI void touch_(unsigned a, const void* base) { asm volatile("global_load_dword a[32+1], %0, %1" ::"v"(a), "s"(base)); }
template <int IMM> DI void gld4t(v4u& dst, unsigned voff, const void* sbase) { asm volatile("global_load_dwordx4 %0, %1, %2 offset:%3 sc1" : "+v"(dst) : "v"(voff), "s"(sbase), "i"(IMM)); }
template <int N> DI void vm_wait() { asm volatile("s_waitcnt vmcnt(%0)" ::"i"(N) : "memory"); }
DI float dot2a(unsigned a, unsigned b, float c) {
    return __builtin_amdgcn_fdot2_f32_bf16(__builtin_bit_cast(bf16x2v, a), __builtin_bit_cast(bf16x2v, b), c, false);
}
DI void disc(unsigned l0, unsigned l1, unsigned l2, f32x2& lam, f32x2& co) {
    const float dt = rbf(__builtin_amdgcn_exp2f(lo16f(l2) * 1.44269504088896f));
    const float lre = lo16f(l0), lim = lo16f(l1);
    const float e_ = __builtin_amdgcn_exp2f(dt * lre * 1.44269504088896f);
    const float rv = __builtin_amdgcn_fractf(dt * lim * 0.159154943091895f);
    lam = f32x2{e_ * __builtin_amdgcn_cosf(rv), e_ * __builtin_amdgcn_sinf(rv)};
    const float nr = lam.x - 1.0f, ni = lam.y, id = __builtin_amdgcn_rcpf(lre * lre + lim * lim);
    co = f32x2{(nr * lre + ni * lim) * id, (ni * lre - nr * lim) * id};
}
DI f32x4 unpk4(v2u u) { return f32x4{lo16f(u.x), hi16f(u.x), lo16f(u.y), hi16f(u.y)}; }
#define D2 "v_dot2_f32_bf16 "
#define CP "v_cvt_pk_bf16_f32 "
DI void d2x4(unsigned* o, unsigned a0, unsigned a1, unsigned a2, unsigned a3, unsigned b0, unsigned b1, unsigned b2, unsigned b3) {
    asm volatile(D2 "%0, %4, %8, 0\n\t" D2 "%1, %5, %9, 0\n\t" D2 "%2, %6, %10, 0\n\t" D2 "%3, %7, %11, 0"
                 : "=&v"(o[0]), "=&v"(o[1]), "=&v"(o[2]), "=&v"(o[3]) : "v"(a0), "v"(a1), "v"(a2), "v"(a3), "v"(b0), "v"(b1), "v"(b2), "v"(b3));
}
DI void cpx4(unsigned* o, unsigned a0, unsigned a1, unsigned a2, unsigned a3, unsigned b0, unsigned b1, unsigned b2, unsigned b3) {
    asm volatile(CP "%0, %4, %8\n\t" CP "%1, %5, %9\n\t" CP "%2, %6, %10\n\t" CP "%3, %7, %11"
                 : "=&v"(o[0]), "=&v"(o[1]), "=&v"(o[2]), "=&v"(o[3]) : "v"(a0), "v"(a1), "v"(a2), "v"(a3), "v"(b0), "v"(b1), "v"(b2), "v"(b3));
}
DI void cpx4i(unsigned* t, const unsigned* ka) {
    asm volatile(CP "%0, %0, %4\n\t" CP "%1, %1, %5\n\t" CP "%2, %2, %6\n\t" CP "%3, %3, %7"
                 : "+v"(t[0]), "+v"(t[1]), "+v"(t[2]), "+v"(t[3]) : "v"(ka[0]), "v"(ka[1]), "v"(ka[2]), "v"(ka[3]));
}

__global__ __launch_bounds__(256, 1) __attribute__((aligned(4096))) void k_main(Params p) {
    __shared__ __attribute__((aligned(16))) char smem[LDS_BYTES];
    const rsrc_t rs = __builtin_amdgcn_make_buffer_rsrc((void*)p.ws, 0, WS_BYTES, 0x00020000);
    const rsrc_t ro = __builtin_amdgcn_make_buffer_rsrc((void*)p.out, 0, S_ * D_ * 4, 0x00020000);
    const int w0 = __builtin_amdgcn_readfirstlane(threadIdx.x >> 6), b0 = blockIdx.x;
    bool dead = false;
    asm volatile("" ::: "a255");
    float SW[5];
    f32x4 xin[5];
    v2u gt[5];
    f32x4 acc[2][5];
    f32x4 rr[2];
    float tt[2][5][4];
    Lm lam1, lamq;
    f32x2 l8, l32, l128;
    u64 late;
    v4u cA[8], cB[8];
    int sgn_, sg4, ab[4], bb, zpa, uwa, spa, tbw, tbr, tbp, tbq, cwa, cw4, spl, dst[3];
    unsigned w2o, wro, fwo, fko, xso, gso, fgo1, fho1, fho, hso, eo, es, ho[4];
    bool cv0;
    const char* gn;
    rsrc_t rn;
    const char* kvtb;
    const char* w2b0[(NW2P + 4) / 5];
    int otw, otr0, otr4;
    int mt1_;
    auto addrs = [&](auto layer_, int lane, int w, int b) __attribute__((always_inline)) {
        constexpr int layer = layer_.value;
        const int c = lane & 15, q = lane >> 4;
        TILES()
        static_assert(l_sg(0, 0) == SC + 0xA900 && l_sg(0, 1) == SC + 0xB400 && l_sg(0, 2) == SC + 0xCE00 && l_sg(0, 3) == SC + 0xDC00, "");
        sgn_ = (layer ? l_sg(1, 0) + w * INBOX : SC + (int)((0xDC00CE00B400A900ull >> (16 * w)) & 0xffff)) + (q >> 1) * 640 + (q & 1) * 8;
        const int c4 = (c >> 1) * 16 + (c & 1) * 4;
        sg4 = sgn_ + c4; sgn_ += c * 16;
        const int pl = lane < 40 ? lane : 39, tbb = l_tb(layer, 0) + w * (layer ? INBOX : 5120);
        spl = SC + pl * 16;
        eo = OFF_EG + (unsigned)(layer * 163840 + nt1 * 640 + pl * 16);
        es = eo + (unsigned)(mt1 * 10240);
        spa = l_rs(layer, 0) + (2 * (mt2 & 1) + (w >> 1)) * (layer ? INBOX : 256) + (2 * (w & 1) + (c >> 3)) * 64 + (c & 7) * 4;
        tbw = tbb + c * 64 + ((q ^ ((c >> 1) & 3)) * 16);
        tbr = tbb + (2 * (c >> 2) + (q & 1)) * 64 + (((2 * (q >> 1) + ((c >> 1) & 1)) ^ (c >> 2)) * 16) + (c & 1) * 8;
        hso = N_HF + (unsigned)((layer * 8 + 2 * w + (q >> 1)) * 5 * NB_ + (2 * nt1 + (q & 1)) * 256 + c * 16);
        tbp = tbb + (8 * (q & 1) + (c >> 2)) * 64 + (((2 * (q >> 1) + ((c >> 1) & 1)) ^ (c >> 3)) * 16) + (c & 1) * 8;
        tbq = tbp ^ 32;
        cwa = L_CW + c * 16; cw4 = L_CW + c4;
        pin(sg4); pin(tbp); pin(tbq); pin(cw4);
        fho1 = lane ? kNone : N_FH + (unsigned)(layer * NB_ + (w >> 1) * 4096 + (nt1 * 2 + (w & 1)) * 128);
        fho = N_FH + (unsigned)(layer * NB_ + ((mt2 & 1) * 4 + w) * 1024 + (lane >> 3) * 128);
        pin(sgn_); pin(spl); pin(eo); pin(es); pin(spa); pin(tbw); pin(tbr); pin(hso); pin(cwa); pin(fho1); pin(fho);
        const unsigned hb_ = N_HF + (unsigned)((layer * 8 + 4 * (mt2 & 1)) * 5 * NB_ + w * 2048);
#pragma unroll
        for (int m = 0; m < 4; ++m) { ho[m] = hb_ + (unsigned)(((m + 1 + w) & 3) * 5 * NB_ + lane * 16); pin(ho[m]); }
#pragma unroll
        for (int m = 0; m < 3; ++m) { dst[m] = SC + ((w + 1 + m) & 3) * INBOX + m * 5120 + lane * 16; pin(dst[m]); }
        mt1_ = mt1;
        if constexpr (layer == 0) {
            const int r0 = SC + (w < 3 ? w : 0) * REGSZ, y0 = 2031 + 8 * q - c, l34 = lane < 33 ? lane : 33;
#pragma unroll
            for (int i = 0; i < 4; ++i) { ab[i] = r0 + ((y0 & 1) ? CV_O : CV_E) + ((y0 + 1) >> 1) * 4 - 448 - 512 - 1024 * i; pin(ab[i]); }
            bb = r0 + 32 * c + 16 * q;
            zpa = w < 3 ? r0 + l34 * 16 + (l34 >= 30 ? CV_E + 4096 - 480 : 0) + (l34 >= 32 ? CV_O + 4100 - 32 - CV_E - 4096 : 0) : SC + 47360 + lane * 16;
            uwa = (w < 3 ? r0 : SC + 47360 - 480) + lane * 16;
            xso = (unsigned)((64 * mt2 + 16 * w + (lane >> 2)) * D_ + 80 * nt2 + 8 * ((lane & 3) ^ q)) * 2u;
            gso = OFF_GT + (unsigned)((80 * nt2 + i32 + 32 * w) * GSTR + (q & 1) * 512 + (c >> 2) * 128 + (q >> 1) * 64 + (c & 3) * 16);
            fgo1 = lane == 0 && i32 + 32 * w < 80 ? OFF_FG + (unsigned)((nt2 * 96 + i32 + 32 * w) * 128) : kNone;
            pin(bb); pin(zpa); pin(uwa); pin(xso); pin(gso); pin(fgo1);
        }
        SCHED();
    };
    unsigned tag0, tag1;
    v2u tagv;
#define FLAG(off) { if (lane == 0) st64(tagv, rs, off); }
#define ISTAG(f) (((f.x ^ tag0) | (f.y ^ tag1)) == 0u)
#define KW_REQ() { ld_ur(wro, kvtb); static_for<NW2P>(LAMBDA(i_) { gld_w2<i_.value>(w2o, p.ws + 2048 + (i_.value / 5) * 5120); }); }
#define FW_OK() { if (__builtin_expect(late != 0ull, 0)) { WAVE_WAIT({ const v2u fk_ = ld64(rs, fko); const v2u fw_ = ld64(rs, fwo); ok = ISTAG(fk_) && ISTAG(fw_); }) KW_REQ() } }
#define WF(g, s) *(const v4u*)(smem + wa[(s) & 1][(g) < 4 ? (g) & 1 : 2] + ((g) & 2) * 20480 + ((s) >> 1) * 1024)
#define TT_GAP(n) { constexpr int mb_ = (n) / 10, nb_ = ((n) % 10) / 2, e_ = ((n) & 1) * 2; \
        mul2(tt[mb_][nb_][e_], tt[mb_][nb_][e_ + 1], rm_[4 * mb_ + e_], rm_[4 * mb_ + e_ + 1], SW[nb_]); }
    {
        const int tid = threadIdx.x, lane = tid & 63, w = w0, b = b0;
        const int c = lane & 15, q = lane >> 4;
        TILES()
        STAMP(0)
        const unsigned lds0 = (unsigned)(size_t)(LAS char*)smem;
        unsigned nonce;
        unsigned cpf0;
        {
            u64 pc;
            int len;
            asm volatile("s_getpc_b64 %0\nkpf_begin:\n\ts_mov_b32 %1, kpf_end-kpf_begin\n\ts_max_i32 %1, %1, kpf_end2-kpf_begin\n\ts_max_i32 %1, %1, kpf_end3-kpf_begin" : "=s"(pc), "=s"(len)::"scc");
            const int o0 = (((j & 15) * 4 + w) + 64 * (lane >> 3)) * 128;
            asm volatile("global_load_dword %0, %1, %2" : "=v"(cpf0) : "v"(o0 < len ? o0 : 0), "s"(pc));
        }
        const bool cv = 32 * w + i32 < 80;
        const int chn = 80 * nt2 + i32 + 32 * (cv ? w : 0);
        const bool kw = 32 * w + 31 - i32 < 80;
        const int ku = 80 * nt2 + (w == 2 ? 64 + (i32 & 15) : i32 + 32 * (w & 1)), kc = ku >> 6, kr = ku & 63;
        const int lc = cv ? lane : 8;
        const unsigned xl = lds0 + (unsigned)(w * REGSZ);
        v4u ax[15], sa[5], cr[2], wr[4];
        unsigned uL[3], uM[3], sdq, kso;
        const int cu_ = (b < 200 ? b : 0) * 4 + w, cdb = cu_ / 20, ccg = cu_ % 20;
        {
            unsigned cpo = (unsigned)(32 * ccg + (lane & 31)) * 2u, cpm = (unsigned)(40 * nt1 + (lane < 39 ? lane : 39)) * 2u;
            unsigned k1 = (unsigned)(32 * kr + 5 + (lane >> 3)) * 1280u + (unsigned)(64 * kc + 8 * (lane & 7)) * 2u, k0 = k1 - 10240u, k4 = lane < 24 ? k1 + 30720u : kNone;
            unsigned vo = k1 - 6400u + (unsigned)(lane >> 3) * 3840u;
            pin(cpo); pin(cpm); pin(k0); pin(k1); pin(k4); pin(vo);
            SCHED();
            gldu16(uL[0], cpo, p.Lre); gldu16(uL[1], cpo, p.Lim); gldu16(uL[2], cpo, p.logdt);
            gldu16(uM[0], cpm, p.Lre); gldu16(uM[1], cpm, p.Lim); gldu16(uM[2], cpm, p.logdt);
            const v4u rk = {(unsigned)(u64)p.k, (unsigned)((u64)p.k >> 32) & 0xffffu, S_ * D_ * 2u, 0x00020000u};
            ld_kv(ax, 32 * w + 31 - i32, k0, k1, k4, vo, rk, p.v, xl);
            SCHED();
        }
        const unsigned gof = OFF_GRP + (unsigned)mt1 * 0x200000u - ((unsigned)(u64)p.ws & 0x1fffffu);
        unsigned vote = 4u, side;
        unsigned wu;
        asm volatile("s_load_dword %0, %1, 0x0" : "=s"(wu) : "s"(p.ws + (gof + 0x1ff000u)));
        SCHED();
        auto small_ = [&]() __attribute__((always_inline)) {
            kso = (unsigned)(512 * kc + lane * 4);
            dma_1<L_EAR + 2048, 0>(kso, p.shk, xl); dma_1<L_EAR + 2304, 256>(kso, p.shk, xl);
            gld1s(nonce, kso & 4u, p.ws + OFF_NONCE);
            SCHED();
        };
        if (SMP == 0) small_();
        const u16* sab = w < 2 ? p.x0 : w == 2 ? p.Bre : p.Bim;
        unsigned sao;
        {
            int r0 = w < 2 ? 128 * mt1 + 8 * nt1 + 4 * w : 40 * nt1 + mt1, r1 = w < 2 ? 1 : 16, r2 = w < 2 ? 2047 : 40 * nt1 + 32 + (mt1 >> 1);
            pins(sab); pins(r0); pins(r1); pins(r2);
            const int r = r0 + q * r1;
            sao = (unsigned)((r < r2 ? r : r2) * 1280 + c * 16);
            pin(sao);
        }
        if (SAF == 0) static_for<5>(LAMBDA(i) { gld<256 * i.value>(sa[i.value], sao, sab); });
        SCHED();
        if (w == TW0 && !kw) tour<0>();
        STAMP(22)
        const int row8 = lane >> 3;
        const unsigned wvo = (unsigned)(40 * nt1 + row8) * 1280u + (unsigned)(((lane & 7) ^ ((w & 1) | ((row8 >> 1) << 1))) * 16 + (w >> 1) * 128);
        const char* const wbase = (const char*)((w & 1) ? p.Bim : p.Bre);
        const unsigned wl = lds0 + (unsigned)(((w & 1) * 10 + (w >> 1)) * 1024);
        const unsigned xvo0 = (unsigned)(8 * (row8 >> 1) + 2 * (row8 & 1)) * 1280u + (unsigned)(((lane & 7) ^ ((row8 >> 1) << 1)) * 16);
        const unsigned xvo1 = (xvo0 ^ 16u) + 1280u;
        const char* const xbase = (const char*)p.x0 + (size_t)(128 * mt1 + 32 * w) * 1280;
        unsigned xv[4] = {xvo0, xvo1, xvo0 + 5120u, xvo1 + 5120u}, wv[5];
#pragma unroll
        for (int i = 0; i < 5; ++i) { wv[i] = wvo + i * 10240u; pin(wv[i]); if (i < 4) pin(xv[i]); }
#define DMA_M0(it) SETM0<dma_loff(it) - dma_goff(it)>((it) < 40 ? xl : wl)
#define DMA_LD1(it, nx_) GLD1<dma_goff(it), dma_loff(nx_) - dma_goff(nx_)>((it) < 40 ? xv[(it) & 3] : wv[(it) < 40 ? 0 : ((it) - 100) % 5], (it) < 40 ? xbase : wbase, (nx_) < 40 ? xl : wl)
#define DMA_LD(it) GLD0<dma_goff(it)>((it) < 40 ? xv[(it) & 3] : wv[(it) < 40 ? 0 : ((it) - 100) % 5], (it) < 40 ? xbase : wbase)
        const int swz = (c & 1) | ((c >> 2) << 1);
        int wa[2][3];
#pragma unroll
        for (int i = 0; i < 2; ++i) {
            wa[i][0] = (c >> 3) * 20480 + (c & 7) * 128 + (((4 * i + q) ^ (c & 6)) * 16); wa[i][1] = (wa[i][0] ^ 16) + 10240;
            wa[i][2] = 81920 + (c & 1) * 10240 + (c >> 1) * 128 + (((4 * i + q) ^ ((c & 1) | ((c >> 2) << 1))) * 16);
            pin(wa[i][0]); pin(wa[i][1]); pin(wa[i][2]);
        }
        int xlane0 = L_XR + w * REGSZ + (c & 1) * 1024 + (c >> 1) * 128 + ((q ^ swz) * 16);
        int xlane1 = L_XR + w * REGSZ + (c & 1) * 1024 + (c >> 1) * 128 + (((4 + q) ^ swz) * 16);
        unsigned cvo = (unsigned)((16 * cdb + (lane >> 2)) * D_ + 32 * ccg + 8 * ((lane & 3) ^ q)) * 2u, for0, for1;
        int ear = L_EAR + w * REGSZ + c * 64 + (q ^ (c >> 2)) * 16, eas = L_EAR + 2048 + w * REGSZ + (lane & 7) * 64;
        int kra = L_XR + w * REGSZ + (lane >> 3) * 512 + (lane & 7) * 16;
        pin(ear); pin(eas); pin(kra);
        {
            const int G = 4 * ccg + q, nt = G / 5, g5 = G - 5 * nt;
            for0 = OFF_W2F + (unsigned)(((cdb / 5) * 200 + cdb % 5) * 1024 + c * 16 + (10 * (nt >> 2) + 2 * (g5 & 6) + ((nt >> 1) & 1)) * 5120 + (2 * (nt & 1) + (g5 & 1)) * 256);
            for1 = for0 + (g5 < 4 ? 10240u : 256u);
        }
        unsigned sto = OFF_ST + (unsigned)((mt1 * 16 + 4 * w + q) * 256 + c * 8), swo = OFF_ST + (unsigned)(((lane >> 2) * 16 + nt1) * 256 + 128 + (lane & 3) * 16);
        unsigned vto = OFF_VT + (unsigned)(mt1 * 512 + c * 32 + (q & 1) * 8);
        unsigned kvo = OFF_KVT + (unsigned)((64 * kc + 8 * (lane >> 3) + ((lane >> 2) & 1)) * KSTR + (8 * kc + (lane >> 3)) * KPS + (32 * kr + 8 * (lane & 3)) * 2);
        int kbw = L_KB + w * 1024 + (lane & 7) * 128 + (((lane >> 3) * 8) ^ ((lane & 7) * 16));
        int kbr = L_KB + w * 1024 + (lane >> 3) * 128 + (((lane & 7) * 16) ^ ((lane >> 3) * 16));
        const unsigned dum = kNone;
        unsigned fko1 = OFF_FK + (unsigned)((64 * kc + kr) * 32);
        if (!kw) kvo = dum;
        if (b >= 200) { for0 = dum; for1 = dum; cvo = (unsigned)((b * 4 + w) * 128); }
        wro = (unsigned)(chn * 4096 + lc * 16);
        cv0 = cv;
        pin(kvo); pin(kbw); pin(kbr); pin(fko1);
        pin(xlane0); pin(xlane1); pin(cvo); pin(for0); pin(for1); pin(sto); pin(swo); pin(vto); pin(wro);
        SCHED();
        auto pre = LAMBDA(i_) {
            constexpr int it = p1_dma(i_.value);
            SCHED();
            DMA_M0(it);
            asm volatile("s_nop 0");
            DMA_LD(it);
            SCHED();
        };
        fko = OFF_FK + (unsigned)(((chn & ~63) + (cv ? lane : 0)) * 32);
        { const int e_ = (lane & 7) < w ? w : (lane & 7) > w + 4 ? w + 4 : (lane & 7);
          fwo = OFF_FW + (unsigned)(((5 * nt2 + (lane < 40 ? lane >> 3 : 4)) * 20 + 4 * w + e_) * 32); }
        w2o = OFF_W2F + (unsigned)((nt2 * 40 + 10 * w) * 5120 + lane * 16);
        int ime = SC + (w < 3 ? w * REGSZ + CV_E : 43264) + 1008 - 16 * lane, imo = SC + (w < 3 ? w * REGSZ + CV_O + 4 : 47360) + 1008 - 16 * lane, im3 = w < 3 ? ime + 2048 : imo;
        int tca = L_TAB + c * 136, tcb = tca + 8 + 32 * w + 8 * q, tcc = L_TAB + (32 + (c >> 1)) * 136, tcd = tcc + 8 + 32 * w + 8 * q, tcl = L_TAB + (lane < 40 ? lane : 39) * 136, coa = L_CO + w * REGSZ + q * 64, rsa = l_rs(0, 0) + w * 256 + lane * 4;
        unsigned sgn = (c & 1) ? 0u : 0x80000000u;
        fko1 = kw && lane < 32 ? fko1 : OFF_FW + (unsigned)((b * 4 + w) * 32);
        pin(fko); pin(fwo); pin(w2o); pin(fko1); pin(ime); pin(imo); pin(im3); pin(tca); pin(tcb); pin(tcc); pin(tcd); pin(tcl); pin(coa); pin(rsa); pin(sgn);
        SCHED();
        auto cw_ = [&]() __attribute__((always_inline)) {
            dma_n<L_EAR>(cvo, p.Cre, xl); dma_n<L_EAR + 1024>(cvo, p.Cim, xl);
            dma_1<L_EAR + 2560, 0>((unsigned)(160 * nt2 + (lane < 39 ? lane : 39) * 4), p.Dskip, xl);
            ld_wr(wr, 32 * w + i32, wro, p.ssmk);
            gldu16(sdq, (unsigned)chn * 2u, p.ssmD);
        };
        if (SMP == 1) small_();
        static_assert(SMP == 0 || SAF > 0, "");
        if (SAF == 1) static_for<5>(LAMBDA(i) { gld<256 * i.value>(sa[i.value], sao, sab); });
        if (CWP == 0) cw_();
        STAMP(33)
        asm volatile("s_waitcnt vmcnt(%5)\n\ts_cmp_ge_u32 %4, 80\n\ts_cbranch_scc0 1f\n\ts_waitcnt vmcnt(%6)\n1:" : "+v"(uL[0]), "+v"(uL[1]), "+v"(uL[2]), "+v"(cpf0) : "s"(32 * w + 31 - i32), "i"(12 + (SAF < 2 ? 5 : 0) + (CWP ? 0 : 8)), "i"(3 + (SAF < 2 ? 5 : 0) + (CWP ? 0 : 8)) : "memory", "scc");
        pin(uM[0]); pin(uM[1]); pin(uM[2]);
        {
            f32x2 lam, co;
            disc(uL[0], uL[1], uL[2], lam, co);
            *(f32x2*)(smem + L_CO + w * REGSZ + (lane & 31) * 8) = 2.0f * co;
        }
        SCHED();
        STAMP(35)
        if (SAF == 2) static_for<5>(LAMBDA(i) { gld<256 * i.value>(sa[i.value], sao, sab); });
        asm volatile("s_waitcnt lgkmcnt(0)" : "+s"(wu)::"memory");
        if (CWP == 1) cw_();
        if (APOS == 0) addrs(std::integral_constant<int, 0>{}, lane, w, b);
        if (SAF == 3) static_for<5>(LAMBDA(i) { gld<256 * i.value>(sa[i.value], sao, sab); });
        if (CWP == 2) cw_();
        v4u kvt[4];
        unsigned ka[8], kt[8], se[8], vs[8], ko4[4], one2 = 0x3F803F80u;
        pin(one2);
        auto kvop = LAMBDA(n_) {
            constexpr int n = n_.value, h = n ? (n - 1) / 29 : -1, u = n ? (n - 1) % 29 : 26;
            if constexpr (u < 2) d2x4(ka + 4 * u, ax[0][h], ax[1][h], ax[2][h], ax[3][h], se[u], se[u], se[u], se[u]);
            else if constexpr (u == 8) { const v4u sk = ax[11 + h]; se[4] = sk[0] >> 16; se[5] = sk[2] & 0xffff0000u; se[6] = sk[0] & 0xffffu; se[7] = sk[2] << 16; pin(se[4]); pin(se[5]); pin(se[6]); pin(se[7]); }
            else if constexpr (u < 21) {
                constexpr int v = u < 8 ? u - 2 : u - 3, tp = 1 + v / 6, st = v % 6, e = st & 1;
                if constexpr (st < 2) d2x4(kt + 4 * e, ax[tp][h], ax[tp + 1][h], ax[tp + 2][h], ax[tp + 3][h], se[2 * tp + e], se[2 * tp + e], se[2 * tp + e], se[2 * tp + e]);
                else if constexpr (st < 4) cpx4i(kt + 4 * e, ka + 4 * e);
                else d2x4(ka + 4 * e, kt[4 * e], kt[4 * e + 1], kt[4 * e + 2], kt[4 * e + 3], one2, one2, one2, one2);
            }
            else if constexpr (u == 21) { vs[0] = ax[7][h] & 0xffffu; vs[1] = ax[8][h] << 16; vs[2] = ax[9][h] & 0xffffu; vs[3] = ax[10][h] << 16; pin(vs[0]); pin(vs[1]); pin(vs[2]); pin(vs[3]); }
            else if constexpr (u == 22) { vs[4] = ax[7][h] >> 16; vs[5] = ax[8][h] & 0xffff0000u; vs[6] = ax[9][h] >> 16; vs[7] = ax[10][h] & 0xffff0000u; pin(vs[4]); pin(vs[5]); pin(vs[6]); pin(vs[7]); }
            else if constexpr (u == 23) cpx4(ko4, ka[0], ka[2], ka[4], ka[6], ka[1], ka[3], ka[5], ka[7]);
            else if constexpr (u < 26) { constexpr int e = u - 24; d2x4(kt + 4 * e, ko4[2 * e], ko4[2 * e], ko4[2 * e + 1], ko4[2 * e + 1], vs[4 * e], vs[4 * e + 1], vs[4 * e + 2], vs[4 * e + 3]); }
            else if constexpr (u == 26) {
                if constexpr (h < 3) { const v4u sk = ax[12 + h]; se[0] = sk[1] >> 16; se[1] = sk[3] & 0xffff0000u; se[2] = sk[1] & 0xffffu; se[3] = sk[3] << 16; pin(se[0]); pin(se[1]); pin(se[2]); pin(se[3]); }
                else asm volatile("s_nop 2");
            }
            else if constexpr (u == 27) {
                cpx4(ko4, kt[0], kt[2], kt[4], kt[6], kt[1], kt[3], kt[5], kt[7]);
                *(v2u*)(smem + kbw) = v2u{ko4[0], ko4[1]};
                *(v2u*)(smem + (kbw ^ 64)) = v2u{ko4[2], ko4[3]};
            }
            else kvt[h] = *(const v4u*)(smem + kbr);
            SCHED();
        };
        auto deal = [&](auto a_, auto b_, auto n_, auto i_) __attribute__((always_inline)) {
            constexpr int a = a_.value, m = b_.value - a, lo = m * i_.value / n_.value, hi = m * (i_.value + 1) / n_.value;
            static_for<hi - lo>(LAMBDA(e_) { pre(std::integral_constant<int, a + lo + e_.value>{}); });
        };
#define IC(n) std::integral_constant<int, (n)>{}
        static_for<kQ0>(pre);
        static_assert(kQ0 == 0 && KD0 >= 8, "");
        asm volatile("s_waitcnt vmcnt(%4)" : "+v"(ax[7]), "+v"(ax[8]), "+v"(ax[9]), "+v"(ax[10]) : "i"(13 + kQ0) : "memory");
        ax[11] = *(const v4u*)(smem + eas);
        SCHED();
#pragma unroll
        for (int i = 0; i < 7; ++i) { ax[i] = *(const v4u*)(smem + kra + i * 128); SCHED(); }
#pragma unroll
        for (int i = 1; i < 4; ++i) ax[11 + i] = *(const v4u*)(smem + eas + i * 16);
        SCHED();
        pin(nonce);
        STAMP(30)
        unsigned tch = 0u;
        auto kvs = LAMBDA(n_) {
            site<1000 + n_.value>(); SCHED();
            if constexpr (n_.value == KD0) LGKM0()
            kvop(n_);
            if constexpr (n_.value >= KD0) deal(IC(kQ0), IC(kQ0 + kQ1), IC(117 - KD0), IC(n_.value - KD0));
        };
        static_assert((KST - 1) % 29 == 0, "");
        if (kw) static_for<KST>(kvs);
        else {
            {
                const unsigned t_ = (unsigned)__builtin_amdgcn_s_memtime(), f_ = ((nt1 ^ w) & 1) << 13;
                const unsigned o_ = gof + N_END + (t_ & 0x1e0u) * 512u + (unsigned)(nt1 * 512 + (w & 1) * 256) + (t_ & 0x10u) * 8u;
                const int r_ = race(p.ws + (o_ + f_), p.ws + (o_ + (f_ ^ 0x2000u)));
                vote += f_ ? r_ : -r_;
            }
            if (__builtin_expect(w == 3, !W3O)) {
                f32x2 lam, co, lp[16];
                disc(uM[0], uM[1], uM[2], lam, co);
                lp[0] = cmul(lam, lam); lp[0] = cmul(lp[0], lp[0]); lp[1] = cmul(lp[0], lp[0]);
                lp[0] = f32x2{1.f, 0.f};
                lp[2] = cmul(lp[1], lp[1]); lp[3] = cmul(lp[2], lp[1]); lp[4] = cmul(lp[2], lp[2]);
                const f32x2 z2 = {0.f, 0.f};
                f32x2 t4[4];
                cfma4(t4, lp[4], lp[1], lp[2], lp[3], lp[4], z2, z2, z2, z2);
                lp[5] = t4[0]; lp[6] = t4[1]; lp[7] = t4[2]; lp[8] = t4[3];
                cfma4(t4, lp[8], lp[1], lp[2], lp[3], lp[4], z2, z2, z2, z2);
                lp[9] = t4[0]; lp[10] = t4[1]; lp[11] = t4[2]; lp[12] = t4[3];
                cfma4(t4, lp[8], lp[5], lp[6], lp[7], lp[7], z2, z2, z2, z2);
                lp[13] = t4[0]; lp[14] = t4[1]; lp[15] = t4[2];
                f32x2* tl = (f32x2*)(smem + tcl);
                tl[0] = lam;
#pragma unroll
                for (int i = 0; i < 16; ++i) tl[1 + i] = lp[i];
                SCHED();
                asm volatile("kpf_end3:");
            }
            static_for<kQ1a>(LAMBDA(i_) { pre(IC(kQ0 + i_.value)); });
            undef4(kvt);
            asm volatile("" : "=v"(se[0]), "=v"(se[1]), "=v"(se[2]), "=v"(se[3]));
        }
        tag0 = __builtin_amdgcn_readlane(nonce, 0) + 1u; tag1 = __builtin_amdgcn_readlane(nonce + (tag0 == 0u ? 1u : 0u), 1);
        tagv = v2u{tag0, tag1}; pin(tagv);
        {
            asm volatile("s_waitcnt vmcnt(%5)" : "+v"(sa[0]), "+v"(sa[1]), "+v"(sa[2]), "+v"(sa[3]), "+v"(sa[4]) : "i"(8 + kQ0 + kQ1a) : "memory");
            float s1[2] = {0.f, 0.f}, s2[2] = {0.f, 0.f};
#pragma unroll
            for (int i = 0; i < 5; ++i)
#pragma unroll
                for (int e = 0; e < 4; ++e) { s1[e & 1] = dot2a(sa[i][e], one2, s1[e & 1]); s2[e & 1] = dot2a(sa[i][e], sa[i][e], s2[e & 1]); }
            const float a1 = rowsum16(s1[0] + s1[1]), a2 = rowsum16(s2[0] + s2[1]);
            const float mean = a1 * (1.0f / D_);
            const float var = fmaxf(a2 * (1.0f / D_) - mean * mean, 0.f);
            const float ri = __builtin_amdgcn_rcpf(rbf(__builtin_amdgcn_sqrtf(rbf(rbf(var) + 1e-5f))));
            v2u sv = {__float_as_uint(w >= 2 ? a1 : c ? -ri * rbf(mean) : ri), tag0};
            int o0 = w < 2 ? (mt1 * 16 + nt1) * 256 + 32 * w : ((mt1 >> 2) * 16 + nt1) * 256 + 112 + (mt1 & 3) * 16 + 8 * w, o1 = w < 2 ? 8 : 16384, o2 = w < 2 ? 64 : 0;
            pins(o0); pins(o1); pins(o2);
            unsigned so_ = c < 2 ? OFF_ST + (unsigned)(o0 + q * o1 + c * o2) : kNone;
            if (w >= 2 && lane == 2) { sv.x = vote; so_ = OFF_VT + (unsigned)((mt1 * 16 + nt1) * 32 + (w & 1) * 8); }
            pin(sv); pin(so_);
            st64(sv, rs, so_);
            SCHED();
        }
        bool kw2 = kw;
        if (kw2) {
            static_for<117 - KST>(LAMBDA(n_) { kvs(IC(KST + n_.value)); });
        } else {
            if (__builtin_expect(w == 3, !W3O)) {
            const int l = lane, l3 = l & 3;
            unsigned a = gof + N_SG + (unsigned)((((l >> 3) & 1) * 2 + (mt2 & 1)) * NB_ + ((l >> 1) & 3) * 2048 + nt2 * 256 + (l & 1) * 128);
            { unsigned t_ = OFF_EG + (unsigned)((l >= 21 ? 163840 - 640 : 0) + (mt1 * 16 + nt1) * 640 + (l < 26 ? l - 16 : 5) * 128); pin(t_); a = l >= 16 ? t_ : a; }
            { unsigned t_ = gof + N_PS + (unsigned)((((mt2 & 1) * 4 + l3) * 8 + nt2) * 128); pin(t_); a = l >= 28 ? t_ : a; }
            { unsigned t_ = gof + N_FH + (unsigned)(((l >> 2) & 1) * NB_ + (l3 >> 1) * 4096 + (nt1 * 2 + (l3 & 1)) * 128); pin(t_); a = l >= 32 ? t_ : a; }
            { unsigned t_ = gof + N_FX + (unsigned)(((2 * (mt2 & 1) + (l3 >> 1)) * 16 + nt2 * 2 + (l3 & 1)) * 128); pin(t_); a = l >= 40 ? t_ : a; }
            { unsigned t_ = OFF_FG + (unsigned)((nt2 * 96 + i32 + 16 * ((l3 + 1) & 6)) * 128); pin(t_); a = l >= 48 ? t_ : a; }
            const unsigned big = (l & 7) < 5 ? OFF_KVT + (unsigned)((l & 1) * 640 * 4608 + (80 * nt2 + i32) * GSTR) : OFF_W2F + (unsigned)(nt2 * 204800 + ((l & 7) - 5) * 98304);
            a = l >= 56 ? big : a;
            tch = a;
            pin(tch);
            asm volatile("kpf_end2:");
        }
            static_for<kQ1 - kQ1a>(LAMBDA(i_) { pre(IC(kQ0 + kQ1a + i_.value)); });
            if (w == TW) tour<1>();
            skip<SKIP0>();
            undef4(kvt);
        }
        SCHED();
        STAMP(34)
        if (APOS == 1) addrs(std::integral_constant<int, 0>{}, lane, w, b);
        v4u orr, oi;
        unsigned ie[4][4], io[4][4], iw;
        auto img = LAMBDA(n_) {
            constexpr int i = n_.value / 10, u = n_.value % 10;
            if constexpr (u == 0) { if constexpr (i > 0) ror1(iw, wr[i - 1].w); else { iw = 0u; pin(iw); } }
            else if constexpr (u < 5) rot1(ie[i][u - 1], wr[i][4 - u]);
            else if constexpr (u == 5) shr1b(iw, wr[i].w);
            else if constexpr (u < 9) mix1(io[i][u - 6], wr[i][9 - u], wr[i][8 - u]);
            else mix1(io[i][3], wr[i].x, iw);
        };
        auto tap0 = [&]() __attribute__((always_inline)) {
            wr[0].x = cvtpk(lo16f(wr[0].x) + (lane ? 0.f : lo16f(sdq)), hi16f(wr[0].x));
            pin(wr[0]);
            SCHED();
        };
        {
            static_assert(kQ0 + kQ1 <= 18, "");
            vm_wait<6 + kQ0 + kQ1>();
            cr[0] = *(const v4u*)(smem + ear); cr[1] = *(const v4u*)(smem + ear + 1024);
            static_for<5>(LAMBDA(i) { lds2a<22 + 2 * i.value, 2560 + 32 * i.value>(lds0 + (unsigned)(L_EAR + w * REGSZ + q * 8)); });
            asm volatile("s_waitcnt lgkmcnt(0)" : "+v"(cr[0]), "+v"(cr[1])::"memory");
            static_for<kQ2>(LAMBDA(i_) { pre(IC(kQ0 + kQ1 + i_.value)); });
            static_for<4>(LAMBDA(e_) {
                constexpr int e = e_.value;
                const f32x4 co = *(const f32x4*)(smem + coa + e * 16);
                const f32x2 o0 = lo16f(cr[0][e]) * f32x2{co.x, -co.y} - lo16f(cr[1][e]) * f32x2{co.y, co.x};
                const f32x2 o1 = hi16f(cr[0][e]) * f32x2{co.z, -co.w} - hi16f(cr[1][e]) * f32x2{co.w, co.z};
                orr[e] = cvtpk(o0.x, o1.x); oi[e] = cvtpk(o0.y, o1.y);
            });
            pin(orr); pin(oi);
            SCHED();
            static_for<NPD - kQ0 - kQ1 - kQ2>(LAMBDA(i_) { pre(IC(kQ0 + kQ1 + kQ2 + i_.value)); });
            for (int i = 0; i < 4; ++i) st128(kvt[i], rs, kvo + i * 2 * KSTR);
            st128(orr, rs, for0); st128(oi, rs, for1);
        }
        SCHED();
        STAMP(23)
        if (w == 3) tour<7>();
        asm volatile("s_waitcnt vmcnt(%5) lgkmcnt(0)" : "+v"(wr[0]), "+v"(wr[1]), "+v"(wr[2]), "+v"(wr[3]), "+v"(sdq) : "i"(P1_PRE - 1 - p1_pos(3)) : "memory");
        __builtin_amdgcn_s_barrier();
        asm volatile("" ::: "memory");
        STAMP(24)
        site<10>();
        v4u wf[2][5], af[2][2][2];
#pragma unroll
        for (int nb = 0; nb < 5; ++nb) wf[0][nb] = WF(nb, 0);
#pragma unroll
        for (int i = 0; i < 4; ++i) af[0][i >> 1][i & 1] = *(const v4u*)(smem + ((i >> 1) ? xlane1 : xlane0) + (i & 1) * 2048);
        SCHED();
        tap0();
        site<11>();
        kvtb = p.ws + OFF_KVT + (chn * KPAD + (chn >> 3) * KPS);
        pins(kvtb);
#pragma unroll
        for (int i = 0; i < (NW2P + 4) / 5; ++i) { w2b0[i] = p.ws + 2048 + i * 5120; pins(w2b0[i]); }
        v2u sg, tc[2], fk, fw, vt;
        v4u sw0;
        v2u zq[2];
        float rr_[8];
        unsigned sx[4], sz[4];
        u64 odd = 0xAAAAAAAAAAAAAAAAull;
        pins(odd);
        auto filler = LAMBDA(f_) {
            constexpr int f = f_.value;
            if constexpr (f < 0) {}
            else if constexpr (f < 40) img(IC(f));
            else if constexpr (f == 48) { lam1.r = __uint_as_float(tc[0].x); xor1(lam1.s, tc[0].y, sgn); }
            else if constexpr (f == 49) { lamq.r = __uint_as_float(tc[1].x); xor1(lamq.s, tc[1].y, sgn); }
            else if constexpr (f < 52) zero2(zq[f - 50]);
            else if constexpr (f == 52) { sx[0] = sw0.x; mov1(sx[1], sx[0]); }
            else if constexpr (f == 53) { sz[0] = sw0.z; mov1(sz[1], sz[0]); }
            else if constexpr (f < 56) bc1<f - 54>(rr_[f - 54], sg.x);
            else if constexpr (f == 56) sw16(sx[0], sx[1]);
            else if constexpr (f == 57) sw16(sz[0], sz[1]);
            else if constexpr (f < 60) mov1(sx[f - 56], sx[f - 58]);
            else if constexpr (f < 62) mov1(sz[f - 58], sz[f - 60]);
            else if constexpr (f < 64) sw32(sx[f - 62], sx[f - 60]);
            else if constexpr (f < 66) sw32(sz[f - 64], sz[f - 62]);
            else if constexpr (f < 72) {
                bc1<f - 64>(rr_[f - 64], sg.x);
                if constexpr (f == 66) { SW[0] = __uint_as_float(sx[0]); SW[1] = __uint_as_float(sz[0]); SW[2] = __uint_as_float(sx[1]); SW[3] = __uint_as_float(sz[1]); }
                if constexpr (f == 71) sel1(SW[4], sx[2], sz[2], odd);
            }
            else mulb<8 + (f - 72) % 8>(tt[(f - 72) % 8 / 4][(f - 72) / 8][(f - 72) % 4], sg.x, SW[(f - 72) / 8]);
        };
        auto kstep = LAMBDA(s_) {
            constexpr int s = s_.value, ph = s >> 2, s4 = s & 3, cu = s & 1, nx = cu ^ 1, jj = s >> 1, pp = jj & 1;
            if constexpr (s4 == 3 && ph < 4) {
                constexpr int pw = p1_pos(104 + 5 * (ph + 1)), px = p1_pos(4 * (jj + 1) + 3), pm = pw > px ? pw : px;
                constexpr int vm = p1_lim(p1_cnt(s, 0) - 1 - pm);
                asm volatile("" ::: "memory");
                __builtin_amdgcn_s_waitcnt((vm & 15) | ((vm >> 4) << 14) | 0x70);
                __builtin_amdgcn_s_barrier();
                asm volatile("" ::: "memory");
                STAMP(25 + ph)
                SCHED();
            }
            if constexpr (s == KSC) {
                static_assert(s4 == 3 && p1_pos(204) < p1_pos(4 * (jj + 1) + 3), "");
                pin(sg); pin(sw0); pin(vt);
                if (__builtin_expect(__builtin_amdgcn_ballot_w64((__builtin_amdgcn_bitop3_b32(sg.y, sw0.y, tag0, 0x7E) | __builtin_amdgcn_bitop3_b32(vt.y, sw0.w, tag0, 0x7E)) != 0u) != 0ull, 0))
                    WAVE_WAIT({ sg = ld64(rs, sto); sw0 = ld128(rs, swo); vt = ld64(rs, vto); ok = ((sg.y ^ tag0) | (sw0.y ^ tag0) | (sw0.w ^ tag0) | (vt.y ^ tag0)) == 0u; })
                ORDER();
                STAMP(7)
            }
            if constexpr (s == kW2S && KUS > kW2S) { vm_wait<p1_cnt(s, 0) - 1 - p1_pos(208)>(); SCHED(); }
            if constexpr (s == KUS) {
                static_assert(p1_pos(206) > p1_pos(208), "");
                wait_fkn<p1_cnt(s, 0) - 1 - p1_pos(206)>(fk, fw);
                if (!USP) ld_ur(wro, kvtb);
                SCHED();
            }
            if constexpr (s4 != 3 || ph == 4) lgkm<(s == 0 ? 0 : p1_nl(s - 1, 5 - cu, 10) - 1)>();
            SCHED();
            static_for<10>(LAMBDA(g_) {
                constexpr int g = g_.value, mb = g / 5, k = g % 5, it = p1_item(s, g), nt_ = g < 9 ? p1_item(s, g + 1) : -1;
                constexpr bool d0 = it >= 0 && it < 200, d1 = nt_ >= 0 && nt_ < 200;
                if constexpr (s == 0) mf0(acc[mb][k], af[pp][cu][mb], wf[cu][k]); else mf(acc[mb][k], af[pp][cu][mb], wf[cu][k]);
                SCHED();
                if constexpr (s < 19) {
                    if constexpr (g < 5) wf[nx][g] = WF(g, s + 1);
                    if constexpr (cu == 1 && g == 5) vm_wait<p1_lim(p1_cnt(s, 5) - 1 - p1_pos(4 * (jj + 1) + 3))>();
                    if constexpr (cu == 1 && g >= 5 && g < 9)
                        af[pp ^ 1][(g - 5) >> 1][(g - 5) & 1] = *(const v4u*)(smem + (((g - 5) >> 1) ? xlane1 : xlane0) + ((jj + 1) % 3) * 4096 + ((g - 5) & 1) * 2048);
                }
                if constexpr (cu == 0 && g == XG0 - 1 && s > 0) lgkm<p1_nl(s - 1, 8, 10) + p1_nl(s, 0, XG0 - 1)>();
                constexpr int lq = p1_lds(s, g);
                if constexpr (lq >= 4 && lq < 8) (lq == 4 ? lam1.a : lq == 5 ? lam1.b : lq == 6 ? lamq.a : lamq.b) = *(const f32x2*)(smem + (lq < 6 ? tca : tcb) + (lq & 1) * 2176);
                else if constexpr (lq == 8 || lq == 9) tc[lq - 8] = *(const v2u*)(smem + (lq == 8 ? tcc : tcd));
                else if constexpr (lq == 10) { const f32x2* tp = (const f32x2*)(smem + tcl); l8 = tp[2]; l32 = tp[5]; }
                else if constexpr (lq == 11) l128 = *(const f32x2*)(smem + tcl + 72);
                else if constexpr (lq >= 30 && lq < 33) *(v4u*)(smem + (lq < 32 ? ime + 1024 * (lq - 30) : im3)) = v4u{ie[33 - lq][0], ie[33 - lq][1], ie[33 - lq][2], ie[33 - lq][3]};
                else if constexpr (lq >= 33 && lq < 37) *(v4u*)(smem + imo + 1024 * (36 - lq)) = v4u{io[lq - 33][0], io[lq - 33][1], io[lq - 33][2], io[lq - 33][3]};
                else if constexpr (lq == 37) *(v4u*)(smem + im3 + 1024) = v4u{ie[0][0], ie[0][1], ie[0][2], ie[0][3]};
                else if constexpr (lq == 38) *(unsigned*)(smem + rsa) = sg.x;
                else if constexpr (lq == 39) *(v4u*)(smem + zpa) = v4u{zq[0].x, zq[0].y, zq[1].x, zq[1].y};
                static_for<p1_cap(s, g)>(LAMBDA(e_) { filler(IC(p1_fill(s, g, e_.value))); });
                if constexpr (s == 18 && g == 9) ne2(late, tagv, fk, fw);
                if constexpr (s == 19 && g == 5) count_(side, vt.x);
                if constexpr (s == 19 && g == 2) { if (__builtin_expect(late != 0ull, 0)) poll2(fko, fwo, p.ws); }
                if constexpr (it >= 0) {
                    if constexpr (d0 && d1) DMA_LD1(it, d1 ? nt_ : 0);
                    else if constexpr (d0) DMA_LD(it);
                    else if constexpr (it == 202) gld2s<0>(sg, sto, p.ws);
                    else if constexpr (it == 203) gld4s<0>(sw0, swo, p.ws);
                    else if constexpr (it == 204) gld2s<0>(vt, vto, p.ws);
                    else if constexpr (it == 206) gld2s<0>(fk, fko, p.ws);
                    else if constexpr (it == 208) gld2s<0>(fw, fwo, p.ws);
                    else if constexpr (it == 209) { unsigned t2 = tch + side; asm volatile("v_cndmask_b32 %0, %1, %0, %2" : "+v"(t2) : "v"(tch), "s"(0x0000FFFFF000FFFFull)); touch_(t2, p.ws); }
                    else if constexpr (it >= 210 && it < 214) ld_ur1<it - 210>(wro, kvtb);
                    else if constexpr (it >= 500) gld_w2<it % 50>(w2o, w2b0[(it % 50) / 5]);
                    else { vm_wait<p1_cnt(s, g) - P1_PRE>(); st64(tagv, rs, fko1); }
                }
                if constexpr (d1 && !d0) DMA_M0(d1 ? nt_ : 0);
                site<2000 + 10 * s + g>();
                SCHED();
            });
        };
        static_for<20>(kstep);
        static_for<NFILL - p1_fill(19, 10, 0)>(LAMBDA(i_) { filler(IC(p1_fill(19, 10, 0) + i_.value)); });
        SCHED();
        rr[0] = f32x4{rr_[0], rr_[1], rr_[2], rr_[3]}; rr[1] = f32x4{rr_[4], rr_[5], rr_[6], rr_[7]};
        ACC_FENCE10(acc[0], acc[1]);
        gn = p.ws + (gof + side);
        rn = __builtin_amdgcn_make_buffer_rsrc((void*)gn, 0, N_END, 0x00020000);
        STAMP(1)
    }

    auto layer_body = LAMBDA(layer_) {
        constexpr int layer = layer_.value;
        const int sga = sgn_;
        const bool cv = layer == 0 && cv0;
        v2u fh;
        unsigned ks[5];
        f32x4 ca[8], a2[4][5];
        unsigned gp[4][4];
        auto gst = LAMBDA(i_) {
            constexpr int i = i_.value >> 2, u = i_.value & 3;
            if constexpr (u < 2) cp2(gp[i][2 * u], gp[i][2 * u + 1], ca[2 * i + u]);
            else if constexpr (u == 2) swp2n(gp[i][0], gp[i][2], gp[i][1], gp[i][3]);
            else st128(v4u{gp[i][0], gp[i][1], gp[i][2], gp[i][3]}, rs, gso + i * 1024);
        };
        float rsr, rsm;
        St bv[8];
        {
            St a0, a1;
            auto w2s = LAMBDA(n_) {
                constexpr int lo = KSA * n_.value / 4, hi = KSA * (n_.value + 1) / 4;
                if constexpr (layer == 0) { static_for<hi - lo>(LAMBDA(e_) { constexpr int i = NW2L + lo + e_.value; gld_w2<i>(w2o, w2b0[i / 5]); }); SCHED(); }
            };
            static_for<2>(LAMBDA(m_) {
                constexpr int mb = m_.value;
#pragma unroll
                for (int i = 0; i < 4; ++i) fma2p(bv[4 * mb + i].a, acc[mb][0][i], acc[mb][1][i], rr[mb][i], tt[mb][0][i], tt[mb][1][i]);
#pragma unroll
                for (int i = 0; i < 4; ++i) fma2p(bv[4 * mb + i].b, acc[mb][2][i], acc[mb][3][i], rr[mb][i], tt[mb][2][i], tt[mb][3][i]);
                const f32x4 v = __builtin_elementwise_fma(acc[mb][4], rr[mb], f32x4{tt[mb][4][0], tt[mb][4][1], tt[mb][4][2], tt[mb][4][3]});
#pragma unroll
                for (int i = 0; i < 4; ++i) bv[4 * mb + i].c = v[i];
                SCHED();
                w2s(IC(2 * mb));
                if constexpr (mb == 0) run3(a0, bv[0], bv + 1, lam1); else run4(a1, a0, bv + 4, lam1);
                SCHED();
                if constexpr (mb == 0) w2s(IC(1));
            });
            *(f32x2*)(smem + sga) = a1.a; *(f32x2*)(smem + sga + 256) = a1.b; *(float*)(smem + sg4 + 512) = a1.c;
            SCHED();
            w2s(IC(3));
        }
        SCHED();
        const int w_ = w0;
        {
        const int w = w_, mt1 = mt1_;
        if (layer == 0 && __builtin_expect(late != 0ull, 0)) {
            unsigned f0_, f1_, f2_, f3_;
            asm volatile("s_waitcnt vmcnt(0)\n\t" AR("%0", 36, 0) AR("%1", 36, 1) AR("%2", 36, 2) AR("%3", 36, 3) : "=v"(f0_), "=v"(f1_), "=v"(f2_), "=v"(f3_)::"memory");
            late = __builtin_amdgcn_ballot_w64(((f0_ ^ tag0) | (f1_ ^ tag1) | (f2_ ^ tag0) | (f3_ ^ tag1)) != 0u);
            ORDER();
            if (late == 0ull) KW_REQ()
        }
        asm volatile("s_waitcnt lgkmcnt(0)" ::: "memory");
        __builtin_amdgcn_s_barrier();
        asm volatile("" ::: "memory");
        STAMP(layer ? 14 : 2)
#pragma unroll
        for (int s = 0; s < 5; ++s) ks[s] = (unsigned)s * NB_;
        constexpr int NW2A = NW2P + 7 * kCR1 + kXA, NW3 = NW2P + kN3E + kN3;
        auto w2ld = LAMBDA(i_) {
            constexpr int i = i_.value;
            gld_w2<i>(w2o, p.ws + 2048 + (i / 5) * 5120);
        };
        auto w2a = LAMBDA(i_) {
            constexpr int k = i_.value;
            if constexpr (k < kXA) { w2ld(std::integral_constant<int, NW2P + 2 * k>{}); w2ld(std::integral_constant<int, NW2P + 2 * k + 1>{}); }
            else w2ld(std::integral_constant<int, NW2P + kXA + k>{});
        };
        unsigned xtb = (unsigned)(size_t)(LAS char*)smem + (unsigned)(l_tb(0, 0) + w * 5120);
        auto w2x = LAMBDA(i_) {
            constexpr int i = NW2A + i_.value;
            if constexpr (i < 50) w2ld(std::integral_constant<int, i>{});
            else if constexpr (i < 53) dma_x<(i - 50) * 1024, (i < 52 ? (i - 50) * 64 : 96)>(xso, p.x0, xtb);
        };
        auto w2b = LAMBDA(i_) {
            constexpr int k = i_.value - 7 * kCR1;
            constexpr int ne = KW2E < 53 - NW2A ? KW2E : 53 - NW2A;
            if constexpr (k < ne) w2x(std::integral_constant<int, k>{});
            constexpr int k1 = k - kFHK - 1;
            if constexpr (k1 >= 0 && k1 < KW2T) { w2x(std::integral_constant<int, ne + 2 * k1>{}); w2x(std::integral_constant<int, ne + 2 * k1 + 1>{}); }
            else if constexpr (k1 >= 0) w2x(std::integral_constant<int, ne + KW2T + k1>{});
            static_assert(ne <= kFHK + 1 && 53 - NW2A - ne - KW2T + kFHK < 7 * (8 - kCR1) - kFHP + 1, "");
            if constexpr (k == kFHK) { vm_wait<ne>(); st64(tagv, rn, fho1); STAMP(39) }
            if constexpr (k == kFHK - 1) { STAMP(38) }
            if constexpr (k == 7 * (8 - kCR1) - kFHP) poll1(fho, gn);
            static_assert(NW2A + 7 * (8 - kCR1) >= 53 && NW2A <= 50 && NW3 <= NW2A, "");
        };
        auto partb = [&]() __attribute__((always_inline)) {
            const float* sp = (const float*)(smem + spa);
            rsr = sp[0]; rsm = sp[8];
            if (layer == 1) { pin(rsr); pin(rsm); }
            if (layer == 1) {
                static_for<5>(LAMBDA(n_) {
                    constexpr int nb = n_.value;
                    unsigned d0, d1;
                    rda<22 + 2 * nb>(d0); rda<23 + 2 * nb>(d1);
                    a2[3][nb] = xin[nb] + (xin[nb] * rsr + rsm) * unpk4(v2u{d0, d1}); pin(a2[3][nb]);
                });
            }
        };
        constexpr int LBW = layer ? 2 : 3;
        if (w == 3) {
            site<3 + 10 * layer>(); SCHED();
            f32x4 s4[8];
#pragma unroll
            for (int i = 0; i < 8; ++i) { s4[i] = *(const f32x4*)(smem + spl + (l_sg(layer, i & 3) - SC + (i >> 2) * 640)); SCHED(); }
            f32x2 sv[16], x[4], P[4][4];
#pragma unroll
            for (int i = 0; i < 16; ++i) { const f32x4 t = s4[(i >> 2) + 2 * (i & 2)]; sv[i] = (i & 1) ? f32x2{t.z, t.w} : f32x2{t.x, t.y}; }
            unsigned er, ei;
            {
                f32x2 u[4];
                cfma4n(u, l8, sv[0], sv[4], sv[8], sv[12], sv[1], sv[5], sv[9], sv[13]);
                agg(er, ei, x, P[0][2], u, sv, l8, l32, l128);
            }
            st128(v4u{er, tag0, ei, tag0}, rs, es);
            SCHED();
            P[0][0] = f32x2{0.f, 0.f}; P[0][1] = x[0]; P[0][3] = cfma(l32, P[0][2], x[2]);
            if (layer == 0) { FW_OK() static_for<kN3E>(LAMBDA(i_) { w2ld(std::integral_constant<int, NW2P + i_.value>{}); }); }
#pragma unroll
            for (int kk = 1; kk < 4; ++kk) cfma4(P[kk], l8, P[kk - 1][0], P[kk - 1][1], P[kk - 1][2], P[kk - 1][3], sv[kk - 1], sv[kk + 3], sv[kk + 7], sv[kk + 11]);
#pragma unroll
            for (int i = 0; i < 8; ++i) { const f32x2 a = P[2 * (i >> 2)][i & 3], b_ = P[2 * (i >> 2) + 1][i & 3]; *(f32x4*)(smem + spl + (l_sg(layer, i & 3) - SC + (i >> 2) * 640)) = f32x4{a.x, a.y, b_.x, b_.y}; }
            partb();
        }
        int w1_ = w, w3_ = w;
        if (layer == 1) { pins(w1_); pins(w3_); }
        if (layer == 1 && w1_ < LBW) { site<4 + 10 * layer>(); SCHED(); partb(); if constexpr (layer == 1) { if (w == 0) tour<6>(); if constexpr (LBR == 1) room<3000>(); } }
        if (w3_ == LBW) {
            site<5 + 10 * layer>(); SCHED();
            f32x2 wg[17], t4[4];
            unsigned ea[15], tg[15];
            v4u eg[15];
            constexpr bool early = (LBE >> layer) & 1;
            const f32x2 z2 = {0.f, 0.f};
#pragma unroll
            for (int i = 0; i < 15; ++i) { ea[i] = (unsigned)(mt1 - 1 - i) * 10240u; pins(ea[i]); ea[i] = ea[i] < kNone ? ea[i] : kNone; pins(ea[i]); }
            if constexpr (early) {
                __builtin_amdgcn_s_sleep(layer ? KLB1 : KLB0);
#pragma unroll
                for (int i = 0; i < 15; ++i) eg[i] = ld128s(rs, eo, ea[i]);
                SCHED();
            }
            wg[1] = cmul(l128, l128); wg[2] = cmul(wg[1], wg[1]); wg[3] = cmul(wg[2], wg[1]); wg[4] = cmul(wg[2], wg[2]);
#pragma unroll
            for (int i = 4; i < 16; i += 4) {
                cfma4(t4, wg[i], wg[1], wg[2], wg[3], wg[4], z2, z2, z2, z2);
#pragma unroll
                for (int e = 0; e < 4; ++e) wg[i + 1 + e] = t4[e];
            }
#pragma unroll
            for (int i = 0; i < 15; ++i) { if (i) pin(wg[i]); tg[i] = mt1 - 1 - i < 0 ? 0u : tag0; pins(tg[i]); }
            if (layer == 1) partb();
            if constexpr (layer == 0) tour<3>();
            f32x2 H0, H1;
            unsigned spins = dead ? kSpinCap : 0u;
            if constexpr (layer == 1 && LBR == 0) { room<1800>(); site<16>(); }
            if constexpr (!early) __builtin_amdgcn_s_sleep(layer ? KLB1 : KLB0);
            SCHED();
            auto eval = [&](const v4u (&e)[15]) __attribute__((always_inline)) {
                unsigned x = __builtin_amdgcn_bitop3_b32(e[0].y, e[0].w, tg[0], 0x7E);
                H0 = f32x2{__uint_as_float(e[0].x), __uint_as_float(e[0].z)}; H1 = f32x2{0.f, 0.f};
#pragma unroll
                for (int i = 1; i < 15; i += 2) {
                    x |= __builtin_amdgcn_bitop3_b32(e[i].y, e[i].w, tg[i], 0x7E) | __builtin_amdgcn_bitop3_b32(e[i + 1].y, e[i + 1].w, tg[i + 1], 0x7E);
                    pin(x);
                    gacc2(H0, H1, wg[i], wg[i + 1], e[i], e[i + 1]);
                    SCHED();
                }
                *(f32x2*)(smem + spl + (L_CW - SC)) = H0 + H1;
                return __builtin_amdgcn_ballot_w64(x != 0u) != 0ull;
            };
            bool again = true;
            if constexpr (early) again = eval(eg);
            if (again) {
                bool miss;
#pragma nounroll
                do {
                    asm volatile("" ::: "memory");
                    v4u e2[15];
#pragma unroll
                    for (int i = 0; i < 15; ++i) e2[i] = ld128s(rs, eo, ea[i]);
                    SCHED();
                    miss = eval(e2);
                    if (__builtin_expect(miss, 0) && ++spins > kSpinCap) { dead = true; miss = false; }
                } while (miss);
            }
            ORDER();
            STAMP(layer ? 36 : 37)
            if (layer == 0) {
                static_for<NW3 - NW2P - kN3E>(LAMBDA(i_) { w2ld(std::integral_constant<int, NW2P + kN3E + i_.value>{}); });
            }
        }
        int w2_ = w;
        pins(w2_);
        if constexpr (layer == 0) site<21>();
        if (layer == 0) UNDEF8(ca);
        if (layer == 0 && w2_ != LBW) {
            if (cv) {
                site<2>(); SCHED();
                partb();
                {
                    FW_OK()
                    st_ur((unsigned)(size_t)(LAS char*)smem + (unsigned)uwa);
                    CV_LOAD_A(cA, 0, 0, 0) CV_LOAD_A(cA, 0, 0, 1)
                    SCHED();
                    static_for<8>(LAMBDA(a_) { constexpr int a = a_.value; CV_LOAD_B(cB, a, 0) SCHED(); });
                    CV_LOAD_A(cA, 1, 0, 0) CV_LOAD_A(cA, 1, 0, 1)
                    SCHED();
                }
                STAMP(32)
                conv_part<0, kCR1, CPF>(ca, cA, cB, smem, ab, bb, w2a, LAMBDA(i_) {});
            } else {
                if constexpr (layer == 0) tour<5>();
                FW_OK()
                static_for<NW2A - NW2P>(LAMBDA(i_) { w2ld(std::integral_constant<int, NW2P + i_.value>{}); __builtin_amdgcn_s_sleep(1); });
                partb();
                if constexpr (layer == 0) room<3900>();
            }
        }
        asm volatile("s_waitcnt lgkmcnt(0)" ::: "memory");
        __builtin_amdgcn_s_barrier();
        asm volatile("" ::: "memory");
        STAMP(layer ? 15 : 4)
        {
            St cw, ex, hv[8];
            cw.a = *(const f32x2*)(smem + cwa); cw.b = *(const f32x2*)(smem + cwa + 256); cw.c = *(const float*)(smem + cw4 + 512);
            ex.a = *(const f32x2*)(smem + sga); ex.b = *(const f32x2*)(smem + sga + 256); ex.c = *(const float*)(smem + sg4 + 512);
            SCHED();
#pragma unroll
            for (int s = 1; s < 5; ++s) pins(ks[s]);
            SCHED();
            keep14(hv, cw, ex, bv, lamq, lam1);
            keep4(hv + 4, hv[3], bv + 4, lam1);
            SCHED();
            v2u ta[5], td[5];
            f32x2 hb[9];
            static_for<5 + SBL>(LAMBDA(r_) {
                constexpr int r = r_.value, rd = r < 2 ? r : r == 2 ? 4 : r - 1, ro_ = r - SBL < 2 ? r - SBL : r - SBL == 2 ? 4 : r - SBL - 1;
                if constexpr (r < 5) {
                    float e[8];
                    unsigned o[4];
#pragma unroll
                    for (int i = 0; i < 8; ++i) e[i] = rd == 4 ? hv[i].c : rd < 2 ? hv[i].a[rd & 1] : hb[i + 1][rd & 1];
                    if constexpr (r == 0) b3cv(hb, o, cw.b, ex.b, bv[0].b, bv[1].b, lamq.b, lam1.b, e);
                    else if constexpr (r < 3) b3cv(hb + 3 * r, o, hb[3 * r - 1], bv[3 * r - 1].b, bv[3 * r].b, bv[3 * r + 1].b, lam1.b, lam1.b, e);
                    else for (int i = 0; i < 4; ++i) o[i] = cvtpk(e[2 * i], e[2 * i + 1]);
                    *(v4u*)(smem + tbw + rd * 1024) = v4u{o[0], o[1], o[2], o[3]};
                    SCHED();
                    if constexpr (r < 3) { bfin(hb[3 * r + 2], hb[3 * r + 1], lam1.b); SCHED(); }
                    ta[rd] = __builtin_bit_cast(v2u, trread(smem, (rd < 4 ? tbp : tbr) + rd * 1024));
                    td[rd] = __builtin_bit_cast(v2u, trread(smem, (rd < 4 ? tbq + 256 : tbr + 512) + rd * 1024));
                    SCHED();
                }
                if constexpr (r >= SBL) { __builtin_amdgcn_raw_buffer_store_b128(v4u{ta[ro_].x, ta[ro_].y, td[ro_].x, td[ro_].y}, rn, hso, ks[ro_], SC1); SCHED(); }
            });
        }
        if (layer == 1) { DRAIN(); st64(tagv, rn, fho1); }
        STAMP(layer ? 16 : 5)
        if (cv) {
            if constexpr (layer == 0) site<6>();
            SCHED();
            if constexpr (!CPF) {
                CV_LOAD_A(cA, 0, kCR1, 0) CV_LOAD_A(cA, 0, kCR1, 1)
                SCHED();
                static_for<8>(LAMBDA(a_) { constexpr int a = a_.value; CV_LOAD_B(cB, a, kCR1) SCHED(); });
                static_for<7>(LAMBDA(a_) { constexpr int a = a_.value + 1; CV_LOAD_A(cA, a, kCR1, 0) CV_LOAD_A(cA, a, kCR1, 1) });
                SCHED();
            }
            conv_part<kCR1, 8, false>(ca, cA, cB, smem, ab, bb, w2b, LAMBDA(g_) {
                constexpr int g = g_.value;
                if constexpr ((g == 14 || g == 16 || g == 18 || g == 20)) gst(std::integral_constant<int, (g - 14) / 2>{});
                if constexpr ((g == 26 || g == 28 || g == 30 || g == 32)) gst(std::integral_constant<int, 4 + (g - 26) / 2>{});
            });
            asm volatile("" : "+v"(ca[0]), "+v"(ca[1]), "+v"(ca[2]), "+v"(ca[3]), "+v"(ca[4]), "+v"(ca[5]), "+v"(ca[6]), "+v"(ca[7]));
            static_for<8>(LAMBDA(i_) {
                constexpr int n = 8 + i_.value;
                if constexpr (n == 9) asm volatile("s_nop 0");
                if constexpr ((n & 3) == 2) asm volatile("s_nop 1");
                gst(std::integral_constant<int, n>{}); SCHED();
            });
        } else if (layer == 0) {
            constexpr int ne = KW2E < 53 - NW2A ? KW2E : 53 - NW2A;
            static_for<54 - NW2A>(LAMBDA(i_) {
                if constexpr (i_.value == ne) { vm_wait<ne>(); st64(tagv, rn, fho1); }
                if constexpr (i_.value < 53 - NW2A) { w2x(i_); __builtin_amdgcn_s_sleep(1); }
            });
            if (w == 3) { static_for<NW2A - NW3>(LAMBDA(i_) { w2ld(std::integral_constant<int, NW3 + i_.value>{}); __builtin_amdgcn_s_sleep(1); }); if constexpr (layer == 0) tour<2>(); }
            poll1(fho, gn);
            if constexpr (layer == 0) room<1000>();
            DRAIN();
        }
        if (layer == 0) { unsigned f0_, f1_; asm volatile("s_waitcnt vmcnt(%2)\n\t" AR("%0", 36, 0) AR("%1", 36, 1) : "=v"(f0_), "=v"(f1_) : "i"(4) : "memory"); fh = v2u{f0_, f1_}; }
        STAMP(layer ? 29 : 6)
        }

            v2u pt[4];
            if (layer == 0) {
                u64 hl;
                ne1(hl, tagv, fh);
                if (__builtin_expect(hl != 0ull, 0)) {
                    gld2t(fh, fho, gn); wait1(fh); st64(tagv, rs, fgo1);
                    WAVE_RETRY(ISTAG(fh), fh = ld64(rn, fho))
                }
                ORDER();
            }
            else { room<3400>(); site<18>(); pipe_flag<KPH0>(pt, fho, gn, tagv, dead); ORDER(); }
            STAMP(layer ? 17 : 31)
            site<7 + 10 * layer>(); SCHED();
            constexpr int RING2 = layer ? KR2 : KR20;
            v4u ar[RING2];
#pragma unroll
            for (int i = 0; i < KI2; ++i) { ar[i] = ld128s(rn, ho[i / 10] + (i & 1) * 1024, ks[i % 10 >> 1]); SCHED(); }
        int b_o = b0, w_o = w0;
        asm volatile("" : "+s"(b_o), "+s"(w_o));
        const int b = b_o, w = w_o;
        TILES()
        v4u gl[3];
        v2u f0;
        unsigned fgo, sgo, sgs, glo, gw, gr, srco;
        u64 glate = 0ull;
        const char *gb1 = p.ws + 32 * GSTR, *gb2 = p.ws + 48 * GSTR;
        {
            unsigned xs_[3][4], dq[2];
            float zx[4], zd[4], zt[4], zz[5][4];
            unsigned sA = SC + w * INBOX, s1 = N_SG + (layer * 2 + (mt2 & 1)) * NB_ + w * 2048, s2 = s1 + nt2 * 256, sF = OFF_FG + (nt2 * 96 + 4 * w) * 128, sG = OFF_GT + (80 * nt2 + 4 * w) * GSTR + mt2 * 128,
                     sW = sA + 8192 + (w & 1) * 128, sR = SC + 8192 + w * 32, sX = l_tb(0, 0) + w * 5120, k160 = 160, kIn = INBOX;
            pins(sA); pins(s1); pins(s2);
            if (layer == 0) { pins(sF); pins(sG); pins(sW); pins(sR); pins(sX); pins(k160); pins(kIn); pins(gb1); pins(gb2); }
            unsigned l16, c16, q0, q1, c2, xr0, xr1, ta_, tb_;
#define A1(n_, v_, e_) else if constexpr (n == n_) { v_ = e_; pin(v_); }
            auto adr = LAMBDA(n_) {
                constexpr int n = n_.value + (layer && n_.value >= 4 ? 12 : 0);
                if constexpr (n == 0) { l16 = (unsigned)dst[0] & 1023u; pin(l16); }
                A1(1, c16, l16 & 0xf0u) A1(2, q0, (l16 >> 8) & 1u) A1(3, q1, l16 >> 9)
                A1(4, c2, (l16 >> 6) & 3u) A1(5, xr0, (c16 << 2) + sX) A1(6, ta_, q1 ^ c2) A1(7, xr0, (q0 << 3) + xr0) A1(8, xr0, (ta_ << 4) + xr0) A1(9, xr1, xr0 ^ 32u)
                else if constexpr (n >= 10 && n < 15) { constexpr int nb = n - 10; const v2u t = *(const v2u*)(smem + ((nb & 5) ? xr1 : xr0) + (nb >> 1) * 1024); xs_[nb >> 1][2 * (nb & 1)] = t.x; xs_[nb >> 1][2 * (nb & 1) + 1] = t.y; }
                A1(16, srco, l16 + sA) A1(17, ta_, l16 & 0x300u) A1(18, sgo, s1 + l16 + ta_) A1(19, sgs, (q0 << 3) + c16) A1(20, ta_, (q1 << 30) + s2) A1(21, sgs, sgs + ta_)
                else if constexpr (layer == 1) {}
                A1(22, ta_, l16 < 624u ? l16 : 624u) A1(23, tb_, ta_ & 0x380u) A1(24, ta_, ta_ & 0x60u) A1(25, fgo, (tb_ << 4) + sF) A1(26, fgo, (ta_ << 2) + fgo)
                A1(27, glo, q1 * (16u * GSTR) + sG) A1(28, ta_, l16 & 0x180u) A1(29, tb_, l16 & 0x70u) A1(30, glo, ta_ * (GSTR / 128u) + glo) A1(31, glo, glo + tb_)
                A1(32, ta_, l16 >> 7) A1(33, tb_, l16 & 0x30u) A1(34, gw, (tb_ << 1) + sW) A1(35, gw, __umul24(ta_, k160) + gw) A1(36, ta_, c2 & 1u) A1(37, gw, (ta_ << 4) + gw)
                A1(38, ta_, l16 >> 8) A1(39, gr, (q0 << 7) + sR) A1(40, gr, __umul24(ta_, kIn) + gr) A1(41, gr, __umul24(c2, k160) + gr) A1(42, tb_, tb_ >> 1) A1(43, gr, gr + tb_)
            };
            SCHED();
            if constexpr (STAMPS == 1) { STAMP(layer ? 50 : 40) }
            static_for<40>(LAMBDA(i_) {
                constexpr int i = i_.value, m = i / 10, s = i % 10;
                static_for<5>(LAMBDA(k_) {
                    constexpr int kk = k_.value, pv = ring_req(5 * i + kk, KI2, RING2, 0);
                    if constexpr (s == 0 && m < 3) mfa0<kk>(a2[m][kk], ar[i % RING2]); else mfa<5 * s + kk>(a2[m][kk], ar[i % RING2]);
                    SCHED();
                    if constexpr (i == 0 && kk == 0 && layer == 1) { pin(pt[0]); pin(pt[1]); pin(pt[2]); pin(pt[3]); }
                    if constexpr (g2_adr(i, kk) >= 0) adr(IC(g2_adr(i, kk)));
                    if constexpr (layer == 0 && i >= 1 && i < 28 && (kk & 1)) {
                        constexpr int n = 2 * (i - 1) + (kk >> 1) - 9, nb = n / 9, u = n % 9 - 1;
                        if constexpr (n < 0) {}
                        else if constexpr (u < 0) { rda<22 + 2 * nb>(dq[0]); rda<23 + 2 * nb>(dq[1]); }
                        else if constexpr (u < 2) unp2(zx[2 * u], zx[2 * u + 1], xs_[nb >> 1][2 * (nb & 1) + u]);
                        else if constexpr (u < 4) unp2(zd[2 * u - 4], zd[2 * u - 3], dq[u - 2]);
                        else if constexpr (u < 6) fma2(zt[2 * u - 8], zt[2 * u - 7], zx[2 * u - 8], zx[2 * u - 7], rsr, rsr, rsm, rsm);
                        else fma2(zz[nb][2 * u - 12], zz[nb][2 * u - 11], zt[2 * u - 12], zt[2 * u - 11], zd[2 * u - 12], zd[2 * u - 11], zx[2 * u - 12], zx[2 * u - 11]);
                    }
                    if constexpr (layer == 0 && i == 0 && kk == 1) { vm_wait<KI2 - 1 + (ring_req(0, KI2, RING2, 0) >= 0)>(); st64(tagv, rs, fgo1); }
                    if constexpr (layer == 0 && i == kGPI && kk == 2) gld2s<0>(f0, fgo, p.ws);
                    if constexpr (layer == 0 && i == kGCI) {
                        static_assert(kGCI + KR20 > 40, "");
                        if constexpr (kk == 0) { __builtin_amdgcn_s_waitcnt(0x0F70); pin(f0); gld4s<0>(gl[0], glo, p.ws); }
                        if constexpr (kk == 1) { gld4s<0>(gl[1], glo, gb1); gld4s<0>(gl[2], glo, gb2); }
                        if constexpr (kk == 3) ne1(glate, tagv, f0);
                    }
                    if constexpr (pv >= 0) ar[pv % RING2] = ld128s(rn, ho[pv / 10] + (pv & 1) * 1024, ks[pv % 10 >> 1]);
                    if constexpr (kk == 2 && m >= 1 && s >= 1 && s < 6) *(f32x4*)(smem + dst[m - 1] + (s - 1) * 1024) = a2[m - 1][s - 1];
                    site<4000 + 1000 * layer + 5 * i + kk>();
                    if constexpr (i >= 37) a2[i - 37][kk] = *(const f32x4*)(smem + srco + (i - 37) * 5120 + kk * 1024);
                    SCHED();
                });
                if constexpr (i == 10) { asm volatile("s_waitcnt lgkmcnt(0)" ::: "memory"); __builtin_amdgcn_s_barrier(); asm volatile("" ::: "memory"); }
                if constexpr (i == 28 && layer == 0) {
#pragma unroll
                    for (int nb = 0; nb < 5; ++nb) { a2[3][nb] = f32x4{zz[nb][0], zz[nb][1], zz[nb][2], zz[nb][3]}; pin(a2[3][nb]); }
                }
                if constexpr (i == 36) {
                    asm volatile("s_waitcnt lgkmcnt(0)" ::: "memory");
                    __builtin_amdgcn_s_barrier(); asm volatile("" ::: "memory");
                }
                if constexpr (STAMPS == 1 && (i == 0 || i == 9 || i == 10 || i == 15 || i == 25 || i == 35 || i == 36)) { STAMP((layer ? 51 : 41) + (i == 0 ? 0 : i == 9 ? 1 : i == 10 ? 2 : i == 15 ? 3 : i == 25 ? 4 : i == 35 ? 5 : 6)) }
                SCHED();
            });
            STAMP(layer ? 18 : 8)
        }
#pragma unroll
        for (int nb = 0; nb < 5; ++nb) a2[0][nb] = (a2[0][nb] + a2[1][nb]) + a2[2][nb];
        asm volatile("s_nop 1" : "+v"(a2[3][0]), "+v"(a2[3][1]), "+v"(a2[3][2]), "+v"(a2[3][3]), "+v"(a2[3][4]),
                     "+v"(a2[0][0]), "+v"(a2[0][1]), "+v"(a2[0][2]), "+v"(a2[0][3]), "+v"(a2[0][4]));
        float ty;
        {
            f32x4 s1, s2;
#pragma unroll
            for (int nb = 0; nb < 5; ++nb) {
                const f32x4 y = a2[3][nb] + a2[0][nb];
                xin[nb] = y;
                if (nb == 0) { s1 = y; s2 = y * y; } else { s1 += y; s2 += y * y; }
            }
            ty = xsum2h(hsum4(s1), hsum4(s2));
            st64(v2u{__float_as_uint(ty), tag0}, rn, sgs);
        }
        SCHED();
        STAMP(layer ? 19 : 9)
        if (layer == 0 && __builtin_expect(glate != 0ull, 0)) {
            asm volatile("s_waitcnt vmcnt(0)" : "+v"(gl[0]), "+v"(gl[1]), "+v"(gl[2])::"memory");
            WAVE_WAIT({ f0 = ld64(rs, fgo); ok = ISTAG(f0); })
            gld4t<0>(gl[0], glo, p.ws); gld4t<0>(gl[1], glo, gb1); gld4t<0>(gl[2], glo, gb2);
            asm volatile("s_waitcnt vmcnt(0)" : "+v"(gl[0]), "+v"(gl[1]), "+v"(gl[2])::"memory");
        }
        if (layer == 0) {
            asm volatile("s_waitcnt vmcnt(1)" : "+v"(gl[0]), "+v"(gl[1]), "+v"(gl[2])::"memory");
            *(v4u*)(smem + gw) = gl[0]; *(v4u*)(smem + gw + 1280) = gl[1]; *(v4u*)(smem + gw + 1920) = gl[2];
            asm volatile("s_waitcnt lgkmcnt(0)" ::: "memory");
            __builtin_amdgcn_s_barrier(); asm volatile("" ::: "memory");
#pragma unroll
            for (int nb = 0; nb < 5; ++nb) gt[nb] = __builtin_bit_cast(v2u, trread(smem, gr + 640 * nb));
        } else {
            ACC_TIE5(gt);
            if constexpr (OTR) {
#pragma unroll
                for (int nb = 0; nb < 5; ++nb) *(f32x4*)(smem + otw + nb * 64) = xin[nb];
#pragma unroll
                for (int nb = 0; nb < 5; ++nb) xin[nb] = *(const f32x4*)(smem + (nb < 4 ? otr0 + (nb & 1) * 8 * 336 + (nb >> 1) * 128 : otr4));
            }
        }
        SCHED();
        auto sgp = LAMBDA(p_) {
            constexpr int P = layer ? SGP1 : SGP0;
            static_for<3>(LAMBDA(k_) { constexpr int d = P >> (16 - 8 * k_.value); if constexpr (((d >> 4) & 15) == p_.value) { gran_req<32 + 8 * k_.value, d & 15>(sgo, gn); SCHED(); } });
        };
        sgp(IC(0));
        f32x4 gf[5], yg[5];
#pragma unroll
        for (int nb = 0; nb < 5; ++nb) { gf[nb] = unpk4(gt[nb]); yg[nb] = xin[nb] * gf[nb]; pin(gf[nb]); pin(yg[nb]); }
        SCHED();
        sgp(IC(1));
        int lane_o;
        asm volatile("v_mbcnt_lo_u32_b32 %0, -1, 0\n\tv_mbcnt_hi_u32_b32 %0, -1, %0" : "=v"(lane_o));
        const int lane = lane_o, c = lane & 15, q = lane >> 4;
        unsigned xs[3];
#pragma unroll
        for (int i = 0; i < 3; ++i) {
            const int ch = 10 * nt2 + 4 * i + 2 * (q & 1) + (q >> 1);
            xs[i] = N_X2 + (unsigned)((2 * (mt2 & 1) + (w >> 1)) * 5 * NB_ + (ch >> 4) * NB_ + ((c >> 2) & 1) * 4096 + (ch & 15) * 256 + (8 * (w & 1) + 4 * (c >> 3) + (c & 3)) * 16);
        }
        unsigned pso = q < 2 ? N_PS + (unsigned)((((mt2 & 1) * 4 + w) * 8 + nt2) * 128 + c * 8 + q * 4) : kNone;
        unsigned fxo1 = N_FX + (unsigned)(((2 * (mt2 & 1) + (w >> 1)) * 16 + nt2 * 2 + (w & 1)) * 128);
        unsigned oo = OTR ? (unsigned)(((64 * mt2 + 16 * w + (lane >> 3)) * D_ + 80 * nt2) * 4 + (nt2 & 1) * 64 + (lane & 7) * 16) : (unsigned)(((64 * mt2 + 16 * w + c) * D_ + 80 * nt2 + 4 * q) * 4);
        unsigned oo1 = oo + 8 * D_ * 4, oo4 = (unsigned)(((64 * mt2 + 16 * w + (lane >> 3) + (lane & 4) * 2) * D_ + 80 * nt2) * 4 + 256 - (nt2 & 1) * 256 + (lane & 3) * 16);
        int bpi = (lane >> 3) * 4;
        u64 up = 0xF0F0F0F0F0F0F0F0ull;
        xs[2] = (q & 1) ? kNone : xs[2];
        fxo1 = lane ? kNone : fxo1;
        if (layer == 0) { pin(xs[0]); pin(xs[1]); pin(xs[2]); pin(pso); pin(fxo1); } else { pin(oo); if (OTR) { pin(oo1); pin(oo4); pin(bpi); pins(up); } }
        SCHED();
        {
            float a, d;
            sgp(IC(2));
            room<(layer ? 900 : 1500)>(); site<9 + 10 * layer>();
            pipe_gran(a, d, sgo, gn, tag0, dead);
            STAMP(layer ? 20 : 10)
            xsum2(a, d);
            const float me = a * (1.0f / D_);
            const float ri = __builtin_amdgcn_rsqf(fmaxf(d * (1.0f / D_) - me * me, 0.f) + 1e-5f), mr = -me * ri;
            float rv[3] = {ri, ri, ri}, mv[3] = {mr, mr, mr};
            if constexpr (layer == 1 && OTR) {
#pragma unroll
                for (int i = 0; i < 2; ++i) { rv[i] = __int_as_float(__builtin_amdgcn_ds_bpermute(bpi + 32 * i, __float_as_int(ri))); mv[i] = __int_as_float(__builtin_amdgcn_ds_bpermute(bpi + 32 * i, __float_as_int(mr))); }
                SCHED();
            }
            static_for<5>(LAMBDA(n_) {
                constexpr int nb = layer ? n_.value : (n_.value + 4) % 5;
                if constexpr (layer == 1 && OTR && nb == 4) { sel1(rv[2], __float_as_uint(rv[0]), __float_as_uint(rv[1]), up); sel1(mv[2], __float_as_uint(mv[0]), __float_as_uint(mv[1]), up); }
                xin[nb] = yg[nb] * rv[nb < 4 ? nb & 1 : 2] + (gf[nb] * mv[nb < 4 ? nb & 1 : 2] + xin[nb]);
                if constexpr (layer == 1) { __builtin_amdgcn_raw_buffer_store_b128(__builtin_bit_cast(v4u, xin[nb]), ro, !OTR ? oo + 64 * nb : nb == 4 ? oo4 : ((nb & 1) ? oo1 : oo) + (nb >> 1) * 128, 0, OUTP); SCHED(); }
                else if constexpr (nb & 5) {
                    const f32x4 xa = xin[nb & 6], xb = xin[nb];
                    const auto r0 = __builtin_amdgcn_permlane16_swap(cvtpk(xa.x, xa.y), cvtpk(xb.x, xb.y), false, false);
                    const auto r1 = __builtin_amdgcn_permlane16_swap(cvtpk(xa.z, xa.w), cvtpk(xb.z, xb.w), false, false);
                    st128(v4u{r0[0], r1[0], r0[1], r1[1]}, rn, xs[nb >> 1]);
                    SCHED();
                }
            });
        }
        if constexpr (layer == 0) {
            {
                f32x4 s1, s2;
#pragma unroll
                for (int nb = 0; nb < 5; ++nb) { const f32x4 x = xin[nb]; if (nb == 0) { s1 = x; s2 = x * x; } else { s1 += x; s2 += x * x; } }
                __builtin_amdgcn_raw_buffer_store_b32(__float_as_uint(xsum2h(hsum4(s1), hsum4(s2))), rn, pso, 0, SC1);
            }
            SCHED();
            site<12>();
        int wa[2][3];
#pragma unroll
        for (int i = 0; i < 2; ++i) {
            wa[i][0] = (c >> 3) * 20480 + (c & 7) * 128 + (((4 * i + q) ^ (c & 6)) * 16); wa[i][1] = (wa[i][0] ^ 16) + 10240;
            wa[i][2] = 81920 + (c & 1) * 10240 + (c >> 1) * 128 + (((4 * i + q) ^ ((c & 1) | ((c >> 2) << 1))) * 16);
            pin(wa[i][0]); pin(wa[i][1]); pin(wa[i][2]);
        }
            unsigned fxo = N_FX + (unsigned)(w * 2048 + (lane >> 2) * 128);
            unsigned xo = N_X2 + (unsigned)(w * 5 * NB_ + lane * 16), xo1 = xo + 4096u;
            const int row = 8 * q + (c & 7);
            unsigned po = N_PS + (unsigned)(((2 * w + (row >> 4)) * 8 + 4 * (c >> 3)) * 128 + (row & 15) * 8);
            int rsw = l_rs(1, 0) + w * INBOX + lane * 4;
            pin(rsw); pin(fxo); pin(xo); pin(xo1); pin(po);
            v4u wf[2][5];
#pragma unroll
            for (int nb = 0; nb < 5; ++nb) wf[0][nb] = WF(nb, 0);
            SCHED();
            DRAIN();
            st64(tagv, rn, fxo1);
            if (b == 255 && w == 0) FLAG(OFF_NONCE)
            STAMP(11)
            if (w == 0) tour<4>();
            if constexpr (OTR) {
                const int sh = (nt2 & 1) * 64, r4 = (lane >> 3) + (lane & 4) * 2;
                const int a0 = c * 336 + q * 16, a1 = (lane >> 3) * 336 + sh + (lane & 7) * 16, a2_ = r4 * 336 + 256 - 4 * sh + (lane & 3) * 16;
                char* tw = smem + SC + w * INBOX + 12288;
#pragma unroll
                for (int nb = 0; nb < 5; ++nb) *(v2u*)(tw + a0 / 2 + nb * 32) = gt[nb];
#pragma unroll
                for (int nb = 0; nb < 5; ++nb) gt[nb] = *(const v2u*)(tw + (nb < 4 ? a1 / 2 + (nb & 1) * 8 * 168 + (nb >> 1) * 64 : a2_ / 2));
                otw = SC + w * INBOX + a0; otr0 = SC + w * INBOX + a1; otr4 = SC + w * INBOX + a2_;
                pin(otw); pin(otr0); pin(otr4);
            }
            addrs(std::integral_constant<int, 1>{}, lane, w, b);
            ACC_TIE5(gt);
            v2u pt[4];
            skip<SKIP5>();
            pipe_flag<KPF0>(pt, fxo, gn, tagv, dead);
            ORDER();
            STAMP(12)
            v4u ar[2 * RING1];
#define XF(n) ld128s(rn, (((n) & 1) ? xo1 : xo) + (((n) >> 1) & 3) * 1024, ((n) >> 3) * NB_)
#pragma unroll
            for (int n = 0; n < KI1; ++n) { ar[n] = XF(n); SCHED(); }
            v2u ps[4];
            float t1, t2, rinv, mri, rr_[8], rm_[8];
            static_for<20>(LAMBDA(s_) {
                constexpr int s = s_.value, cu = s & 1, nx = cu ^ 1;
                LGKM0() SCHED();
                static_for<10>(LAMBDA(g_) {
                    constexpr int g = g_.value, mb = g / 5, k = g % 5;
                    if constexpr (s == 0) mf0(acc[mb][k], ar[mb], wf[0][k]); else mf(acc[mb][k], ar[(2 * s + mb) % (2 * RING1)], wf[cu][k]);
                    SCHED();
                    if constexpr (s == 0 && g == 0) { pin(pt[0]); pin(pt[1]); pin(pt[2]); pin(pt[3]); }
                    if constexpr (s < 19 && g < 5) wf[nx][g] = WF(g, s + 1);
                    constexpr int rq = ring_req(10 * s + g, KI1, 2 * RING1, 4);
                    if constexpr (rq >= 100) ps[rq - 100] = ld64(rn, po + (rq - 100) * 128);
                    else if constexpr (rq >= 0) ar[rq % (2 * RING1)] = XF(rq);
                    constexpr int n = g >= 6 ? 4 * (s - KS1) + g - 6 : -1;
                    if constexpr (n == 0) { t1 = __uint_as_float(ps[0].x) + __uint_as_float(ps[1].x); pin(t1); t2 = __uint_as_float(ps[0].y) + __uint_as_float(ps[1].y); pin(t2); }
                    if constexpr (n == 1) { t1 += __uint_as_float(ps[2].x); pin(t1); t2 += __uint_as_float(ps[2].y); pin(t2); }
                    if constexpr (n == 2) { t1 += __uint_as_float(ps[3].x); pin(t1); t2 += __uint_as_float(ps[3].y); pin(t2); }
                    if constexpr (n == 3) { t1 += dpp<0x128>(t1); t2 += dpp<0x128>(t2); pin(t1); pin(t2); }
                    if constexpr (n == 4) { t1 *= (1.0f / D_); pin(t1); t2 *= (1.0f / D_); pin(t2); }
                    if constexpr (n == 5) { t2 = fmaxf(t2 - t1 * t1, 0.f); pin(t2); }
                    if constexpr (n == 6) { rinv = __builtin_amdgcn_rsqf(t2 + 1e-5f); pin(rinv); }
                    if constexpr (n == 8) { mri = -t1 * rinv; pin(mri); }
                    if constexpr (n == 9) { t1 = rinv; mrg8(t1, mri); }
                    if constexpr (n == 10) *(float*)(smem + rsw) = t1;
                    if constexpr (n >= 11 && n < 19) bc2b<n - 11>(rr_[n - 11], rm_[n - 11], rinv, mri);
                    if constexpr (n >= 19 && n < 39) TT_GAP(n - 19)
                    site<6000 + 10 * s + g>();
                    SCHED();
                });
            });
            rr[0] = f32x4{rr_[0], rr_[1], rr_[2], rr_[3]}; rr[1] = f32x4{rr_[4], rr_[5], rr_[6], rr_[7]};
            ACC_FENCE10(acc[0], acc[1]);
            STAMP(13)
        } else {
            STAMP(21)
            STAMP_DUMP()
            asm volatile("kpf_end:");
        }
    };
    layer_body(std::integral_constant<int, 0>{});
    layer_body(std::integral_constant<int, 1>{});
}

extern "C" void kernel_launch(void* const* d_in, const int* in_sizes, int n_in,
                              void* d_out, int out_size, void* d_ws, size_t ws_size,
                              hipStream_t stream) {
    Params p;
    p.x0 = (const u16*)d_in[0];
    p.Lre = (const u16*)d_in[1];
    p.Lim = (const u16*)d_in[2];
    p.Bre = (const u16*)d_in[3];
    p.Bim = (const u16*)d_in[4];
    p.Cre = (const u16*)d_in[5];
    p.Cim = (const u16*)d_in[6];
    p.logdt = (const u16*)d_in[7];
    p.Dskip = (const u16*)d_in[8];
    p.k = (const u16*)d_in[10];
    p.v = (const u16*)d_in[11];
    p.shk = (const u16*)d_in[12];
    p.ssmk = (const u16*)d_in[13];
    p.ssmD = (const u16*)d_in[14];
    p.ws = (char*)d_ws;
    p.out = (float*)d_out;
    k_main<<<256, 256, 0, stream>>>(p);
}
```

```cpp
#include <hip/hip_runtime.h>
#include <stdint.h>
#include <utility>

#define STAMPS 0

typedef unsigned short u16;
typedef unsigned long long u64;
typedef unsigned v4u __attribute__((ext_vector_type(4)));
typedef unsigned v2u __attribute__((ext_vector_type(2)));
typedef float f32x4 __attribute__((ext_vector_type(4)));
typedef float f32x2 __attribute__((ext_vector_type(2)));
typedef short s16x4 __attribute__((ext_vector_type(4)));
typedef __bf16 bf16x2v __attribute__((ext_vector_type(2)));
#define LAS __attribute__((address_space(3)))
#define DI __device__ __forceinline__

constexpr int S_ = 2048, D_ = 640;
#define KR1 4
#define KR2 8
constexpr int RING1 = KR1;
#define KCR1 4
constexpr int kCR1 = KCR1;
#define KXA 0
#define KN3E 16
#define KN3 12
constexpr int kXA = KXA;
constexpr int kN3E = KN3E, kN3 = KN3;
#define KFHK 9
#define KFHP 7
constexpr int kFHK = KFHK, kFHP = KFHP;
#define KGPI 31
#define KGCI 36
#define CPF 0
#define LBE 0
#define LBR 1
#define W3O 1
#define KI2 6
#define KI1 6
#define KS1 8
#define RSL 2
#define KR20 10
constexpr int kGPI = KGPI, kGCI = KGCI;

#define KPAD 0
#define KPS 0
#define GPAD 0
constexpr int KSTR = 4096 + KPAD, GSTR = 4096 + GPAD;
constexpr unsigned OFF_NONCE = 0;
constexpr unsigned OFF_ST = 4096;
constexpr unsigned OFF_FW = OFF_ST + 65536;
constexpr unsigned OFF_FK = OFF_FW + 131072;
constexpr unsigned OFF_VT = OFF_FK + 65536;
constexpr unsigned OFF_FG = OFF_FK + 81920;
constexpr unsigned OFF_EG = OFF_FG + 98304;
constexpr unsigned OFF_KVT = OFF_EG + 327680;
constexpr unsigned OFF_GT = OFF_KVT + 640 * 4608;
constexpr unsigned OFF_W2F = OFF_GT + 640 * 4608;
constexpr unsigned WS_BYTES = OFF_W2F + 1638400;
constexpr unsigned OFF_GRP = 0x2000000, NB_ = 16384;
constexpr unsigned N_HF = 0;
constexpr unsigned N_X2 = 80 * NB_;
constexpr unsigned N_SG = 100 * NB_;
constexpr unsigned N_PS = 108 * NB_;
constexpr unsigned N_FH = 110 * NB_;
constexpr unsigned N_FX = 114 * NB_;
constexpr unsigned N_END = 116 * NB_;
constexpr unsigned kNone = 0x40000000u;
constexpr unsigned OFF_DBG = 0x1800000;
#define TILES() const int xq = b & 7, j = b >> 3, mt1 = 2 * xq + (j >> 4), nt1 = j & 15, mt2 = 4 * xq + (j >> 3), nt2 = j & 7, i32 = 16 * (mt2 & 1) + mt1;

constexpr int SC = 102400;
constexpr int REGSZ = 13056;
constexpr int L_XR = SC;
constexpr int L_CO = SC + 12288;
constexpr int L_KB = SC + 51712;
constexpr int L_TAB = SC + 55808;
constexpr int L_CW = SC + 39168;
constexpr int INBOX = 15360;
constexpr int l_rs(int l, int w) { return l ? SC + w * INBOX : L_KB + w * 256; }
constexpr int l_sg(int l, int w) { return l ? SC + w * INBOX + 256 : SC + (w == 0 ? 43264 : w == 1 ? 46080 : w == 2 ? 52736 : 56320); }
constexpr int l_tb(int l, int w) { return l ? SC + w * INBOX + 2048 : SC + 40960 + w * 5120; }
constexpr int LDS_BYTES = 163840;
constexpr int CV_E = 4608;
constexpr int CV_O = CV_E + (1056 + 15) * 4;

struct Params {
    const u16 *x0, *Lre, *Lim, *Bre, *Bim, *Cre, *Cim, *logdt, *Dskip, *k, *v, *shk, *ssmk, *ssmD;
    char* ws;
    float* out;
};

#define MF "v_mfma_f32_16x16x32_bf16 "
DI void mf(f32x4& c, v4u a, v4u b) { asm(MF "%0, %1, %2, %0" : "+v"(c) : "v"(a), "v"(b)); }
DI void mf0(f32x4& c, v4u a, v4u b) { asm(MF "%0, %1, %2, 0" : "=&v"(c) : "v"(a), "v"(b)); }
constexpr int W2R = 56;
#define WA(n) "a[%c" #n ":%c" #n "+3]"
template <int R> DI void mfa(f32x4& c, v4u a) { asm(MF "%0, " WA(2) ", %1, %0" : "+v"(c) : "v"(a), "i"(W2R + 4 * R)); }
template <int R> DI void mfa0(f32x4& c, v4u a) { asm(MF "%0, " WA(2) ", %1, 0" : "=&v"(c) : "v"(a), "i"(W2R + 4 * R)); }
#define ACC_FENCE10(c, d) asm volatile("" : "+v"(c[0]), "+v"(c[1]), "+v"(c[2]), "+v"(c[3]), "+v"(c[4]), "+v"(d[0]), "+v"(d[1]), "+v"(d[2]), "+v"(d[3]), "+v"(d[4]))
#define ACC_TIE5(c) asm volatile("" : "+v"(c[0]), "+v"(c[1]), "+v"(c[2]), "+v"(c[3]), "+v"(c[4]))
#define QP " quad_perm:[1,0,3,2] row_mask:0xf bank_mask:0xf"
struct St { f32x2 a, b; float c; };
struct Lm { f32x2 a, b; float r, s; };
#define PK1 " op_sel_hi:[0,1,1]\n\t"
#define PK2 " op_sel:[1,1,0] op_sel_hi:[1,0,1] neg_lo:[1,0,0]"
#define ST_(t0, t1, t2, h0, h1, h2, b0, b1, b2, l0, l1, l2, l3)                                                                                        \
    "v_pk_fma_f32 %" #t0 ", %" #l0 ", %" #h0 ", %" #b0 PK1 "v_pk_fma_f32 %" #t1 ", %" #l1 ", %" #h1 ", %" #b1 PK1 "v_fma_f32 %" #t2 ", %" #l2 ", %" #h2 ", %" #b2 "\n\t" \
    "v_pk_fma_f32 %" #t0 ", %" #l0 ", %" #h0 ", %" #t0 PK2 "\n\tv_pk_fma_f32 %" #t1 ", %" #l1 ", %" #h1 ", %" #t1 PK2 "\n\tv_fmac_f32_dpp %" #t2 ", %" #h2 ", %" #l3 QP "\n\t"
#define SO(t) "=&v"(t.a), "=&v"(t.b), "=&v"(t.c)
#define SI(t) "v"(t.a), "v"(t.b), "v"(t.c)
#define LI(l) "v"(l.a), "v"(l.b), "v"(l.r), "v"(l.s)
DI void run3(St& t, const St& h, const St* b, const Lm& l) {
    St u;
    asm(ST_(0, 1, 2, 6, 7, 8, 9, 10, 11, 18, 19, 20, 21) ST_(3, 4, 5, 0, 1, 2, 12, 13, 14, 18, 19, 20, 21) ST_(0, 1, 2, 3, 4, 5, 15, 16, 17, 18, 19, 20, 21)
        : SO(t), SO(u) : SI(h), SI(b[0]), SI(b[1]), SI(b[2]), LI(l));
}
DI void run4(St& t, const St& h, const St* b, const Lm& l) {
    St u;
    asm(ST_(3, 4, 5, 6, 7, 8, 9, 10, 11, 21, 22, 23, 24) ST_(0, 1, 2, 3, 4, 5, 12, 13, 14, 21, 22, 23, 24) ST_(3, 4, 5, 0, 1, 2, 15, 16, 17, 21, 22, 23, 24) ST_(0, 1, 2, 3, 4, 5, 18, 19, 20, 21, 22, 23, 24)
        : SO(t), SO(u) : SI(h), SI(b[0]), SI(b[1]), SI(b[2]), SI(b[3]), LI(l));
}
#define AC_(t0, t2, h0, h2, b0, b2, l0, l2, l3)                                                                                   \
    "v_pk_fma_f32 %" #t0 ", %" #l0 ", %" #h0 ", %" #b0 PK1 "v_fma_f32 %" #t2 ", %" #l2 ", %" #h2 ", %" #b2 "\n\t"               \
    "v_pk_fma_f32 %" #t0 ", %" #l0 ", %" #h0 ", %" #t0 PK2 "\n\tv_fmac_f32_dpp %" #t2 ", %" #h2 ", %" #l3 QP "\n\t"
#define AO(t) "=&v"(t.a), "=&v"(t.c)
#define AI(t) "v"(t.a), "v"(t.c)
DI void keep14(St* t, const St& h, const St& x, const St* b, const Lm& lq, const Lm& l) {
    St u;
    asm(AC_(8, 9, 10, 11, 12, 13, 22, 23, 24) AC_(0, 1, 8, 9, 14, 15, 25, 26, 27) AC_(2, 3, 0, 1, 16, 17, 25, 26, 27) AC_(4, 5, 2, 3, 18, 19, 25, 26, 27) AC_(6, 7, 4, 5, 20, 21, 25, 26, 27)
        : AO(t[0]), AO(t[1]), AO(t[2]), AO(t[3]), AO(u) : AI(h), AI(x), AI(b[0]), AI(b[1]), AI(b[2]), AI(b[3]), "v"(lq.a), "v"(lq.r), "v"(lq.s), "v"(l.a), "v"(l.r), "v"(l.s));
}
DI void keep4(St* t, const St& h, const St* b, const Lm& l) {
    asm(AC_(0, 1, 8, 9, 10, 11, 18, 19, 20) AC_(2, 3, 0, 1, 12, 13, 18, 19, 20) AC_(4, 5, 2, 3, 14, 15, 18, 19, 20) AC_(6, 7, 4, 5, 16, 17, 18, 19, 20)
        : AO(t[0]), AO(t[1]), AO(t[2]), AO(t[3]) : AI(h), AI(b[0]), AI(b[1]), AI(b[2]), AI(b[3]), "v"(l.a), "v"(l.r), "v"(l.s));
}
DI void b3cv(f32x2* t, unsigned* o, f32x2 h, f32x2 b0, f32x2 b1, f32x2 b2, f32x2 l0, f32x2 l, const float* e) {
    asm("v_pk_fma_f32 %0, %11, %7, %8" PK1 "v_cvt_pk_bf16_f32 %3, %13, %14\n\tv_pk_fma_f32 %0, %11, %7, %0" PK2 "\n\tv_cvt_pk_bf16_f32 %4, %15, %16\n\t"
        "v_pk_fma_f32 %1, %12, %0, %9" PK1 "v_cvt_pk_bf16_f32 %5, %17, %18\n\tv_pk_fma_f32 %1, %12, %0, %1" PK2 "\n\tv_cvt_pk_bf16_f32 %6, %19, %20\n\t"
        "v_pk_fma_f32 %2, %12, %1, %10" PK1
        : "=&v"(t[0]), "=&v"(t[1]), "=&v"(t[2]), "=&v"(o[0]), "=&v"(o[1]), "=&v"(o[2]), "=&v"(o[3])
        : "v"(h), "v"(b0), "v"(b1), "v"(b2), "v"(l0), "v"(l), "v"(e[0]), "v"(e[1]), "v"(e[2]), "v"(e[3]), "v"(e[4]), "v"(e[5]), "v"(e[6]), "v"(e[7]));
}
DI void bfin(f32x2& t, f32x2 h, f32x2 l) { asm("v_pk_fma_f32 %0, %2, %1, %0" PK2 : "+v"(t) : "v"(h), "v"(l)); }
#define UNDEF8(c) asm volatile("" : "=v"(c[0]), "=v"(c[1]), "=v"(c[2]), "=v"(c[3]), "=v"(c[4]), "=v"(c[5]), "=v"(c[6]), "=v"(c[7]))
DI unsigned cvtpk(float lo, float hi) {
    f32x2 v = {lo, hi};
    return __builtin_bit_cast(unsigned, __builtin_convertvector(v, bf16x2v));
}
DI float lo16f(unsigned u) { return __uint_as_float(u << 16); }
DI float hi16f(unsigned u) { return __uint_as_float(u & 0xffff0000u); }
DI float rbf(float v) { return lo16f(cvtpk(v, 0.f)); }
template <int CTRL> DI float dpp(float x) {
    return __int_as_float(__builtin_amdgcn_update_dpp(0, __float_as_int(x), CTRL, 0xF, 0xF, true));
}
DI float rowsum16(float x) {
    x += dpp<0x128>(x); x += dpp<0x124>(x); x += dpp<0x122>(x); x += dpp<0x121>(x);
    return x;
}
DI float xsum32(float x) { auto r = __builtin_amdgcn_permlane32_swap(__float_as_uint(x), __float_as_uint(x), false, false); return __uint_as_float(r[0]) + __uint_as_float(r[1]); }
DI void xsum2(float& a, float& d) {
    auto r = __builtin_amdgcn_permlane16_swap(__float_as_uint(a), __float_as_uint(d), false, false);
    const float s = __uint_as_float(r[0]) + __uint_as_float(r[1]);
    const float t = xsum32(s);
    auto u = __builtin_amdgcn_permlane16_swap(__float_as_uint(t), __float_as_uint(t), false, false);
    a = __uint_as_float(u[0]); d = __uint_as_float(u[1]);
}
DI float xsum2h(float a, float d) {
    auto r = __builtin_amdgcn_permlane16_swap(__float_as_uint(a), __float_as_uint(d), false, false);
    return xsum32(__uint_as_float(r[0]) + __uint_as_float(r[1]));
}
DI float hsum4(f32x4 v) { return (v.x + v.y) + (v.z + v.w); }
typedef __amdgpu_buffer_rsrc_t rsrc_t;
constexpr int SC1 = 16;
DI v4u ld128(rsrc_t rs, unsigned off) { return __builtin_amdgcn_raw_buffer_load_b128(rs, off, 0, SC1); }
DI v4u ld128s(rsrc_t rs, unsigned off, unsigned soff) { return __builtin_amdgcn_raw_buffer_load_b128(rs, off, soff, SC1); }
DI v2u ld64(rsrc_t rs, unsigned off) { return __builtin_amdgcn_raw_buffer_load_b64(rs, off, 0, SC1); }
DI void st128(v4u v, rsrc_t rs, unsigned off) { __builtin_amdgcn_raw_buffer_store_b128(v, rs, off, 0, SC1); }
DI void st64(v2u v, rsrc_t rs, unsigned off) { __builtin_amdgcn_raw_buffer_store_b64(v, rs, off, 0, SC1); }
#define LGKM0() { __builtin_amdgcn_s_waitcnt(0xC07F); asm volatile("" ::: "memory"); }
#define DRAIN() asm volatile("s_waitcnt vmcnt(0)" ::: "memory")
#define ORDER() __builtin_amdgcn_fence(__ATOMIC_ACQUIRE, "wavefront")
#define SCHED() __builtin_amdgcn_sched_barrier(0)
constexpr unsigned kSpinCap = 1u << 17;
#define WAVE_WAIT(...)                                                      \
    {                                                                       \
        unsigned spins_ = dead ? kSpinCap : 0u;                             \
        _Pragma("nounroll") for (;;) {                                      \
            asm volatile("" ::: "memory");                                  \
            bool ok;                                                        \
            __VA_ARGS__;                                                    \
            if (__builtin_amdgcn_ballot_w64(!ok) == 0ull) break;            \
            if (++spins_ > kSpinCap) { dead = true; break; }                \
            __builtin_amdgcn_s_sleep(1);                                    \
        }                                                                   \
        ORDER();                                                            \
    }
#define KPD1 1
#define KPDS 5
#define KPDS0 5
#define KPF0 1
#define KPH0 12
#define KPF1 4
constexpr int PD1 = KPD1, PDS = KPDS, PDS0 = KPDS0;
#define PL(r) "global_load_dwordx2 " r ", %5, %6 sc1\n\t"
#define PS_(r) "s_waitcnt vmcnt(1)\n\tv_cmp_ne_u64 vcc, %7, " r "\n\ts_cbranch_vccz 2f\n\t" PL(r)
template <int D0> DI void pipe_flag(v2u (&t)[4], unsigned off, const void* base, v2u tag, bool& dead) {
    unsigned n = dead ? 0u : kSpinCap;
    asm volatile("s_sleep %8\n\t" PL("%0") "s_sleep %9\n\t" PL("%1")
                 "1:\n\t" PS_("%0") PS_("%1") "s_sub_u32 %4, %4, 1\n\ts_cbranch_scc0 1b\n2:"
                 : "=&v"(t[0]), "=&v"(t[1]), "=&v"(t[2]), "=&v"(t[3]), "+s"(n) : "v"(off), "s"(base), "v"(tag), "i"(D0), "i"(KPF1) : "memory", "vcc", "scc");
    dead = __builtin_amdgcn_readfirstlane(n) == ~0u;
}
#define WAVE_RETRY(OK, ...) { if (__builtin_expect(__builtin_amdgcn_ballot_w64(!(OK)) != 0ull, 0)) WAVE_WAIT({ __VA_ARGS__; ok = OK; }) ORDER(); }
#define STAMP(i)
#define STAMP_DUMP()

#define PADS 9000, 1000, 1059, 1088, 2085, 9001, 9002, 4071, 9, 9003, 6, 9004, 9005, 6107, 14, 15, 9006, 18, 19, 9007, 21, 2, 9008
#define TW 2
#define TW0 2
#define KLB0 0
#define KLB1 4
#define SKIP0 0
#define SKIP5 0
constexpr int kPads[] = {PADS};
constexpr int pad_next(int id) {
    for (unsigned i = 0; i + 1 < sizeof(kPads) / sizeof(int); ++i) if (kPads[i] == id) return kPads[i + 1];
    return -1;
}
constexpr bool pads_unique() {
    for (unsigned i = 0; i < sizeof(kPads) / sizeof(int); ++i)
        for (unsigned k = 0; k < i; ++k) if (kPads[i] == kPads[k]) return false;
    return true;
}
static_assert(pads_unique(), "");
template <int ID> DI void site() {
    if constexpr (pad_next(ID) >= 0) asm volatile("kp%c0:\n\ts_cbranch_execz kp%c1" ::"i"(ID), "i"(pad_next(ID)));
    else asm volatile("");
}
template <int T> DI void tour() {
    if constexpr (pad_next(9000 + T) >= 0 && pad_next(9000 + T) != 9001 + T) asm volatile("s_mov_b64 exec, 0\n\ts_branch kp%c0\nkp%c1:\n\ts_mov_b64 exec, -1" ::"i"(pad_next(9000 + T)), "i"(9001 + T));
}
template <int N> DI void skip() { if constexpr (N > 0) asm volatile("s_branch 1f\n\t.rept %c0\n\ts_nop 0\n\t.endr\n1:" ::"i"(N)); }
template <int N> DI void room() { asm volatile("s_branch 1f\n\t.p2align 12, , %c0\n1:" ::"i"(N)); }
template <class F, int... I> DI void static_for_impl(std::integer_sequence<int, I...>, F&& f) { (f(std::integral_constant<int, I>{}), ...); }
template <int N, class F> DI void static_for(F&& f) { static_for_impl(std::make_integer_sequence<int, N>{}, f); }
#define LAMBDA(v) [&](auto v) __attribute__((always_inline))
DI f32x2 cmul(f32x2 a, f32x2 b) {
    f32x2 t;
    asm("v_pk_mul_f32 %0, %1, %2 op_sel_hi:[0,1]\n\ts_nop 0\n\tv_pk_fma_f32 %0, %1, %2, %0" PK2 "\n\ts_nop 0" : "=&v"(t) : "v"(a), "v"(b));
    return t;
}
DI f32x2 cfma(f32x2 a, f32x2 b, f32x2 c) {
    f32x2 t;
    asm("v_pk_fma_f32 %0, %1, %2, %3 op_sel_hi:[0,1,1]\n\ts_nop 0\n\tv_pk_fma_f32 %0, %1, %2, %0" PK2 "\n\ts_nop 0" : "=&v"(t) : "v"(a), "v"(b), "v"(c));
    return t;
}
DI void cfma4(f32x2 (&t)[4], f32x2 a, f32x2 b0, f32x2 b1, f32x2 b2, f32x2 b3, f32x2 c0, f32x2 c1, f32x2 c2, f32x2 c3) {
    asm("v_pk_fma_f32 %0, %4, %5, %9 op_sel_hi:[0,1,1]\n\tv_pk_fma_f32 %1, %4, %6, %10 op_sel_hi:[0,1,1]\n\tv_pk_fma_f32 %2, %4, %7, %11 op_sel_hi:[0,1,1]\n\tv_pk_fma_f32 %3, %4, %8, %12 op_sel_hi:[0,1,1]\n\t"
        "v_pk_fma_f32 %0, %4, %5, %0" PK2 "\n\tv_pk_fma_f32 %1, %4, %6, %1" PK2 "\n\tv_pk_fma_f32 %2, %4, %7, %2" PK2 "\n\tv_pk_fma_f32 %3, %4, %8, %3" PK2 "\n\ts_nop 0"
        : "=&v"(t[0]), "=&v"(t[1]), "=&v"(t[2]), "=&v"(t[3]) : "v"(a), "v"(b0), "v"(b1), "v"(b2), "v"(b3), "v"(c0), "v"(c1), "v"(c2), "v"(c3));
}
#define C4(t0, t1, t2, t3, a, b0, b1, b2, b3, c0, c1, c2, c3)                                                                                                         \
    "v_pk_fma_f32 %" #t0 ", %" #a ", %" #b0 ", %" #c0 PK1 "v_pk_fma_f32 %" #t1 ", %" #a ", %" #b1 ", %" #c1 PK1 "v_pk_fma_f32 %" #t2 ", %" #a ", %" #b2 ", %" #c2 PK1 "v_pk_fma_f32 %" #t3 ", %" #a ", %" #b3 ", %" #c3 PK1 \
    "v_pk_fma_f32 %" #t0 ", %" #a ", %" #b0 ", %" #t0 PK2 "\n\tv_pk_fma_f32 %" #t1 ", %" #a ", %" #b1 ", %" #t1 PK2 "\n\tv_pk_fma_f32 %" #t2 ", %" #a ", %" #b2 ", %" #t2 PK2 "\n\tv_pk_fma_f32 %" #t3 ", %" #a ", %" #b3 ", %" #t3 PK2 "\n\t"
DI void cfma4n(f32x2 (&t)[4], f32x2 a, f32x2 b0, f32x2 b1, f32x2 b2, f32x2 b3, f32x2 c0, f32x2 c1, f32x2 c2, f32x2 c3) {
    asm(C4(0, 1, 2, 3, 4, 5, 6, 7, 8, 9, 10, 11, 12) : "=&v"(t[0]), "=&v"(t[1]), "=&v"(t[2]), "=&v"(t[3]) : "v"(a), "v"(b0), "v"(b1), "v"(b2), "v"(b3), "v"(c0), "v"(c1), "v"(c2), "v"(c3));
}
DI void agg(unsigned& er, unsigned& ei, f32x2 (&x)[4], f32x2& a, const f32x2 (&u)[4], const f32x2 (&s)[16], f32x2 l8, f32x2 l32, f32x2 l64) {
    f32x2 v[4], b;
    asm(C4(4, 5, 6, 7, 22, 10, 11, 12, 13, 14, 15, 16, 17) C4(0, 1, 2, 3, 22, 4, 5, 6, 7, 18, 19, 20, 21)
        "v_pk_fma_f32 %8, %23, %0, %1" PK1 "v_pk_fma_f32 %9, %23, %2, %3" PK1 "v_pk_fma_f32 %8, %23, %0, %8" PK2 "\n\tv_pk_fma_f32 %9, %23, %2, %9" PK2
        : "=&v"(x[0]), "=&v"(x[1]), "=&v"(x[2]), "=&v"(x[3]), "=&v"(v[0]), "=&v"(v[1]), "=&v"(v[2]), "=&v"(v[3]), "=&v"(a), "=&v"(b)
        : "v"(u[0]), "v"(u[1]), "v"(u[2]), "v"(u[3]), "v"(s[2]), "v"(s[6]), "v"(s[10]), "v"(s[14]), "v"(s[3]), "v"(s[7]), "v"(s[11]), "v"(s[15]), "v"(l8), "v"(l32));
    asm("s_nop 0\n\tv_fma_f32 %0, %2, %6, %4\n\tv_fma_f32 %1, %2, %7, %5\n\tv_fma_f32 %0, -%3, %7, %0\n\tv_fma_f32 %1, %3, %6, %1"
        : "=&v"(er), "=&v"(ei) : "v"(a.x), "v"(a.y), "v"(b.x), "v"(b.y), "v"(l64.x), "v"(l64.y));
}
DI void gacc2(f32x2& h0, f32x2& h1, f32x2 w0, f32x2 w1, v4u e0, v4u e1) {
    const v2u a0 = {e0.x, e0.y}, b0 = {e0.z, e0.w}, a1 = {e1.x, e1.y}, b1 = {e1.z, e1.w};
    asm("v_pk_fma_f32 %0, %4, %2, %0 op_sel_hi:[0,1,1]\n\tv_pk_fma_f32 %1, %6, %3, %1 op_sel_hi:[0,1,1]\n\t"
        "v_pk_fma_f32 %0, %5, %2, %0 op_sel:[0,1,0] op_sel_hi:[0,0,1] neg_lo:[1,0,0]\n\tv_pk_fma_f32 %1, %7, %3, %1 op_sel:[0,1,0] op_sel_hi:[0,0,1] neg_lo:[1,0,0]"
        : "+v"(h0), "+v"(h1) : "v"(w0), "v"(w1), "v"(a0), "v"(b0), "v"(a1), "v"(b1));
}
DI s16x4 trread(char* smem, int byteoff) {
    return __builtin_amdgcn_ds_read_tr16_b64_v4i16((LAS s16x4*)(smem + byteoff));
}

#define CV_LOAD_A(A, a, rr_, hf) { const unsigned* ap_ = (const unsigned*)(smem + ab[(a) >> 1] + (((a) & 1) ? 0 : 512) + 64 * (7 - (rr_)) + 8 * (hf)); A[a][2 * (hf)] = ap_[0]; A[a][2 * (hf) + 1] = ap_[1]; }
#define CV_LOAD_B(B, e, rr_) { B[e] = *(const v4u*)(smem + bb + 64 * (7 - (rr_)) + 512 * (e)); }
template <int R, bool PF, class F, class G>
DI void conv_round(f32x4 (&ca)[8], v4u (&A)[8], const v4u (&B)[8], v4u (&An)[8], v4u (&Bn)[8], const char* smem, const int (&ab)[4], int bb, F&& fill, G&& last) {
    static_for<36>(LAMBDA(g_) {
        constexpr int g = g_.value;
        constexpr int a = g < 8 ? 0 : g < 15 ? 1 : g < 21 ? 2 : g < 26 ? 3 : g < 30 ? 4 : g < 33 ? 5 : g < 35 ? 6 : 7;
        constexpr int e = g - (a * 8 - a * (a - 1) / 2);
        if constexpr (R == 0 && a == 0) mf0(ca[e], A[0], B[e]); else mf(ca[a + e], A[a], B[e]);
        SCHED();
        if constexpr (R == 0 && g < 16 && (g & 3) != 3) { constexpr int n = g - (g >> 2); CV_LOAD_A(A, 2 + (n >> 1), 0, n & 1) SCHED(); }
        if constexpr (PF && g < 32 && (g & 3) != 3) { if constexpr ((g & 3) == 2) CV_LOAD_B(Bn, g >> 2, R + 1) else CV_LOAD_A(An, g >> 2, R + 1, g & 1) SCHED(); }
        site<3000 + 36 * R + g>();
        if constexpr (R == 7) { last(g_); SCHED(); }
        if constexpr ((g & 3) == 3 && g < 28) { fill(std::integral_constant<int, 7 * R + g / 4>{}); SCHED(); }
    });
}
template <int R0, int R1, bool PFL, class F, class G>
DI void conv_part(f32x4 (&ca)[8], v4u (&A0)[8], v4u (&B0)[8], const char* smem, const int (&ab)[4], int bb, F&& fill, G&& last) {
    v4u A1[8], B1[8];
    static_for<R1 - R0>(LAMBDA(r_) {
        constexpr int r = r_.value;
        if constexpr (r & 1) conv_round<R0 + r, (R0 + r + 1 < R1 || PFL)>(ca, A1, B1, A0, B0, smem, ab, bb, fill, last);
        else conv_round<R0 + r, (R0 + r + 1 < R1 || PFL)>(ca, A0, B0, A1, B1, smem, ab, bb, fill, last);
    });
}
template <int IMM> DI void gld(v4u& dst, unsigned voff, const void* sbase) {
    asm volatile("global_load_dwordx4 %0, %1, %2 offset:%3" : "=v"(dst) : "v"(voff), "s"(sbase), "i"(IMM));
}
#define OUTP 16
DI void gldu16(unsigned& dst, unsigned voff, const void* sbase) { asm volatile("global_load_ushort %0, %1, %2" : "=v"(dst) : "v"(voff), "s"(sbase)); }
DI void gld1s(unsigned& dst, unsigned voff, const void* sbase) { asm volatile("global_load_dword %0, %1, %2 sc1" : "=v"(dst) : "v"(voff), "s"(sbase)); }
template <int V> DI void SETM0(unsigned ldsbase) { asm volatile("s_add_u32 m0, %0, %1" ::"s"(ldsbase), "i"(V)); }
template <int GOFF> DI void GLD0(unsigned voff, const char* sbase) { asm volatile("global_load_lds_dwordx4 %0, %1 offset:%2" ::"v"(voff), "s"(sbase), "i"(GOFF) : "memory"); }
template <int GOFF, int V> DI void GLD1(unsigned voff, const char* sbase, unsigned ldsbase) {
    asm volatile("global_load_lds_dwordx4 %0, %1 offset:%3\n\ts_add_u32 m0, %2, %4" ::"v"(voff), "s"(sbase), "s"(ldsbase), "i"(GOFF), "i"(V) : "memory");
}
template <int L, int G> DI void dma_x(unsigned voff, const void* sbase, unsigned ldsbase) {
    asm volatile("s_add_u32 m0, %2, %4\n\ts_nop 0\n\tglobal_load_lds_dwordx4 %0, %1 offset:%3" ::"v"(voff), "s"(sbase), "s"(ldsbase), "i"(G), "i"(L - G) : "memory", "scc");
}
DI void ld_kv(v4u (&a)[15], int on  , unsigned k0, unsigned k1, unsigned k4, unsigned vo, v4u rk, const void* v, unsigned lds) {
    unsigned t;
    asm volatile("s_cmp_ge_u32 %5, 80\n\ts_cbranch_scc1 1f\n\ts_add_u32 m0, %12, %13\n\ts_movk_i32 %4, 0x2800\n\t"
                 "buffer_load_dwordx4 %6, %10, 0 offen nt lds\n\ts_add_u32 m0, m0, 0x400\n\tglobal_load_dwordx4 %0, %9, %11 nt\n\t"
                 "buffer_load_dwordx4 %7, %10, 0 offen nt lds\n\ts_add_u32 m0, m0, 0x400\n\tglobal_load_dwordx4 %1, %9, %11 offset:1280 nt\n\t"
                 "buffer_load_dwordx4 %7, %10, %4 offen nt lds\n\ts_add_u32 m0, m0, 0x400\n\ts_movk_i32 %4, 0x5000\n\tglobal_load_dwordx4 %2, %9, %11 offset:2560 nt\n\t"
                 "buffer_load_dwordx4 %7, %10, %4 offen nt lds\n\ts_add_u32 m0, m0, 0x400\n\tglobal_load_dwordx4 %3, %9, %11 offset:3840 nt\n\t"
                 "buffer_load_dwordx4 %8, %10, 0 offen nt lds\n1:"
                 : "=&v"(a[7]), "=&v"(a[8]), "=&v"(a[9]), "=&v"(a[10]), "=&s"(t)
                 : "s"(on), "v"(k0), "v"(k1), "v"(k4), "v"(vo), "s"(rk), "s"(v), "s"(lds), "i"(L_XR) : "scc", "memory");
}
DI int race(const char* a, const char* b) {
    unsigned u, v;
    int r;
    asm volatile("s_waitcnt lgkmcnt(0)\n\ts_load_dword %1, %3, 0x0\n\ts_load_dword %2, %4, 0x0\n\ts_waitcnt lgkmcnt(1)\n\ts_sleep %5\n\t"
                 "s_getreg_b32 %0, hwreg(HW_REG_IB_STS, 8, 4)\n\ts_waitcnt lgkmcnt(0)"
                 : "=&s"(r), "=&s"(u), "=&s"(v) : "s"(a), "s"(b), "i"(RSL) : "memory");
    return r;
}
DI void ld_wr(v4u (&a)[4], int on, unsigned o, const void* base) {
    asm volatile("s_cmp_ge_u32 %4, 80\n\ts_cbranch_scc0 1f\n\t.rept 4\n\tglobal_load_dword a[32+1], %5, %6\n\t.endr\n\ts_branch 2f\n1:\n\t"
                 "global_load_dwordx4 %0, %5, %6 nt\n\tglobal_load_dwordx4 %1, %5, %6 offset:1024 nt\n\tglobal_load_dwordx4 %2, %5, %6 offset:2048 nt\n\tglobal_load_dwordx4 %3, %5, %6 offset:3072 nt\n2:"
                 : "=&v"(a[0]), "=&v"(a[1]), "=&v"(a[2]), "=&v"(a[3]) : "s"(on), "v"(o), "s"(base) : "scc");
}
template <int L, int G> DI void dma_1(unsigned voff, const void* sbase, unsigned ldsbase) {
    asm volatile("s_add_u32 m0, %2, %4\n\ts_nop 0\n\tglobal_load_lds_dword %0, %1 offset:%3" ::"v"(voff), "s"(sbase), "s"(ldsbase), "i"(G), "i"(L - G) : "memory", "scc");
}
template <int L> DI void dma_n(unsigned voff, const void* sbase, unsigned ldsbase) {
    asm volatile("s_add_u32 m0, %2, %3\n\ts_nop 0\n\tglobal_load_lds_dwordx4 %0, %1 nt" ::"v"(voff), "s"(sbase), "s"(ldsbase), "i"(L) : "memory", "scc");
}
template <int A, int O> DI void lds2a(unsigned a) { asm volatile("ds_read_b64 a[%c1:%c1+1], %0 offset:%2" ::"v"(a), "i"(A), "i"(O) : "memory"); }
constexpr int L_EAR = L_XR + 8192;
constexpr int dma_goff(int it) { return it < 40 ? (it >> 2) * 128 : ((it - 100) / 5) * 256; }
constexpr int dma_loff(int it) { return it < 40 ? L_XR + ((it >> 2) % 3) * 4096 + (it & 3) * 1024 : ((it - 100) % 5) * 20480 + ((it - 100) / 5) * 2048; }
#define KFS 4
#define APOS 0
#define CWP 2
#define SAF 2
#define KD0 30
#define KST 59
#define KSL 5
#define KSC 11
#define XG0 5
#define XGS 1
#define WOD 1
#define USP 1
#define KSG 0
#define SGP0 0x122021
#define SGP1 0x202121
#define SBL 1
#define SMP 1
#define KW2E 0
#define KW2T 2
#define OTR 1
#define KQ0 0
#define KQ1 18
#define KQ2 4
#define KQ3 0
#define NPD 27
constexpr int kQ0 = KQ0, kQ1 = KQ1, kQ2 = KQ2, kQ3 = KQ3;
#define KW2S 16
#define KW2M 0x42
#define KFKS 13
#define KFKG 6
#define KUS 17
#define KTS 19
#define KFWP 13
constexpr int kW2S = KW2S, kW2M = KW2M;
#define KSA 4
constexpr int NW2L = (20 - kW2S) * __builtin_popcount(kW2M), NW2P = NW2L + KSA;
constexpr int P1_PRE = NPD + 7;
constexpr int p1_dma(int i) { return i < 5 ? 100 + i : i < 13 ? i - 5 : i < 18 ? 105 + (i - 13) : i < 22 ? 8 + (i - 18) : i < NPD ? 110 + (i - 22) : 400; }
constexpr int kQ1a = KST > KD0 ? kQ1 * (KST - KD0) / (117 - KD0) : 0;
constexpr int p1_pre(int i) { return i < kQ0 + kQ1a ? p1_dma(i) : i == kQ0 + kQ1a ? 400 : p1_dma(i - 1); }
constexpr int p1_item(int s, int g) {
    if (s == KFS && g == (XG0 == 5 ? 4 : 9)) return 324;
    if (WOD ? (s & 1) && s < 10 && (g == 3 || g == 8) : g == 9 && s < 10) return WOD ? 114 + s + (g == 8) : 115 + s;
    if (s == KSL && (g == KSG || g == KSG + 1)) return 202 + g - KSG;
    if (s == KSL && g == 6) return 204;
    if (s == KFWP && g == 1) return 208;
    if (s == KFKS && g == KFKG) return 206;
    if (USP && s == KUS && (g == 0 || g == 2 || g == 4 || g == 7)) return 210 + g / 2;
    if (s == KTS && g == 9) return 209;
    if (s >= kW2S && ((kW2M >> g) & 1)) return 500 + (s - kW2S) * __builtin_popcount(kW2M) + __builtin_popcount(kW2M & ((1 << g) - 1));
    if ((s & 1) == 0 && s <= 12 && g >= XG0 && (g - XG0) % XGS == 0 && (g - XG0) / XGS < 4) return 4 * (s / 2 + 3) + (g - XG0) / XGS;
    return -1;
}
constexpr int p1_cnt(int s, int g) {
    int n = P1_PRE;
    for (int t = 0; t <= s; ++t)
        for (int u = 0; u < 10; ++u) {
            if (t == s && u >= g) break;
            if (p1_item(t, u) >= 0) ++n;
            if (!USP && t == KUS && u == 0) n += 4;
        }
    return n;
}
constexpr int p1_pos(int code) {
    for (int i = 0; i < P1_PRE; ++i) if (p1_pre(i) == code) return i;
    for (int t = 0; t < 20; ++t)
        for (int u = 0; u < 10; ++u) if (p1_item(t, u) == code) return p1_cnt(t, u);
    return -1000;
}
constexpr int p1_lds(int s, int g) {
    constexpr int t0 = XG0 == 5 ? 0 : 5;
    if (s >= 2 && s < 10 && !(s & 1) && g >= t0 && g < t0 + 2) return 2 + s + g - t0;
    if (s == KSC + 1 && g == t0 + 2) return 38;
    if (s >= 14 && s < 19 && !(s & 1) && (g == 5 || g == 8)) return 30 + (s - 14) + g / 8;
    if (s == 19 && (g == 0 || g == 4)) return 36 + g / 4;
    if (s == 19 && g == 7) return 39;
    return -1;
}
constexpr int p1_nl(int s, int g0, int g1) {
    int n = 0;
    for (int g = g0; g < g1; ++g) n += (s < 19 && g < 5) + ((s & 1) && s < 19 && g >= 5 && g < 9) + (p1_lds(s, g) >= 0);
    return n;
}
constexpr int p1_cap(int s, int g) {
    int u = p1_nl(s, g, g + 1) + (g == 9 && s < 19) + ((s & 1) && s < 19 && g == 5) + ((s & 1) == 0 && g == XG0 - 1);
    const int it = p1_item(s, g), nt = g < 9 ? p1_item(s, g + 1) : -1;
    u += (it >= 0) + (nt >= 0 && nt < 200) + (it == 324);
    if ((s == 19 && g == 2) || (g == 9 && ((s & 3) == 2 || s == KUS - 1 || s == 18))) u += 2;
    return u < 2 ? 2 - u : 0;
}
constexpr int NFILL = 112;
constexpr int p1_from(int f) { return f < 40 ? 0 : f < 52 ? 9 : f < 72 ? KSC : 14; }
constexpr int p1_fill(int s, int g, int e) {
    int f = 0;
    for (int t = 0; t <= s; ++t)
        for (int u = 0; u < 10; ++u)
            for (int v = 0; v < p1_cap(t, u); ++v) {
                const bool go = f < NFILL && p1_from(f) <= t;
                if (t == s && u == g && v == e) return go ? f : -1;
                f += go;
                if (f == 40) f = 48;
            }
    return f;
}
static_assert(p1_fill(13, 10, 0) >= 52, "");
constexpr bool p1_all() {
    for (int i = 0; i < 40; ++i) if (p1_pos(i) < 0) return false;
    for (int i = 100; i < 125; ++i) if (p1_pos(i) < 0) return false;
    for (int i = 0; i < NW2L; ++i) if (p1_pos(500 + i) < 0) return false;
    return p1_pos(202) >= 0 && p1_pos(203) >= 0 && p1_pos(206) >= 0 && p1_pos(208) >= 0 && p1_pos(209) >= 0 && p1_pos(324) >= 0;
}
static_assert(p1_all() && p1_lds(14, 5) == 30 && p1_lds(14, 8) == 31 && p1_lds(16, 5) == 32, "");
constexpr int ring_req(int G, int I, int R, int X) {
    int n = I, e = 0;
    for (int t = 0; t <= G; ++t) {
        int r = -1;
        if (t % 5 == 0 || t % 5 == 3) {
            if (n < 40 && 5 * (n - R) + 4 < t) r = n++;
            else if (e < X && t % 10 == 3) r = 100 + e++;
        }
        if (t == G) return r;
    }
    return -1;
}
constexpr int g2_adr(int i, int kk) {
    int n = 0;
    for (int t = 0; t <= i; ++t)
        for (int u = 0; u < 5; ++u) {
            const bool ok = u == 4 || (u == 2 && (t < 11 || t > 15)) || (u == 3 && t < 5);
            if (t == i && u == kk) return ok ? n : -1;
            n += ok;
        }
    return -1;
}
constexpr int p1_lim(int n) { return n > 63 ? 63 : n; }
template <int IMM> DI void gld4s(v4u& dst, unsigned voff, const void* sbase) { asm volatile("global_load_dwordx4 %0, %1, %2 offset:%3 sc1" : "=v"(dst) : "v"(voff), "s"(sbase), "i"(IMM)); }
template <int IMM> DI void gld2s(v2u& dst, unsigned voff, const void* sbase) { asm volatile("global_load_dwordx2 %0, %1, %2 offset:%3 sc1" : "=v"(dst) : "v"(voff), "s"(sbase), "i"(IMM)); }
template <class T> DI void pin(T& v) { asm volatile("" : "+v"(v)); }
DI void mul2(float& a, float& b, float x, float y, float s) { asm volatile("v_mul_f32 %0, %2, %4\n\tv_mul_f32 %1, %3, %4" : "=&v"(a), "=&v"(b) : "v"(x), "v"(y), "v"(s)); }
DI void rot1(unsigned& a, unsigned x) { asm volatile("v_alignbit_b32 %0, %1, %1, 16" : "=v"(a) : "v"(x)); }
DI void mix1(unsigned& a, unsigned x, unsigned y) { asm volatile("v_bfi_b32 %0, %3, %1, %2" : "=v"(a) : "v"(x), "v"(y), "s"(0xffffu)); }
DI void ror1(unsigned& o, unsigned prev) { asm volatile("v_mov_b32_dpp %0, %1 wave_ror:1 row_mask:0xf bank_mask:0xf" : "=v"(o) : "v"(prev)); }
DI void shr1b(unsigned& o, unsigned x) { asm volatile("v_mov_b32_dpp %0, %1 wave_shr:1 row_mask:0xf bank_mask:0xf" : "+v"(o) : "v"(x)); }
DI void xor1(float& a, unsigned x, unsigned s) { asm volatile("v_xor_b32 %0, %1, %2" : "=v"(a) : "v"(x), "v"(s)); }
template <int G> DI void bc1(float& a, unsigned x) { asm volatile("v_mov_b32_dpp %0, %1 row_newbcast:%2 row_mask:0xf bank_mask:0xf" : "=v"(a) : "v"(x), "i"(G)); }
template <int G> DI void mulb(float& o, unsigned x, float y) { asm volatile("v_mul_f32_dpp %0, %1, %2 row_newbcast:%3 row_mask:0xf bank_mask:0xf" : "=v"(o) : "v"(x), "v"(y), "i"(G)); }
template <int N> DI void lgkm() { __builtin_amdgcn_s_waitcnt(0xC07F | (N << 8)); asm volatile("" ::: "memory"); }
DI void unp2(float& a, float& b, unsigned x) { asm volatile("v_lshlrev_b32 %0, 16, %2\n\tv_and_b32 %1, 0xffff0000, %2" : "=&v"(a), "=&v"(b) : "v"(x)); }
#define GL(a) "global_load_dwordx4 a[" #a ":" #a "+3], %7, %8 sc1\n\tglobal_load_dwordx4 a[" #a "+4:" #a "+7], %7, %8 offset:256 sc1\n\t"
#define AR(d, a, o) "v_accvgpr_read_b32 " d ", a[" #a "+" #o "]\n\t"
#define GS_(a) "s_waitcnt vmcnt(4)\n\t" AR("%2", a, 1) AR("%3", a, 3) AR("%4", a, 5) AR("%5", a, 7) AR("%0", a, 0) AR("%1", a, 2)                              \
    "v_xor_b32 %2, %9, %2\n\tv_bitop3_b32 %4, %4, %9, %2 bitop3:0xbe\n\tv_bitop3_b32 %4, %3, %9, %4 bitop3:0xbe\n\tv_bitop3_b32 %4, %5, %9, %4 bitop3:0xbe\n\t" \
    AR("%2", a, 4) AR("%3", a, 6) "v_cmp_ne_u32 vcc, 0, %4\n\ts_cbranch_vccz 2f\n\t" GL(a)
template <int A, int D> DI void gran_req(unsigned off, const void* base) {
    if constexpr (D > 0) __builtin_amdgcn_s_sleep(D);
    asm volatile("global_load_dwordx4 a[%c2:%c2+3], %0, %1 sc1\n\tglobal_load_dwordx4 a[%c2+4:%c2+7], %0, %1 offset:256 sc1" ::"v"(off), "s"(base), "i"(A) : "memory");
}
DI void pipe_gran(float& a, float& d, unsigned off, const void* base, unsigned tag, bool& dead) {
    unsigned n = dead ? 0u : kSpinCap, y, z;
    float a1, d1;
    asm volatile("1:\n\t" GS_(32) GS_(40) GS_(48) "s_sub_u32 %6, %6, 1\n\ts_cbranch_scc0 1b\n2:"
                 : "=&v"(a), "=&v"(d), "=&v"(a1), "=&v"(d1), "=&v"(y), "=&v"(z), "+s"(n) : "v"(off), "s"(base), "s"(tag) : "memory", "vcc", "scc");
    dead = __builtin_amdgcn_readfirstlane(n) == ~0u;
    a += a1; d += d1;
}
DI void fma2(float& a, float& b, float x, float y, float s, float t, float u, float v) {
    asm volatile("v_fma_f32 %0, %2, %4, %6\n\tv_fma_f32 %1, %3, %5, %7" : "=&v"(a), "=&v"(b) : "v"(x), "v"(y), "v"(s), "v"(t), "v"(u), "v"(v));
}
DI void fma2p(f32x2& o, float x, float y, float r, float u, float v) {
    asm("v_fmac_f32 %0, %1, %2" : "+v"(u) : "v"(x), "v"(r));
    asm("v_fmac_f32 %0, %1, %2" : "+v"(v) : "v"(y), "v"(r));
    o = f32x2{u, v};
}
DI void swp2n(unsigned& a, unsigned& b, unsigned& c, unsigned& d) { asm volatile("v_permlane16_swap_b32 %0, %1\n\tv_permlane16_swap_b32 %2, %3" : "+v"(a), "+v"(b), "+v"(c), "+v"(d)); }
DI void cp2(unsigned& a, unsigned& b, f32x4 v) { asm volatile("v_cvt_pk_bf16_f32 %0, %2, %3\n\tv_cvt_pk_bf16_f32 %1, %4, %5" : "=&v"(a), "=v"(b) : "v"(v.x), "v"(v.y), "v"(v.z), "v"(v.w)); }
DI void zero2(v2u& a) { asm volatile("v_mov_b64 %0, 0" : "=v"(a)); }
DI void mov1(unsigned& a, unsigned b) { asm volatile("v_mov_b32 %0, %1" : "=v"(a) : "v"(b)); }
DI void sw16(unsigned& a, unsigned& b) { asm volatile("v_permlane16_swap_b32 %0, %1" : "+v"(a), "+v"(b)); }
DI void sw32(unsigned& a, unsigned& b) { asm volatile("v_permlane32_swap_b32 %0, %1" : "+v"(a), "+v"(b)); }
DI void mrg8(float& a, float b) { asm volatile("v_mov_b32_dpp %0, %1 quad_perm:[0,1,2,3] row_mask:0xf bank_mask:0xc" : "+v"(a) : "v"(b)); }
DI void sel1(float& o, unsigned a, unsigned b, u64 m) { asm volatile("v_cndmask_b32 %0, %1, %2, %3" : "=v"(o) : "v"(a), "v"(b), "s"(m)); }
DI void count_(unsigned& side, unsigned& fine, unsigned v) {
    u64 m;
    asm volatile("v_cmp_lt_u16 vcc, 4, %3\n\tv_cmp_gt_u16 %1, 4, %3\n\ts_bcnt1_i32_b64 %0, vcc\n\ts_bcnt1_i32_b64 %2, %1\n\ts_cmp_gt_u32 %0, %2\n\tv_readfirstlane_b32 %2, %3\n\ts_cselect_b32 %0, 0x2000, 0\n\t"
                 "s_lshr_b32 %2, %2, 2\n\ts_and_b32 %2, %2, 0x4000"
                 : "=&s"(side), "=&s"(m), "=&s"(fine) : "v"(v) : "vcc", "scc");
}
DI void ne1(u64& m, v2u tag, v2u a) { asm volatile("v_cmp_ne_u64 %0, %1, %2" : "=s"(m) : "v"(tag), "v"(a)); }
DI void ne2(u64& m, v2u tag, v2u a, v2u b) { asm volatile("v_cmp_ne_u64 vcc, %1, %2\n\tv_cmp_ne_u64 %0, %1, %3\n\ts_or_b64 %0, %0, vcc" : "=&s"(m) : "v"(tag), "v"(a), "v"(b) : "vcc", "scc"); }
template <int G> DI void bc2b(float& a, float& b, float x, float y) {
    asm volatile("v_mov_b32_dpp %0, %2 row_newbcast:%4 row_mask:0xf bank_mask:0xf\n\tv_mov_b32_dpp %1, %3 row_newbcast:%4 row_mask:0xf bank_mask:0xf" : "=&v"(a), "=&v"(b) : "v"(x), "v"(y), "i"(G));
}
template <class T> DI void pins(T& v) { asm volatile("" : "+s"(v)); }
template <int I> DI void gld_w2(unsigned voff, const void* sbase) {
    asm volatile("global_load_dwordx4 " WA(2) ", %0, %1 offset:%3 sc1" ::"v"(voff), "s"(sbase), "i"(W2R + 4 * I), "i"((I % 5) * 1024 - 2048));
}
DI void gld2t(v2u& dst, unsigned voff, const void* sbase) { asm volatile("global_load_dwordx2 %0, %1, %2 sc1" : "+v"(dst) : "v"(voff), "s"(sbase)); }
DI void undef4(v4u (&x)[4]) { asm volatile("" : "=v"(x[0]), "=v"(x[1]), "=v"(x[2]), "=v"(x[3])); }
DI void wait1(v2u& v) { asm volatile("s_waitcnt vmcnt(0)" : "+v"(v)::"memory"); }
template <int N> DI void wait_fkn(v2u& v, v2u& u) { asm volatile("s_waitcnt vmcnt(%2)" : "+v"(v), "+v"(u) : "i"(N) : "memory"); }
DI void ld_ur(unsigned o, const void* base) {
    asm volatile("global_load_dwordx4 a[40:40+3], %0, %1 sc1\n\tglobal_load_dwordx4 a[44:44+3], %0, %1 offset:1024 sc1\n\t"
                 "global_load_dwordx4 a[48:48+3], %0, %1 offset:2048 sc1\n\tglobal_load_dwordx4 a[52:52+3], %0, %1 offset:3072 sc1" ::"v"(o), "s"(base));
}
template <int I> DI void ld_ur1(unsigned o, const void* base) { asm volatile("global_load_dwordx4 a[%c2:%c2+3], %0, %1 offset:%3 sc1" ::"v"(o), "s"(base), "i"(40 + 4 * I), "i"(1024 * I)); }
constexpr int kUY = KSA + (USP ? p1_cnt(20, 0) - 1 - p1_pos(213) : p1_cnt(20, 0) - p1_cnt(KUS, 0) - 4);
DI void st_ur(unsigned a) {
    asm volatile("s_waitcnt vmcnt(%1)\n\tds_write_b128 %0, a[40:40+3] offset:480\n\tds_write_b128 %0, a[44:44+3] offset:1504\n\t"
                 "ds_write_b128 %0, a[48:48+3] offset:2528\n\tds_write_b128 %0, a[52:52+3] offset:3552" ::"v"(a), "i"(kUY < NW2P ? kUY : NW2P) : "memory");
}
template <int A> DI void rda(unsigned& v) { asm volatile("v_accvgpr_read_b32 %0, a[%c1+0]" : "=v"(v) : "i"(A)); }
DI void poll1(unsigned a, const void* base) { asm volatile("global_load_dwordx2 a[36:36+1], %0, %1 sc1" ::"v"(a), "s"(base)); }
DI void poll2(unsigned a, unsigned b, const void* base) { asm volatile("global_load_dwordx2 a[36:36+1], %0, %2 sc1\n\tglobal_load_dwordx2 a[36+2:36+3], %1, %2 sc1" ::"v"(a), "v"(b), "s"(base)); }
DI void touch_(unsigned a, const void* base) { asm volatile("global_load_dword a[32+1], %0, %1" ::"v"(a), "s"(base)); }
template <int IMM> DI void gld4t(v4u& dst, unsigned voff, const void* sbase) { asm volatile("global_load_dwordx4 %0, %1, %2 offset:%3 sc1" : "+v"(dst) : "v"(voff), "s"(sbase), "i"(IMM)); }
template <int N> DI void vm_wait() { asm volatile("s_waitcnt vmcnt(%0)" ::"i"(N) : "memory"); }
DI float dot2a(unsigned a, unsigned b, float c) {
    return __builtin_amdgcn_fdot2_f32_bf16(__builtin_bit_cast(bf16x2v, a), __builtin_bit_cast(bf16x2v, b), c, false);
}
DI void disc(unsigned l0, unsigned l1, unsigned l2, f32x2& lam, f32x2& co) {
    const float dt = rbf(__builtin_amdgcn_exp2f(lo16f(l2) * 1.44269504088896f));
    const float lre = lo16f(l0), lim = lo16f(l1);
    const float e_ = __builtin_amdgcn_exp2f(dt * lre * 1.44269504088896f);
    const float rv = __builtin_amdgcn_fractf(dt * lim * 0.159154943091895f);
    lam = f32x2{e_ * __builtin_amdgcn_cosf(rv), e_ * __builtin_amdgcn_sinf(rv)};
    const float nr = lam.x - 1.0f, ni = lam.y, id = __builtin_amdgcn_rcpf(lre * lre + lim * lim);
    co = f32x2{(nr * lre + ni * lim) * id, (ni * lre - nr * lim) * id};
}
DI f32x4 unpk4(v2u u) { return f32x4{lo16f(u.x), hi16f(u.x), lo16f(u.y), hi16f(u.y)}; }
#define D2 "v_dot2_f32_bf16 "
#define CP "v_cvt_pk_bf16_f32 "
DI void d2x4(unsigned* o, unsigned a0, unsigned a1, unsigned a2, unsigned a3, unsigned b0, unsigned b1, unsigned b2, unsigned b3) {
    asm volatile(D2 "%0, %4, %8, 0\n\t" D2 "%1, %5, %9, 0\n\t" D2 "%2, %6, %10, 0\n\t" D2 "%3, %7, %11, 0"
                 : "=&v"(o[0]), "=&v"(o[1]), "=&v"(o[2]), "=&v"(o[3]) : "v"(a0), "v"(a1), "v"(a2), "v"(a3), "v"(b0), "v"(b1), "v"(b2), "v"(b3));
}
DI void cpx4(unsigned* o, unsigned a0, unsigned a1, unsigned a2, unsigned a3, unsigned b0, unsigned b1, unsigned b2, unsigned b3) {
    asm volatile(CP "%0, %4, %8\n\t" CP "%1, %5, %9\n\t" CP "%2, %6, %10\n\t" CP "%3, %7, %11"
                 : "=&v"(o[0]), "=&v"(o[1]), "=&v"(o[2]), "=&v"(o[3]) : "v"(a0), "v"(a1), "v"(a2), "v"(a3), "v"(b0), "v"(b1), "v"(b2), "v"(b3));
}
DI void cpx4i(unsigned* t, const unsigned* ka) {
    asm volatile(CP "%0, %0, %4\n\t" CP "%1, %1, %5\n\t" CP "%2, %2, %6\n\t" CP "%3, %3, %7"
                 : "+v"(t[0]), "+v"(t[1]), "+v"(t[2]), "+v"(t[3]) : "v"(ka[0]), "v"(ka[1]), "v"(ka[2]), "v"(ka[3]));
}

__global__ __launch_bounds__(256, 1) __attribute__((aligned(4096))) void k_main(Params p) {
    __shared__ __attribute__((aligned(16))) char smem[LDS_BYTES];
    const rsrc_t rs = __builtin_amdgcn_make_buffer_rsrc((void*)p.ws, 0, WS_BYTES, 0x00020000);
    const rsrc_t ro = __builtin_amdgcn_make_buffer_rsrc((void*)p.out, 0, S_ * D_ * 4, 0x00020000);
    const int w0 = __builtin_amdgcn_readfirstlane(threadIdx.x >> 6), b0 = blockIdx.x;
    bool dead = false;
    asm volatile("" ::: "a255");
    float SW[5];
    f32x4 xin[5];
    v2u gt[5];
    f32x4 acc[2][5];
    f32x4 rr[2];
    float tt[2][5][4];
    Lm lam1, lamq;
    f32x2 l8, l32, l128;
    u64 late;
    v4u cA[8], cB[8];
    int sgn_, sg4, ab[4], bb, zpa, uwa, spa, tbw, tbr, tbp, tbq, cwa, cw4, spl, dst[3];
    unsigned w2o, wro, fwo, fko, xso, gso, fgo1, fho1, fho, hso, eo, es, ho[4];
    bool cv0;
    unsigned fine;
    const char* gf_;
    const char* gn;
    rsrc_t rn;
    const char* kvtb;
    const char* w2b0[(NW2P + 4) / 5];
    int otw, otr0, otr4;
    int mt1_;
    auto addrs = [&](auto layer_, int lane, int w, int b) __attribute__((always_inline)) {
        constexpr int layer = layer_.value;
        const int c = lane & 15, q = lane >> 4;
        TILES()
        static_assert(l_sg(0, 0) == SC + 0xA900 && l_sg(0, 1) == SC + 0xB400 && l_sg(0, 2) == SC + 0xCE00 && l_sg(0, 3) == SC + 0xDC00, "");
        sgn_ = (layer ? l_sg(1, 0) + w * INBOX : SC + (int)((0xDC00CE00B400A900ull >> (16 * w)) & 0xffff)) + (q >> 1) * 640 + (q & 1) * 8;
        const int c4 = (c >> 1) * 16 + (c & 1) * 4;
        sg4 = sgn_ + c4; sgn_ += c * 16;
        const int pl = lane < 40 ? lane : 39, tbb = l_tb(layer, 0) + w * (layer ? INBOX : 5120);
        spl = SC + pl * 16;
        eo = OFF_EG + (unsigned)(layer * 163840 + nt1 * 640 + pl * 16);
        es = eo + (unsigned)(mt1 * 10240);
        spa = l_rs(layer, 0) + (2 * (mt2 & 1) + (w >> 1)) * (layer ? INBOX : 256) + (2 * (w & 1) + (c >> 3)) * 64 + (c & 7) * 4;
        tbw = tbb + c * 64 + ((q ^ ((c >> 1) & 3)) * 16);
        tbr = tbb + (2 * (c >> 2) + (q & 1)) * 64 + (((2 * (q >> 1) + ((c >> 1) & 1)) ^ (c >> 2)) * 16) + (c & 1) * 8;
        hso = N_HF + (unsigned)((layer * 8 + 2 * w + (q >> 1)) * 5 * NB_ + (2 * nt1 + (q & 1)) * 256 + c * 16);
        tbp = tbb + (8 * (q & 1) + (c >> 2)) * 64 + (((2 * (q >> 1) + ((c >> 1) & 1)) ^ (c >> 3)) * 16) + (c & 1) * 8;
        tbq = tbp ^ 32;
        cwa = L_CW + c * 16; cw4 = L_CW + c4;
        pin(sg4); pin(tbp); pin(tbq); pin(cw4);
        fho1 = lane ? kNone : N_FH + (unsigned)(layer * 2 * NB_ + (w >> 1) * 4096 + (nt1 * 2 + (w & 1)) * 128);
        fho = N_FH + (unsigned)(layer * 2 * NB_ + ((mt2 & 1) * 4 + w) * 1024 + (lane >> 3) * 128);
        pin(sgn_); pin(spl); pin(eo); pin(es); pin(spa); pin(tbw); pin(tbr); pin(hso); pin(cwa); pin(fho1); pin(fho);
        const unsigned hb_ = N_HF + (unsigned)((layer * 8 + 4 * (mt2 & 1)) * 5 * NB_ + w * 2048);
#pragma unroll
        for (int m = 0; m < 4; ++m) { ho[m] = hb_ + (unsigned)(((m + 1 + w) & 3) * 5 * NB_ + lane * 16); pin(ho[m]); }
#pragma unroll
        for (int m = 0; m < 3; ++m) { dst[m] = SC + ((w + 1 + m) & 3) * INBOX + m * 5120 + lane * 16; pin(dst[m]); }
        mt1_ = mt1;
        if constexpr (layer == 0) {
            const int r0 = SC + (w < 3 ? w : 0) * REGSZ, y0 = 2031 + 8 * q - c, l34 = lane < 33 ? lane : 33;
#pragma unroll
            for (int i = 0; i < 4; ++i) { ab[i] = r0 + ((y0 & 1) ? CV_O : CV_E) + ((y0 + 1) >> 1) * 4 - 448 - 512 - 1024 * i; pin(ab[i]); }
            bb = r0 + 32 * c + 16 * q;
            zpa = w < 3 ? r0 + l34 * 16 + (l34 >= 30 ? CV_E + 4096 - 480 : 0) + (l34 >= 32 ? CV_O + 4100 - 32 - CV_E - 4096 : 0) : SC + 47360 + lane * 16;
            uwa = (w < 3 ? r0 : SC + 47360 - 480) + lane * 16;
            xso = (unsigned)((64 * mt2 + 16 * w + (lane >> 2)) * D_ + 80 * nt2 + 8 * ((lane & 3) ^ q)) * 2u;
            gso = OFF_GT + (unsigned)((80 * nt2 + i32 + 32 * w) * GSTR + (q & 1) * 512 + (c >> 2) * 128 + (q >> 1) * 64 + (c & 3) * 16);
            fgo1 = lane == 0 && i32 + 32 * w < 80 ? OFF_FG + (unsigned)((nt2 * 96 + i32 + 32 * w) * 128) : kNone;
            pin(bb); pin(zpa); pin(uwa); pin(xso); pin(gso); pin(fgo1);
        }
        SCHED();
    };
    unsigned tag0, tag1;
    v2u tagv;
#define FLAG(off) { if (lane == 0) st64(tagv, rs, off); }
#define ISTAG(f) (((f.x ^ tag0) | (f.y ^ tag1)) == 0u)
#define KW_REQ() { ld_ur(wro, kvtb); static_for<NW2P>(LAMBDA(i_) { gld_w2<i_.value>(w2o, p.ws + 2048 + (i_.value / 5) * 5120); }); }
#define FW_OK() { if (__builtin_expect(late != 0ull, 0)) { WAVE_WAIT({ const v2u fk_ = ld64(rs, fko); const v2u fw_ = ld64(rs, fwo); ok = ISTAG(fk_) && ISTAG(fw_); }) KW_REQ() } }
#define WF(g, s) *(const v4u*)(smem + wa[(s) & 1][(g) < 4 ? (g) & 1 : 2] + ((g) & 2) * 20480 + ((s) >> 1) * 1024)
#define TT_GAP(n) { constexpr int mb_ = (n) / 10, nb_ = ((n) % 10) / 2, e_ = ((n) & 1) * 2; \
        mul2(tt[mb_][nb_][e_], tt[mb_][nb_][e_ + 1], rm_[4 * mb_ + e_], rm_[4 * mb_ + e_ + 1], SW[nb_]); }
    {
        const int tid = threadIdx.x, lane = tid & 63, w = w0, b = b0;
        const int c = lane & 15, q = lane >> 4;
        TILES()
        STAMP(0)
        const unsigned lds0 = (unsigned)(size_t)(LAS char*)smem;
        unsigned nonce;
        unsigned cpf0;
        {
            u64 pc;
            int len;
            asm volatile("s_getpc_b64 %0\nkpf_begin:\n\ts_mov_b32 %1, kpf_end-kpf_begin\n\ts_max_i32 %1, %1, kpf_end2-kpf_begin\n\ts_max_i32 %1, %1, kpf_end3-kpf_begin" : "=s"(pc), "=s"(len)::"scc");
            const int o0 = (((j & 15) * 4 + w) + 64 * (lane >> 3)) * 128;
            asm volatile("global_load_dword %0, %1, %2" : "=v"(cpf0) : "v"(o0 < len ? o0 : 0), "s"(pc));
        }
        const bool cv = 32 * w + i32 < 80;
        const int chn = 80 * nt2 + i32 + 32 * (cv ? w : 0);
        const bool kw = 32 * w + 31 - i32 < 80;
        const int ku = 80 * nt2 + (w == 2 ? 64 + (i32 & 15) : i32 + 32 * (w & 1)), kc = ku >> 6, kr = ku & 63;
        const int lc = cv ? lane : 8;
        const unsigned xl = lds0 + (unsigned)(w * REGSZ);
        v4u ax[15], sa[5], cr[2], wr[4];
        unsigned uL[3], uM[3], sdq, kso;
        const int cu_ = (b < 200 ? b : 0) * 4 + w, cdb = cu_ / 20, ccg = cu_ % 20;
        {
            unsigned cpo = (unsigned)(32 * ccg + (lane & 31)) * 2u, cpm = (unsigned)(40 * nt1 + (lane < 39 ? lane : 39)) * 2u;
            unsigned k1 = (unsigned)(32 * kr + 5 + (lane >> 3)) * 1280u + (unsigned)(64 * kc + 8 * (lane & 7)) * 2u, k0 = k1 - 10240u, k4 = lane < 24 ? k1 + 30720u : kNone;
            unsigned vo = k1 - 6400u + (unsigned)(lane >> 3) * 3840u;
            pin(cpo); pin(cpm); pin(k0); pin(k1); pin(k4); pin(vo);
            SCHED();
            gldu16(uL[0], cpo, p.Lre); gldu16(uL[1], cpo, p.Lim); gldu16(uL[2], cpo, p.logdt);
            gldu16(uM[0], cpm, p.Lre); gldu16(uM[1], cpm, p.Lim); gldu16(uM[2], cpm, p.logdt);
            const v4u rk = {(unsigned)(u64)p.k, (unsigned)((u64)p.k >> 32) & 0xffffu, S_ * D_ * 2u, 0x00020000u};
            ld_kv(ax, 32 * w + 31 - i32, k0, k1, k4, vo, rk, p.v, xl);
            SCHED();
        }
        const unsigned gof = OFF_GRP + (unsigned)mt1 * 0x200000u - ((unsigned)(u64)p.ws & 0x1fffffu);
        unsigned vote = 4u, side;
        SCHED();
        auto small_ = [&]() __attribute__((always_inline)) {
            kso = (unsigned)(512 * kc + lane * 4);
            dma_1<L_EAR + 2048, 0>(kso, p.shk, xl); dma_1<L_EAR + 2304, 256>(kso, p.shk, xl);
            gld1s(nonce, kso & 4u, p.ws + OFF_NONCE);
            SCHED();
        };
        if (SMP == 0) small_();
        const u16* sab = w < 2 ? p.x0 : w == 2 ? p.Bre : p.Bim;
        unsigned sao;
        {
            int r0 = w < 2 ? 128 * mt1 + 8 * nt1 + 4 * w : 40 * nt1 + mt1, r1 = w < 2 ? 1 : 16, r2 = w < 2 ? 2047 : 40 * nt1 + 32 + (mt1 >> 1);
            pins(sab); pins(r0); pins(r1); pins(r2);
            const int r = r0 + q * r1;
            sao = (unsigned)((r < r2 ? r : r2) * 1280 + c * 16);
            pin(sao);
        }
        if (SAF == 0) static_for<5>(LAMBDA(i) { gld<256 * i.value>(sa[i.value], sao, sab); });
        SCHED();
        if (w == TW0 && !kw) tour<0>();
        STAMP(22)
        const int row8 = lane >> 3;
        const unsigned wvo = (unsigned)(40 * nt1 + row8) * 1280u + (unsigned)(((lane & 7) ^ ((w & 1) | ((row8 >> 1) << 1))) * 16 + (w >> 1) * 128);
        const char* const wbase = (const char*)((w & 1) ? p.Bim : p.Bre);
        const unsigned wl = lds0 + (unsigned)(((w & 1) * 10 + (w >> 1)) * 1024);
        const unsigned xvo0 = (unsigned)(8 * (row8 >> 1) + 2 * (row8 & 1)) * 1280u + (unsigned)(((lane & 7) ^ ((row8 >> 1) << 1)) * 16);
        const unsigned xvo1 = (xvo0 ^ 16u) + 1280u;
        const char* const xbase = (const char*)p.x0 + (size_t)(128 * mt1 + 32 * w) * 1280;
        unsigned xv[4] = {xvo0, xvo1, xvo0 + 5120u, xvo1 + 5120u}, wv[5];
#pragma unroll
        for (int i = 0; i < 5; ++i) { wv[i] = wvo + i * 10240u; pin(wv[i]); if (i < 4) pin(xv[i]); }
#define DMA_M0(it) SETM0<dma_loff(it) - dma_goff(it)>((it) < 40 ? xl : wl)
#define DMA_LD1(it, nx_) GLD1<dma_goff(it), dma_loff(nx_) - dma_goff(nx_)>((it) < 40 ? xv[(it) & 3] : wv[(it) < 40 ? 0 : ((it) - 100) % 5], (it) < 40 ? xbase : wbase, (nx_) < 40 ? xl : wl)
#define DMA_LD(it) GLD0<dma_goff(it)>((it) < 40 ? xv[(it) & 3] : wv[(it) < 40 ? 0 : ((it) - 100) % 5], (it) < 40 ? xbase : wbase)
        const int swz = (c & 1) | ((c >> 2) << 1);
        int wa[2][3];
#pragma unroll
        for (int i = 0; i < 2; ++i) {
            wa[i][0] = (c >> 3) * 20480 + (c & 7) * 128 + (((4 * i + q) ^ (c & 6)) * 16); wa[i][1] = (wa[i][0] ^ 16) + 10240;
            wa[i][2] = 81920 + (c & 1) * 10240 + (c >> 1) * 128 + (((4 * i + q) ^ ((c & 1) | ((c >> 2) << 1))) * 16);
            pin(wa[i][0]); pin(wa[i][1]); pin(wa[i][2]);
        }
        int xlane0 = L_XR + w * REGSZ + (c & 1) * 1024 + (c >> 1) * 128 + ((q ^ swz) * 16);
        int xlane1 = L_XR + w * REGSZ + (c & 1) * 1024 + (c >> 1) * 128 + (((4 + q) ^ swz) * 16);
        unsigned cvo = (unsigned)((16 * cdb + (lane >> 2)) * D_ + 32 * ccg + 8 * ((lane & 3) ^ q)) * 2u, for0, for1;
        int ear = L_EAR + w * REGSZ + c * 64 + (q ^ (c >> 2)) * 16, eas = L_EAR + 2048 + w * REGSZ + (lane & 7) * 64;
        int kra = L_XR + w * REGSZ + (lane >> 3) * 512 + (lane & 7) * 16;
        pin(ear); pin(eas); pin(kra);
        {
            const int G = 4 * ccg + q, nt = G / 5, g5 = G - 5 * nt;
            for0 = OFF_W2F + (unsigned)(((cdb / 5) * 200 + cdb % 5) * 1024 + c * 16 + (10 * (nt >> 2) + 2 * (g5 & 6) + ((nt >> 1) & 1)) * 5120 + (2 * (nt & 1) + (g5 & 1)) * 256);
            for1 = for0 + (g5 < 4 ? 10240u : 256u);
        }
        unsigned sto = OFF_ST + (unsigned)((mt1 * 16 + 4 * w + q) * 256 + c * 8), swo = OFF_ST + (unsigned)(((lane >> 2) * 16 + nt1) * 256 + 128 + (lane & 3) * 16);
        unsigned vto = OFF_VT + (unsigned)(mt1 * 512 + c * 32 + (q & 1) * 8);
        unsigned kvo = OFF_KVT + (unsigned)((64 * kc + 8 * (lane >> 3) + ((lane >> 2) & 1)) * KSTR + (8 * kc + (lane >> 3)) * KPS + (32 * kr + 8 * (lane & 3)) * 2);
        int kbw = L_KB + w * 1024 + (lane & 7) * 128 + (((lane >> 3) * 8) ^ ((lane & 7) * 16));
        int kbr = L_KB + w * 1024 + (lane >> 3) * 128 + (((lane & 7) * 16) ^ ((lane >> 3) * 16));
        const unsigned dum = kNone;
        unsigned fko1 = OFF_FK + (unsigned)((64 * kc + kr) * 32);
        if (!kw) kvo = dum;
        if (b >= 200) { for0 = dum; for1 = dum; cvo = (unsigned)((b * 4 + w) * 128); }
        wro = (unsigned)(chn * 4096 + lc * 16);
        cv0 = cv;
        pin(kvo); pin(kbw); pin(kbr); pin(fko1);
        pin(xlane0); pin(xlane1); pin(cvo); pin(for0); pin(for1); pin(sto); pin(swo); pin(vto); pin(wro);
        SCHED();
        auto pre = LAMBDA(i_) {
            constexpr int it = p1_dma(i_.value);
            SCHED();
            DMA_M0(it);
            asm volatile("s_nop 0");
            DMA_LD(it);
            SCHED();
        };
        fko = OFF_FK + (unsigned)(((chn & ~63) + (cv ? lane : 0)) * 32);
        { const int e_ = (lane & 7) < w ? w : (lane & 7) > w + 4 ? w + 4 : (lane & 7);
          fwo = OFF_FW + (unsigned)(((5 * nt2 + (lane < 40 ? lane >> 3 : 4)) * 20 + 4 * w + e_) * 32); }
        w2o = OFF_W2F + (unsigned)((nt2 * 40 + 10 * w) * 5120 + lane * 16);
        int ime = SC + (w < 3 ? w * REGSZ + CV_E : 43264) + 1008 - 16 * lane, imo = SC + (w < 3 ? w * REGSZ + CV_O + 4 : 47360) + 1008 - 16 * lane, im3 = w < 3 ? ime + 2048 : imo;
        int tca = L_TAB + c * 136, tcb = tca + 8 + 32 * w + 8 * q, tcc = L_TAB + (32 + (c >> 1)) * 136, tcd = tcc + 8 + 32 * w + 8 * q, tcl = L_TAB + (lane < 40 ? lane : 39) * 136, coa = L_CO + w * REGSZ + q * 64, rsa = l_rs(0, 0) + w * 256 + lane * 4;
        unsigned sgn = (c & 1) ? 0u : 0x80000000u;
        fko1 = kw && lane < 32 ? fko1 : OFF_FW + (unsigned)((b * 4 + w) * 32);
        pin(fko); pin(fwo); pin(w2o); pin(fko1); pin(ime); pin(imo); pin(im3); pin(tca); pin(tcb); pin(tcc); pin(tcd); pin(tcl); pin(coa); pin(rsa); pin(sgn);
        SCHED();
        auto cw_ = [&]() __attribute__((always_inline)) {
            dma_n<L_EAR>(cvo, p.Cre, xl); dma_n<L_EAR + 1024>(cvo, p.Cim, xl);
            dma_1<L_EAR + 2560, 0>((unsigned)(160 * nt2 + (lane < 39 ? lane : 39) * 4), p.Dskip, xl);
            ld_wr(wr, 32 * w + i32, wro, p.ssmk);
            gldu16(sdq, (unsigned)chn * 2u, p.ssmD);
        };
        if (SMP == 1) small_();
        static_assert(SMP == 0 || SAF > 0, "");
        if (SAF == 1) static_for<5>(LAMBDA(i) { gld<256 * i.value>(sa[i.value], sao, sab); });
        if (CWP == 0) cw_();
        STAMP(33)
        asm volatile("s_waitcnt vmcnt(%5)\n\ts_cmp_ge_u32 %4, 80\n\ts_cbranch_scc0 1f\n\ts_waitcnt vmcnt(%6)\n1:" : "+v"(uL[0]), "+v"(uL[1]), "+v"(uL[2]), "+v"(cpf0) : "s"(32 * w + 31 - i32), "i"(12 + (SAF < 2 ? 5 : 0) + (CWP ? 0 : 8)), "i"(3 + (SAF < 2 ? 5 : 0) + (CWP ? 0 : 8)) : "memory", "scc");
        pin(uM[0]); pin(uM[1]); pin(uM[2]);
        {
            f32x2 lam, co;
            disc(uL[0], uL[1], uL[2], lam, co);
            *(f32x2*)(smem + L_CO + w * REGSZ + (lane & 31) * 8) = 2.0f * co;
        }
        SCHED();
        STAMP(35)
        if (SAF == 2) static_for<5>(LAMBDA(i) { gld<256 * i.value>(sa[i.value], sao, sab); });
        if (CWP == 1) cw_();
        if (APOS == 0) addrs(std::integral_constant<int, 0>{}, lane, w, b);
        if (SAF == 3) static_for<5>(LAMBDA(i) { gld<256 * i.value>(sa[i.value], sao, sab); });
        if (CWP == 2) cw_();
        v4u kvt[4];
        unsigned ka[8], kt[8], se[8], vs[8], ko4[4], one2 = 0x3F803F80u;
        pin(one2);
        auto kvop = LAMBDA(n_) {
            constexpr int n = n_.value, h = n ? (n - 1) / 29 : -1, u = n ? (n - 1) % 29 : 26;
            if constexpr (u < 2) d2x4(ka + 4 * u, ax[0][h], ax[1][h], ax[2][h], ax[3][h], se[u], se[u], se[u], se[u]);
            else if constexpr (u == 8) { const v4u sk = ax[11 + h]; se[4] = sk[0] >> 16; se[5] = sk[2] & 0xffff0000u; se[6] = sk[0] & 0xffffu; se[7] = sk[2] << 16; pin(se[4]); pin(se[5]); pin(se[6]); pin(se[7]); }
            else if constexpr (u < 21) {
                constexpr int v = u < 8 ? u - 2 : u - 3, tp = 1 + v / 6, st = v % 6, e = st & 1;
                if constexpr (st < 2) d2x4(kt + 4 * e, ax[tp][h], ax[tp + 1][h], ax[tp + 2][h], ax[tp + 3][h], se[2 * tp + e], se[2 * tp + e], se[2 * tp + e], se[2 * tp + e]);
                else if constexpr (st < 4) cpx4i(kt + 4 * e, ka + 4 * e);
                else d2x4(ka + 4 * e, kt[4 * e], kt[4 * e + 1], kt[4 * e + 2], kt[4 * e + 3], one2, one2, one2, one2);
            }
            else if constexpr (u == 21) { vs[0] = ax[7][h] & 0xffffu; vs[1] = ax[8][h] << 16; vs[2] = ax[9][h] & 0xffffu; vs[3] = ax[10][h] << 16; pin(vs[0]); pin(vs[1]); pin(vs[2]); pin(vs[3]); }
            else if constexpr (u == 22) { vs[4] = ax[7][h] >> 16; vs[5] = ax[8][h] & 0xffff0000u; vs[6] = ax[9][h] >> 16; vs[7] = ax[10][h] & 0xffff0000u; pin(vs[4]); pin(vs[5]); pin(vs[6]); pin(vs[7]); }
            else if constexpr (u == 23) cpx4(ko4, ka[0], ka[2], ka[4], ka[6], ka[1], ka[3], ka[5], ka[7]);
            else if constexpr (u < 26) { constexpr int e = u - 24; d2x4(kt + 4 * e, ko4[2 * e], ko4[2 * e], ko4[2 * e + 1], ko4[2 * e + 1], vs[4 * e], vs[4 * e + 1], vs[4 * e + 2], vs[4 * e + 3]); }
            else if constexpr (u == 26) {
                if constexpr (h < 3) { const v4u sk = ax[12 + h]; se[0] = sk[1] >> 16; se[1] = sk[3] & 0xffff0000u; se[2] = sk[1] & 0xffffu; se[3] = sk[3] << 16; pin(se[0]); pin(se[1]); pin(se[2]); pin(se[3]); }
                else asm volatile("s_nop 2");
            }
            else if constexpr (u == 27) {
                cpx4(ko4, kt[0], kt[2], kt[4], kt[6], kt[1], kt[3], kt[5], kt[7]);
                *(v2u*)(smem + kbw) = v2u{ko4[0], ko4[1]};
                *(v2u*)(smem + (kbw ^ 64)) = v2u{ko4[2], ko4[3]};
            }
            else kvt[h] = *(const v4u*)(smem + kbr);
            SCHED();
        };
        auto deal = [&](auto a_, auto b_, auto n_, auto i_) __attribute__((always_inline)) {
            constexpr int a = a_.value, m = b_.value - a, lo = m * i_.value / n_.value, hi = m * (i_.value + 1) / n_.value;
            static_for<hi - lo>(LAMBDA(e_) { pre(std::integral_constant<int, a + lo + e_.value>{}); });
        };
#define IC(n) std::integral_constant<int, (n)>{}
        static_for<kQ0>(pre);
        static_assert(kQ0 == 0 && KD0 >= 8, "");
        asm volatile("s_waitcnt vmcnt(%4)" : "+v"(ax[7]), "+v"(ax[8]), "+v"(ax[9]), "+v"(ax[10]) : "i"(13 + kQ0) : "memory");
        ax[11] = *(const v4u*)(smem + eas);
        SCHED();
#pragma unroll
        for (int i = 0; i < 7; ++i) { ax[i] = *(const v4u*)(smem + kra + i * 128); SCHED(); }
#pragma unroll
        for (int i = 1; i < 4; ++i) ax[11 + i] = *(const v4u*)(smem + eas + i * 16);
        SCHED();
        pin(nonce);
        STAMP(30)
        unsigned tch = 0u;
        auto kvs = LAMBDA(n_) {
            site<1000 + n_.value>(); SCHED();
            if constexpr (n_.value == KD0) LGKM0()
            kvop(n_);
            if constexpr (n_.value >= KD0) deal(IC(kQ0), IC(kQ0 + kQ1), IC(117 - KD0), IC(n_.value - KD0));
        };
        static_assert((KST - 1) % 29 == 0, "");
        if (kw) static_for<KST>(kvs);
        else {
            {
                const unsigned t_ = (unsigned)__builtin_amdgcn_s_memtime(), f_ = ((nt1 ^ w) & 1) << 13;
                const unsigned o_ = gof + N_END + (t_ & 0xe0u) * 512u + (unsigned)(nt1 * 512 + (w & 1) * 256) + (t_ & 0x10u) * 8u;
                const int r_ = race(p.ws + (o_ + f_), p.ws + (o_ + (f_ ^ 0x2000u)));
                vote += f_ ? r_ : -r_;
            }
            if (__builtin_expect(w == 3, !W3O)) {
                f32x2 lam, co, lp[16];
                disc(uM[0], uM[1], uM[2], lam, co);
                lp[0] = cmul(lam, lam); lp[0] = cmul(lp[0], lp[0]); lp[1] = cmul(lp[0], lp[0]);
                lp[0] = f32x2{1.f, 0.f};
                lp[2] = cmul(lp[1], lp[1]); lp[3] = cmul(lp[2], lp[1]); lp[4] = cmul(lp[2], lp[2]);
                const f32x2 z2 = {0.f, 0.f};
                f32x2 t4[4];
                cfma4(t4, lp[4], lp[1], lp[2], lp[3], lp[4], z2, z2, z2, z2);
                lp[5] = t4[0]; lp[6] = t4[1]; lp[7] = t4[2]; lp[8] = t4[3];
                cfma4(t4, lp[8], lp[1], lp[2], lp[3], lp[4], z2, z2, z2, z2);
                lp[9] = t4[0]; lp[10] = t4[1]; lp[11] = t4[2]; lp[12] = t4[3];
                cfma4(t4, lp[8], lp[5], lp[6], lp[7], lp[7], z2, z2, z2, z2);
                lp[13] = t4[0]; lp[14] = t4[1]; lp[15] = t4[2];
                f32x2* tl = (f32x2*)(smem + tcl);
                tl[0] = lam;
#pragma unroll
                for (int i = 0; i < 16; ++i) tl[1 + i] = lp[i];
                SCHED();
                asm volatile("kpf_end3:");
            }
            static_for<kQ1a>(LAMBDA(i_) { pre(IC(kQ0 + i_.value)); });
            undef4(kvt);
            asm volatile("" : "=v"(se[0]), "=v"(se[1]), "=v"(se[2]), "=v"(se[3]));
        }
        tag0 = __builtin_amdgcn_readlane(nonce, 0) + 1u; tag1 = __builtin_amdgcn_readlane(nonce + (tag0 == 0u ? 1u : 0u), 1);
        tagv = v2u{tag0, tag1}; pin(tagv);
        {
            asm volatile("s_waitcnt vmcnt(%5)" : "+v"(sa[0]), "+v"(sa[1]), "+v"(sa[2]), "+v"(sa[3]), "+v"(sa[4]) : "i"(8 + kQ0 + kQ1a) : "memory");
            float s1[2] = {0.f, 0.f}, s2[2] = {0.f, 0.f};
#pragma unroll
            for (int i = 0; i < 5; ++i)
#pragma unroll
                for (int e = 0; e < 4; ++e) { s1[e & 1] = dot2a(sa[i][e], one2, s1[e & 1]); s2[e & 1] = dot2a(sa[i][e], sa[i][e], s2[e & 1]); }
            const float a1 = rowsum16(s1[0] + s1[1]), a2 = rowsum16(s2[0] + s2[1]);
            const float mean = a1 * (1.0f / D_);
            const float var = fmaxf(a2 * (1.0f / D_) - mean * mean, 0.f);
            const float ri = __builtin_amdgcn_rcpf(rbf(__builtin_amdgcn_sqrtf(rbf(rbf(var) + 1e-5f))));
            v2u sv = {__float_as_uint(w >= 2 ? a1 : c ? -ri * rbf(mean) : ri), tag0};
            int o0 = w < 2 ? (mt1 * 16 + nt1) * 256 + 32 * w : ((mt1 >> 2) * 16 + nt1) * 256 + 112 + (mt1 & 3) * 16 + 8 * w, o1 = w < 2 ? 8 : 16384, o2 = w < 2 ? 64 : 0;
            pins(o0); pins(o1); pins(o2);
            unsigned so_ = c < 2 ? OFF_ST + (unsigned)(o0 + q * o1 + c * o2) : kNone;
            if (w >= 2 && lane == 2) { unsigned xc_; asm volatile("s_getreg_b32 %0, hwreg(HW_REG_XCC_ID, 0, 1)" : "=s"(xc_)); sv.x = vote | (xc_ << 16); so_ = OFF_VT + (unsigned)((mt1 * 16 + nt1) * 32 + (w & 1) * 8); }
            pin(sv); pin(so_);
            st64(sv, rs, so_);
            SCHED();
        }
        bool kw2 = kw;
        if (kw2) {
            static_for<117 - KST>(LAMBDA(n_) { kvs(IC(KST + n_.value)); });
        } else {
            if (__builtin_expect(w == 3, !W3O)) {
            const int l = lane, l3 = l & 3;
            unsigned a = gof + N_SG + (unsigned)((((l >> 3) & 1) * 2 + (mt2 & 1)) * 2 * NB_ + ((l >> 1) & 3) * 2048 + nt2 * 256 + (l & 1) * 128);
            { unsigned t_ = OFF_EG + (unsigned)((l >= 21 ? 163840 - 640 : 0) + (mt1 * 16 + nt1) * 640 + (l < 26 ? l - 16 : 5) * 128); pin(t_); a = l >= 16 ? t_ : a; }
            { unsigned t_ = gof + N_PS + (unsigned)((((mt2 & 1) * 4 + l3) * 8 + nt2) * 128); pin(t_); a = l >= 28 ? t_ : a; }
            { unsigned t_ = gof + N_FH + (unsigned)(((l >> 2) & 1) * 2 * NB_ + (l3 >> 1) * 4096 + (nt1 * 2 + (l3 & 1)) * 128); pin(t_); a = l >= 32 ? t_ : a; }
            { unsigned t_ = gof + N_FX + (unsigned)(((2 * (mt2 & 1) + (l3 >> 1)) * 16 + nt2 * 2 + (l3 & 1)) * 128); pin(t_); a = l >= 40 ? t_ : a; }
            { unsigned t_ = OFF_FG + (unsigned)((nt2 * 96 + i32 + 16 * ((l3 + 1) & 6)) * 128); pin(t_); a = l >= 48 ? t_ : a; }
            const unsigned big = (l & 7) < 5 ? OFF_KVT + (unsigned)((l & 1) * 640 * 4608 + (80 * nt2 + i32) * GSTR) : OFF_W2F + (unsigned)(nt2 * 204800 + ((l & 7) - 5) * 98304);
            a = l >= 56 ? big : a;
            tch = a;
            pin(tch);
            asm volatile("kpf_end2:");
        }
            static_for<kQ1 - kQ1a>(LAMBDA(i_) { pre(IC(kQ0 + kQ1a + i_.value)); });
            if (w == TW) tour<1>();
            skip<SKIP0>();
            undef4(kvt);
        }
        SCHED();
        STAMP(34)
        if (APOS == 1) addrs(std::integral_constant<int, 0>{}, lane, w, b);
        v4u orr, oi;
        unsigned ie[4][4], io[4][4], iw;
        auto img = LAMBDA(n_) {
            constexpr int i = n_.value / 10, u = n_.value % 10;
            if constexpr (u == 0) { if constexpr (i > 0) ror1(iw, wr[i - 1].w); else { iw = 0u; pin(iw); } }
            else if constexpr (u < 5) rot1(ie[i][u - 1], wr[i][4 - u]);
            else if constexpr (u == 5) shr1b(iw, wr[i].w);
            else if constexpr (u < 9) mix1(io[i][u - 6], wr[i][9 - u], wr[i][8 - u]);
            else mix1(io[i][3], wr[i].x, iw);
        };
        auto tap0 = [&]() __attribute__((always_inline)) {
            wr[0].x = cvtpk(lo16f(wr[0].x) + (lane ? 0.f : lo16f(sdq)), hi16f(wr[0].x));
            pin(wr[0]);
            SCHED();
        };
        {
            static_assert(kQ0 + kQ1 <= 18, "");
            vm_wait<6 + kQ0 + kQ1>();
            cr[0] = *(const v4u*)(smem + ear); cr[1] = *(const v4u*)(smem + ear + 1024);
            static_for<5>(LAMBDA(i) { lds2a<22 + 2 * i.value, 2560 + 32 * i.value>(lds0 + (unsigned)(L_EAR + w * REGSZ + q * 8)); });
            asm volatile("s_waitcnt lgkmcnt(0)" : "+v"(cr[0]), "+v"(cr[1])::"memory");
            static_for<kQ2>(LAMBDA(i_) { pre(IC(kQ0 + kQ1 + i_.value)); });
            static_for<4>(LAMBDA(e_) {
                constexpr int e = e_.value;
                const f32x4 co = *(const f32x4*)(smem + coa + e * 16);
                const f32x2 o0 = lo16f(cr[0][e]) * f32x2{co.x, -co.y} - lo16f(cr[1][e]) * f32x2{co.y, co.x};
                const f32x2 o1 = hi16f(cr[0][e]) * f32x2{co.z, -co.w} - hi16f(cr[1][e]) * f32x2{co.w, co.z};
                orr[e] = cvtpk(o0.x, o1.x); oi[e] = cvtpk(o0.y, o1.y);
            });
            pin(orr); pin(oi);
            SCHED();
            static_for<NPD - kQ0 - kQ1 - kQ2>(LAMBDA(i_) { pre(IC(kQ0 + kQ1 + kQ2 + i_.value)); });
            for (int i = 0; i < 4; ++i) st128(kvt[i], rs, kvo + i * 2 * KSTR);
            st128(orr, rs, for0); st128(oi, rs, for1);
        }
        SCHED();
        STAMP(23)
        if (w == 3) tour<7>();
        asm volatile("s_waitcnt vmcnt(%5) lgkmcnt(0)" : "+v"(wr[0]), "+v"(wr[1]), "+v"(wr[2]), "+v"(wr[3]), "+v"(sdq) : "i"(P1_PRE - 1 - p1_pos(3)) : "memory");
        __builtin_amdgcn_s_barrier();
        asm volatile("" ::: "memory");
        STAMP(24)
        site<10>();
        v4u wf[2][5], af[2][2][2];
#pragma unroll
        for (int nb = 0; nb < 5; ++nb) wf[0][nb] = WF(nb, 0);
#pragma unroll
        for (int i = 0; i < 4; ++i) af[0][i >> 1][i & 1] = *(const v4u*)(smem + ((i >> 1) ? xlane1 : xlane0) + (i & 1) * 2048);
        SCHED();
        tap0();
        site<11>();
        kvtb = p.ws + OFF_KVT + (chn * KPAD + (chn >> 3) * KPS);
        pins(kvtb);
#pragma unroll
        for (int i = 0; i < (NW2P + 4) / 5; ++i) { w2b0[i] = p.ws + 2048 + i * 5120; pins(w2b0[i]); }
        v2u sg, tc[2], fk, fw, vt;
        v4u sw0;
        v2u zq[2];
        float rr_[8];
        unsigned sx[4], sz[4];
        u64 odd = 0xAAAAAAAAAAAAAAAAull;
        pins(odd);
        auto filler = LAMBDA(f_) {
            constexpr int f = f_.value;
            if constexpr (f < 0) {}
            else if constexpr (f < 40) img(IC(f));
            else if constexpr (f == 48) { lam1.r = __uint_as_float(tc[0].x); xor1(lam1.s, tc[0].y, sgn); }
            else if constexpr (f == 49) { lamq.r = __uint_as_float(tc[1].x); xor1(lamq.s, tc[1].y, sgn); }
            else if constexpr (f < 52) zero2(zq[f - 50]);
            else if constexpr (f == 52) { sx[0] = sw0.x; mov1(sx[1], sx[0]); }
            else if constexpr (f == 53) { sz[0] = sw0.z; mov1(sz[1], sz[0]); }
            else if constexpr (f < 56) bc1<f - 54>(rr_[f - 54], sg.x);
            else if constexpr (f == 56) sw16(sx[0], sx[1]);
            else if constexpr (f == 57) sw16(sz[0], sz[1]);
            else if constexpr (f < 60) mov1(sx[f - 56], sx[f - 58]);
            else if constexpr (f < 62) mov1(sz[f - 58], sz[f - 60]);
            else if constexpr (f < 64) sw32(sx[f - 62], sx[f - 60]);
            else if constexpr (f < 66) sw32(sz[f - 64], sz[f - 62]);
            else if constexpr (f < 72) {
                bc1<f - 64>(rr_[f - 64], sg.x);
                if constexpr (f == 66) { SW[0] = __uint_as_float(sx[0]); SW[1] = __uint_as_float(sz[0]); SW[2] = __uint_as_float(sx[1]); SW[3] = __uint_as_float(sz[1]); }
                if constexpr (f == 71) sel1(SW[4], sx[2], sz[2], odd);
            }
            else mulb<8 + (f - 72) % 8>(tt[(f - 72) % 8 / 4][(f - 72) / 8][(f - 72) % 4], sg.x, SW[(f - 72) / 8]);
        };
        auto kstep = LAMBDA(s_) {
            constexpr int s = s_.value, ph = s >> 2, s4 = s & 3, cu = s & 1, nx = cu ^ 1, jj = s >> 1, pp = jj & 1;
            if constexpr (s4 == 3 && ph < 4) {
                constexpr int pw = p1_pos(104 + 5 * (ph + 1)), px = p1_pos(4 * (jj + 1) + 3), pm = pw > px ? pw : px;
                constexpr int vm = p1_lim(p1_cnt(s, 0) - 1 - pm);
                asm volatile("" ::: "memory");
                __builtin_amdgcn_s_waitcnt((vm & 15) | ((vm >> 4) << 14) | 0x70);
                __builtin_amdgcn_s_barrier();
                asm volatile("" ::: "memory");
                STAMP(25 + ph)
                SCHED();
            }
            if constexpr (s == KSC) {
                static_assert(s4 == 3 && p1_pos(204) < p1_pos(4 * (jj + 1) + 3), "");
                pin(sg); pin(sw0); pin(vt);
                if (__builtin_expect(__builtin_amdgcn_ballot_w64((__builtin_amdgcn_bitop3_b32(sg.y, sw0.y, tag0, 0x7E) | __builtin_amdgcn_bitop3_b32(vt.y, sw0.w, tag0, 0x7E)) != 0u) != 0ull, 0))
                    WAVE_WAIT({ sg = ld64(rs, sto); sw0 = ld128(rs, swo); vt = ld64(rs, vto); ok = ((sg.y ^ tag0) | (sw0.y ^ tag0) | (sw0.w ^ tag0) | (vt.y ^ tag0)) == 0u; })
                ORDER();
                STAMP(7)
            }
            if constexpr (s == kW2S && KUS > kW2S) { vm_wait<p1_cnt(s, 0) - 1 - p1_pos(208)>(); SCHED(); }
            if constexpr (s == KUS) {
                static_assert(p1_pos(206) > p1_pos(208), "");
                wait_fkn<p1_cnt(s, 0) - 1 - p1_pos(206)>(fk, fw);
                if (!USP) ld_ur(wro, kvtb);
                SCHED();
            }
            if constexpr (s4 != 3 || ph == 4) lgkm<(s == 0 ? 0 : p1_nl(s - 1, 5 - cu, 10) - 1)>();
            SCHED();
            static_for<10>(LAMBDA(g_) {
                constexpr int g = g_.value, mb = g / 5, k = g % 5, it = p1_item(s, g), nt_ = g < 9 ? p1_item(s, g + 1) : -1;
                constexpr bool d0 = it >= 0 && it < 200, d1 = nt_ >= 0 && nt_ < 200;
                if constexpr (s == 0) mf0(acc[mb][k], af[pp][cu][mb], wf[cu][k]); else mf(acc[mb][k], af[pp][cu][mb], wf[cu][k]);
                SCHED();
                if constexpr (s < 19) {
                    if constexpr (g < 5) wf[nx][g] = WF(g, s + 1);
                    if constexpr (cu == 1 && g == 5) vm_wait<p1_lim(p1_cnt(s, 5) - 1 - p1_pos(4 * (jj + 1) + 3))>();
                    if constexpr (cu == 1 && g >= 5 && g < 9)
                        af[pp ^ 1][(g - 5) >> 1][(g - 5) & 1] = *(const v4u*)(smem + (((g - 5) >> 1) ? xlane1 : xlane0) + ((jj + 1) % 3) * 4096 + ((g - 5) & 1) * 2048);
                }
                if constexpr (cu == 0 && g == XG0 - 1 && s > 0) lgkm<p1_nl(s - 1, 8, 10) + p1_nl(s, 0, XG0 - 1)>();
                constexpr int lq = p1_lds(s, g);
                if constexpr (lq >= 4 && lq < 8) (lq == 4 ? lam1.a : lq == 5 ? lam1.b : lq == 6 ? lamq.a : lamq.b) = *(const f32x2*)(smem + (lq < 6 ? tca : tcb) + (lq & 1) * 2176);
                else if constexpr (lq == 8 || lq == 9) tc[lq - 8] = *(const v2u*)(smem + (lq == 8 ? tcc : tcd));
                else if constexpr (lq == 10) { const f32x2* tp = (const f32x2*)(smem + tcl); l8 = tp[2]; l32 = tp[5]; }
                else if constexpr (lq == 11) l128 = *(const f32x2*)(smem + tcl + 72);
                else if constexpr (lq >= 30 && lq < 33) *(v4u*)(smem + (lq < 32 ? ime + 1024 * (lq - 30) : im3)) = v4u{ie[33 - lq][0], ie[33 - lq][1], ie[33 - lq][2], ie[33 - lq][3]};
                else if constexpr (lq >= 33 && lq < 37) *(v4u*)(smem + imo + 1024 * (36 - lq)) = v4u{io[lq - 33][0], io[lq - 33][1], io[lq - 33][2], io[lq - 33][3]};
                else if constexpr (lq == 37) *(v4u*)(smem + im3 + 1024) = v4u{ie[0][0], ie[0][1], ie[0][2], ie[0][3]};
                else if constexpr (lq == 38) *(unsigned*)(smem + rsa) = sg.x;
                else if constexpr (lq == 39) *(v4u*)(smem + zpa) = v4u{zq[0].x, zq[0].y, zq[1].x, zq[1].y};
                static_for<p1_cap(s, g)>(LAMBDA(e_) { filler(IC(p1_fill(s, g, e_.value))); });
                if constexpr (s == 18 && g == 9) ne2(late, tagv, fk, fw);
                if constexpr (s == 19 && g == 5) count_(side, fine, vt.x);
                if constexpr (s == 19 && g == 2) { if (__builtin_expect(late != 0ull, 0)) poll2(fko, fwo, p.ws); }
                if constexpr (it >= 0) {
                    if constexpr (d0 && d1) DMA_LD1(it, d1 ? nt_ : 0);
                    else if constexpr (d0) DMA_LD(it);
                    else if constexpr (it == 202) gld2s<0>(sg, sto, p.ws);
                    else if constexpr (it == 203) gld4s<0>(sw0, swo, p.ws);
                    else if constexpr (it == 204) gld2s<0>(vt, vto, p.ws);
                    else if constexpr (it == 206) gld2s<0>(fk, fko, p.ws);
                    else if constexpr (it == 208) gld2s<0>(fw, fwo, p.ws);
                    else if constexpr (it == 209) { unsigned t2 = tch + (side + fine); asm volatile("v_cndmask_b32 %0, %1, %0, %2" : "+v"(t2) : "v"(tch), "s"(0x0000FFFFF000FFFFull)); touch_(t2, p.ws); }
                    else if constexpr (it >= 210 && it < 214) ld_ur1<it - 210>(wro, kvtb);
                    else if constexpr (it >= 500) gld_w2<it % 50>(w2o, w2b0[(it % 50) / 5]);
                    else { vm_wait<p1_cnt(s, g) - P1_PRE>(); st64(tagv, rs, fko1); }
                }
                if constexpr (d1 && !d0) DMA_M0(d1 ? nt_ : 0);
                site<2000 + 10 * s + g>();
                SCHED();
            });
        };
        static_for<20>(kstep);
        static_for<NFILL - p1_fill(19, 10, 0)>(LAMBDA(i_) { filler(IC(p1_fill(19, 10, 0) + i_.value)); });
        SCHED();
        rr[0] = f32x4{rr_[0], rr_[1], rr_[2], rr_[3]}; rr[1] = f32x4{rr_[4], rr_[5], rr_[6], rr_[7]};
        ACC_FENCE10(acc[0], acc[1]);
        gn = p.ws + (gof + side); gf_ = gn + fine;
        rn = __builtin_amdgcn_make_buffer_rsrc((void*)gn, 0, N_END, 0x00020000);
        STAMP(1)
    }

    auto layer_body = LAMBDA(layer_) {
        constexpr int layer = layer_.value;
        const int sga = sgn_;
        const bool cv = layer == 0 && cv0;
        v2u fh;
        unsigned ks[5];
        f32x4 ca[8], a2[4][5];
        unsigned gp[4][4];
        auto gst = LAMBDA(i_) {
            constexpr int i = i_.value >> 2, u = i_.value & 3;
            if constexpr (u < 2) cp2(gp[i][2 * u], gp[i][2 * u + 1], ca[2 * i + u]);
            else if constexpr (u == 2) swp2n(gp[i][0], gp[i][2], gp[i][1], gp[i][3]);
            else st128(v4u{gp[i][0], gp[i][1], gp[i][2], gp[i][3]}, rs, gso + i * 1024);
        };
        float rsr, rsm;
        St bv[8];
        {
            St a0, a1;
            auto w2s = LAMBDA(n_) {
                constexpr int lo = KSA * n_.value / 4, hi = KSA * (n_.value + 1) / 4;
                if constexpr (layer == 0) { static_for<hi - lo>(LAMBDA(e_) { constexpr int i = NW2L + lo + e_.value; gld_w2<i>(w2o, w2b0[i / 5]); }); SCHED(); }
            };
            static_for<2>(LAMBDA(m_) {
                constexpr int mb = m_.value;
#pragma unroll
                for (int i = 0; i < 4; ++i) fma2p(bv[4 * mb + i].a, acc[mb][0][i], acc[mb][1][i], rr[mb][i], tt[mb][0][i], tt[mb][1][i]);
#pragma unroll
                for (int i = 0; i < 4; ++i) fma2p(bv[4 * mb + i].b, acc[mb][2][i], acc[mb][3][i], rr[mb][i], tt[mb][2][i], tt[mb][3][i]);
                const f32x4 v = __builtin_elementwise_fma(acc[mb][4], rr[mb], f32x4{tt[mb][4][0], tt[mb][4][1], tt[mb][4][2], tt[mb][4][3]});
#pragma unroll
                for (int i = 0; i < 4; ++i) bv[4 * mb + i].c = v[i];
                SCHED();
                w2s(IC(2 * mb));
                if constexpr (mb == 0) run3(a0, bv[0], bv + 1, lam1); else run4(a1, a0, bv + 4, lam1);
                SCHED();
                if constexpr (mb == 0) w2s(IC(1));
            });
            *(f32x2*)(smem + sga) = a1.a; *(f32x2*)(smem + sga + 256) = a1.b; *(float*)(smem + sg4 + 512) = a1.c;
            SCHED();
            w2s(IC(3));
        }
        SCHED();
        const int w_ = w0;
        {
        const int w = w_, mt1 = mt1_;
        if (layer == 0 && __builtin_expect(late != 0ull, 0)) {
            unsigned f0_, f1_, f2_, f3_;
            asm volatile("s_waitcnt vmcnt(0)\n\t" AR("%0", 36, 0) AR("%1", 36, 1) AR("%2", 36, 2) AR("%3", 36, 3) : "=v"(f0_), "=v"(f1_), "=v"(f2_), "=v"(f3_)::"memory");
            late = __builtin_amdgcn_ballot_w64(((f0_ ^ tag0) | (f1_ ^ tag1) | (f2_ ^ tag0) | (f3_ ^ tag1)) != 0u);
            ORDER();
            if (late == 0ull) KW_REQ()
        }
        asm volatile("s_waitcnt lgkmcnt(0)" ::: "memory");
        __builtin_amdgcn_s_barrier();
        asm volatile("" ::: "memory");
        STAMP(layer ? 14 : 2)
#pragma unroll
        for (int s = 0; s < 5; ++s) ks[s] = (unsigned)s * NB_;
        constexpr int NW2A = NW2P + 7 * kCR1 + kXA, NW3 = NW2P + kN3E + kN3;
        auto w2ld = LAMBDA(i_) {
            constexpr int i = i_.value;
            gld_w2<i>(w2o, p.ws + 2048 + (i / 5) * 5120);
        };
        auto w2a = LAMBDA(i_) {
            constexpr int k = i_.value;
            if constexpr (k < kXA) { w2ld(std::integral_constant<int, NW2P + 2 * k>{}); w2ld(std::integral_constant<int, NW2P + 2 * k + 1>{}); }
            else w2ld(std::integral_constant<int, NW2P + kXA + k>{});
        };
        unsigned xtb = (unsigned)(size_t)(LAS char*)smem + (unsigned)(l_tb(0, 0) + w * 5120);
        auto w2x = LAMBDA(i_) {
            constexpr int i = NW2A + i_.value;
            if constexpr (i < 50) w2ld(std::integral_constant<int, i>{});
            else if constexpr (i < 53) dma_x<(i - 50) * 1024, (i < 52 ? (i - 50) * 64 : 96)>(xso, p.x0, xtb);
        };
        auto w2b = LAMBDA(i_) {
            constexpr int k = i_.value - 7 * kCR1;
            constexpr int ne = KW2E < 53 - NW2A ? KW2E : 53 - NW2A;
            if constexpr (k < ne) w2x(std::integral_constant<int, k>{});
            constexpr int k1 = k - kFHK - 1;
            if constexpr (k1 >= 0 && k1 < KW2T) { w2x(std::integral_constant<int, ne + 2 * k1>{}); w2x(std::integral_constant<int, ne + 2 * k1 + 1>{}); }
            else if constexpr (k1 >= 0) w2x(std::integral_constant<int, ne + KW2T + k1>{});
            static_assert(ne <= kFHK + 1 && 53 - NW2A - ne - KW2T + kFHK < 7 * (8 - kCR1) - kFHP + 1, "");
            if constexpr (k == kFHK) { vm_wait<ne>(); __builtin_amdgcn_raw_buffer_store_b64(tagv, rn, fho1, fine, SC1); STAMP(39) }
            if constexpr (k == kFHK - 1) { STAMP(38) }
            if constexpr (k == 7 * (8 - kCR1) - kFHP) poll1(fho, gf_);
            static_assert(NW2A + 7 * (8 - kCR1) >= 53 && NW2A <= 50 && NW3 <= NW2A, "");
        };
        auto partb = [&]() __attribute__((always_inline)) {
            const float* sp = (const float*)(smem + spa);
            rsr = sp[0]; rsm = sp[8];
            if (layer == 1) { pin(rsr); pin(rsm); }
            if (layer == 1) {
                static_for<5>(LAMBDA(n_) {
                    constexpr int nb = n_.value;
                    unsigned d0, d1;
                    rda<22 + 2 * nb>(d0); rda<23 + 2 * nb>(d1);
                    a2[3][nb] = xin[nb] + (xin[nb] * rsr + rsm) * unpk4(v2u{d0, d1}); pin(a2[3][nb]);
                });
            }
        };
        constexpr int LBW = layer ? 2 : 3;
        if (w == 3) {
            site<3 + 10 * layer>(); SCHED();
            f32x4 s4[8];
#pragma unroll
            for (int i = 0; i < 8; ++i) { s4[i] = *(const f32x4*)(smem + spl + (l_sg(layer, i & 3) - SC + (i >> 2) * 640)); SCHED(); }
            f32x2 sv[16], x[4], P[4][4];
#pragma unroll
            for (int i = 0; i < 16; ++i) { const f32x4 t = s4[(i >> 2) + 2 * (i & 2)]; sv[i] = (i & 1) ? f32x2{t.z, t.w} : f32x2{t.x, t.y}; }
            unsigned er, ei;
            {
                f32x2 u[4];
                cfma4n(u, l8, sv[0], sv[4], sv[8], sv[12], sv[1], sv[5], sv[9], sv[13]);
                agg(er, ei, x, P[0][2], u, sv, l8, l32, l128);
            }
            st128(v4u{er, tag0, ei, tag0}, rs, es);
            SCHED();
            P[0][0] = f32x2{0.f, 0.f}; P[0][1] = x[0]; P[0][3] = cfma(l32, P[0][2], x[2]);
            if (layer == 0) { FW_OK() static_for<kN3E>(LAMBDA(i_) { w2ld(std::integral_constant<int, NW2P + i_.value>{}); }); }
#pragma unroll
            for (int kk = 1; kk < 4; ++kk) cfma4(P[kk], l8, P[kk - 1][0], P[kk - 1][1], P[kk - 1][2], P[kk - 1][3], sv[kk - 1], sv[kk + 3], sv[kk + 7], sv[kk + 11]);
#pragma unroll
            for (int i = 0; i < 8; ++i) { const f32x2 a = P[2 * (i >> 2)][i & 3], b_ = P[2 * (i >> 2) + 1][i & 3]; *(f32x4*)(smem + spl + (l_sg(layer, i & 3) - SC + (i >> 2) * 640)) = f32x4{a.x, a.y, b_.x, b_.y}; }
            partb();
        }
        int w1_ = w, w3_ = w;
        if (layer == 1) { pins(w1_); pins(w3_); }
        if (layer == 1 && w1_ < LBW) { site<4 + 10 * layer>(); SCHED(); partb(); if constexpr (layer == 1) { if (w == 0) tour<6>(); if constexpr (LBR == 1) room<3000>(); } }
        if (w3_ == LBW) {
            site<5 + 10 * layer>(); SCHED();
            f32x2 wg[17], t4[4];
            unsigned ea[15], tg[15];
            v4u eg[15];
            constexpr bool early = (LBE >> layer) & 1;
            const f32x2 z2 = {0.f, 0.f};
#pragma unroll
            for (int i = 0; i < 15; ++i) { ea[i] = (unsigned)(mt1 - 1 - i) * 10240u; pins(ea[i]); ea[i] = ea[i] < kNone ? ea[i] : kNone; pins(ea[i]); }
            if constexpr (early) {
                __builtin_amdgcn_s_sleep(layer ? KLB1 : KLB0);
#pragma unroll
                for (int i = 0; i < 15; ++i) eg[i] = ld128s(rs, eo, ea[i]);
                SCHED();
            }
            wg[1] = cmul(l128, l128); wg[2] = cmul(wg[1], wg[1]); wg[3] = cmul(wg[2], wg[1]); wg[4] = cmul(wg[2], wg[2]);
#pragma unroll
            for (int i = 4; i < 16; i += 4) {
                cfma4(t4, wg[i], wg[1], wg[2], wg[3], wg[4], z2, z2, z2, z2);
#pragma unroll
                for (int e = 0; e < 4; ++e) wg[i + 1 + e] = t4[e];
            }
#pragma unroll
            for (int i = 0; i < 15; ++i) { if (i) pin(wg[i]); tg[i] = mt1 - 1 - i < 0 ? 0u : tag0; pins(tg[i]); }
            if (layer == 1) partb();
            if constexpr (layer == 0) tour<3>();
            f32x2 H0, H1;
            unsigned spins = dead ? kSpinCap : 0u;
            if constexpr (layer == 1 && LBR == 0) { room<1800>(); site<16>(); }
            if constexpr (!early) __builtin_amdgcn_s_sleep(layer ? KLB1 : KLB0);
            SCHED();
            auto eval = [&](const v4u (&e)[15]) __attribute__((always_inline)) {
                unsigned x = __builtin_amdgcn_bitop3_b32(e[0].y, e[0].w, tg[0], 0x7E);
                H0 = f32x2{__uint_as_float(e[0].x), __uint_as_float(e[0].z)}; H1 = f32x2{0.f, 0.f};
#pragma unroll
                for (int i = 1; i < 15; i += 2) {
                    x |= __builtin_amdgcn_bitop3_b32(e[i].y, e[i].w, tg[i], 0x7E) | __builtin_amdgcn_bitop3_b32(e[i + 1].y, e[i + 1].w, tg[i + 1], 0x7E);
                    pin(x);
                    gacc2(H0, H1, wg[i], wg[i + 1], e[i], e[i + 1]);
                    SCHED();
                }
                *(f32x2*)(smem + spl + (L_CW - SC)) = H0 + H1;
                return __builtin_amdgcn_ballot_w64(x != 0u) != 0ull;
            };
            bool again = true;
            if constexpr (early) again = eval(eg);
            if (again) {
                bool miss;
#pragma nounroll
                do {
                    asm volatile("" ::: "memory");
                    v4u e2[15];
#pragma unroll
                    for (int i = 0; i < 15; ++i) e2[i] = ld128s(rs, eo, ea[i]);
                    SCHED();
                    miss = eval(e2);
                    if (__builtin_expect(miss, 0) && ++spins > kSpinCap) { dead = true; miss = false; }
                } while (miss);
            }
            ORDER();
            STAMP(layer ? 36 : 37)
            if (layer == 0) {
                static_for<NW3 - NW2P - kN3E>(LAMBDA(i_) { w2ld(std::integral_constant<int, NW2P + kN3E + i_.value>{}); });
            }
        }
        int w2_ = w;
        pins(w2_);
        if constexpr (layer == 0) site<21>();
        if (layer == 0) UNDEF8(ca);
        if (layer == 0 && w2_ != LBW) {
            if (cv) {
                site<2>(); SCHED();
                partb();
                {
                    FW_OK()
                    st_ur((unsigned)(size_t)(LAS char*)smem + (unsigned)uwa);
                    CV_LOAD_A(cA, 0, 0, 0) CV_LOAD_A(cA, 0, 0, 1)
                    SCHED();
                    static_for<8>(LAMBDA(a_) { constexpr int a = a_.value; CV_LOAD_B(cB, a, 0) SCHED(); });
                    CV_LOAD_A(cA, 1, 0, 0) CV_LOAD_A(cA, 1, 0, 1)
                    SCHED();
                }
                STAMP(32)
                conv_part<0, kCR1, CPF>(ca, cA, cB, smem, ab, bb, w2a, LAMBDA(i_) {});
            } else {
                if constexpr (layer == 0) tour<5>();
                FW_OK()
                static_for<NW2A - NW2P>(LAMBDA(i_) { w2ld(std::integral_constant<int, NW2P + i_.value>{}); __builtin_amdgcn_s_sleep(1); });
                partb();
                if constexpr (layer == 0) room<3900>();
            }
        }
        asm volatile("s_waitcnt lgkmcnt(0)" ::: "memory");
        __builtin_amdgcn_s_barrier();
        asm volatile("" ::: "memory");
        STAMP(layer ? 15 : 4)
        {
            St cw, ex, hv[8];
            cw.a = *(const f32x2*)(smem + cwa); cw.b = *(const f32x2*)(smem + cwa + 256); cw.c = *(const float*)(smem + cw4 + 512);
            ex.a = *(const f32x2*)(smem + sga); ex.b = *(const f32x2*)(smem + sga + 256); ex.c = *(const float*)(smem + sg4 + 512);
            SCHED();
#pragma unroll
            for (int s = 1; s < 5; ++s) pins(ks[s]);
            SCHED();
            keep14(hv, cw, ex, bv, lamq, lam1);
            keep4(hv + 4, hv[3], bv + 4, lam1);
            SCHED();
            v2u ta[5], td[5];
            f32x2 hb[9];
            static_for<5 + SBL>(LAMBDA(r_) {
                constexpr int r = r_.value, rd = r < 2 ? r : r == 2 ? 4 : r - 1, ro_ = r - SBL < 2 ? r - SBL : r - SBL == 2 ? 4 : r - SBL - 1;
                if constexpr (r < 5) {
                    float e[8];
                    unsigned o[4];
#pragma unroll
                    for (int i = 0; i < 8; ++i) e[i] = rd == 4 ? hv[i].c : rd < 2 ? hv[i].a[rd & 1] : hb[i + 1][rd & 1];
                    if constexpr (r == 0) b3cv(hb, o, cw.b, ex.b, bv[0].b, bv[1].b, lamq.b, lam1.b, e);
                    else if constexpr (r < 3) b3cv(hb + 3 * r, o, hb[3 * r - 1], bv[3 * r - 1].b, bv[3 * r].b, bv[3 * r + 1].b, lam1.b, lam1.b, e);
                    else for (int i = 0; i < 4; ++i) o[i] = cvtpk(e[2 * i], e[2 * i + 1]);
                    *(v4u*)(smem + tbw + rd * 1024) = v4u{o[0], o[1], o[2], o[3]};
                    SCHED();
                    if constexpr (r < 3) { bfin(hb[3 * r + 2], hb[3 * r + 1], lam1.b); SCHED(); }
                    ta[rd] = __builtin_bit_cast(v2u, trread(smem, (rd < 4 ? tbp : tbr) + rd * 1024));
                    td[rd] = __builtin_bit_cast(v2u, trread(smem, (rd < 4 ? tbq + 256 : tbr + 512) + rd * 1024));
                    SCHED();
                }
                if constexpr (r >= SBL) { __builtin_amdgcn_raw_buffer_store_b128(v4u{ta[ro_].x, ta[ro_].y, td[ro_].x, td[ro_].y}, rn, hso, ks[ro_], SC1); SCHED(); }
            });
        }
        if (layer == 1) { DRAIN(); __builtin_amdgcn_raw_buffer_store_b64(tagv, rn, fho1, fine, SC1); }
        STAMP(layer ? 16 : 5)
        if (cv) {
            if constexpr (layer == 0) site<6>();
            SCHED();
            if constexpr (!CPF) {
                CV_LOAD_A(cA, 0, kCR1, 0) CV_LOAD_A(cA, 0, kCR1, 1)
                SCHED();
                static_for<8>(LAMBDA(a_) { constexpr int a = a_.value; CV_LOAD_B(cB, a, kCR1) SCHED(); });
                static_for<7>(LAMBDA(a_) { constexpr int a = a_.value + 1; CV_LOAD_A(cA, a, kCR1, 0) CV_LOAD_A(cA, a, kCR1, 1) });
                SCHED();
            }
            conv_part<kCR1, 8, false>(ca, cA, cB, smem, ab, bb, w2b, LAMBDA(g_) {
                constexpr int g = g_.value;
                if constexpr ((g == 14 || g == 16 || g == 18 || g == 20)) gst(std::integral_constant<int, (g - 14) / 2>{});
                if constexpr ((g == 26 || g == 28 || g == 30 || g == 32)) gst(std::integral_constant<int, 4 + (g - 26) / 2>{});
            });
            asm volatile("" : "+v"(ca[0]), "+v"(ca[1]), "+v"(ca[2]), "+v"(ca[3]), "+v"(ca[4]), "+v"(ca[5]), "+v"(ca[6]), "+v"(ca[7]));
            static_for<8>(LAMBDA(i_) {
                constexpr int n = 8 + i_.value;
                if constexpr (n == 9) asm volatile("s_nop 0");
                if constexpr ((n & 3) == 2) asm volatile("s_nop 1");
                gst(std::integral_constant<int, n>{}); SCHED();
            });
        } else if (layer == 0) {
            constexpr int ne = KW2E < 53 - NW2A ? KW2E : 53 - NW2A;
            static_for<54 - NW2A>(LAMBDA(i_) {
                if constexpr (i_.value == ne) { vm_wait<ne>(); __builtin_amdgcn_raw_buffer_store_b64(tagv, rn, fho1, fine, SC1); }
                if constexpr (i_.value < 53 - NW2A) { w2x(i_); __builtin_amdgcn_s_sleep(1); }
            });
            if (w == 3) { static_for<NW2A - NW3>(LAMBDA(i_) { w2ld(std::integral_constant<int, NW3 + i_.value>{}); __builtin_amdgcn_s_sleep(1); }); if constexpr (layer == 0) tour<2>(); }
            poll1(fho, gf_);
            if constexpr (layer == 0) room<1000>();
            DRAIN();
        }
        if (layer == 0) { unsigned f0_, f1_; asm volatile("s_waitcnt vmcnt(%2)\n\t" AR("%0", 36, 0) AR("%1", 36, 1) : "=v"(f0_), "=v"(f1_) : "i"(4) : "memory"); fh = v2u{f0_, f1_}; }
        STAMP(layer ? 29 : 6)
        }

            v2u pt[4];
            if (layer == 0) {
                u64 hl;
                ne1(hl, tagv, fh);
                if (__builtin_expect(hl != 0ull, 0)) {
                    gld2t(fh, fho, gf_); wait1(fh); st64(tagv, rs, fgo1);
                    WAVE_RETRY(ISTAG(fh), fh = ld64(rn, fho + fine))
                }
                ORDER();
            }
            else { room<3400>(); site<18>(); pipe_flag<KPH0>(pt, fho, gf_, tagv, dead); ORDER(); }
            STAMP(layer ? 17 : 31)
            site<7 + 10 * layer>(); SCHED();
            constexpr int RING2 = layer ? KR2 : KR20;
            v4u ar[RING2];
#pragma unroll
            for (int i = 0; i < KI2; ++i) { ar[i] = ld128s(rn, ho[i / 10] + (i & 1) * 1024, ks[i % 10 >> 1]); SCHED(); }
        int b_o = b0, w_o = w0;
        asm volatile("" : "+s"(b_o), "+s"(w_o));
        const int b = b_o, w = w_o;
        TILES()
        v4u gl[3];
        v2u f0;
        unsigned fgo, sgo, sgs, glo, gw, gr, srco;
        u64 glate = 0ull;
        const char *gb1 = p.ws + 32 * GSTR, *gb2 = p.ws + 48 * GSTR;
        {
            unsigned xs_[3][4], dq[2];
            float zx[4], zd[4], zt[4], zz[5][4];
            unsigned sA = SC + w * INBOX, s1 = N_SG + fine + (layer * 2 + (mt2 & 1)) * 2 * NB_ + w * 2048, s2 = s1 + nt2 * 256, sF = OFF_FG + (nt2 * 96 + 4 * w) * 128, sG = OFF_GT + (80 * nt2 + 4 * w) * GSTR + mt2 * 128,
                     sW = sA + 8192 + (w & 1) * 128, sR = SC + 8192 + w * 32, sX = l_tb(0, 0) + w * 5120, k160 = 160, kIn = INBOX;
            pins(sA); pins(s1); pins(s2);
            if (layer == 0) { pins(sF); pins(sG); pins(sW); pins(sR); pins(sX); pins(k160); pins(kIn); pins(gb1); pins(gb2); }
            unsigned l16, c16, q0, q1, c2, xr0, xr1, ta_, tb_;
#define A1(n_, v_, e_) else if constexpr (n == n_) { v_ = e_; pin(v_); }
            auto adr = LAMBDA(n_) {
                constexpr int n = n_.value + (layer && n_.value >= 4 ? 12 : 0);
                if constexpr (n == 0) { l16 = (unsigned)dst[0] & 1023u; pin(l16); }
                A1(1, c16, l16 & 0xf0u) A1(2, q0, (l16 >> 8) & 1u) A1(3, q1, l16 >> 9)
                A1(4, c2, (l16 >> 6) & 3u) A1(5, xr0, (c16 << 2) + sX) A1(6, ta_, q1 ^ c2) A1(7, xr0, (q0 << 3) + xr0) A1(8, xr0, (ta_ << 4) + xr0) A1(9, xr1, xr0 ^ 32u)
                else if constexpr (n >= 10 && n < 15) { constexpr int nb = n - 10; const v2u t = *(const v2u*)(smem + ((nb & 5) ? xr1 : xr0) + (nb >> 1) * 1024); xs_[nb >> 1][2 * (nb & 1)] = t.x; xs_[nb >> 1][2 * (nb & 1) + 1] = t.y; }
                A1(16, srco, l16 + sA) A1(17, ta_, l16 & 0x300u) A1(18, sgo, s1 + l16 + ta_) A1(19, sgs, (q0 << 3) + c16) A1(20, ta_, (q1 << 30) + s2) A1(21, sgs, sgs + ta_)
                else if constexpr (layer == 1) {}
                A1(22, ta_, l16 < 624u ? l16 : 624u) A1(23, tb_, ta_ & 0x380u) A1(24, ta_, ta_ & 0x60u) A1(25, fgo, (tb_ << 4) + sF) A1(26, fgo, (ta_ << 2) + fgo)
                A1(27, glo, q1 * (16u * GSTR) + sG) A1(28, ta_, l16 & 0x180u) A1(29, tb_, l16 & 0x70u) A1(30, glo, ta_ * (GSTR / 128u) + glo) A1(31, glo, glo + tb_)
                A1(32, ta_, l16 >> 7) A1(33, tb_, l16 & 0x30u) A1(34, gw, (tb_ << 1) + sW) A1(35, gw, __umul24(ta_, k160) + gw) A1(36, ta_, c2 & 1u) A1(37, gw, (ta_ << 4) + gw)
                A1(38, ta_, l16 >> 8) A1(39, gr, (q0 << 7) + sR) A1(40, gr, __umul24(ta_, kIn) + gr) A1(41, gr, __umul24(c2, k160) + gr) A1(42, tb_, tb_ >> 1) A1(43, gr, gr + tb_)
            };
            SCHED();
            if constexpr (STAMPS == 1) { STAMP(layer ? 50 : 40) }
            static_for<40>(LAMBDA(i_) {
                constexpr int i = i_.value, m = i / 10, s = i % 10;
                static_for<5>(LAMBDA(k_) {
                    constexpr int kk = k_.value, pv = ring_req(5 * i + kk, KI2, RING2, 0);
                    if constexpr (s == 0 && m < 3) mfa0<kk>(a2[m][kk], ar[i % RING2]); else mfa<5 * s + kk>(a2[m][kk], ar[i % RING2]);
                    SCHED();
                    if constexpr (i == 0 && kk == 0 && layer == 1) { pin(pt[0]); pin(pt[1]); pin(pt[2]); pin(pt[3]); }
                    if constexpr (g2_adr(i, kk) >= 0) adr(IC(g2_adr(i, kk)));
                    if constexpr (layer == 0 && i >= 1 && i < 28 && (kk & 1)) {
                        constexpr int n = 2 * (i - 1) + (kk >> 1) - 9, nb = n / 9, u = n % 9 - 1;
                        if constexpr (n < 0) {}
                        else if constexpr (u < 0) { rda<22 + 2 * nb>(dq[0]); rda<23 + 2 * nb>(dq[1]); }
                        else if constexpr (u < 2) unp2(zx[2 * u], zx[2 * u + 1], xs_[nb >> 1][2 * (nb & 1) + u]);
                        else if constexpr (u < 4) unp2(zd[2 * u - 4], zd[2 * u - 3], dq[u - 2]);
                        else if constexpr (u < 6) fma2(zt[2 * u - 8], zt[2 * u - 7], zx[2 * u - 8], zx[2 * u - 7], rsr, rsr, rsm, rsm);
                        else fma2(zz[nb][2 * u - 12], zz[nb][2 * u - 11], zt[2 * u - 12], zt[2 * u - 11], zd[2 * u - 12], zd[2 * u - 11], zx[2 * u - 12], zx[2 * u - 11]);
                    }
                    if constexpr (layer == 0 && i == 0 && kk == 1) { vm_wait<KI2 - 1 + (ring_req(0, KI2, RING2, 0) >= 0)>(); st64(tagv, rs, fgo1); }
                    if constexpr (layer == 0 && i == kGPI && kk == 2) gld2s<0>(f0, fgo, p.ws);
                    if constexpr (layer == 0 && i == kGCI) {
                        static_assert(kGCI + KR20 > 40, "");
                        if constexpr (kk == 0) { __builtin_amdgcn_s_waitcnt(0x0F70); pin(f0); gld4s<0>(gl[0], glo, p.ws); }
                        if constexpr (kk == 1) { gld4s<0>(gl[1], glo, gb1); gld4s<0>(gl[2], glo, gb2); }
                        if constexpr (kk == 3) ne1(glate, tagv, f0);
                    }
                    if constexpr (pv >= 0) ar[pv % RING2] = ld128s(rn, ho[pv / 10] + (pv & 1) * 1024, ks[pv % 10 >> 1]);
                    if constexpr (kk == 2 && m >= 1 && s >= 1 && s < 6) *(f32x4*)(smem + dst[m - 1] + (s - 1) * 1024) = a2[m - 1][s - 1];
                    site<4000 + 1000 * layer + 5 * i + kk>();
                    if constexpr (i >= 37) a2[i - 37][kk] = *(const f32x4*)(smem + srco + (i - 37) * 5120 + kk * 1024);
                    SCHED();
                });
                if constexpr (i == 10) { asm volatile("s_waitcnt lgkmcnt(0)" ::: "memory"); __builtin_amdgcn_s_barrier(); asm volatile("" ::: "memory"); }
                if constexpr (i == 28 && layer == 0) {
#pragma unroll
                    for (int nb = 0; nb < 5; ++nb) { a2[3][nb] = f32x4{zz[nb][0], zz[nb][1], zz[nb][2], zz[nb][3]}; pin(a2[3][nb]); }
                }
                if constexpr (i == 36) {
                    asm volatile("s_waitcnt lgkmcnt(0)" ::: "memory");
                    __builtin_amdgcn_s_barrier(); asm volatile("" ::: "memory");
                }
                if constexpr (STAMPS == 1 && (i == 0 || i == 9 || i == 10 || i == 15 || i == 25 || i == 35 || i == 36)) { STAMP((layer ? 51 : 41) + (i == 0 ? 0 : i == 9 ? 1 : i == 10 ? 2 : i == 15 ? 3 : i == 25 ? 4 : i == 35 ? 5 : 6)) }
                SCHED();
            });
            STAMP(layer ? 18 : 8)
        }
#pragma unroll
        for (int nb = 0; nb < 5; ++nb) a2[0][nb] = (a2[0][nb] + a2[1][nb]) + a2[2][nb];
        asm volatile("s_nop 1" : "+v"(a2[3][0]), "+v"(a2[3][1]), "+v"(a2[3][2]), "+v"(a2[3][3]), "+v"(a2[3][4]),
                     "+v"(a2[0][0]), "+v"(a2[0][1]), "+v"(a2[0][2]), "+v"(a2[0][3]), "+v"(a2[0][4]));
        float ty;
        {
            f32x4 s1, s2;
#pragma unroll
            for (int nb = 0; nb < 5; ++nb) {
                const f32x4 y = a2[3][nb] + a2[0][nb];
                xin[nb] = y;
                if (nb == 0) { s1 = y; s2 = y * y; } else { s1 += y; s2 += y * y; }
            }
            ty = xsum2h(hsum4(s1), hsum4(s2));
            st64(v2u{__float_as_uint(ty), tag0}, rn, sgs);
        }
        SCHED();
        STAMP(layer ? 19 : 9)
        if (layer == 0 && __builtin_expect(glate != 0ull, 0)) {
            asm volatile("s_waitcnt vmcnt(0)" : "+v"(gl[0]), "+v"(gl[1]), "+v"(gl[2])::"memory");
            WAVE_WAIT({ f0 = ld64(rs, fgo); ok = ISTAG(f0); })
            gld4t<0>(gl[0], glo, p.ws); gld4t<0>(gl[1], glo, gb1); gld4t<0>(gl[2], glo, gb2);
            asm volatile("s_waitcnt vmcnt(0)" : "+v"(gl[0]), "+v"(gl[1]), "+v"(gl[2])::"memory");
        }
        if (layer == 0) {
            asm volatile("s_waitcnt vmcnt(1)" : "+v"(gl[0]), "+v"(gl[1]), "+v"(gl[2])::"memory");
            *(v4u*)(smem + gw) = gl[0]; *(v4u*)(smem + gw + 1280) = gl[1]; *(v4u*)(smem + gw + 1920) = gl[2];
            asm volatile("s_waitcnt lgkmcnt(0)" ::: "memory");
            __builtin_amdgcn_s_barrier(); asm volatile("" ::: "memory");
#pragma unroll
            for (int nb = 0; nb < 5; ++nb) gt[nb] = __builtin_bit_cast(v2u, trread(smem, gr + 640 * nb));
        } else {
            ACC_TIE5(gt);
            if constexpr (OTR) {
#pragma unroll
                for (int nb = 0; nb < 5; ++nb) *(f32x4*)(smem + otw + nb * 64) = xin[nb];
#pragma unroll
                for (int nb = 0; nb < 5; ++nb) xin[nb] = *(const f32x4*)(smem + (nb < 4 ? otr0 + (nb & 1) * 8 * 336 + (nb >> 1) * 128 : otr4));
            }
        }
        SCHED();
        auto sgp = LAMBDA(p_) {
            constexpr int P = layer ? SGP1 : SGP0;
            static_for<3>(LAMBDA(k_) { constexpr int d = P >> (16 - 8 * k_.value); if constexpr (((d >> 4) & 15) == p_.value) { gran_req<32 + 8 * k_.value, d & 15>(sgo, gn); SCHED(); } });
        };
        sgp(IC(0));
        f32x4 gf[5], yg[5];
#pragma unroll
        for (int nb = 0; nb < 5; ++nb) { gf[nb] = unpk4(gt[nb]); yg[nb] = xin[nb] * gf[nb]; pin(gf[nb]); pin(yg[nb]); }
        SCHED();
        sgp(IC(1));
        int lane_o;
        asm volatile("v_mbcnt_lo_u32_b32 %0, -1, 0\n\tv_mbcnt_hi_u32_b32 %0, -1, %0" : "=v"(lane_o));
        const int lane = lane_o, c = lane & 15, q = lane >> 4;
        unsigned xs[3];
#pragma unroll
        for (int i = 0; i < 3; ++i) {
            const int ch = 10 * nt2 + 4 * i + 2 * (q & 1) + (q >> 1);
            xs[i] = N_X2 + (unsigned)((2 * (mt2 & 1) + (w >> 1)) * 5 * NB_ + (ch >> 4) * NB_ + ((c >> 2) & 1) * 4096 + (ch & 15) * 256 + (8 * (w & 1) + 4 * (c >> 3) + (c & 3)) * 16);
        }
        unsigned pso = q < 2 ? N_PS + fine + (unsigned)((((mt2 & 1) * 4 + w) * 8 + nt2) * 128 + c * 8 + q * 4) : kNone;
        unsigned fxo1 = N_FX + fine + (unsigned)(((2 * (mt2 & 1) + (w >> 1)) * 16 + nt2 * 2 + (w & 1)) * 128);
        unsigned oo = OTR ? (unsigned)(((64 * mt2 + 16 * w + (lane >> 3)) * D_ + 80 * nt2) * 4 + (nt2 & 1) * 64 + (lane & 7) * 16) : (unsigned)(((64 * mt2 + 16 * w + c) * D_ + 80 * nt2 + 4 * q) * 4);
        unsigned oo1 = oo + 8 * D_ * 4, oo4 = (unsigned)(((64 * mt2 + 16 * w + (lane >> 3) + (lane & 4) * 2) * D_ + 80 * nt2) * 4 + 256 - (nt2 & 1) * 256 + (lane & 3) * 16);
        int bpi = (lane >> 3) * 4;
        u64 up = 0xF0F0F0F0F0F0F0F0ull;
        xs[2] = (q & 1) ? kNone : xs[2];
        fxo1 = lane ? kNone : fxo1;
        if (layer == 0) { pin(xs[0]); pin(xs[1]); pin(xs[2]); pin(pso); pin(fxo1); } else { pin(oo); if (OTR) { pin(oo1); pin(oo4); pin(bpi); pins(up); } }
        SCHED();
        {
            float a, d;
            sgp(IC(2));
            room<(layer ? 900 : 1500)>(); site<9 + 10 * layer>();
            pipe_gran(a, d, sgo, gn, tag0, dead);
            STAMP(layer ? 20 : 10)
            xsum2(a, d);
            const float me = a * (1.0f / D_);
            const float ri = __builtin_amdgcn_rsqf(fmaxf(d * (1.0f / D_) - me * me, 0.f) + 1e-5f), mr = -me * ri;
            float rv[3] = {ri, ri, ri}, mv[3] = {mr, mr, mr};
            if constexpr (layer == 1 && OTR) {
#pragma unroll
                for (int i = 0; i < 2; ++i) { rv[i] = __int_as_float(__builtin_amdgcn_ds_bpermute(bpi + 32 * i, __float_as_int(ri))); mv[i] = __int_as_float(__builtin_amdgcn_ds_bpermute(bpi + 32 * i, __float_as_int(mr))); }
                SCHED();
            }
            static_for<5>(LAMBDA(n_) {
                constexpr int nb = layer ? n_.value : (n_.value + 4) % 5;
                if constexpr (layer == 1 && OTR && nb == 4) { sel1(rv[2], __float_as_uint(rv[0]), __float_as_uint(rv[1]), up); sel1(mv[2], __float_as_uint(mv[0]), __float_as_uint(mv[1]), up); }
                xin[nb] = yg[nb] * rv[nb < 4 ? nb & 1 : 2] + (gf[nb] * mv[nb < 4 ? nb & 1 : 2] + xin[nb]);
                if constexpr (layer == 1) { __builtin_amdgcn_raw_buffer_store_b128(__builtin_bit_cast(v4u, xin[nb]), ro, !OTR ? oo + 64 * nb : nb == 4 ? oo4 : ((nb & 1) ? oo1 : oo) + (nb >> 1) * 128, 0, OUTP); SCHED(); }
                else if constexpr (nb & 5) {
                    const f32x4 xa = xin[nb & 6], xb = xin[nb];
                    const auto r0 = __builtin_amdgcn_permlane16_swap(cvtpk(xa.x, xa.y), cvtpk(xb.x, xb.y), false, false);
                    const auto r1 = __builtin_amdgcn_permlane16_swap(cvtpk(xa.z, xa.w), cvtpk(xb.z, xb.w), false, false);
                    st128(v4u{r0[0], r1[0], r0[1], r1[1]}, rn, xs[nb >> 1]);
                    SCHED();
                }
            });
        }
        if constexpr (layer == 0) {
            {
                f32x4 s1, s2;
#pragma unroll
                for (int nb = 0; nb < 5; ++nb) { const f32x4 x = xin[nb]; if (nb == 0) { s1 = x; s2 = x * x; } else { s1 += x; s2 += x * x; } }
                __builtin_amdgcn_raw_buffer_store_b32(__float_as_uint(xsum2h(hsum4(s1), hsum4(s2))), rn, pso, 0, SC1);
            }
            SCHED();
            site<12>();
        int wa[2][3];
#pragma unroll
        for (int i = 0; i < 2; ++i) {
            wa[i][0] = (c >> 3) * 20480 + (c & 7) * 128 + (((4 * i + q) ^ (c & 6)) * 16); wa[i][1] = (wa[i][0] ^ 16) + 10240;
            wa[i][2] = 81920 + (c & 1) * 10240 + (c >> 1) * 128 + (((4 * i + q) ^ ((c & 1) | ((c >> 2) << 1))) * 16);
            pin(wa[i][0]); pin(wa[i][1]); pin(wa[i][2]);
        }
            unsigned fxo = N_FX + fine + (unsigned)(w * 2048 + (lane >> 2) * 128);
            unsigned xo = N_X2 + (unsigned)(w * 5 * NB_ + lane * 16), xo1 = xo + 4096u;
            const int row = 8 * q + (c & 7);
            unsigned po = N_PS + fine + (unsigned)(((2 * w + (row >> 4)) * 8 + 4 * (c >> 3)) * 128 + (row & 15) * 8);
            int rsw = l_rs(1, 0) + w * INBOX + lane * 4;
            pin(rsw); pin(fxo); pin(xo); pin(xo1); pin(po);
            v4u wf[2][5];
#pragma unroll
            for (int nb = 0; nb < 5; ++nb) wf[0][nb] = WF(nb, 0);
            SCHED();
            DRAIN();
            st64(tagv, rn, fxo1);
            if (b == 255 && w == 0) FLAG(OFF_NONCE)
            STAMP(11)
            if (w == 0) tour<4>();
            if constexpr (OTR) {
                const int sh = (nt2 & 1) * 64, r4 = (lane >> 3) + (lane & 4) * 2;
                const int a0 = c * 336 + q * 16, a1 = (lane >> 3) * 336 + sh + (lane & 7) * 16, a2_ = r4 * 336 + 256 - 4 * sh + (lane & 3) * 16;
                char* tw = smem + SC + w * INBOX + 12288;
#pragma unroll
                for (int nb = 0; nb < 5; ++nb) *(v2u*)(tw + a0 / 2 + nb * 32) = gt[nb];
#pragma unroll
                for (int nb = 0; nb < 5; ++nb) gt[nb] = *(const v2u*)(tw + (nb < 4 ? a1 / 2 + (nb & 1) * 8 * 168 + (nb >> 1) * 64 : a2_ / 2));
                otw = SC + w * INBOX + a0; otr0 = SC + w * INBOX + a1; otr4 = SC + w * INBOX + a2_;
                pin(otw); pin(otr0); pin(otr4);
            }
            addrs(std::integral_constant<int, 1>{}, lane, w, b);
            ACC_TIE5(gt);
            v2u pt[4];
            skip<SKIP5>();
            pipe_flag<KPF0>(pt, fxo, gn, tagv, dead);
            ORDER();
            STAMP(12)
            v4u ar[2 * RING1];
#define XF(n) ld128s(rn, (((n) & 1) ? xo1 : xo) + (((n) >> 1) & 3) * 1024, ((n) >> 3) * NB_)
#pragma unroll
            for (int n = 0; n < KI1; ++n) { ar[n] = XF(n); SCHED(); }
            v2u ps[4];
            float t1, t2, rinv, mri, rr_[8], rm_[8];
            static_for<20>(LAMBDA(s_) {
                constexpr int s = s_.value, cu = s & 1, nx = cu ^ 1;
                LGKM0() SCHED();
                static_for<10>(LAMBDA(g_) {
                    constexpr int g = g_.value, mb = g / 5, k = g % 5;
                    if constexpr (s == 0) mf0(acc[mb][k], ar[mb], wf[0][k]); else mf(acc[mb][k], ar[(2 * s + mb) % (2 * RING1)], wf[cu][k]);
                    SCHED();
                    if constexpr (s == 0 && g == 0) { pin(pt[0]); pin(pt[1]); pin(pt[2]); pin(pt[3]); }
                    if constexpr (s < 19 && g < 5) wf[nx][g] = WF(g, s + 1);
                    constexpr int rq = ring_req(10 * s + g, KI1, 2 * RING1, 4);
                    if constexpr (rq >= 100) ps[rq - 100] = ld64(rn, po + (rq - 100) * 128);
                    else if constexpr (rq >= 0) ar[rq % (2 * RING1)] = XF(rq);
                    constexpr int n = g >= 6 ? 4 * (s - KS1) + g - 6 : -1;
                    if constexpr (n == 0) { t1 = __uint_as_float(ps[0].x) + __uint_as_float(ps[1].x); pin(t1); t2 = __uint_as_float(ps[0].y) + __uint_as_float(ps[1].y); pin(t2); }
                    if constexpr (n == 1) { t1 += __uint_as_float(ps[2].x); pin(t1); t2 += __uint_as_float(ps[2].y); pin(t2); }
                    if constexpr (n == 2) { t1 += __uint_as_float(ps[3].x); pin(t1); t2 += __uint_as_float(ps[3].y); pin(t2); }
                    if constexpr (n == 3) { t1 += dpp<0x128>(t1); t2 += dpp<0x128>(t2); pin(t1); pin(t2); }
                    if constexpr (n == 4) { t1 *= (1.0f / D_); pin(t1); t2 *= (1.0f / D_); pin(t2); }
                    if constexpr (n == 5) { t2 = fmaxf(t2 - t1 * t1, 0.f); pin(t2); }
                    if constexpr (n == 6) { rinv = __builtin_amdgcn_rsqf(t2 + 1e-5f); pin(rinv); }
                    if constexpr (n == 8) { mri = -t1 * rinv; pin(mri); }
                    if constexpr (n == 9) { t1 = rinv; mrg8(t1, mri); }
                    if constexpr (n == 10) *(float*)(smem + rsw) = t1;
                    if constexpr (n >= 11 && n < 19) bc2b<n - 11>(rr_[n - 11], rm_[n - 11], rinv, mri);
                    if constexpr (n >= 19 && n < 39) TT_GAP(n - 19)
                    site<6000 + 10 * s + g>();
                    SCHED();
                });
            });
            rr[0] = f32x4{rr_[0], rr_[1], rr_[2], rr_[3]}; rr[1] = f32x4{rr_[4], rr_[5], rr_[6], rr_[7]};
            ACC_FENCE10(acc[0], acc[1]);
            STAMP(13)
        } else {
            STAMP(21)
            STAMP_DUMP()
            asm volatile("kpf_end:");
        }
    };
    layer_body(std::integral_constant<int, 0>{});
    layer_body(std::integral_constant<int, 1>{});
}

extern "C" void kernel_launch(void* const* d_in, const int* in_sizes, int n_in,
                              void* d_out, int out_size, void* d_ws, size_t ws_size,
                              hipStream_t stream) {
    Params p;
    p.x0 = (const u16*)d_in[0];
    p.Lre = (const u16*)d_in[1];
    p.Lim = (const u16*)d_in[2];
    p.Bre = (const u16*)d_in[3];
    p.Bim = (const u16*)d_in[4];
    p.Cre = (const u16*)d_in[5];
    p.Cim = (const u16*)d_in[6];
    p.logdt = (const u16*)d_in[7];
    p.Dskip = (const u16*)d_in[8];
    p.k = (const u16*)d_in[10];
    p.v = (const u16*)d_in[11];
    p.shk = (const u16*)d_in[12];
    p.ssmk = (const u16*)d_in[13];
    p.ssmD = (const u16*)d_in[14];
    p.ws = (char*)d_ws;
    p.out = (float*)d_out;
    k_main<<<256, 256, 0, stream>>>(p);
}
```

```cpp
#include <hip/hip_runtime.h>
#include <stdint.h>
#include <utility>

#define STAMPS 0

typedef unsigned short u16;
typedef unsigned long long u64;
typedef unsigned v4u __attribute__((ext_vector_type(4)));
typedef unsigned v2u __attribute__((ext_vector_type(2)));
typedef float f32x4 __attribute__((ext_vector_type(4)));
typedef float f32x2 __attribute__((ext_vector_type(2)));
typedef short s16x4 __attribute__((ext_vector_type(4)));
typedef __bf16 bf16x2v __attribute__((ext_vector_type(2)));
#define LAS __attribute__((address_space(3)))
#define DI __device__ __forceinline__

constexpr int S_ = 2048, D_ = 640;
#define KR1 4
#define KR2 8
constexpr int RING1 = KR1;
#define KCR1 4
constexpr int kCR1 = KCR1;
#define KXA 0
#define KN3E 16
#define KN3 12
constexpr int kXA = KXA;
constexpr int kN3E = KN3E, kN3 = KN3;
#define KFHK 8
#define KFHP 7
constexpr int kFHK = KFHK, kFHP = KFHP;
#define KGPI 31
#define KGCI 36
#define CPF 1
#define LBE 0
#define LBR 1
#define W3O 1
#define KI2 6
#define KI1 6
#define KS1 8
#define RSL 2
#define KR20 10
constexpr int kGPI = KGPI, kGCI = KGCI;

#define KPAD 0
#define KPS 0
#define GPAD 0
constexpr int KSTR = 4096 + KPAD, GSTR = 4096 + GPAD;
constexpr unsigned OFF_NONCE = 0;
constexpr unsigned OFF_ST = 4096;
constexpr unsigned OFF_FW = OFF_ST + 65536;
constexpr unsigned OFF_FK = OFF_FW + 131072;
constexpr unsigned OFF_VT = OFF_FK + 65536;
constexpr unsigned OFF_FG = OFF_FK + 81920;
constexpr unsigned OFF_EG = OFF_FG + 98304;
constexpr unsigned OFF_KVT = OFF_EG + 327680;
constexpr unsigned OFF_GT = OFF_KVT + 640 * 4608;
constexpr unsigned OFF_W2F = OFF_GT + 640 * 4608;
constexpr unsigned WS_BYTES = OFF_W2F + 1638400;
constexpr unsigned OFF_GRP = 0x2000000, NB_ = 16384;
constexpr unsigned N_HF = 0;
constexpr unsigned N_X2 = 80 * NB_;
constexpr unsigned N_SG = 100 * NB_;
constexpr unsigned N_PS = 108 * NB_;
constexpr unsigned N_FH = 110 * NB_;
constexpr unsigned N_FX = 114 * NB_;
constexpr unsigned N_END = 116 * NB_;
constexpr unsigned kNone = 0x40000000u;
constexpr unsigned OFF_DBG = 0x1800000;
#define TILES() const int xq = b & 7, j = b >> 3, mt1 = 2 * xq + (j >> 4), nt1 = j & 15, mt2 = 4 * xq + (j >> 3), nt2 = j & 7, i32 = 16 * (mt2 & 1) + mt1;

constexpr int SC = 102400;
constexpr int REGSZ = 13056;
constexpr int L_XR = SC;
constexpr int L_CO = SC + 12288;
constexpr int L_KB = SC + 51712;
constexpr int L_TAB = SC + 55808;
constexpr int L_CW = SC + 39168;
constexpr int INBOX = 15360;
constexpr int l_rs(int l, int w) { return l ? SC + w * INBOX : L_KB + w * 256; }
constexpr int l_sg(int l, int w) { return l ? SC + w * INBOX + 256 : SC + (w == 0 ? 43264 : w == 1 ? 46080 : w == 2 ? 52736 : 56320); }
constexpr int l_tb(int l, int w) { return l ? SC + w * INBOX + 2048 : SC + 40960 + w * 5120; }
constexpr int LDS_BYTES = 163840;
constexpr int CV_E = 4608;
constexpr int CV_O = CV_E + (1056 + 15) * 4;

struct Params {
    const u16 *x0, *Lre, *Lim, *Bre, *Bim, *Cre, *Cim, *logdt, *Dskip, *k, *v, *shk, *ssmk, *ssmD;
    char* ws;
    float* out;
};

#define MF "v_mfma_f32_16x16x32_bf16 "
DI void mf(f32x4& c, v4u a, v4u b) { asm(MF "%0, %1, %2, %0" : "+v"(c) : "v"(a), "v"(b)); }
DI void mf0(f32x4& c, v4u a, v4u b) { asm(MF "%0, %1, %2, 0" : "=&v"(c) : "v"(a), "v"(b)); }
constexpr int W2R = 56;
#define WA(n) "a[%c" #n ":%c" #n "+3]"
template <int R> DI void mfa(f32x4& c, v4u a) { asm(MF "%0, " WA(2) ", %1, %0" : "+v"(c) : "v"(a), "i"(W2R + 4 * R)); }
template <int R> DI void mfa0(f32x4& c, v4u a) { asm(MF "%0, " WA(2) ", %1, 0" : "=&v"(c) : "v"(a), "i"(W2R + 4 * R)); }
#define ACC_FENCE10(c, d) asm volatile("" : "+v"(c[0]), "+v"(c[1]), "+v"(c[2]), "+v"(c[3]), "+v"(c[4]), "+v"(d[0]), "+v"(d[1]), "+v"(d[2]), "+v"(d[3]), "+v"(d[4]))
#define ACC_TIE5(c) asm volatile("" : "+v"(c[0]), "+v"(c[1]), "+v"(c[2]), "+v"(c[3]), "+v"(c[4]))
#define QP " quad_perm:[1,0,3,2] row_mask:0xf bank_mask:0xf"
struct St { f32x2 a, b; float c; };
struct Lm { f32x2 a, b; float r, s; };
#define PK1 " op_sel_hi:[0,1,1]\n\t"
#define PK2 " op_sel:[1,1,0] op_sel_hi:[1,0,1] neg_lo:[1,0,0]"
#define ST_(t0, t1, t2, h0, h1, h2, b0, b1, b2, l0, l1, l2, l3)                                                                                        \
    "v_pk_fma_f32 %" #t0 ", %" #l0 ", %" #h0 ", %" #b0 PK1 "v_pk_fma_f32 %" #t1 ", %" #l1 ", %" #h1 ", %" #b1 PK1 "v_fma_f32 %" #t2 ", %" #l2 ", %" #h2 ", %" #b2 "\n\t" \
    "v_pk_fma_f32 %" #t0 ", %" #l0 ", %" #h0 ", %" #t0 PK2 "\n\tv_pk_fma_f32 %" #t1 ", %" #l1 ", %" #h1 ", %" #t1 PK2 "\n\tv_fmac_f32_dpp %" #t2 ", %" #h2 ", %" #l3 QP "\n\t"
#define SO(t) "=&v"(t.a), "=&v"(t.b), "=&v"(t.c)
#define SI(t) "v"(t.a), "v"(t.b), "v"(t.c)
#define LI(l) "v"(l.a), "v"(l.b), "v"(l.r), "v"(l.s)
DI void run3(St& t, const St& h, const St* b, const Lm& l) {
    St u;
    asm(ST_(0, 1, 2, 6, 7, 8, 9, 10, 11, 18, 19, 20, 21) ST_(3, 4, 5, 0, 1, 2, 12, 13, 14, 18, 19, 20, 21) ST_(0, 1, 2, 3, 4, 5, 15, 16, 17, 18, 19, 20, 21)
        : SO(t), SO(u) : SI(h), SI(b[0]), SI(b[1]), SI(b[2]), LI(l));
}
DI void run4(St& t, const St& h, const St* b, const Lm& l) {
    St u;
    asm(ST_(3, 4, 5, 6, 7, 8, 9, 10, 11, 21, 22, 23, 24) ST_(0, 1, 2, 3, 4, 5, 12, 13, 14, 21, 22, 23, 24) ST_(3, 4, 5, 0, 1, 2, 15, 16, 17, 21, 22, 23, 24) ST_(0, 1, 2, 3, 4, 5, 18, 19, 20, 21, 22, 23, 24)
        : SO(t), SO(u) : SI(h), SI(b[0]), SI(b[1]), SI(b[2]), SI(b[3]), LI(l));
}
#define AC_(t0, t2, h0, h2, b0, b2, l0, l2, l3)                                                                                   \
    "v_pk_fma_f32 %" #t0 ", %" #l0 ", %" #h0 ", %" #b0 PK1 "v_fma_f32 %" #t2 ", %" #l2 ", %" #h2 ", %" #b2 "\n\t"               \
    "v_pk_fma_f32 %" #t0 ", %" #l0 ", %" #h0 ", %" #t0 PK2 "\n\tv_fmac_f32_dpp %" #t2 ", %" #h2 ", %" #l3 QP "\n\t"
#define AO(t) "=&v"(t.a), "=&v"(t.c)
#define AI(t) "v"(t.a), "v"(t.c)
DI void keep14(St* t, const St& h, const St& x, const St* b, const Lm& lq, const Lm& l) {
    St u;
    asm(AC_(8, 9, 10, 11, 12, 13, 22, 23, 24) AC_(0, 1, 8, 9, 14, 15, 25, 26, 27) AC_(2, 3, 0, 1, 16, 17, 25, 26, 27) AC_(4, 5, 2, 3, 18, 19, 25, 26, 27) AC_(6, 7, 4, 5, 20, 21, 25, 26, 27)
        : AO(t[0]), AO(t[1]), AO(t[2]), AO(t[3]), AO(u) : AI(h), AI(x), AI(b[0]), AI(b[1]), AI(b[2]), AI(b[3]), "v"(lq.a), "v"(lq.r), "v"(lq.s), "v"(l.a), "v"(l.r), "v"(l.s));
}
DI void keep4(St* t, const St& h, const St* b, const Lm& l) {
    asm(AC_(0, 1, 8, 9, 10, 11, 18, 19, 20) AC_(2, 3, 0, 1, 12, 13, 18, 19, 20) AC_(4, 5, 2, 3, 14, 15, 18, 19, 20) AC_(6, 7, 4, 5, 16, 17, 18, 19, 20)
        : AO(t[0]), AO(t[1]), AO(t[2]), AO(t[3]) : AI(h), AI(b[0]), AI(b[1]), AI(b[2]), AI(b[3]), "v"(l.a), "v"(l.r), "v"(l.s));
}
DI void b3cv(f32x2* t, unsigned* o, f32x2 h, f32x2 b0, f32x2 b1, f32x2 b2, f32x2 l0, f32x2 l, const float* e) {
    asm("v_pk_fma_f32 %0, %11, %7, %8" PK1 "v_cvt_pk_bf16_f32 %3, %13, %14\n\tv_pk_fma_f32 %0, %11, %7, %0" PK2 "\n\tv_cvt_pk_bf16_f32 %4, %15, %16\n\t"
        "v_pk_fma_f32 %1, %12, %0, %9" PK1 "v_cvt_pk_bf16_f32 %5, %17, %18\n\tv_pk_fma_f32 %1, %12, %0, %1" PK2 "\n\tv_cvt_pk_bf16_f32 %6, %19, %20\n\t"
        "v_pk_fma_f32 %2, %12, %1, %10" PK1
        : "=&v"(t[0]), "=&v"(t[1]), "=&v"(t[2]), "=&v"(o[0]), "=&v"(o[1]), "=&v"(o[2]), "=&v"(o[3])
        : "v"(h), "v"(b0), "v"(b1), "v"(b2), "v"(l0), "v"(l), "v"(e[0]), "v"(e[1]), "v"(e[2]), "v"(e[3]), "v"(e[4]), "v"(e[5]), "v"(e[6]), "v"(e[7]));
}
DI void bfin(f32x2& t, f32x2 h, f32x2 l) { asm("v_pk_fma_f32 %0, %2, %1, %0" PK2 : "+v"(t) : "v"(h), "v"(l)); }
#define UNDEF8(c) asm volatile("" : "=v"(c[0]), "=v"(c[1]), "=v"(c[2]), "=v"(c[3]), "=v"(c[4]), "=v"(c[5]), "=v"(c[6]), "=v"(c[7]))
DI unsigned cvtpk(float lo, float hi) {
    f32x2 v = {lo, hi};
    return __builtin_bit_cast(unsigned, __builtin_convertvector(v, bf16x2v));
}
DI float lo16f(unsigned u) { return __uint_as_float(u << 16); }
DI float hi16f(unsigned u) { return __uint_as_float(u & 0xffff0000u); }
DI float rbf(float v) { return lo16f(cvtpk(v, 0.f)); }
template <int CTRL> DI float dpp(float x) {
    return __int_as_float(__builtin_amdgcn_update_dpp(0, __float_as_int(x), CTRL, 0xF, 0xF, true));
}
DI float rowsum16(float x) {
    x += dpp<0x128>(x); x += dpp<0x124>(x); x += dpp<0x122>(x); x += dpp<0x121>(x);
    return x;
}
DI float xsum32(float x) { auto r = __builtin_amdgcn_permlane32_swap(__float_as_uint(x), __float_as_uint(x), false, false); return __uint_as_float(r[0]) + __uint_as_float(r[1]); }
DI void xsum2(float& a, float& d) {
    auto r = __builtin_amdgcn_permlane16_swap(__float_as_uint(a), __float_as_uint(d), false, false);
    const float s = __uint_as_float(r[0]) + __uint_as_float(r[1]);
    const float t = xsum32(s);
    auto u = __builtin_amdgcn_permlane16_swap(__float_as_uint(t), __float_as_uint(t), false, false);
    a = __uint_as_float(u[0]); d = __uint_as_float(u[1]);
}
DI float xsum2h(float a, float d) {
    auto r = __builtin_amdgcn_permlane16_swap(__float_as_uint(a), __float_as_uint(d), false, false);
    return xsum32(__uint_as_float(r[0]) + __uint_as_float(r[1]));
}
DI float hsum4(f32x4 v) { return (v.x + v.y) + (v.z + v.w); }
typedef __amdgpu_buffer_rsrc_t rsrc_t;
constexpr int SC1 = 16;
DI v4u ld128(rsrc_t rs, unsigned off) { return __builtin_amdgcn_raw_buffer_load_b128(rs, off, 0, SC1); }
DI v4u ld128s(rsrc_t rs, unsigned off, unsigned soff) { return __builtin_amdgcn_raw_buffer_load_b128(rs, off, soff, SC1); }
DI v2u ld64(rsrc_t rs, unsigned off) { return __builtin_amdgcn_raw_buffer_load_b64(rs, off, 0, SC1); }
DI void st128(v4u v, rsrc_t rs, unsigned off) { __builtin_amdgcn_raw_buffer_store_b128(v, rs, off, 0, SC1); }
DI void st64(v2u v, rsrc_t rs, unsigned off) { __builtin_amdgcn_raw_buffer_store_b64(v, rs, off, 0, SC1); }
#define LGKM0() { __builtin_amdgcn_s_waitcnt(0xC07F); asm volatile("" ::: "memory"); }
#define DRAIN() asm volatile("s_waitcnt vmcnt(0)" ::: "memory")
#define ORDER() __builtin_amdgcn_fence(__ATOMIC_ACQUIRE, "wavefront")
#define SCHED() __builtin_amdgcn_sched_barrier(0)
constexpr unsigned kSpinCap = 1u << 17;
#define WAVE_WAIT(...)                                                      \
    {                                                                       \
        unsigned spins_ = dead ? kSpinCap : 0u;                             \
        _Pragma("nounroll") for (;;) {                                      \
            asm volatile("" ::: "memory");                                  \
            bool ok;                                                        \
            __VA_ARGS__;                                                    \
            if (__builtin_amdgcn_ballot_w64(!ok) == 0ull) break;            \
            if (++spins_ > kSpinCap) { dead = true; break; }                \
            __builtin_amdgcn_s_sleep(1);                                    \
        }                                                                   \
        ORDER();                                                            \
    }
#define KPD1 1
#define KPDS 5
#define KPDS0 5
#define KPF0 1
#define KPH0 12
#define KPF1 4
constexpr int PD1 = KPD1, PDS = KPDS, PDS0 = KPDS0;
#define PL(r) "global_load_dwordx2 " r ", %5, %6 sc1\n\t"
#define PS_(r) "s_waitcnt vmcnt(1)\n\tv_cmp_ne_u64 vcc, %7, " r "\n\ts_cbranch_vccz 2f\n\t" PL(r)
template <int D0> DI void pipe_flag(v2u (&t)[4], unsigned off, const void* base, v2u tag, bool& dead) {
    unsigned n = dead ? 0u : kSpinCap;
    asm volatile("s_sleep %8\n\t" PL("%0") "s_sleep %9\n\t" PL("%1")
                 "1:\n\t" PS_("%0") PS_("%1") "s_sub_u32 %4, %4, 1\n\ts_cbranch_scc0 1b\n2:"
                 : "=&v"(t[0]), "=&v"(t[1]), "=&v"(t[2]), "=&v"(t[3]), "+s"(n) : "v"(off), "s"(base), "v"(tag), "i"(D0), "i"(KPF1) : "memory", "vcc", "scc");
    dead = __builtin_amdgcn_readfirstlane(n) == ~0u;
}
#define WAVE_RETRY(OK, ...) { if (__builtin_expect(__builtin_amdgcn_ballot_w64(!(OK)) != 0ull, 0)) WAVE_WAIT({ __VA_ARGS__; ok = OK; }) ORDER(); }
#define STAMP(i)
#define STAMP_DUMP()

#define PADS 9000, 1000, 1059, 1088, 2085, 9001, 9002, 4081, 9, 9003, 6, 9004, 9005, 6107, 14, 15, 9006, 18, 19, 9007, 21, 2, 9008
#define TW 2
#define TW0 2
#define KLB0 0
#define KLB1 4
#define SKIP0 0
#define SKIP5 0
constexpr int kPads[] = {PADS};
constexpr int pad_next(int id) {
    for (unsigned i = 0; i + 1 < sizeof(kPads) / sizeof(int); ++i) if (kPads[i] == id) return kPads[i + 1];
    return -1;
}
constexpr bool pads_unique() {
    for (unsigned i = 0; i < sizeof(kPads) / sizeof(int); ++i)
        for (unsigned k = 0; k < i; ++k) if (kPads[i] == kPads[k]) return false;
    return true;
}
static_assert(pads_unique(), "");
template <int ID> DI void site() {
    if constexpr (pad_next(ID) >= 0) asm volatile("kp%c0:\n\ts_cbranch_execz kp%c1" ::"i"(ID), "i"(pad_next(ID)));
    else asm volatile("");
}
template <int T> DI void tour() {
    if constexpr (pad_next(9000 + T) >= 0 && pad_next(9000 + T) != 9001 + T) asm volatile("s_mov_b64 exec, 0\n\ts_branch kp%c0\nkp%c1:\n\ts_mov_b64 exec, -1" ::"i"(pad_next(9000 + T)), "i"(9001 + T));
}
template <int N> DI void skip() { if constexpr (N > 0) asm volatile("s_branch 1f\n\t.rept %c0\n\ts_nop 0\n\t.endr\n1:" ::"i"(N)); }
template <int N> DI void room() { asm volatile("s_branch 1f\n\t.p2align 12, , %c0\n1:" ::"i"(N)); }
template <class F, int... I> DI void static_for_impl(std::integer_sequence<int, I...>, F&& f) { (f(std::integral_constant<int, I>{}), ...); }
template <int N, class F> DI void static_for(F&& f) { static_for_impl(std::make_integer_sequence<int, N>{}, f); }
#define LAMBDA(v) [&](auto v) __attribute__((always_inline))
DI f32x2 cmul(f32x2 a, f32x2 b) {
    f32x2 t;
    asm("v_pk_mul_f32 %0, %1, %2 op_sel_hi:[0,1]\n\ts_nop 0\n\tv_pk_fma_f32 %0, %1, %2, %0" PK2 "\n\ts_nop 0" : "=&v"(t) : "v"(a), "v"(b));
    return t;
}
DI f32x2 cfma(f32x2 a, f32x2 b, f32x2 c) {
    f32x2 t;
    asm("v_pk_fma_f32 %0, %1, %2, %3 op_sel_hi:[0,1,1]\n\ts_nop 0\n\tv_pk_fma_f32 %0, %1, %2, %0" PK2 "\n\ts_nop 0" : "=&v"(t) : "v"(a), "v"(b), "v"(c));
    return t;
}
DI void cfma4(f32x2 (&t)[4], f32x2 a, f32x2 b0, f32x2 b1, f32x2 b2, f32x2 b3, f32x2 c0, f32x2 c1, f32x2 c2, f32x2 c3) {
    asm("v_pk_fma_f32 %0, %4, %5, %9 op_sel_hi:[0,1,1]\n\tv_pk_fma_f32 %1, %4, %6, %10 op_sel_hi:[0,1,1]\n\tv_pk_fma_f32 %2, %4, %7, %11 op_sel_hi:[0,1,1]\n\tv_pk_fma_f32 %3, %4, %8, %12 op_sel_hi:[0,1,1]\n\t"
        "v_pk_fma_f32 %0, %4, %5, %0" PK2 "\n\tv_pk_fma_f32 %1, %4, %6, %1" PK2 "\n\tv_pk_fma_f32 %2, %4, %7, %2" PK2 "\n\tv_pk_fma_f32 %3, %4, %8, %3" PK2 "\n\ts_nop 0"
        : "=&v"(t[0]), "=&v"(t[1]), "=&v"(t[2]), "=&v"(t[3]) : "v"(a), "v"(b0), "v"(b1), "v"(b2), "v"(b3), "v"(c0), "v"(c1), "v"(c2), "v"(c3));
}
#define C4(t0, t1, t2, t3, a, b0, b1, b2, b3, c0, c1, c2, c3)                                                                                                         \
    "v_pk_fma_f32 %" #t0 ", %" #a ", %" #b0 ", %" #c0 PK1 "v_pk_fma_f32 %" #t1 ", %" #a ", %" #b1 ", %" #c1 PK1 "v_pk_fma_f32 %" #t2 ", %" #a ", %" #b2 ", %" #c2 PK1 "v_pk_fma_f32 %" #t3 ", %" #a ", %" #b3 ", %" #c3 PK1 \
    "v_pk_fma_f32 %" #t0 ", %" #a ", %" #b0 ", %" #t0 PK2 "\n\tv_pk_fma_f32 %" #t1 ", %" #a ", %" #b1 ", %" #t1 PK2 "\n\tv_pk_fma_f32 %" #t2 ", %" #a ", %" #b2 ", %" #t2 PK2 "\n\tv_pk_fma_f32 %" #t3 ", %" #a ", %" #b3 ", %" #t3 PK2 "\n\t"
DI void cfma4n(f32x2 (&t)[4], f32x2 a, f32x2 b0, f32x2 b1, f32x2 b2, f32x2 b3, f32x2 c0, f32x2 c1, f32x2 c2, f32x2 c3) {
    asm(C4(0, 1, 2, 3, 4, 5, 6, 7, 8, 9, 10, 11, 12) : "=&v"(t[0]), "=&v"(t[1]), "=&v"(t[2]), "=&v"(t[3]) : "v"(a), "v"(b0), "v"(b1), "v"(b2), "v"(b3), "v"(c0), "v"(c1), "v"(c2), "v"(c3));
}
DI void agg(unsigned& er, unsigned& ei, f32x2 (&x)[4], f32x2& a, const f32x2 (&u)[4], const f32x2 (&s)[16], f32x2 l8, f32x2 l32, f32x2 l64) {
    f32x2 v[4], b;
    asm(C4(4, 5, 6, 7, 22, 10, 11, 12, 13, 14, 15, 16, 17) C4(0, 1, 2, 3, 22, 4, 5, 6, 7, 18, 19, 20, 21)
        "v_pk_fma_f32 %8, %23, %0, %1" PK1 "v_pk_fma_f32 %9, %23, %2, %3" PK1 "v_pk_fma_f32 %8, %23, %0, %8" PK2 "\n\tv_pk_fma_f32 %9, %23, %2, %9" PK2
        : "=&v"(x[0]), "=&v"(x[1]), "=&v"(x[2]), "=&v"(x[3]), "=&v"(v[0]), "=&v"(v[1]), "=&v"(v[2]), "=&v"(v[3]), "=&v"(a), "=&v"(b)
        : "v"(u[0]), "v"(u[1]), "v"(u[2]), "v"(u[3]), "v"(s[2]), "v"(s[6]), "v"(s[10]), "v"(s[14]), "v"(s[3]), "v"(s[7]), "v"(s[11]), "v"(s[15]), "v"(l8), "v"(l32));
    asm("s_nop 0\n\tv_fma_f32 %0, %2, %6, %4\n\tv_fma_f32 %1, %2, %7, %5\n\tv_fma_f32 %0, -%3, %7, %0\n\tv_fma_f32 %1, %3, %6, %1"
        : "=&v"(er), "=&v"(ei) : "v"(a.x), "v"(a.y), "v"(b.x), "v"(b.y), "v"(l64.x), "v"(l64.y));
}
DI void gacc2(f32x2& h0, f32x2& h1, f32x2 w0, f32x2 w1, v4u e0, v4u e1) {
    const v2u a0 = {e0.x, e0.y}, b0 = {e0.z, e0.w}, a1 = {e1.x, e1.y}, b1 = {e1.z, e1.w};
    asm("v_pk_fma_f32 %0, %4, %2, %0 op_sel_hi:[0,1,1]\n\tv_pk_fma_f32 %1, %6, %3, %1 op_sel_hi:[0,1,1]\n\t"
        "v_pk_fma_f32 %0, %5, %2, %0 op_sel:[0,1,0] op_sel_hi:[0,0,1] neg_lo:[1,0,0]\n\tv_pk_fma_f32 %1, %7, %3, %1 op_sel:[0,1,0] op_sel_hi:[0,0,1] neg_lo:[1,0,0]"
        : "+v"(h0), "+v"(h1) : "v"(w0), "v"(w1), "v"(a0), "v"(b0), "v"(a1), "v"(b1));
}
DI s16x4 trread(char* smem, int byteoff) {
    return __builtin_amdgcn_ds_read_tr16_b64_v4i16((LAS s16x4*)(smem + byteoff));
}

#define CV_LOAD_A(A, a, rr_, hf) { const unsigned* ap_ = (const unsigned*)(smem + ab[(a) >> 1] + (((a) & 1) ? 0 : 512) + 64 * (7 - (rr_)) + 8 * (hf)); A[a][2 * (hf)] = ap_[0]; A[a][2 * (hf) + 1] = ap_[1]; }
#define CV_LOAD_B(B, e, rr_) { B[e] = *(const v4u*)(smem + bb + 64 * (7 - (rr_)) + 512 * (e)); }
template <int R, bool PF, class F, class G>
DI void conv_round(f32x4 (&ca)[8], v4u (&A)[8], const v4u (&B)[8], v4u (&An)[8], v4u (&Bn)[8], const char* smem, const int (&ab)[4], int bb, F&& fill, G&& last) {
    static_for<36>(LAMBDA(g_) {
        constexpr int g = g_.value;
        constexpr int a = g < 8 ? 0 : g < 15 ? 1 : g < 21 ? 2 : g < 26 ? 3 : g < 30 ? 4 : g < 33 ? 5 : g < 35 ? 6 : 7;
        constexpr int e = g - (a * 8 - a * (a - 1) / 2);
        if constexpr (R == 0 && a == 0) mf0(ca[e], A[0], B[e]); else mf(ca[a + e], A[a], B[e]);
        SCHED();
        if constexpr (R == 0 && g < 16 && (g & 3) != 3) { constexpr int n = g - (g >> 2); CV_LOAD_A(A, 2 + (n >> 1), 0, n & 1) SCHED(); }
        if constexpr (PF && g < 32 && (g & 3) != 3) { if constexpr ((g & 3) == 2) CV_LOAD_B(Bn, g >> 2, R + 1) else CV_LOAD_A(An, g >> 2, R + 1, g & 1) SCHED(); }
        site<3000 + 36 * R + g>();
        if constexpr (R == 7) { last(g_); SCHED(); }
        if constexpr ((g & 3) == 3 && g < 28) { fill(std::integral_constant<int, 7 * R + g / 4>{}); SCHED(); }
    });
}
template <int R0, int R1, bool PFL, class F, class G>
DI void conv_part(f32x4 (&ca)[8], v4u (&A0)[8], v4u (&B0)[8], const char* smem, const int (&ab)[4], int bb, F&& fill, G&& last) {
    v4u A1[8], B1[8];
    static_for<R1 - R0>(LAMBDA(r_) {
        constexpr int r = r_.value;
        if constexpr (r & 1) conv_round<R0 + r, (R0 + r + 1 < R1 || PFL)>(ca, A1, B1, A0, B0, smem, ab, bb, fill, last);
        else conv_round<R0 + r, (R0 + r + 1 < R1 || PFL)>(ca, A0, B0, A1, B1, smem, ab, bb, fill, last);
    });
}
template <int IMM> DI void gld(v4u& dst, unsigned voff, const void* sbase) {
    asm volatile("global_load_dwordx4 %0, %1, %2 offset:%3" : "=v"(dst) : "v"(voff), "s"(sbase), "i"(IMM));
}
#define OUTP 16
DI void gldu16(unsigned& dst, unsigned voff, const void* sbase) { asm volatile("global_load_ushort %0, %1, %2" : "=v"(dst) : "v"(voff), "s"(sbase)); }
DI void gld1s(unsigned& dst, unsigned voff, const void* sbase) { asm volatile("global_load_dword %0, %1, %2 sc1" : "=v"(dst) : "v"(voff), "s"(sbase)); }
template <int V> DI void SETM0(unsigned ldsbase) { asm volatile("s_add_u32 m0, %0, %1" ::"s"(ldsbase), "i"(V)); }
template <int GOFF> DI void GLD0(unsigned voff, const char* sbase) { asm volatile("global_load_lds_dwordx4 %0, %1 offset:%2" ::"v"(voff), "s"(sbase), "i"(GOFF) : "memory"); }
template <int GOFF, int V> DI void GLD1(unsigned voff, const char* sbase, unsigned ldsbase) {
    asm volatile("global_load_lds_dwordx4 %0, %1 offset:%3\n\ts_add_u32 m0, %2, %4" ::"v"(voff), "s"(sbase), "s"(ldsbase), "i"(GOFF), "i"(V) : "memory");
}
template <int L, int G> DI void dma_x(unsigned voff, const void* sbase, unsigned ldsbase) {
    asm volatile("s_add_u32 m0, %2, %4\n\ts_nop 0\n\tglobal_load_lds_dwordx4 %0, %1 offset:%3" ::"v"(voff), "s"(sbase), "s"(ldsbase), "i"(G), "i"(L - G) : "memory", "scc");
}
DI void ld_kv(v4u (&a)[15], int on  , unsigned k0, unsigned k1, unsigned k4, unsigned vo, v4u rk, const void* v, unsigned lds) {
    unsigned t;
    asm volatile("s_cmp_ge_u32 %5, 80\n\ts_cbranch_scc1 1f\n\ts_add_u32 m0, %12, %13\n\ts_movk_i32 %4, 0x2800\n\t"
                 "buffer_load_dwordx4 %6, %10, 0 offen nt lds\n\ts_add_u32 m0, m0, 0x400\n\tglobal_load_dwordx4 %0, %9, %11 nt\n\t"
                 "buffer_load_dwordx4 %7, %10, 0 offen nt lds\n\ts_add_u32 m0, m0, 0x400\n\tglobal_load_dwordx4 %1, %9, %11 offset:1280 nt\n\t"
                 "buffer_load_dwordx4 %7, %10, %4 offen nt lds\n\ts_add_u32 m0, m0, 0x400\n\ts_movk_i32 %4, 0x5000\n\tglobal_load_dwordx4 %2, %9, %11 offset:2560 nt\n\t"
                 "buffer_load_dwordx4 %7, %10, %4 offen nt lds\n\ts_add_u32 m0, m0, 0x400\n\tglobal_load_dwordx4 %3, %9, %11 offset:3840 nt\n\t"
                 "buffer_load_dwordx4 %8, %10, 0 offen nt lds\n1:"
                 : "=&v"(a[7]), "=&v"(a[8]), "=&v"(a[9]), "=&v"(a[10]), "=&s"(t)
                 : "s"(on), "v"(k0), "v"(k1), "v"(k4), "v"(vo), "s"(rk), "s"(v), "s"(lds), "i"(L_XR) : "scc", "memory");
}
DI int race(const char* a, const char* b) {
    unsigned u, v;
    int r;
    asm volatile("s_waitcnt lgkmcnt(0)\n\ts_load_dword %1, %3, 0x0\n\ts_load_dword %2, %4, 0x0\n\ts_waitcnt lgkmcnt(1)\n\ts_sleep %5\n\t"
                 "s_getreg_b32 %0, hwreg(HW_REG_IB_STS, 8, 4)\n\ts_waitcnt lgkmcnt(0)"
                 : "=&s"(r), "=&s"(u), "=&s"(v) : "s"(a), "s"(b), "i"(RSL) : "memory");
    return r;
}
DI void ld_wr(v4u (&a)[4], int on, unsigned o, const void* base) {
    asm volatile("s_cmp_ge_u32 %4, 80\n\ts_cbranch_scc0 1f\n\t.rept 4\n\tglobal_load_dword a[32+1], %5, %6\n\t.endr\n\ts_branch 2f\n1:\n\t"
                 "global_load_dwordx4 %0, %5, %6 nt\n\tglobal_load_dwordx4 %1, %5, %6 offset:1024 nt\n\tglobal_load_dwordx4 %2, %5, %6 offset:2048 nt\n\tglobal_load_dwordx4 %3, %5, %6 offset:3072 nt\n2:"
                 : "=&v"(a[0]), "=&v"(a[1]), "=&v"(a[2]), "=&v"(a[3]) : "s"(on), "v"(o), "s"(base) : "scc");
}
template <int L, int G> DI void dma_1(unsigned voff, const void* sbase, unsigned ldsbase) {
    asm volatile("s_add_u32 m0, %2, %4\n\ts_nop 0\n\tglobal_load_lds_dword %0, %1 offset:%3" ::"v"(voff), "s"(sbase), "s"(ldsbase), "i"(G), "i"(L - G) : "memory", "scc");
}
template <int L> DI void dma_n(unsigned voff, const void* sbase, unsigned ldsbase) {
    asm volatile("s_add_u32 m0, %2, %3\n\ts_nop 0\n\tglobal_load_lds_dwordx4 %0, %1 nt" ::"v"(voff), "s"(sbase), "s"(ldsbase), "i"(L) : "memory", "scc");
}
template <int A, int O> DI void lds2a(unsigned a) { asm volatile("ds_read_b64 a[%c1:%c1+1], %0 offset:%2" ::"v"(a), "i"(A), "i"(O) : "memory"); }
constexpr int L_EAR = L_XR + 8192;
constexpr int dma_goff(int it) { return it < 40 ? (it >> 2) * 128 : ((it - 100) / 5) * 256; }
constexpr int dma_loff(int it) { return it < 40 ? L_XR + ((it >> 2) % 3) * 4096 + (it & 3) * 1024 : ((it - 100) % 5) * 20480 + ((it - 100) / 5) * 2048; }
#define KFS 4
#define APOS 0
#define CWP 2
#define SAF 2
#define KD0 30
#define KST 59
#define KSL 5
#define KSC 11
#define XG0 5
#define XGS 1
#define WOD 1
#define USP 1
#define KSG 0
#define SGP0 0x122021
#define SGP1 0x202121
#define SBL 1
#define SMP 1
#define KW2E 0
#define KW2T 1
#define OTR 1
#define KQ0 0
#define KQ1 18
#define KQ2 4
#define KQ3 0
#define NPD 27
constexpr int kQ0 = KQ0, kQ1 = KQ1, kQ2 = KQ2, kQ3 = KQ3;
#define KW2S 16
#define KW2M 0x42
#define KFKS 13
#define KFKG 6
#define KUS 17
#define KTS 19
#define KFWP 13
constexpr int kW2S = KW2S, kW2M = KW2M;
#define KSA 4
constexpr int NW2L = (20 - kW2S) * __builtin_popcount(kW2M), NW2P = NW2L + KSA;
constexpr int P1_PRE = NPD + 7;
constexpr int p1_dma(int i) { return i < 5 ? 100 + i : i < 13 ? i - 5 : i < 18 ? 105 + (i - 13) : i < 22 ? 8 + (i - 18) : i < NPD ? 110 + (i - 22) : 400; }
constexpr int kQ1a = KST > KD0 ? kQ1 * (KST - KD0) / (117 - KD0) : 0;
constexpr int p1_pre(int i) { return i < kQ0 + kQ1a ? p1_dma(i) : i == kQ0 + kQ1a ? 400 : p1_dma(i - 1); }
constexpr int p1_item(int s, int g) {
    if (s == KFS && g == (XG0 == 5 ? 4 : 9)) return 324;
    if (WOD ? (s & 1) && s < 10 && (g == 3 || g == 8) : g == 9 && s < 10) return WOD ? 114 + s + (g == 8) : 115 + s;
    if (s == KSL && (g == KSG || g == KSG + 1)) return 202 + g - KSG;
    if (s == KSL && g == 6) return 204;
    if (s == KFWP && g == 1) return 208;
    if (s == KFKS && g == KFKG) return 206;
    if (USP && s == KUS && (g == 0 || g == 2 || g == 4 || g == 7)) return 210 + g / 2;
    if (s == KTS && g == 9) return 209;
    if (s >= kW2S && ((kW2M >> g) & 1)) return 500 + (s - kW2S) * __builtin_popcount(kW2M) + __builtin_popcount(kW2M & ((1 << g) - 1));
    if ((s & 1) == 0 && s <= 12 && g >= XG0 && (g - XG0) % XGS == 0 && (g - XG0) / XGS < 4) return 4 * (s / 2 + 3) + (g - XG0) / XGS;
    return -1;
}
constexpr int p1_cnt(int s, int g) {
    int n = P1_PRE;
    for (int t = 0; t <= s; ++t)
        for (int u = 0; u < 10; ++u) {
            if (t == s && u >= g) break;
            if (p1_item(t, u) >= 0) ++n;
            if (!USP && t == KUS && u == 0) n += 4;
        }
    return n;
}
constexpr int p1_pos(int code) {
    for (int i = 0; i < P1_PRE; ++i) if (p1_pre(i) == code) return i;
    for (int t = 0; t < 20; ++t)
        for (int u = 0; u < 10; ++u) if (p1_item(t, u) == code) return p1_cnt(t, u);
    return -1000;
}
constexpr int p1_lds(int s, int g) {
    constexpr int t0 = XG0 == 5 ? 0 : 5;
    if (s >= 2 && s < 10 && !(s & 1) && g >= t0 && g < t0 + 2) return 2 + s + g - t0;
    if (s == KSC + 1 && g == t0 + 2) return 38;
    if (s >= 14 && s < 19 && !(s & 1) && (g == 5 || g == 8)) return 30 + (s - 14) + g / 8;
    if (s == 19 && (g == 0 || g == 4)) return 36 + g / 4;
    if (s == 19 && g == 7) return 39;
    return -1;
}
constexpr int p1_nl(int s, int g0, int g1) {
    int n = 0;
    for (int g = g0; g < g1; ++g) n += (s < 19 && g < 5) + ((s & 1) && s < 19 && g >= 5 && g < 9) + (p1_lds(s, g) >= 0);
    return n;
}
constexpr int p1_cap(int s, int g) {
    int u = p1_nl(s, g, g + 1) + (g == 9 && s < 19) + ((s & 1) && s < 19 && g == 5) + ((s & 1) == 0 && g == XG0 - 1);
    const int it = p1_item(s, g), nt = g < 9 ? p1_item(s, g + 1) : -1;
    u += (it >= 0) + (nt >= 0 && nt < 200) + (it == 324);
    if ((s == 19 && g == 2) || (g == 9 && ((s & 3) == 2 || s == KUS - 1 || s == 18))) u += 2;
    return u < 2 ? 2 - u : 0;
}
constexpr int NFILL = 112;
constexpr int p1_from(int f) { return f < 40 ? 0 : f < 52 ? 9 : f < 72 ? KSC : 14; }
constexpr int p1_fill(int s, int g, int e) {
    int f = 0;
    for (int t = 0; t <= s; ++t)
        for (int u = 0; u < 10; ++u)
            for (int v = 0; v < p1_cap(t, u); ++v) {
                const bool go = f < NFILL && p1_from(f) <= t;
                if (t == s && u == g && v == e) return go ? f : -1;
                f += go;
                if (f == 40) f = 48;
            }
    return f;
}
static_assert(p1_fill(13, 10, 0) >= 52, "");
constexpr bool p1_all() {
    for (int i = 0; i < 40; ++i) if (p1_pos(i) < 0) return false;
    for (int i = 100; i < 125; ++i) if (p1_pos(i) < 0) return false;
    for (int i = 0; i < NW2L; ++i) if (p1_pos(500 + i) < 0) return false;
    return p1_pos(202) >= 0 && p1_pos(203) >= 0 && p1_pos(206) >= 0 && p1_pos(208) >= 0 && p1_pos(209) >= 0 && p1_pos(324) >= 0;
}
static_assert(p1_all() && p1_lds(14, 5) == 30 && p1_lds(14, 8) == 31 && p1_lds(16, 5) == 32, "");
constexpr int ring_req(int G, int I, int R, int X) {
    int n = I, e = 0;
    for (int t = 0; t <= G; ++t) {
        int r = -1;
        if (t % 5 == 0 || t % 5 == 3) {
            if (n < 40 && 5 * (n - R) + 4 < t) r = n++;
            else if (e < X && t % 10 == 3) r = 100 + e++;
        }
        if (t == G) return r;
    }
    return -1;
}
constexpr int g2_adr(int i, int kk) {
    int n = 0;
    for (int t = 0; t <= i; ++t)
        for (int u = 0; u < 5; ++u) {
            const bool ok = u == 4 || (u == 2 && (t < 11 || t > 15)) || (u == 3 && t < 5);
            if (t == i && u == kk) return ok ? n : -1;
            n += ok;
        }
    return -1;
}
constexpr int p1_lim(int n) { return n > 63 ? 63 : n; }
template <int IMM> DI void gld4s(v4u& dst, unsigned voff, const void* sbase) { asm volatile("global_load_dwordx4 %0, %1, %2 offset:%3 sc1" : "=v"(dst) : "v"(voff), "s"(sbase), "i"(IMM)); }
template <int IMM> DI void gld2s(v2u& dst, unsigned voff, const void* sbase) { asm volatile("global_load_dwordx2 %0, %1, %2 offset:%3 sc1" : "=v"(dst) : "v"(voff), "s"(sbase), "i"(IMM)); }
template <class T> DI void pin(T& v) { asm volatile("" : "+v"(v)); }
DI void mul2(float& a, float& b, float x, float y, float s) { asm volatile("v_mul_f32 %0, %2, %4\n\tv_mul_f32 %1, %3, %4" : "=&v"(a), "=&v"(b) : "v"(x), "v"(y), "v"(s)); }
DI void rot1(unsigned& a, unsigned x) { asm volatile("v_alignbit_b32 %0, %1, %1, 16" : "=v"(a) : "v"(x)); }
DI void mix1(unsigned& a, unsigned x, unsigned y) { asm volatile("v_bfi_b32 %0, %3, %1, %2" : "=v"(a) : "v"(x), "v"(y), "s"(0xffffu)); }
DI void ror1(unsigned& o, unsigned prev) { asm volatile("v_mov_b32_dpp %0, %1 wave_ror:1 row_mask:0xf bank_mask:0xf" : "=v"(o) : "v"(prev)); }
DI void shr1b(unsigned& o, unsigned x) { asm volatile("v_mov_b32_dpp %0, %1 wave_shr:1 row_mask:0xf bank_mask:0xf" : "+v"(o) : "v"(x)); }
DI void xor1(float& a, unsigned x, unsigned s) { asm volatile("v_xor_b32 %0, %1, %2" : "=v"(a) : "v"(x), "v"(s)); }
template <int G> DI void bc1(float& a, unsigned x) { asm volatile("v_mov_b32_dpp %0, %1 row_newbcast:%2 row_mask:0xf bank_mask:0xf" : "=v"(a) : "v"(x), "i"(G)); }
template <int G> DI void mulb(float& o, unsigned x, float y) { asm volatile("v_mul_f32_dpp %0, %1, %2 row_newbcast:%3 row_mask:0xf bank_mask:0xf" : "=v"(o) : "v"(x), "v"(y), "i"(G)); }
template <int N> DI void lgkm() { __builtin_amdgcn_s_waitcnt(0xC07F | (N << 8)); asm volatile("" ::: "memory"); }
DI void unp2(float& a, float& b, unsigned x) { asm volatile("v_lshlrev_b32 %0, 16, %2\n\tv_and_b32 %1, 0xffff0000, %2" : "=&v"(a), "=&v"(b) : "v"(x)); }
#define GL(a) "global_load_dwordx4 a[" #a ":" #a "+3], %7, %8 sc1\n\tglobal_load_dwordx4 a[" #a "+4:" #a "+7], %7, %8 offset:256 sc1\n\t"
#define AR(d, a, o) "v_accvgpr_read_b32 " d ", a[" #a "+" #o "]\n\t"
#define GS_(a) "s_waitcnt vmcnt(4)\n\t" AR("%2", a, 1) AR("%3", a, 3) AR("%4", a, 5) AR("%5", a, 7) AR("%0", a, 0) AR("%1", a, 2)                              \
    "v_xor_b32 %2, %9, %2\n\tv_bitop3_b32 %4, %4, %9, %2 bitop3:0xbe\n\tv_bitop3_b32 %4, %3, %9, %4 bitop3:0xbe\n\tv_bitop3_b32 %4, %5, %9, %4 bitop3:0xbe\n\t" \
    AR("%2", a, 4) AR("%3", a, 6) "v_cmp_ne_u32 vcc, 0, %4\n\ts_cbranch_vccz 2f\n\t" GL(a)
template <int A, int D> DI void gran_req(unsigned off, const void* base) {
    if constexpr (D > 0) __builtin_amdgcn_s_sleep(D);
    asm volatile("global_load_dwordx4 a[%c2:%c2+3], %0, %1 sc1\n\tglobal_load_dwordx4 a[%c2+4:%c2+7], %0, %1 offset:256 sc1" ::"v"(off), "s"(base), "i"(A) : "memory");
}
DI void pipe_gran(float& a, float& d, unsigned off, const void* base, unsigned tag, bool& dead) {
    unsigned n = dead ? 0u : kSpinCap, y, z;
    float a1, d1;
    asm volatile("1:\n\t" GS_(32) GS_(40) GS_(48) "s_sub_u32 %6, %6, 1\n\ts_cbranch_scc0 1b\n2:"
                 : "=&v"(a), "=&v"(d), "=&v"(a1), "=&v"(d1), "=&v"(y), "=&v"(z), "+s"(n) : "v"(off), "s"(base), "s"(tag) : "memory", "vcc", "scc");
    dead = __builtin_amdgcn_readfirstlane(n) == ~0u;
    a += a1; d += d1;
}
DI void fma2(float& a, float& b, float x, float y, float s, float t, float u, float v) {
    asm volatile("v_fma_f32 %0, %2, %4, %6\n\tv_fma_f32 %1, %3, %5, %7" : "=&v"(a), "=&v"(b) : "v"(x), "v"(y), "v"(s), "v"(t), "v"(u), "v"(v));
}
DI void fma2p(f32x2& o, float x, float y, float r, float u, float v) {
    asm("v_fmac_f32 %0, %1, %2" : "+v"(u) : "v"(x), "v"(r));
    asm("v_fmac_f32 %0, %1, %2" : "+v"(v) : "v"(y), "v"(r));
    o = f32x2{u, v};
}
DI void swp2n(unsigned& a, unsigned& b, unsigned& c, unsigned& d) { asm volatile("v_permlane16_swap_b32 %0, %1\n\tv_permlane16_swap_b32 %2, %3" : "+v"(a), "+v"(b), "+v"(c), "+v"(d)); }
DI void cp2(unsigned& a, unsigned& b, f32x4 v) { asm volatile("v_cvt_pk_bf16_f32 %0, %2, %3\n\tv_cvt_pk_bf16_f32 %1, %4, %5" : "=&v"(a), "=v"(b) : "v"(v.x), "v"(v.y), "v"(v.z), "v"(v.w)); }
DI void zero2(v2u& a) { asm volatile("v_mov_b64 %0, 0" : "=v"(a)); }
DI void mov1(unsigned& a, unsigned b) { asm volatile("v_mov_b32 %0, %1" : "=v"(a) : "v"(b)); }
DI void sw16(unsigned& a, unsigned& b) { asm volatile("v_permlane16_swap_b32 %0, %1" : "+v"(a), "+v"(b)); }
DI void sw32(unsigned& a, unsigned& b) { asm volatile("v_permlane32_swap_b32 %0, %1" : "+v"(a), "+v"(b)); }
DI void mrg8(float& a, float b) { asm volatile("v_mov_b32_dpp %0, %1 quad_perm:[0,1,2,3] row_mask:0xf bank_mask:0xc" : "+v"(a) : "v"(b)); }
DI void sel1(float& o, unsigned a, unsigned b, u64 m) { asm volatile("v_cndmask_b32 %0, %1, %2, %3" : "=v"(o) : "v"(a), "v"(b), "s"(m)); }
DI void count_(unsigned& side, unsigned& fine, unsigned v) {
    u64 m;
    asm volatile("v_cmp_lt_u16 vcc, 4, %3\n\tv_cmp_gt_u16 %1, 4, %3\n\ts_bcnt1_i32_b64 %0, vcc\n\ts_bcnt1_i32_b64 %2, %1\n\ts_cmp_gt_u32 %0, %2\n\tv_readfirstlane_b32 %2, %3\n\ts_cselect_b32 %0, 0x2000, 0\n\t"
                 "s_lshr_b32 %2, %2, 2\n\ts_and_b32 %2, %2, 0x4000"
                 : "=&s"(side), "=&s"(m), "=&s"(fine) : "v"(v) : "vcc", "scc");
}
DI void ne1(u64& m, v2u tag, v2u a) { asm volatile("v_cmp_ne_u64 %0, %1, %2" : "=s"(m) : "v"(tag), "v"(a)); }
DI void ne2(u64& m, v2u tag, v2u a, v2u b) { asm volatile("v_cmp_ne_u64 vcc, %1, %2\n\tv_cmp_ne_u64 %0, %1, %3\n\ts_or_b64 %0, %0, vcc" : "=&s"(m) : "v"(tag), "v"(a), "v"(b) : "vcc", "scc"); }
template <int G> DI void bc2b(float& a, float& b, float x, float y) {
    asm volatile("v_mov_b32_dpp %0, %2 row_newbcast:%4 row_mask:0xf bank_mask:0xf\n\tv_mov_b32_dpp %1, %3 row_newbcast:%4 row_mask:0xf bank_mask:0xf" : "=&v"(a), "=&v"(b) : "v"(x), "v"(y), "i"(G));
}
template <class T> DI void pins(T& v) { asm volatile("" : "+s"(v)); }
template <int I> DI void gld_w2(unsigned voff, const void* sbase) {
    asm volatile("global_load_dwordx4 " WA(2) ", %0, %1 offset:%3 sc1" ::"v"(voff), "s"(sbase), "i"(W2R + 4 * I), "i"((I % 5) * 1024 - 2048));
}
DI void gld2t(v2u& dst, unsigned voff, const void* sbase) { asm volatile("global_load_dwordx2 %0, %1, %2 sc1" : "+v"(dst) : "v"(voff), "s"(sbase)); }
DI void undef4(v4u (&x)[4]) { asm volatile("" : "=v"(x[0]), "=v"(x[1]), "=v"(x[2]), "=v"(x[3])); }
DI void wait1(v2u& v) { asm volatile("s_waitcnt vmcnt(0)" : "+v"(v)::"memory"); }
template <int N> DI void wait_fkn(v2u& v, v2u& u) { asm volatile("s_waitcnt vmcnt(%2)" : "+v"(v), "+v"(u) : "i"(N) : "memory"); }
DI void ld_ur(unsigned o, const void* base) {
    asm volatile("global_load_dwordx4 a[40:40+3], %0, %1 sc1\n\tglobal_load_dwordx4 a[44:44+3], %0, %1 offset:1024 sc1\n\t"
                 "global_load_dwordx4 a[48:48+3], %0, %1 offset:2048 sc1\n\tglobal_load_dwordx4 a[52:52+3], %0, %1 offset:3072 sc1" ::"v"(o), "s"(base));
}
template <int I> DI void ld_ur1(unsigned o, const void* base) { asm volatile("global_load_dwordx4 a[%c2:%c2+3], %0, %1 offset:%3 sc1" ::"v"(o), "s"(base), "i"(40 + 4 * I), "i"(1024 * I)); }
constexpr int kUY = KSA + (USP ? p1_cnt(20, 0) - 1 - p1_pos(213) : p1_cnt(20, 0) - p1_cnt(KUS, 0) - 4);
DI void st_ur(unsigned a) {
    asm volatile("s_waitcnt vmcnt(%1)\n\tds_write_b128 %0, a[40:40+3] offset:480\n\tds_write_b128 %0, a[44:44+3] offset:1504\n\t"
                 "ds_write_b128 %0, a[48:48+3] offset:2528\n\tds_write_b128 %0, a[52:52+3] offset:3552" ::"v"(a), "i"(kUY < NW2P ? kUY : NW2P) : "memory");
}
template <int A> DI void rda(unsigned& v) { asm volatile("v_accvgpr_read_b32 %0, a[%c1+0]" : "=v"(v) : "i"(A)); }
DI void poll1(unsigned a, const void* base) { asm volatile("global_load_dwordx2 a[36:36+1], %0, %1 sc1" ::"v"(a), "s"(base)); }
DI void poll2(unsigned a, unsigned b, const void* base) { asm volatile("global_load_dwordx2 a[36:36+1], %0, %2 sc1\n\tglobal_load_dwordx2 a[36+2:36+3], %1, %2 sc1" ::"v"(a), "v"(b), "s"(base)); }
DI void touch_(unsigned a, const void* base) { asm volatile("global_load_dword a[32+1], %0, %1" ::"v"(a), "s"(base)); }
template <int IMM> DI void gld4t(v4u& dst, unsigned voff, const void* sbase) { asm volatile("global_load_dwordx4 %0, %1, %2 offset:%3 sc1" : "+v"(dst) : "v"(voff), "s"(sbase), "i"(IMM)); }
template <int N> DI void vm_wait() { asm volatile("s_waitcnt vmcnt(%0)" ::"i"(N) : "memory"); }
DI float dot2a(unsigned a, unsigned b, float c) {
    return __builtin_amdgcn_fdot2_f32_bf16(__builtin_bit_cast(bf16x2v, a), __builtin_bit_cast(bf16x2v, b), c, false);
}
DI void disc(unsigned l0, unsigned l1, unsigned l2, f32x2& lam, f32x2& co) {
    const float dt = rbf(__builtin_amdgcn_exp2f(lo16f(l2) * 1.44269504088896f));
    const float lre = lo16f(l0), lim = lo16f(l1);
    const float e_ = __builtin_amdgcn_exp2f(dt * lre * 1.44269504088896f);
    const float rv = __builtin_amdgcn_fractf(dt * lim * 0.159154943091895f);
    lam = f32x2{e_ * __builtin_amdgcn_cosf(rv), e_ * __builtin_amdgcn_sinf(rv)};
    const float nr = lam.x - 1.0f, ni = lam.y, id = __builtin_amdgcn_rcpf(lre * lre + lim * lim);
    co = f32x2{(nr * lre + ni * lim) * id, (ni * lre - nr * lim) * id};
}
DI f32x4 unpk4(v2u u) { return f32x4{lo16f(u.x), hi16f(u.x), lo16f(u.y), hi16f(u.y)}; }
#define D2 "v_dot2_f32_bf16 "
#define CP "v_cvt_pk_bf16_f32 "
DI void d2x4(unsigned* o, unsigned a0, unsigned a1, unsigned a2, unsigned a3, unsigned b0, unsigned b1, unsigned b2, unsigned b3) {
    asm volatile(D2 "%0, %4, %8, 0\n\t" D2 "%1, %5, %9, 0\n\t" D2 "%2, %6, %10, 0\n\t" D2 "%3, %7, %11, 0"
                 : "=&v"(o[0]), "=&v"(o[1]), "=&v"(o[2]), "=&v"(o[3]) : "v"(a0), "v"(a1), "v"(a2), "v"(a3), "v"(b0), "v"(b1), "v"(b2), "v"(b3));
}
DI void cpx4(unsigned* o, unsigned a0, unsigned a1, unsigned a2, unsigned a3, unsigned b0, unsigned b1, unsigned b2, unsigned b3) {
    asm volatile(CP "%0, %4, %8\n\t" CP "%1, %5, %9\n\t" CP "%2, %6, %10\n\t" CP "%3, %7, %11"
                 : "=&v"(o[0]), "=&v"(o[1]), "=&v"(o[2]), "=&v"(o[3]) : "v"(a0), "v"(a1), "v"(a2), "v"(a3), "v"(b0), "v"(b1), "v"(b2), "v"(b3));
}
DI void cpx4i(unsigned* t, const unsigned* ka) {
    asm volatile(CP "%0, %0, %4\n\t" CP "%1, %1, %5\n\t" CP "%2, %2, %6\n\t" CP "%3, %3, %7"
                 : "+v"(t[0]), "+v"(t[1]), "+v"(t[2]), "+v"(t[3]) : "v"(ka[0]), "v"(ka[1]), "v"(ka[2]), "v"(ka[3]));
}

__global__ __launch_bounds__(256, 1) __attribute__((aligned(4096))) void k_main(Params p) {
    __shared__ __attribute__((aligned(16))) char smem[LDS_BYTES];
    const rsrc_t rs = __builtin_amdgcn_make_buffer_rsrc((void*)p.ws, 0, WS_BYTES, 0x00020000);
    const rsrc_t ro = __builtin_amdgcn_make_buffer_rsrc((void*)p.out, 0, S_ * D_ * 4, 0x00020000);
    const int w0 = __builtin_amdgcn_readfirstlane(threadIdx.x >> 6), b0 = blockIdx.x;
    bool dead = false;
    asm volatile("" ::: "a255");
    float SW[5];
    f32x4 xin[5];
    v2u gt[5];
    f32x4 acc[2][5];
    f32x4 rr[2];
    float tt[2][5][4];
    Lm lam1, lamq;
    f32x2 l8, l32, l128;
    u64 late;
    v4u cA[8], cB[8];
    int sgn_, sg4, ab[4], bb, zpa, uwa, spa, tbw, tbr, tbp, tbq, cwa, cw4, spl, dst[3];
    unsigned w2o, wro, fwo, fko, xso, gso, fgo1, fho1, fho, hso, eo, es, ho[4];
    bool cv0;
    unsigned fine;
    const char* gf_;
    const char* gn;
    rsrc_t rn;
    const char* kvtb;
    const char* w2b0[(NW2P + 4) / 5];
    int otw, otr0, otr4;
    int mt1_;
    auto addrs = [&](auto layer_, int lane, int w, int b) __attribute__((always_inline)) {
        constexpr int layer = layer_.value;
        const int c = lane & 15, q = lane >> 4;
        TILES()
        static_assert(l_sg(0, 0) == SC + 0xA900 && l_sg(0, 1) == SC + 0xB400 && l_sg(0, 2) == SC + 0xCE00 && l_sg(0, 3) == SC + 0xDC00, "");
        sgn_ = (layer ? l_sg(1, 0) + w * INBOX : SC + (int)((0xDC00CE00B400A900ull >> (16 * w)) & 0xffff)) + (q >> 1) * 640 + (q & 1) * 8;
        const int c4 = (c >> 1) * 16 + (c & 1) * 4;
        sg4 = sgn_ + c4; sgn_ += c * 16;
        const int pl = lane < 40 ? lane : 39, tbb = l_tb(layer, 0) + w * (layer ? INBOX : 5120);
        spl = SC + pl * 16;
        eo = OFF_EG + (unsigned)(layer * 163840 + nt1 * 640 + pl * 16);
        es = eo + (unsigned)(mt1 * 10240);
        spa = l_rs(layer, 0) + (2 * (mt2 & 1) + (w >> 1)) * (layer ? INBOX : 256) + (2 * (w & 1) + (c >> 3)) * 64 + (c & 7) * 4;
        tbw = tbb + c * 64 + ((q ^ ((c >> 1) & 3)) * 16);
        tbr = tbb + (2 * (c >> 2) + (q & 1)) * 64 + (((2 * (q >> 1) + ((c >> 1) & 1)) ^ (c >> 2)) * 16) + (c & 1) * 8;
        hso = N_HF + (unsigned)((layer * 8 + 2 * w + (q >> 1)) * 5 * NB_ + (2 * nt1 + (q & 1)) * 256 + c * 16);
        tbp = tbb + (8 * (q & 1) + (c >> 2)) * 64 + (((2 * (q >> 1) + ((c >> 1) & 1)) ^ (c >> 3)) * 16) + (c & 1) * 8;
        tbq = tbp ^ 32;
        cwa = L_CW + c * 16; cw4 = L_CW + c4;
        pin(sg4); pin(tbp); pin(tbq); pin(cw4);
        fho1 = lane ? kNone : N_FH + (unsigned)(layer * 2 * NB_ + (w >> 1) * 4096 + (nt1 * 2 + (w & 1)) * 128);
        fho = N_FH + (unsigned)(layer * 2 * NB_ + ((mt2 & 1) * 4 + w) * 1024 + (lane >> 3) * 128);
        pin(sgn_); pin(spl); pin(eo); pin(es); pin(spa); pin(tbw); pin(tbr); pin(hso); pin(cwa); pin(fho1); pin(fho);
        const unsigned hb_ = N_HF + (unsigned)((layer * 8 + 4 * (mt2 & 1)) * 5 * NB_ + w * 2048);
#pragma unroll
        for (int m = 0; m < 4; ++m) { ho[m] = hb_ + (unsigned)(((m + 1 + w) & 3) * 5 * NB_ + lane * 16); pin(ho[m]); }
#pragma unroll
        for (int m = 0; m < 3; ++m) { dst[m] = SC + ((w + 1 + m) & 3) * INBOX + m * 5120 + lane * 16; pin(dst[m]); }
        mt1_ = mt1;
        if constexpr (layer == 0) {
            const int r0 = SC + (w < 3 ? w : 0) * REGSZ, y0 = 2031 + 8 * q - c, l34 = lane < 33 ? lane : 33;
#pragma unroll
            for (int i = 0; i < 4; ++i) { ab[i] = r0 + ((y0 & 1) ? CV_O : CV_E) + ((y0 + 1) >> 1) * 4 - 448 - 512 - 1024 * i; pin(ab[i]); }
            bb = r0 + 32 * c + 16 * q;
            zpa = w < 3 ? r0 + l34 * 16 + (l34 >= 30 ? CV_E + 4096 - 480 : 0) + (l34 >= 32 ? CV_O + 4100 - 32 - CV_E - 4096 : 0) : SC + 47360 + lane * 16;
            uwa = (w < 3 ? r0 : SC + 47360 - 480) + lane * 16;
            xso = (unsigned)((64 * mt2 + 16 * w + (lane >> 2)) * D_ + 80 * nt2 + 8 * ((lane & 3) ^ q)) * 2u;
            gso = OFF_GT + (unsigned)((80 * nt2 + i32 + 32 * w) * GSTR + (q & 1) * 512 + (c >> 2) * 128 + (q >> 1) * 64 + (c & 3) * 16);
            fgo1 = lane == 0 && i32 + 32 * w < 80 ? OFF_FG + (unsigned)((nt2 * 96 + i32 + 32 * w) * 128) : kNone;
            pin(bb); pin(zpa); pin(uwa); pin(xso); pin(gso); pin(fgo1);
        }
        SCHED();
    };
    unsigned tag0, tag1;
    v2u tagv;
#define FLAG(off) { if (lane == 0) st64(tagv, rs, off); }
#define ISTAG(f) (((f.x ^ tag0) | (f.y ^ tag1)) == 0u)
#define KW_REQ() { ld_ur(wro, kvtb); static_for<NW2P>(LAMBDA(i_) { gld_w2<i_.value>(w2o, p.ws + 2048 + (i_.value / 5) * 5120); }); }
#define FW_OK() { if (__builtin_expect(late != 0ull, 0)) { WAVE_WAIT({ const v2u fk_ = ld64(rs, fko); const v2u fw_ = ld64(rs, fwo); ok = ISTAG(fk_) && ISTAG(fw_); }) KW_REQ() } }
#define WF(g, s) *(const v4u*)(smem + wa[(s) & 1][(g) < 4 ? (g) & 1 : 2] + ((g) & 2) * 20480 + ((s) >> 1) * 1024)
#define TT_GAP(n) { constexpr int mb_ = (n) / 10, nb_ = ((n) % 10) / 2, e_ = ((n) & 1) * 2; \
        mul2(tt[mb_][nb_][e_], tt[mb_][nb_][e_ + 1], rm_[4 * mb_ + e_], rm_[4 * mb_ + e_ + 1], SW[nb_]); }
    {
        const int tid = threadIdx.x, lane = tid & 63, w = w0, b = b0;
        const int c = lane & 15, q = lane >> 4;
        TILES()
        STAMP(0)
        const unsigned lds0 = (unsigned)(size_t)(LAS char*)smem;
        unsigned nonce;
        unsigned cpf0;
        {
            u64 pc;
            int len;
            asm volatile("s_getpc_b64 %0\nkpf_begin:\n\ts_mov_b32 %1, kpf_end-kpf_begin\n\ts_max_i32 %1, %1, kpf_end2-kpf_begin\n\ts_max_i32 %1, %1, kpf_end3-kpf_begin" : "=s"(pc), "=s"(len)::"scc");
            const int o0 = (((j & 15) * 4 + w) + 64 * (lane >> 3)) * 128;
            asm volatile("global_load_dword %0, %1, %2" : "=v"(cpf0) : "v"(o0 < len ? o0 : 0), "s"(pc));
        }
        const bool cv = 32 * w + i32 < 80;
        const int chn = 80 * nt2 + i32 + 32 * (cv ? w : 0);
        const bool kw = 32 * w + 31 - i32 < 80;
        const int ku = 80 * nt2 + (w == 2 ? 64 + (i32 & 15) : i32 + 32 * (w & 1)), kc = ku >> 6, kr = ku & 63;
        const int lc = cv ? lane : 8;
        const unsigned xl = lds0 + (unsigned)(w * REGSZ);
        v4u ax[15], sa[5], cr[2], wr[4];
        unsigned uL[3], uM[3], sdq, kso;
        const int cu_ = (b < 200 ? b : 0) * 4 + w, cdb = cu_ / 20, ccg = cu_ % 20;
        {
            unsigned cpo = (unsigned)(32 * ccg + (lane & 31)) * 2u, cpm = (unsigned)(40 * nt1 + (lane < 39 ? lane : 39)) * 2u;
            unsigned k1 = (unsigned)(32 * kr + 5 + (lane >> 3)) * 1280u + (unsigned)(64 * kc + 8 * (lane & 7)) * 2u, k0 = k1 - 10240u, k4 = lane < 24 ? k1 + 30720u : kNone;
            unsigned vo = k1 - 6400u + (unsigned)(lane >> 3) * 3840u;
            pin(cpo); pin(cpm); pin(k0); pin(k1); pin(k4); pin(vo);
            SCHED();
            gldu16(uL[0], cpo, p.Lre); gldu16(uL[1], cpo, p.Lim); gldu16(uL[2], cpo, p.logdt);
            gldu16(uM[0], cpm, p.Lre); gldu16(uM[1], cpm, p.Lim); gldu16(uM[2], cpm, p.logdt);
            const v4u rk = {(unsigned)(u64)p.k, (unsigned)((u64)p.k >> 32) & 0xffffu, S_ * D_ * 2u, 0x00020000u};
            ld_kv(ax, 32 * w + 31 - i32, k0, k1, k4, vo, rk, p.v, xl);
            SCHED();
        }
        const unsigned gof = OFF_GRP + (unsigned)mt1 * 0x200000u - ((unsigned)(u64)p.ws & 0x1fffffu);
        unsigned vote = 4u, side;
        SCHED();
        auto small_ = [&]() __attribute__((always_inline)) {
            kso = (unsigned)(512 * kc + lane * 4);
            dma_1<L_EAR + 2048, 0>(kso, p.shk, xl); dma_1<L_EAR + 2304, 256>(kso, p.shk, xl);
            gld1s(nonce, kso & 4u, p.ws + OFF_NONCE);
            SCHED();
        };
        if (SMP == 0) small_();
        const u16* sab = w < 2 ? p.x0 : w == 2 ? p.Bre : p.Bim;
        unsigned sao;
        {
            int r0 = w < 2 ? 128 * mt1 + 8 * nt1 + 4 * w : 40 * nt1 + mt1, r1 = w < 2 ? 1 : 16, r2 = w < 2 ? 2047 : 40 * nt1 + 32 + (mt1 >> 1);
            pins(sab); pins(r0); pins(r1); pins(r2);
            const int r = r0 + q * r1;
            sao = (unsigned)((r < r2 ? r : r2) * 1280 + c * 16);
            pin(sao);
        }
        if (SAF == 0) static_for<5>(LAMBDA(i) { gld<256 * i.value>(sa[i.value], sao, sab); });
        SCHED();
        if (w == TW0 && !kw) tour<0>();
        STAMP(22)
        const int row8 = lane >> 3;
        const unsigned wvo = (unsigned)(40 * nt1 + row8) * 1280u + (unsigned)(((lane & 7) ^ ((w & 1) | ((row8 >> 1) << 1))) * 16 + (w >> 1) * 128);
        const char* const wbase = (const char*)((w & 1) ? p.Bim : p.Bre);
        const unsigned wl = lds0 + (unsigned)(((w & 1) * 10 + (w >> 1)) * 1024);
        const unsigned xvo0 = (unsigned)(8 * (row8 >> 1) + 2 * (row8 & 1)) * 1280u + (unsigned)(((lane & 7) ^ ((row8 >> 1) << 1)) * 16);
        const unsigned xvo1 = (xvo0 ^ 16u) + 1280u;
        const char* const xbase = (const char*)p.x0 + (size_t)(128 * mt1 + 32 * w) * 1280;
        unsigned xv[4] = {xvo0, xvo1, xvo0 + 5120u, xvo1 + 5120u}, wv[5];
#pragma unroll
        for (int i = 0; i < 5; ++i) { wv[i] = wvo + i * 10240u; pin(wv[i]); if (i < 4) pin(xv[i]); }
#define DMA_M0(it) SETM0<dma_loff(it) - dma_goff(it)>((it) < 40 ? xl : wl)
#define DMA_LD1(it, nx_) GLD1<dma_goff(it), dma_loff(nx_) - dma_goff(nx_)>((it) < 40 ? xv[(it) & 3] : wv[(it) < 40 ? 0 : ((it) - 100) % 5], (it) < 40 ? xbase : wbase, (nx_) < 40 ? xl : wl)
#define DMA_LD(it) GLD0<dma_goff(it)>((it) < 40 ? xv[(it) & 3] : wv[(it) < 40 ? 0 : ((it) - 100) % 5], (it) < 40 ? xbase : wbase)
        const int swz = (c & 1) | ((c >> 2) << 1);
        int wa[2][3];
#pragma unroll
        for (int i = 0; i < 2; ++i) {
            wa[i][0] = (c >> 3) * 20480 + (c & 7) * 128 + (((4 * i + q) ^ (c & 6)) * 16); wa[i][1] = (wa[i][0] ^ 16) + 10240;
            wa[i][2] = 81920 + (c & 1) * 10240 + (c >> 1) * 128 + (((4 * i + q) ^ ((c & 1) | ((c >> 2) << 1))) * 16);
            pin(wa[i][0]); pin(wa[i][1]); pin(wa[i][2]);
        }
        int xlane0 = L_XR + w * REGSZ + (c & 1) * 1024 + (c >> 1) * 128 + ((q ^ swz) * 16);
        int xlane1 = L_XR + w * REGSZ + (c & 1) * 1024 + (c >> 1) * 128 + (((4 + q) ^ swz) * 16);
        unsigned cvo = (unsigned)((16 * cdb + (lane >> 2)) * D_ + 32 * ccg + 8 * ((lane & 3) ^ q)) * 2u, for0, for1;
        int ear = L_EAR + w * REGSZ + c * 64 + (q ^ (c >> 2)) * 16, eas = L_EAR + 2048 + w * REGSZ + (lane & 7) * 64;
        int kra = L_XR + w * REGSZ + (lane >> 3) * 512 + (lane & 7) * 16;
        pin(ear); pin(eas); pin(kra);
        {
            const int G = 4 * ccg + q, nt = G / 5, g5 = G - 5 * nt;
            for0 = OFF_W2F + (unsigned)(((cdb / 5) * 200 + cdb % 5) * 1024 + c * 16 + (10 * (nt >> 2) + 2 * (g5 & 6) + ((nt >> 1) & 1)) * 5120 + (2 * (nt & 1) + (g5 & 1)) * 256);
            for1 = for0 + (g5 < 4 ? 10240u : 256u);
        }
        unsigned sto = OFF_ST + (unsigned)((mt1 * 16 + 4 * w + q) * 256 + c * 8), swo = OFF_ST + (unsigned)(((lane >> 2) * 16 + nt1) * 256 + 128 + (lane & 3) * 16);
        unsigned vto = OFF_VT + (unsigned)(mt1 * 512 + c * 32 + (q & 1) * 8);
        unsigned kvo = OFF_KVT + (unsigned)((64 * kc + 8 * (lane >> 3) + ((lane >> 2) & 1)) * KSTR + (8 * kc + (lane >> 3)) * KPS + (32 * kr + 8 * (lane & 3)) * 2);
        int kbw = L_KB + w * 1024 + (lane & 7) * 128 + (((lane >> 3) * 8) ^ ((lane & 7) * 16));
        int kbr = L_KB + w * 1024 + (lane >> 3) * 128 + (((lane & 7) * 16) ^ ((lane >> 3) * 16));
        const unsigned dum = kNone;
        unsigned fko1 = OFF_FK + (unsigned)((64 * kc + kr) * 32);
        if (!kw) kvo = dum;
        if (b >= 200) { for0 = dum; for1 = dum; cvo = (unsigned)((b * 4 + w) * 128); }
        wro = (unsigned)(chn * 4096 + lc * 16);
        cv0 = cv;
        pin(kvo); pin(kbw); pin(kbr); pin(fko1);
        pin(xlane0); pin(xlane1); pin(cvo); pin(for0); pin(for1); pin(sto); pin(swo); pin(vto); pin(wro);
        SCHED();
        auto pre = LAMBDA(i_) {
            constexpr int it = p1_dma(i_.value);
            SCHED();
            DMA_M0(it);
            asm volatile("s_nop 0");
            DMA_LD(it);
            SCHED();
        };
        fko = OFF_FK + (unsigned)(((chn & ~63) + (cv ? lane : 0)) * 32);
        { const int e_ = (lane & 7) < w ? w : (lane & 7) > w + 4 ? w + 4 : (lane & 7);
          fwo = OFF_FW + (unsigned)(((5 * nt2 + (lane < 40 ? lane >> 3 : 4)) * 20 + 4 * w + e_) * 32); }
        w2o = OFF_W2F + (unsigned)((nt2 * 40 + 10 * w) * 5120 + lane * 16);
        int ime = SC + (w < 3 ? w * REGSZ + CV_E : 43264) + 1008 - 16 * lane, imo = SC + (w < 3 ? w * REGSZ + CV_O + 4 : 47360) + 1008 - 16 * lane, im3 = w < 3 ? ime + 2048 : imo;
        int tca = L_TAB + c * 136, tcb = tca + 8 + 32 * w + 8 * q, tcc = L_TAB + (32 + (c >> 1)) * 136, tcd = tcc + 8 + 32 * w + 8 * q, tcl = L_TAB + (lane < 40 ? lane : 39) * 136, coa = L_CO + w * REGSZ + q * 64, rsa = l_rs(0, 0) + w * 256 + lane * 4;
        unsigned sgn = (c & 1) ? 0u : 0x80000000u;
        fko1 = kw && lane < 32 ? fko1 : OFF_FW + (unsigned)((b * 4 + w) * 32);
        pin(fko); pin(fwo); pin(w2o); pin(fko1); pin(ime); pin(imo); pin(im3); pin(tca); pin(tcb); pin(tcc); pin(tcd); pin(tcl); pin(coa); pin(rsa); pin(sgn);
        SCHED();
        auto cw_ = [&]() __attribute__((always_inline)) {
            dma_n<L_EAR>(cvo, p.Cre, xl); dma_n<L_EAR + 1024>(cvo, p.Cim, xl);
            dma_1<L_EAR + 2560, 0>((unsigned)(160 * nt2 + (lane < 39 ? lane : 39) * 4), p.Dskip, xl);
            ld_wr(wr, 32 * w + i32, wro, p.ssmk);
            gldu16(sdq, (unsigned)chn * 2u, p.ssmD);
        };
        if (SMP == 1) small_();
        static_assert(SMP == 0 || SAF > 0, "");
        if (SAF == 1) static_for<5>(LAMBDA(i) { gld<256 * i.value>(sa[i.value], sao, sab); });
        if (CWP == 0) cw_();
        STAMP(33)
        asm volatile("s_waitcnt vmcnt(%5)\n\ts_cmp_ge_u32 %4, 80\n\ts_cbranch_scc0 1f\n\ts_waitcnt vmcnt(%6)\n1:" : "+v"(uL[0]), "+v"(uL[1]), "+v"(uL[2]), "+v"(cpf0) : "s"(32 * w + 31 - i32), "i"(12 + (SAF < 2 ? 5 : 0) + (CWP ? 0 : 8)), "i"(3 + (SAF < 2 ? 5 : 0) + (CWP ? 0 : 8)) : "memory", "scc");
        pin(uM[0]); pin(uM[1]); pin(uM[2]);
        {
            f32x2 lam, co;
            disc(uL[0], uL[1], uL[2], lam, co);
            *(f32x2*)(smem + L_CO + w * REGSZ + (lane & 31) * 8) = 2.0f * co;
        }
        SCHED();
        STAMP(35)
        if (SAF == 2) static_for<5>(LAMBDA(i) { gld<256 * i.value>(sa[i.value], sao, sab); });
        if (CWP == 1) cw_();
        if (APOS == 0) addrs(std::integral_constant<int, 0>{}, lane, w, b);
        if (SAF == 3) static_for<5>(LAMBDA(i) { gld<256 * i.value>(sa[i.value], sao, sab); });
        if (CWP == 2) cw_();
        v4u kvt[4];
        unsigned ka[8], kt[8], se[8], vs[8], ko4[4], one2 = 0x3F803F80u;
        pin(one2);
        auto kvop = LAMBDA(n_) {
            constexpr int n = n_.value, h = n ? (n - 1) / 29 : -1, u = n ? (n - 1) % 29 : 26;
            if constexpr (u < 2) d2x4(ka + 4 * u, ax[0][h], ax[1][h], ax[2][h], ax[3][h], se[u], se[u], se[u], se[u]);
            else if constexpr (u == 8) { const v4u sk = ax[11 + h]; se[4] = sk[0] >> 16; se[5] = sk[2] & 0xffff0000u; se[6] = sk[0] & 0xffffu; se[7] = sk[2] << 16; pin(se[4]); pin(se[5]); pin(se[6]); pin(se[7]); }
            else if constexpr (u < 21) {
                constexpr int v = u < 8 ? u - 2 : u - 3, tp = 1 + v / 6, st = v % 6, e = st & 1;
                if constexpr (st < 2) d2x4(kt + 4 * e, ax[tp][h], ax[tp + 1][h], ax[tp + 2][h], ax[tp + 3][h], se[2 * tp + e], se[2 * tp + e], se[2 * tp + e], se[2 * tp + e]);
                else if constexpr (st < 4) cpx4i(kt + 4 * e, ka + 4 * e);
                else d2x4(ka + 4 * e, kt[4 * e], kt[4 * e + 1], kt[4 * e + 2], kt[4 * e + 3], one2, one2, one2, one2);
            }
            else if constexpr (u == 21) { vs[0] = ax[7][h] & 0xffffu; vs[1] = ax[8][h] << 16; vs[2] = ax[9][h] & 0xffffu; vs[3] = ax[10][h] << 16; pin(vs[0]); pin(vs[1]); pin(vs[2]); pin(vs[3]); }
            else if constexpr (u == 22) { vs[4] = ax[7][h] >> 16; vs[5] = ax[8][h] & 0xffff0000u; vs[6] = ax[9][h] >> 16; vs[7] = ax[10][h] & 0xffff0000u; pin(vs[4]); pin(vs[5]); pin(vs[6]); pin(vs[7]); }
            else if constexpr (u == 23) cpx4(ko4, ka[0], ka[2], ka[4], ka[6], ka[1], ka[3], ka[5], ka[7]);
            else if constexpr (u < 26) { constexpr int e = u - 24; d2x4(kt + 4 * e, ko4[2 * e], ko4[2 * e], ko4[2 * e + 1], ko4[2 * e + 1], vs[4 * e], vs[4 * e + 1], vs[4 * e + 2], vs[4 * e + 3]); }
            else if constexpr (u == 26) {
                if constexpr (h < 3) { const v4u sk = ax[12 + h]; se[0] = sk[1] >> 16; se[1] = sk[3] & 0xffff0000u; se[2] = sk[1] & 0xffffu; se[3] = sk[3] << 16; pin(se[0]); pin(se[1]); pin(se[2]); pin(se[3]); }
                else asm volatile("s_nop 2");
            }
            else if constexpr (u == 27) {
                cpx4(ko4, kt[0], kt[2], kt[4], kt[6], kt[1], kt[3], kt[5], kt[7]);
                *(v2u*)(smem + kbw) = v2u{ko4[0], ko4[1]};
                *(v2u*)(smem + (kbw ^ 64)) = v2u{ko4[2], ko4[3]};
            }
            else kvt[h] = *(const v4u*)(smem + kbr);
            SCHED();
        };
        auto deal = [&](auto a_, auto b_, auto n_, auto i_) __attribute__((always_inline)) {
            constexpr int a = a_.value, m = b_.value - a, lo = m * i_.value / n_.value, hi = m * (i_.value + 1) / n_.value;
            static_for<hi - lo>(LAMBDA(e_) { pre(std::integral_constant<int, a + lo + e_.value>{}); });
        };
#define IC(n) std::integral_constant<int, (n)>{}
        static_for<kQ0>(pre);
        static_assert(kQ0 == 0 && KD0 >= 8, "");
        asm volatile("s_waitcnt vmcnt(%4)" : "+v"(ax[7]), "+v"(ax[8]), "+v"(ax[9]), "+v"(ax[10]) : "i"(13 + kQ0) : "memory");
        ax[11] = *(const v4u*)(smem + eas);
        SCHED();
#pragma unroll
        for (int i = 0; i < 7; ++i) { ax[i] = *(const v4u*)(smem + kra + i * 128); SCHED(); }
#pragma unroll
        for (int i = 1; i < 4; ++i) ax[11 + i] = *(const v4u*)(smem + eas + i * 16);
        SCHED();
        pin(nonce);
        STAMP(30)
        unsigned tch = 0u;
        auto kvs = LAMBDA(n_) {
            site<1000 + n_.value>(); SCHED();
            if constexpr (n_.value == KD0) LGKM0()
            kvop(n_);
            if constexpr (n_.value >= KD0) deal(IC(kQ0), IC(kQ0 + kQ1), IC(117 - KD0), IC(n_.value - KD0));
        };
        static_assert((KST - 1) % 29 == 0, "");
        if (kw) static_for<KST>(kvs);
        else {
            {
                const unsigned t_ = (unsigned)__builtin_amdgcn_s_memtime(), f_ = ((nt1 ^ w) & 1) << 13;
                const unsigned o_ = gof + N_END + (t_ & 0xe0u) * 512u + (unsigned)(nt1 * 512 + (w & 1) * 256) + (t_ & 0x10u) * 8u;
                const int r_ = race(p.ws + (o_ + f_), p.ws + (o_ + (f_ ^ 0x2000u)));
                vote += f_ ? r_ : -r_;
            }
            if (__builtin_expect(w == 3, !W3O)) {
                f32x2 lam, co, lp[16];
                disc(uM[0], uM[1], uM[2], lam, co);
                lp[0] = cmul(lam, lam); lp[0] = cmul(lp[0], lp[0]); lp[1] = cmul(lp[0], lp[0]);
                lp[0] = f32x2{1.f, 0.f};
                lp[2] = cmul(lp[1], lp[1]); lp[3] = cmul(lp[2], lp[1]); lp[4] = cmul(lp[2], lp[2]);
                const f32x2 z2 = {0.f, 0.f};
                f32x2 t4[4];
                cfma4(t4, lp[4], lp[1], lp[2], lp[3], lp[4], z2, z2, z2, z2);
                lp[5] = t4[0]; lp[6] = t4[1]; lp[7] = t4[2]; lp[8] = t4[3];
                cfma4(t4, lp[8], lp[1], lp[2], lp[3], lp[4], z2, z2, z2, z2);
                lp[9] = t4[0]; lp[10] = t4[1]; lp[11] = t4[2]; lp[12] = t4[3];
                cfma4(t4, lp[8], lp[5], lp[6], lp[7], lp[7], z2, z2, z2, z2);
                lp[13] = t4[0]; lp[14] = t4[1]; lp[15] = t4[2];
                f32x2* tl = (f32x2*)(smem + tcl);
                tl[0] = lam;
#pragma unroll
                for (int i = 0; i < 16; ++i) tl[1 + i] = lp[i];
                SCHED();
                asm volatile("kpf_end3:");
            }
            static_for<kQ1a>(LAMBDA(i_) { pre(IC(kQ0 + i_.value)); });
            undef4(kvt);
            asm volatile("" : "=v"(se[0]), "=v"(se[1]), "=v"(se[2]), "=v"(se[3]));
        }
        tag0 = __builtin_amdgcn_readlane(nonce, 0) + 1u; tag1 = __builtin_amdgcn_readlane(nonce + (tag0 == 0u ? 1u : 0u), 1);
        tagv = v2u{tag0, tag1}; pin(tagv);
        {
            asm volatile("s_waitcnt vmcnt(%5)" : "+v"(sa[0]), "+v"(sa[1]), "+v"(sa[2]), "+v"(sa[3]), "+v"(sa[4]) : "i"(8 + kQ0 + kQ1a) : "memory");
            float s1[2] = {0.f, 0.f}, s2[2] = {0.f, 0.f};
#pragma unroll
            for (int i = 0; i < 5; ++i)
#pragma unroll
                for (int e = 0; e < 4; ++e) { s1[e & 1] = dot2a(sa[i][e], one2, s1[e & 1]); s2[e & 1] = dot2a(sa[i][e], sa[i][e], s2[e & 1]); }
            const float a1 = rowsum16(s1[0] + s1[1]), a2 = rowsum16(s2[0] + s2[1]);
            const float mean = a1 * (1.0f / D_);
            const float var = fmaxf(a2 * (1.0f / D_) - mean * mean, 0.f);
            const float ri = __builtin_amdgcn_rcpf(rbf(__builtin_amdgcn_sqrtf(rbf(rbf(var) + 1e-5f))));
            v2u sv = {__float_as_uint(w >= 2 ? a1 : c ? -ri * rbf(mean) : ri), tag0};
            int o0 = w < 2 ? (mt1 * 16 + nt1) * 256 + 32 * w : ((mt1 >> 2) * 16 + nt1) * 256 + 112 + (mt1 & 3) * 16 + 8 * w, o1 = w < 2 ? 8 : 16384, o2 = w < 2 ? 64 : 0;
            pins(o0); pins(o1); pins(o2);
            unsigned so_ = c < 2 ? OFF_ST + (unsigned)(o0 + q * o1 + c * o2) : kNone;
            if (w >= 2 && lane == 2) { unsigned xc_; asm volatile("s_getreg_b32 %0, hwreg(HW_REG_XCC_ID, 0, 1)" : "=s"(xc_)); sv.x = vote | (xc_ << 16); so_ = OFF_VT + (unsigned)((mt1 * 16 + nt1) * 32 + (w & 1) * 8); }
            pin(sv); pin(so_);
            st64(sv, rs, so_);
            SCHED();
        }
        bool kw2 = kw;
        if (kw2) {
            static_for<117 - KST>(LAMBDA(n_) { kvs(IC(KST + n_.value)); });
        } else {
            if (__builtin_expect(w == 3, !W3O)) {
            const int l = lane, l3 = l & 3;
            unsigned a = gof + N_SG + (unsigned)((((l >> 3) & 1) * 2 + (mt2 & 1)) * 2 * NB_ + ((l >> 1) & 3) * 2048 + nt2 * 256 + (l & 1) * 128);
            { unsigned t_ = OFF_EG + (unsigned)((l >= 21 ? 163840 - 640 : 0) + (mt1 * 16 + nt1) * 640 + (l < 26 ? l - 16 : 5) * 128); pin(t_); a = l >= 16 ? t_ : a; }
            { unsigned t_ = gof + N_PS + (unsigned)((((mt2 & 1) * 4 + l3) * 8 + nt2) * 128); pin(t_); a = l >= 28 ? t_ : a; }
            { unsigned t_ = gof + N_FH + (unsigned)(((l >> 2) & 1) * 2 * NB_ + (l3 >> 1) * 4096 + (nt1 * 2 + (l3 & 1)) * 128); pin(t_); a = l >= 32 ? t_ : a; }
            { unsigned t_ = gof + N_FX + (unsigned)(((2 * (mt2 & 1) + (l3 >> 1)) * 16 + nt2 * 2 + (l3 & 1)) * 128); pin(t_); a = l >= 40 ? t_ : a; }
            { unsigned t_ = OFF_FG + (unsigned)((nt2 * 96 + i32 + 16 * ((l3 + 1) & 6)) * 128); pin(t_); a = l >= 48 ? t_ : a; }
            const unsigned big = (l & 7) < 5 ? OFF_KVT + (unsigned)((l & 1) * 640 * 4608 + (80 * nt2 + i32) * GSTR) : OFF_W2F + (unsigned)(nt2 * 204800 + ((l & 7) - 5) * 98304);
            a = l >= 56 ? big : a;
            tch = a;
            pin(tch);
            asm volatile("kpf_end2:");
        }
            static_for<kQ1 - kQ1a>(LAMBDA(i_) { pre(IC(kQ0 + kQ1a + i_.value)); });
            if (w == TW) tour<1>();
            skip<SKIP0>();
            undef4(kvt);
        }
        SCHED();
        STAMP(34)
        if (APOS == 1) addrs(std::integral_constant<int, 0>{}, lane, w, b);
        v4u orr, oi;
        unsigned ie[4][4], io[4][4], iw;
        auto img = LAMBDA(n_) {
            constexpr int i = n_.value / 10, u = n_.value % 10;
            if constexpr (u == 0) { if constexpr (i > 0) ror1(iw, wr[i - 1].w); else { iw = 0u; pin(iw); } }
            else if constexpr (u < 5) rot1(ie[i][u - 1], wr[i][4 - u]);
            else if constexpr (u == 5) shr1b(iw, wr[i].w);
            else if constexpr (u < 9) mix1(io[i][u - 6], wr[i][9 - u], wr[i][8 - u]);
            else mix1(io[i][3], wr[i].x, iw);
        };
        auto tap0 = [&]() __attribute__((always_inline)) {
            wr[0].x = cvtpk(lo16f(wr[0].x) + (lane ? 0.f : lo16f(sdq)), hi16f(wr[0].x));
            pin(wr[0]);
            SCHED();
        };
        {
            static_assert(kQ0 + kQ1 <= 18, "");
            vm_wait<6 + kQ0 + kQ1>();
            cr[0] = *(const v4u*)(smem + ear); cr[1] = *(const v4u*)(smem + ear + 1024);
            static_for<5>(LAMBDA(i) { lds2a<22 + 2 * i.value, 2560 + 32 * i.value>(lds0 + (unsigned)(L_EAR + w * REGSZ + q * 8)); });
            asm volatile("s_waitcnt lgkmcnt(0)" : "+v"(cr[0]), "+v"(cr[1])::"memory");
            static_for<kQ2>(LAMBDA(i_) { pre(IC(kQ0 + kQ1 + i_.value)); });
            static_for<4>(LAMBDA(e_) {
                constexpr int e = e_.value;
                const f32x4 co = *(const f32x4*)(smem + coa + e * 16);
                const f32x2 o0 = lo16f(cr[0][e]) * f32x2{co.x, -co.y} - lo16f(cr[1][e]) * f32x2{co.y, co.x};
                const f32x2 o1 = hi16f(cr[0][e]) * f32x2{co.z, -co.w} - hi16f(cr[1][e]) * f32x2{co.w, co.z};
                orr[e] = cvtpk(o0.x, o1.x); oi[e] = cvtpk(o0.y, o1.y);
            });
            pin(orr); pin(oi);
            SCHED();
            static_for<NPD - kQ0 - kQ1 - kQ2>(LAMBDA(i_) { pre(IC(kQ0 + kQ1 + kQ2 + i_.value)); });
            for (int i = 0; i < 4; ++i) st128(kvt[i], rs, kvo + i * 2 * KSTR);
            st128(orr, rs, for0); st128(oi, rs, for1);
        }
        SCHED();
        STAMP(23)
        if (w == 3) tour<7>();
        asm volatile("s_waitcnt vmcnt(%5) lgkmcnt(0)" : "+v"(wr[0]), "+v"(wr[1]), "+v"(wr[2]), "+v"(wr[3]), "+v"(sdq) : "i"(P1_PRE - 1 - p1_pos(3)) : "memory");
        __builtin_amdgcn_s_barrier();
        asm volatile("" ::: "memory");
        STAMP(24)
        site<10>();
        v4u wf[2][5], af[2][2][2];
#pragma unroll
        for (int nb = 0; nb < 5; ++nb) wf[0][nb] = WF(nb, 0);
#pragma unroll
        for (int i = 0; i < 4; ++i) af[0][i >> 1][i & 1] = *(const v4u*)(smem + ((i >> 1) ? xlane1 : xlane0) + (i & 1) * 2048);
        SCHED();
        tap0();
        site<11>();
        kvtb = p.ws + OFF_KVT + (chn * KPAD + (chn >> 3) * KPS);
        pins(kvtb);
#pragma unroll
        for (int i = 0; i < (NW2P + 4) / 5; ++i) { w2b0[i] = p.ws + 2048 + i * 5120; pins(w2b0[i]); }
        v2u sg, tc[2], fk, fw, vt;
        v4u sw0;
        v2u zq[2];
        float rr_[8];
        unsigned sx[4], sz[4];
        u64 odd = 0xAAAAAAAAAAAAAAAAull;
        pins(odd);
        auto filler = LAMBDA(f_) {
            constexpr int f = f_.value;
            if constexpr (f < 0) {}
            else if constexpr (f < 40) img(IC(f));
            else if constexpr (f == 48) { lam1.r = __uint_as_float(tc[0].x); xor1(lam1.s, tc[0].y, sgn); }
            else if constexpr (f == 49) { lamq.r = __uint_as_float(tc[1].x); xor1(lamq.s, tc[1].y, sgn); }
            else if constexpr (f < 52) zero2(zq[f - 50]);
            else if constexpr (f == 52) { sx[0] = sw0.x; mov1(sx[1], sx[0]); }
            else if constexpr (f == 53) { sz[0] = sw0.z; mov1(sz[1], sz[0]); }
            else if constexpr (f < 56) bc1<f - 54>(rr_[f - 54], sg.x);
            else if constexpr (f == 56) sw16(sx[0], sx[1]);
            else if constexpr (f == 57) sw16(sz[0], sz[1]);
            else if constexpr (f < 60) mov1(sx[f - 56], sx[f - 58]);
            else if constexpr (f < 62) mov1(sz[f - 58], sz[f - 60]);
            else if constexpr (f < 64) sw32(sx[f - 62], sx[f - 60]);
            else if constexpr (f < 66) sw32(sz[f - 64], sz[f - 62]);
            else if constexpr (f < 72) {
                bc1<f - 64>(rr_[f - 64], sg.x);
                if constexpr (f == 66) { SW[0] = __uint_as_float(sx[0]); SW[1] = __uint_as_float(sz[0]); SW[2] = __uint_as_float(sx[1]); SW[3] = __uint_as_float(sz[1]); }
                if constexpr (f == 71) sel1(SW[4], sx[2], sz[2], odd);
            }
            else mulb<8 + (f - 72) % 8>(tt[(f - 72) % 8 / 4][(f - 72) / 8][(f - 72) % 4], sg.x, SW[(f - 72) / 8]);
        };
        auto kstep = LAMBDA(s_) {
            constexpr int s = s_.value, ph = s >> 2, s4 = s & 3, cu = s & 1, nx = cu ^ 1, jj = s >> 1, pp = jj & 1;
            if constexpr (s4 == 3 && ph < 4) {
                constexpr int pw = p1_pos(104 + 5 * (ph + 1)), px = p1_pos(4 * (jj + 1) + 3), pm = pw > px ? pw : px;
                constexpr int vm = p1_lim(p1_cnt(s, 0) - 1 - pm);
                asm volatile("" ::: "memory");
                __builtin_amdgcn_s_waitcnt((vm & 15) | ((vm >> 4) << 14) | 0x70);
                __builtin_amdgcn_s_barrier();
                asm volatile("" ::: "memory");
                STAMP(25 + ph)
                SCHED();
            }
            if constexpr (s == KSC) {
                static_assert(s4 == 3 && p1_pos(204) < p1_pos(4 * (jj + 1) + 3), "");
                pin(sg); pin(sw0); pin(vt);
                if (__builtin_expect(__builtin_amdgcn_ballot_w64((__builtin_amdgcn_bitop3_b32(sg.y, sw0.y, tag0, 0x7E) | __builtin_amdgcn_bitop3_b32(vt.y, sw0.w, tag0, 0x7E)) != 0u) != 0ull, 0))
                    WAVE_WAIT({ sg = ld64(rs, sto); sw0 = ld128(rs, swo); vt = ld64(rs, vto); ok = ((sg.y ^ tag0) | (sw0.y ^ tag0) | (sw0.w ^ tag0) | (vt.y ^ tag0)) == 0u; })
                ORDER();
                STAMP(7)
            }
            if constexpr (s == kW2S && KUS > kW2S) { vm_wait<p1_cnt(s, 0) - 1 - p1_pos(208)>(); SCHED(); }
            if constexpr (s == KUS) {
                static_assert(p1_pos(206) > p1_pos(208), "");
                wait_fkn<p1_cnt(s, 0) - 1 - p1_pos(206)>(fk, fw);
                if (!USP) ld_ur(wro, kvtb);
                SCHED();
            }
            if constexpr (s4 != 3 || ph == 4) lgkm<(s == 0 ? 0 : p1_nl(s - 1, 5 - cu, 10) - 1)>();
            SCHED();
            static_for<10>(LAMBDA(g_) {
                constexpr int g = g_.value, mb = g / 5, k = g % 5, it = p1_item(s, g), nt_ = g < 9 ? p1_item(s, g + 1) : -1;
                constexpr bool d0 = it >= 0 && it < 200, d1 = nt_ >= 0 && nt_ < 200;
                if constexpr (s == 0) mf0(acc[mb][k], af[pp][cu][mb], wf[cu][k]); else mf(acc[mb][k], af[pp][cu][mb], wf[cu][k]);
                SCHED();
                if constexpr (s < 19) {
                    if constexpr (g < 5) wf[nx][g] = WF(g, s + 1);
                    if constexpr (cu == 1 && g == 5) vm_wait<p1_lim(p1_cnt(s, 5) - 1 - p1_pos(4 * (jj + 1) + 3))>();
                    if constexpr (cu == 1 && g >= 5 && g < 9)
                        af[pp ^ 1][(g - 5) >> 1][(g - 5) & 1] = *(const v4u*)(smem + (((g - 5) >> 1) ? xlane1 : xlane0) + ((jj + 1) % 3) * 4096 + ((g - 5) & 1) * 2048);
                }
                if constexpr (cu == 0 && g == XG0 - 1 && s > 0) lgkm<p1_nl(s - 1, 8, 10) + p1_nl(s, 0, XG0 - 1)>();
                constexpr int lq = p1_lds(s, g);
                if constexpr (lq >= 4 && lq < 8) (lq == 4 ? lam1.a : lq == 5 ? lam1.b : lq == 6 ? lamq.a : lamq.b) = *(const f32x2*)(smem + (lq < 6 ? tca : tcb) + (lq & 1) * 2176);
                else if constexpr (lq == 8 || lq == 9) tc[lq - 8] = *(const v2u*)(smem + (lq == 8 ? tcc : tcd));
                else if constexpr (lq == 10) { const f32x2* tp = (const f32x2*)(smem + tcl); l8 = tp[2]; l32 = tp[5]; }
                else if constexpr (lq == 11) l128 = *(const f32x2*)(smem + tcl + 72);
                else if constexpr (lq >= 30 && lq < 33) *(v4u*)(smem + (lq < 32 ? ime + 1024 * (lq - 30) : im3)) = v4u{ie[33 - lq][0], ie[33 - lq][1], ie[33 - lq][2], ie[33 - lq][3]};
                else if constexpr (lq >= 33 && lq < 37) *(v4u*)(smem + imo + 1024 * (36 - lq)) = v4u{io[lq - 33][0], io[lq - 33][1], io[lq - 33][2], io[lq - 33][3]};
                else if constexpr (lq == 37) *(v4u*)(smem + im3 + 1024) = v4u{ie[0][0], ie[0][1], ie[0][2], ie[0][3]};
                else if constexpr (lq == 38) *(unsigned*)(smem + rsa) = sg.x;
                else if constexpr (lq == 39) *(v4u*)(smem + zpa) = v4u{zq[0].x, zq[0].y, zq[1].x, zq[1].y};
                static_for<p1_cap(s, g)>(LAMBDA(e_) { filler(IC(p1_fill(s, g, e_.value))); });
                if constexpr (s == 18 && g == 9) ne2(late, tagv, fk, fw);
                if constexpr (s == 19 && g == 5) count_(side, fine, vt.x);
                if constexpr (s == 19 && g == 2) { if (__builtin_expect(late != 0ull, 0)) poll2(fko, fwo, p.ws); }
                if constexpr (it >= 0) {
                    if constexpr (d0 && d1) DMA_LD1(it, d1 ? nt_ : 0);
                    else if constexpr (d0) DMA_LD(it);
                    else if constexpr (it == 202) gld2s<0>(sg, sto, p.ws);
                    else if constexpr (it == 203) gld4s<0>(sw0, swo, p.ws);
                    else if constexpr (it == 204) gld2s<0>(vt, vto, p.ws);
                    else if constexpr (it == 206) gld2s<0>(fk, fko, p.ws);
                    else if constexpr (it == 208) gld2s<0>(fw, fwo, p.ws);
                    else if constexpr (it == 209) { unsigned t2 = tch + (side + fine); asm volatile("v_cndmask_b32 %0, %1, %0, %2" : "+v"(t2) : "v"(tch), "s"(0x0000FFFFF000FFFFull)); touch_(t2, p.ws); }
                    else if constexpr (it >= 210 && it < 214) ld_ur1<it - 210>(wro, kvtb);
                    else if constexpr (it >= 500) gld_w2<it % 50>(w2o, w2b0[(it % 50) / 5]);
                    else { vm_wait<p1_cnt(s, g) - P1_PRE>(); st64(tagv, rs, fko1); }
                }
                if constexpr (d1 && !d0) DMA_M0(d1 ? nt_ : 0);
                site<2000 + 10 * s + g>();
                SCHED();
            });
        };
        static_for<20>(kstep);
        static_for<NFILL - p1_fill(19, 10, 0)>(LAMBDA(i_) { filler(IC(p1_fill(19, 10, 0) + i_.value)); });
        SCHED();
        rr[0] = f32x4{rr_[0], rr_[1], rr_[2], rr_[3]}; rr[1] = f32x4{rr_[4], rr_[5], rr_[6], rr_[7]};
        ACC_FENCE10(acc[0], acc[1]);
        gn = p.ws + (gof + side); gf_ = gn + fine;
        rn = __builtin_amdgcn_make_buffer_rsrc((void*)gn, 0, N_END, 0x00020000);
        STAMP(1)
    }

    auto layer_body = LAMBDA(layer_) {
        constexpr int layer = layer_.value;
        const int sga = sgn_;
        const bool cv = layer == 0 && cv0;
        v2u fh;
        unsigned ks[5];
        f32x4 ca[8], a2[4][5];
        unsigned gp[4][4];
        auto gst = LAMBDA(i_) {
            constexpr int i = i_.value >> 2, u = i_.value & 3;
            if constexpr (u < 2) cp2(gp[i][2 * u], gp[i][2 * u + 1], ca[2 * i + u]);
            else if constexpr (u == 2) swp2n(gp[i][0], gp[i][2], gp[i][1], gp[i][3]);
            else st128(v4u{gp[i][0], gp[i][1], gp[i][2], gp[i][3]}, rs, gso + i * 1024);
        };
        float rsr, rsm;
        St bv[8];
        {
            St a0, a1;
            auto w2s = LAMBDA(n_) {
                constexpr int lo = KSA * n_.value / 4, hi = KSA * (n_.value + 1) / 4;
                if constexpr (layer == 0) { static_for<hi - lo>(LAMBDA(e_) { constexpr int i = NW2L + lo + e_.value; gld_w2<i>(w2o, w2b0[i / 5]); }); SCHED(); }
            };
            static_for<2>(LAMBDA(m_) {
                constexpr int mb = m_.value;
#pragma unroll
                for (int i = 0; i < 4; ++i) fma2p(bv[4 * mb + i].a, acc[mb][0][i], acc[mb][1][i], rr[mb][i], tt[mb][0][i], tt[mb][1][i]);
#pragma unroll
                for (int i = 0; i < 4; ++i) fma2p(bv[4 * mb + i].b, acc[mb][2][i], acc[mb][3][i], rr[mb][i], tt[mb][2][i], tt[mb][3][i]);
                const f32x4 v = __builtin_elementwise_fma(acc[mb][4], rr[mb], f32x4{tt[mb][4][0], tt[mb][4][1], tt[mb][4][2], tt[mb][4][3]});
#pragma unroll
                for (int i = 0; i < 4; ++i) bv[4 * mb + i].c = v[i];
                SCHED();
                w2s(IC(2 * mb));
                if constexpr (mb == 0) run3(a0, bv[0], bv + 1, lam1); else run4(a1, a0, bv + 4, lam1);
                SCHED();
                if constexpr (mb == 0) w2s(IC(1));
            });
            *(f32x2*)(smem + sga) = a1.a; *(f32x2*)(smem + sga + 256) = a1.b; *(float*)(smem + sg4 + 512) = a1.c;
            SCHED();
            w2s(IC(3));
        }
        SCHED();
        const int w_ = w0;
        {
        const int w = w_, mt1 = mt1_;
        if (layer == 0 && __builtin_expect(late != 0ull, 0)) {
            unsigned f0_, f1_, f2_, f3_;
            asm volatile("s_waitcnt vmcnt(0)\n\t" AR("%0", 36, 0) AR("%1", 36, 1) AR("%2", 36, 2) AR("%3", 36, 3) : "=v"(f0_), "=v"(f1_), "=v"(f2_), "=v"(f3_)::"memory");
            late = __builtin_amdgcn_ballot_w64(((f0_ ^ tag0) | (f1_ ^ tag1) | (f2_ ^ tag0) | (f3_ ^ tag1)) != 0u);
            ORDER();
            if (late == 0ull) KW_REQ()
        }
        asm volatile("s_waitcnt lgkmcnt(0)" ::: "memory");
        __builtin_amdgcn_s_barrier();
        asm volatile("" ::: "memory");
        STAMP(layer ? 14 : 2)
#pragma unroll
        for (int s = 0; s < 5; ++s) ks[s] = (unsigned)s * NB_;
        constexpr int NW2A = NW2P + 7 * kCR1 + kXA, NW3 = NW2P + kN3E + kN3;
        auto w2ld = LAMBDA(i_) {
            constexpr int i = i_.value;
            gld_w2<i>(w2o, p.ws + 2048 + (i / 5) * 5120);
        };
        auto w2a = LAMBDA(i_) {
            constexpr int k = i_.value;
            if constexpr (k < kXA) { w2ld(std::integral_constant<int, NW2P + 2 * k>{}); w2ld(std::integral_constant<int, NW2P + 2 * k + 1>{}); }
            else w2ld(std::integral_constant<int, NW2P + kXA + k>{});
        };
        unsigned xtb = (unsigned)(size_t)(LAS char*)smem + (unsigned)(l_tb(0, 0) + w * 5120);
        auto w2x = LAMBDA(i_) {
            constexpr int i = NW2A + i_.value;
            if constexpr (i < 50) w2ld(std::integral_constant<int, i>{});
            else if constexpr (i < 53) dma_x<(i - 50) * 1024, (i < 52 ? (i - 50) * 64 : 96)>(xso, p.x0, xtb);
        };
        auto w2b = LAMBDA(i_) {
            constexpr int k = i_.value - 7 * kCR1;
            constexpr int ne = KW2E < 53 - NW2A ? KW2E : 53 - NW2A;
            if constexpr (k < ne) w2x(std::integral_constant<int, k>{});
            constexpr int k1 = k - kFHK - 1;
            if constexpr (k1 >= 0 && k1 < KW2T) { w2x(std::integral_constant<int, ne + 2 * k1>{}); w2x(std::integral_constant<int, ne + 2 * k1 + 1>{}); }
            else if constexpr (k1 >= 0) w2x(std::integral_constant<int, ne + KW2T + k1>{});
            static_assert(ne <= kFHK + 1 && 53 - NW2A - ne - KW2T + kFHK < 7 * (8 - kCR1) - kFHP + 1, "");
            if constexpr (k == kFHK) { vm_wait<ne>(); __builtin_amdgcn_raw_buffer_store_b64(tagv, rn, fho1, fine, SC1); STAMP(39) }
            if constexpr (k == kFHK - 1) { STAMP(38) }
            if constexpr (k == 7 * (8 - kCR1) - kFHP) poll1(fho, gf_);
            static_assert(NW2A + 7 * (8 - kCR1) >= 53 && NW2A <= 50 && NW3 <= NW2A, "");
        };
        auto partb = [&]() __attribute__((always_inline)) {
            const float* sp = (const float*)(smem + spa);
            rsr = sp[0]; rsm = sp[8];
            if (layer == 1) { pin(rsr); pin(rsm); }
            if (layer == 1) {
                static_for<5>(LAMBDA(n_) {
                    constexpr int nb = n_.value;
                    unsigned d0, d1;
                    rda<22 + 2 * nb>(d0); rda<23 + 2 * nb>(d1);
                    a2[3][nb] = xin[nb] + (xin[nb] * rsr + rsm) * unpk4(v2u{d0, d1}); pin(a2[3][nb]);
                });
            }
        };
        constexpr int LBW = layer ? 2 : 3;
        if (w == 3) {
            site<3 + 10 * layer>(); SCHED();
            f32x4 s4[8];
#pragma unroll
            for (int i = 0; i < 8; ++i) { s4[i] = *(const f32x4*)(smem + spl + (l_sg(layer, i & 3) - SC + (i >> 2) * 640)); SCHED(); }
            f32x2 sv[16], x[4], P[4][4];
#pragma unroll
            for (int i = 0; i < 16; ++i) { const f32x4 t = s4[(i >> 2) + 2 * (i & 2)]; sv[i] = (i & 1) ? f32x2{t.z, t.w} : f32x2{t.x, t.y}; }
            unsigned er, ei;
            {
                f32x2 u[4];
                cfma4n(u, l8, sv[0], sv[4], sv[8], sv[12], sv[1], sv[5], sv[9], sv[13]);
                agg(er, ei, x, P[0][2], u, sv, l8, l32, l128);
            }
            st128(v4u{er, tag0, ei, tag0}, rs, es);
            SCHED();
            P[0][0] = f32x2{0.f, 0.f}; P[0][1] = x[0]; P[0][3] = cfma(l32, P[0][2], x[2]);
            if (layer == 0) { FW_OK() static_for<kN3E>(LAMBDA(i_) { w2ld(std::integral_constant<int, NW2P + i_.value>{}); }); }
#pragma unroll
            for (int kk = 1; kk < 4; ++kk) cfma4(P[kk], l8, P[kk - 1][0], P[kk - 1][1], P[kk - 1][2], P[kk - 1][3], sv[kk - 1], sv[kk + 3], sv[kk + 7], sv[kk + 11]);
#pragma unroll
            for (int i = 0; i < 8; ++i) { const f32x2 a = P[2 * (i >> 2)][i & 3], b_ = P[2 * (i >> 2) + 1][i & 3]; *(f32x4*)(smem + spl + (l_sg(layer, i & 3) - SC + (i >> 2) * 640)) = f32x4{a.x, a.y, b_.x, b_.y}; }
            partb();
        }
        int w1_ = w, w3_ = w;
        if (layer == 1) { pins(w1_); pins(w3_); }
        if (layer == 1 && w1_ < LBW) { site<4 + 10 * layer>(); SCHED(); partb(); if constexpr (layer == 1) { if (w == 0) tour<6>(); if constexpr (LBR == 1) room<3000>(); } }
        if (w3_ == LBW) {
            site<5 + 10 * layer>(); SCHED();
            f32x2 wg[17], t4[4];
            unsigned ea[15], tg[15];
            v4u eg[15];
            constexpr bool early = (LBE >> layer) & 1;
            const f32x2 z2 = {0.f, 0.f};
#pragma unroll
            for (int i = 0; i < 15; ++i) { ea[i] = (unsigned)(mt1 - 1 - i) * 10240u; pins(ea[i]); ea[i] = ea[i] < kNone ? ea[i] : kNone; pins(ea[i]); }
            if constexpr (early) {
                __builtin_amdgcn_s_sleep(layer ? KLB1 : KLB0);
#pragma unroll
                for (int i = 0; i < 15; ++i) eg[i] = ld128s(rs, eo, ea[i]);
                SCHED();
            }
            wg[1] = cmul(l128, l128); wg[2] = cmul(wg[1], wg[1]); wg[3] = cmul(wg[2], wg[1]); wg[4] = cmul(wg[2], wg[2]);
#pragma unroll
            for (int i = 4; i < 16; i += 4) {
                cfma4(t4, wg[i], wg[1], wg[2], wg[3], wg[4], z2, z2, z2, z2);
#pragma unroll
                for (int e = 0; e < 4; ++e) wg[i + 1 + e] = t4[e];
            }
#pragma unroll
            for (int i = 0; i < 15; ++i) { if (i) pin(wg[i]); tg[i] = mt1 - 1 - i < 0 ? 0u : tag0; pins(tg[i]); }
            if (layer == 1) partb();
            if constexpr (layer == 0) tour<3>();
            f32x2 H0, H1;
            unsigned spins = dead ? kSpinCap : 0u;
            if constexpr (layer == 1 && LBR == 0) { room<1800>(); site<16>(); }
            if constexpr (!early) __builtin_amdgcn_s_sleep(layer ? KLB1 : KLB0);
            SCHED();
            auto eval = [&](const v4u (&e)[15]) __attribute__((always_inline)) {
                unsigned x = __builtin_amdgcn_bitop3_b32(e[0].y, e[0].w, tg[0], 0x7E);
                H0 = f32x2{__uint_as_float(e[0].x), __uint_as_float(e[0].z)}; H1 = f32x2{0.f, 0.f};
#pragma unroll
                for (int i = 1; i < 15; i += 2) {
                    x |= __builtin_amdgcn_bitop3_b32(e[i].y, e[i].w, tg[i], 0x7E) | __builtin_amdgcn_bitop3_b32(e[i + 1].y, e[i + 1].w, tg[i + 1], 0x7E);
                    pin(x);
                    gacc2(H0, H1, wg[i], wg[i + 1], e[i], e[i + 1]);
                    SCHED();
                }
                *(f32x2*)(smem + spl + (L_CW - SC)) = H0 + H1;
                return __builtin_amdgcn_ballot_w64(x != 0u) != 0ull;
            };
            bool again = true;
            if constexpr (early) again = eval(eg);
            if (again) {
                bool miss;
#pragma nounroll
                do {
                    asm volatile("" ::: "memory");
                    v4u e2[15];
#pragma unroll
                    for (int i = 0; i < 15; ++i) e2[i] = ld128s(rs, eo, ea[i]);
                    SCHED();
                    miss = eval(e2);
                    if (__builtin_expect(miss, 0) && ++spins > kSpinCap) { dead = true; miss = false; }
                } while (miss);
            }
            ORDER();
            STAMP(layer ? 36 : 37)
            if (layer == 0) {
                static_for<NW3 - NW2P - kN3E>(LAMBDA(i_) { w2ld(std::integral_constant<int, NW2P + kN3E + i_.value>{}); });
            }
        }
        int w2_ = w;
        pins(w2_);
        if constexpr (layer == 0) site<21>();
        if (layer == 0) UNDEF8(ca);
        if (layer == 0 && w2_ != LBW) {
            if (cv) {
                site<2>(); SCHED();
                partb();
                {
                    FW_OK()
                    st_ur((unsigned)(size_t)(LAS char*)smem + (unsigned)uwa);
                    CV_LOAD_A(cA, 0, 0, 0) CV_LOAD_A(cA, 0, 0, 1)
                    SCHED();
                    static_for<8>(LAMBDA(a_) { constexpr int a = a_.value; CV_LOAD_B(cB, a, 0) SCHED(); });
                    CV_LOAD_A(cA, 1, 0, 0) CV_LOAD_A(cA, 1, 0, 1)
                    SCHED();
                }
                STAMP(32)
                conv_part<0, kCR1, CPF>(ca, cA, cB, smem, ab, bb, w2a, LAMBDA(i_) {});
            } else {
                if constexpr (layer == 0) tour<5>();
                FW_OK()
                static_for<NW2A - NW2P>(LAMBDA(i_) { w2ld(std::integral_constant<int, NW2P + i_.value>{}); __builtin_amdgcn_s_sleep(1); });
                partb();
                if constexpr (layer == 0) room<3900>();
            }
        }
        asm volatile("s_waitcnt lgkmcnt(0)" ::: "memory");
        __builtin_amdgcn_s_barrier();
        asm volatile("" ::: "memory");
        STAMP(layer ? 15 : 4)
        {
            St cw, ex, hv[8];
            cw.a = *(const f32x2*)(smem + cwa); cw.b = *(const f32x2*)(smem + cwa + 256); cw.c = *(const float*)(smem + cw4 + 512);
            ex.a = *(const f32x2*)(smem + sga); ex.b = *(const f32x2*)(smem + sga + 256); ex.c = *(const float*)(smem + sg4 + 512);
            SCHED();
#pragma unroll
            for (int s = 1; s < 5; ++s) pins(ks[s]);
            SCHED();
            keep14(hv, cw, ex, bv, lamq, lam1);
            keep4(hv + 4, hv[3], bv + 4, lam1);
            SCHED();
            v2u ta[5], td[5];
            f32x2 hb[9];
            static_for<5 + SBL>(LAMBDA(r_) {
                constexpr int r = r_.value, rd = r < 2 ? r : r == 2 ? 4 : r - 1, ro_ = r - SBL < 2 ? r - SBL : r - SBL == 2 ? 4 : r - SBL - 1;
                if constexpr (r < 5) {
                    float e[8];
                    unsigned o[4];
#pragma unroll
                    for (int i = 0; i < 8; ++i) e[i] = rd == 4 ? hv[i].c : rd < 2 ? hv[i].a[rd & 1] : hb[i + 1][rd & 1];
                    if constexpr (r == 0) b3cv(hb, o, cw.b, ex.b, bv[0].b, bv[1].b, lamq.b, lam1.b, e);
                    else if constexpr (r < 3) b3cv(hb + 3 * r, o, hb[3 * r - 1], bv[3 * r - 1].b, bv[3 * r].b, bv[3 * r + 1].b, lam1.b, lam1.b, e);
                    else for (int i = 0; i < 4; ++i) o[i] = cvtpk(e[2 * i], e[2 * i + 1]);
                    *(v4u*)(smem + tbw + rd * 1024) = v4u{o[0], o[1], o[2], o[3]};
                    SCHED();
                    if constexpr (r < 3) { bfin(hb[3 * r + 2], hb[3 * r + 1], lam1.b); SCHED(); }
                    ta[rd] = __builtin_bit_cast(v2u, trread(smem, (rd < 4 ? tbp : tbr) + rd * 1024));
                    td[rd] = __builtin_bit_cast(v2u, trread(smem, (rd < 4 ? tbq + 256 : tbr + 512) + rd * 1024));
                    SCHED();
                }
                if constexpr (r >= SBL) { __builtin_amdgcn_raw_buffer_store_b128(v4u{ta[ro_].x, ta[ro_].y, td[ro_].x, td[ro_].y}, rn, hso, ks[ro_], SC1); SCHED(); }
            });
        }
        if (layer == 1) { DRAIN(); __builtin_amdgcn_raw_buffer_store_b64(tagv, rn, fho1, fine, SC1); }
        STAMP(layer ? 16 : 5)
        if (cv) {
            if constexpr (layer == 0) site<6>();
            SCHED();
            if constexpr (!CPF) {
                CV_LOAD_A(cA, 0, kCR1, 0) CV_LOAD_A(cA, 0, kCR1, 1)
                SCHED();
                static_for<8>(LAMBDA(a_) { constexpr int a = a_.value; CV_LOAD_B(cB, a, kCR1) SCHED(); });
                static_for<7>(LAMBDA(a_) { constexpr int a = a_.value + 1; CV_LOAD_A(cA, a, kCR1, 0) CV_LOAD_A(cA, a, kCR1, 1) });
                SCHED();
            }
            conv_part<kCR1, 8, false>(ca, cA, cB, smem, ab, bb, w2b, LAMBDA(g_) {
                constexpr int g = g_.value;
                if constexpr ((g == 14 || g == 16 || g == 18 || g == 20)) gst(std::integral_constant<int, (g - 14) / 2>{});
                if constexpr ((g == 26 || g == 28 || g == 30 || g == 32)) gst(std::integral_constant<int, 4 + (g - 26) / 2>{});
            });
            asm volatile("" : "+v"(ca[0]), "+v"(ca[1]), "+v"(ca[2]), "+v"(ca[3]), "+v"(ca[4]), "+v"(ca[5]), "+v"(ca[6]), "+v"(ca[7]));
            static_for<8>(LAMBDA(i_) {
                constexpr int n = 8 + i_.value;
                if constexpr (n == 9) asm volatile("s_nop 0");
                if constexpr ((n & 3) == 2) asm volatile("s_nop 1");
                gst(std::integral_constant<int, n>{}); SCHED();
            });
        } else if (layer == 0) {
            constexpr int ne = KW2E < 53 - NW2A ? KW2E : 53 - NW2A;
            static_for<54 - NW2A>(LAMBDA(i_) {
                if constexpr (i_.value == ne) { vm_wait<ne>(); __builtin_amdgcn_raw_buffer_store_b64(tagv, rn, fho1, fine, SC1); }
                if constexpr (i_.value < 53 - NW2A) { w2x(i_); __builtin_amdgcn_s_sleep(1); }
            });
            if (w == 3) { static_for<NW2A - NW3>(LAMBDA(i_) { w2ld(std::integral_constant<int, NW3 + i_.value>{}); __builtin_amdgcn_s_sleep(1); }); if constexpr (layer == 0) tour<2>(); }
            poll1(fho, gf_);
            if constexpr (layer == 0) room<1000>();
            DRAIN();
        }
        if (layer == 0) { unsigned f0_, f1_; asm volatile("s_waitcnt vmcnt(%2)\n\t" AR("%0", 36, 0) AR("%1", 36, 1) : "=v"(f0_), "=v"(f1_) : "i"(4) : "memory"); fh = v2u{f0_, f1_}; }
        STAMP(layer ? 29 : 6)
        }

            v2u pt[4];
            if (layer == 0) {
                u64 hl;
                ne1(hl, tagv, fh);
                if (__builtin_expect(hl != 0ull, 0)) {
                    gld2t(fh, fho, gf_); wait1(fh); st64(tagv, rs, fgo1);
                    WAVE_RETRY(ISTAG(fh), fh = ld64(rn, fho + fine))
                }
                ORDER();
            }
            else { room<3400>(); site<18>(); pipe_flag<KPH0>(pt, fho, gf_, tagv, dead); ORDER(); }
            STAMP(layer ? 17 : 31)
            site<7 + 10 * layer>(); SCHED();
            constexpr int RING2 = layer ? KR2 : KR20;
            v4u ar[RING2];
#pragma unroll
            for (int i = 0; i < KI2; ++i) { ar[i] = ld128s(rn, ho[i / 10] + (i & 1) * 1024, ks[i % 10 >> 1]); SCHED(); }
        int b_o = b0, w_o = w0;
        asm volatile("" : "+s"(b_o), "+s"(w_o));
        const int b = b_o, w = w_o;
        TILES()
        v4u gl[3];
        v2u f0;
        unsigned fgo, sgo, sgs, glo, gw, gr, srco;
        u64 glate = 0ull;
        const char *gb1 = p.ws + 32 * GSTR, *gb2 = p.ws + 48 * GSTR;
        {
            unsigned xs_[3][4], dq[2];
            float zx[4], zd[4], zt[4], zz[5][4];
            unsigned sA = SC + w * INBOX, s1 = N_SG + fine + (layer * 2 + (mt2 & 1)) * 2 * NB_ + w * 2048, s2 = s1 + nt2 * 256, sF = OFF_FG + (nt2 * 96 + 4 * w) * 128, sG = OFF_GT + (80 * nt2 + 4 * w) * GSTR + mt2 * 128,
                     sW = sA + 8192 + (w & 1) * 128, sR = SC + 8192 + w * 32, sX = l_tb(0, 0) + w * 5120, k160 = 160, kIn = INBOX;
            pins(sA); pins(s1); pins(s2);
            if (layer == 0) { pins(sF); pins(sG); pins(sW); pins(sR); pins(sX); pins(k160); pins(kIn); pins(gb1); pins(gb2); }
            unsigned l16, c16, q0, q1, c2, xr0, xr1, ta_, tb_;
#define A1(n_, v_, e_) else if constexpr (n == n_) { v_ = e_; pin(v_); }
            auto adr = LAMBDA(n_) {
                constexpr int n = n_.value + (layer && n_.value >= 4 ? 12 : 0);
                if constexpr (n == 0) { l16 = (unsigned)dst[0] & 1023u; pin(l16); }
                A1(1, c16, l16 & 0xf0u) A1(2, q0, (l16 >> 8) & 1u) A1(3, q1, l16 >> 9)
                A1(4, c2, (l16 >> 6) & 3u) A1(5, xr0, (c16 << 2) + sX) A1(6, ta_, q1 ^ c2) A1(7, xr0, (q0 << 3) + xr0) A1(8, xr0, (ta_ << 4) + xr0) A1(9, xr1, xr0 ^ 32u)
                else if constexpr (n >= 10 && n < 15) { constexpr int nb = n - 10; const v2u t = *(const v2u*)(smem + ((nb & 5) ? xr1 : xr0) + (nb >> 1) * 1024); xs_[nb >> 1][2 * (nb & 1)] = t.x; xs_[nb >> 1][2 * (nb & 1) + 1] = t.y; }
                A1(16, srco, l16 + sA) A1(17, ta_, l16 & 0x300u) A1(18, sgo, s1 + l16 + ta_) A1(19, sgs, (q0 << 3) + c16) A1(20, ta_, (q1 << 30) + s2) A1(21, sgs, sgs + ta_)
                else if constexpr (layer == 1) {}
                A1(22, ta_, l16 < 624u ? l16 : 624u) A1(23, tb_, ta_ & 0x380u) A1(24, ta_, ta_ & 0x60u) A1(25, fgo, (tb_ << 4) + sF) A1(26, fgo, (ta_ << 2) + fgo)
                A1(27, glo, q1 * (16u * GSTR) + sG) A1(28, ta_, l16 & 0x180u) A1(29, tb_, l16 & 0x70u) A1(30, glo, ta_ * (GSTR / 128u) + glo) A1(31, glo, glo + tb_)
                A1(32, ta_, l16 >> 7) A1(33, tb_, l16 & 0x30u) A1(34, gw, (tb_ << 1) + sW) A1(35, gw, __umul24(ta_, k160) + gw) A1(36, ta_, c2 & 1u) A1(37, gw, (ta_ << 4) + gw)
                A1(38, ta_, l16 >> 8) A1(39, gr, (q0 << 7) + sR) A1(40, gr, __umul24(ta_, kIn) + gr) A1(41, gr, __umul24(c2, k160) + gr) A1(42, tb_, tb_ >> 1) A1(43, gr, gr + tb_)
            };
            SCHED();
            if constexpr (STAMPS == 1) { STAMP(layer ? 50 : 40) }
            static_for<40>(LAMBDA(i_) {
                constexpr int i = i_.value, m = i / 10, s = i % 10;
                static_for<5>(LAMBDA(k_) {
                    constexpr int kk = k_.value, pv = ring_req(5 * i + kk, KI2, RING2, 0);
                    if constexpr (s == 0 && m < 3) mfa0<kk>(a2[m][kk], ar[i % RING2]); else mfa<5 * s + kk>(a2[m][kk], ar[i % RING2]);
                    SCHED();
                    if constexpr (i == 0 && kk == 0 && layer == 1) { pin(pt[0]); pin(pt[1]); pin(pt[2]); pin(pt[3]); }
                    if constexpr (g2_adr(i, kk) >= 0) adr(IC(g2_adr(i, kk)));
                    if constexpr (layer == 0 && i >= 1 && i < 28 && (kk & 1)) {
                        constexpr int n = 2 * (i - 1) + (kk >> 1) - 9, nb = n / 9, u = n % 9 - 1;
                        if constexpr (n < 0) {}
                        else if constexpr (u < 0) { rda<22 + 2 * nb>(dq[0]); rda<23 + 2 * nb>(dq[1]); }
                        else if constexpr (u < 2) unp2(zx[2 * u], zx[2 * u + 1], xs_[nb >> 1][2 * (nb & 1) + u]);
                        else if constexpr (u < 4) unp2(zd[2 * u - 4], zd[2 * u - 3], dq[u - 2]);
                        else if constexpr (u < 6) fma2(zt[2 * u - 8], zt[2 * u - 7], zx[2 * u - 8], zx[2 * u - 7], rsr, rsr, rsm, rsm);
                        else fma2(zz[nb][2 * u - 12], zz[nb][2 * u - 11], zt[2 * u - 12], zt[2 * u - 11], zd[2 * u - 12], zd[2 * u - 11], zx[2 * u - 12], zx[2 * u - 11]);
                    }
                    if constexpr (layer == 0 && i == 0 && kk == 1) { vm_wait<KI2 - 1 + (ring_req(0, KI2, RING2, 0) >= 0)>(); st64(tagv, rs, fgo1); }
                    if constexpr (layer == 0 && i == kGPI && kk == 2) gld2s<0>(f0, fgo, p.ws);
                    if constexpr (layer == 0 && i == kGCI) {
                        static_assert(kGCI + KR20 > 40, "");
                        if constexpr (kk == 0) { __builtin_amdgcn_s_waitcnt(0x0F70); pin(f0); gld4s<0>(gl[0], glo, p.ws); }
                        if constexpr (kk == 1) { gld4s<0>(gl[1], glo, gb1); gld4s<0>(gl[2], glo, gb2); }
                        if constexpr (kk == 3) ne1(glate, tagv, f0);
                    }
                    if constexpr (pv >= 0) ar[pv % RING2] = ld128s(rn, ho[pv / 10] + (pv & 1) * 1024, ks[pv % 10 >> 1]);
                    if constexpr (kk == 2 && m >= 1 && s >= 1 && s < 6) *(f32x4*)(smem + dst[m - 1] + (s - 1) * 1024) = a2[m - 1][s - 1];
                    site<4000 + 1000 * layer + 5 * i + kk>();
                    if constexpr (i >= 37) a2[i - 37][kk] = *(const f32x4*)(smem + srco + (i - 37) * 5120 + kk * 1024);
                    SCHED();
                });
                if constexpr (i == 10) { asm volatile("s_waitcnt lgkmcnt(0)" ::: "memory"); __builtin_amdgcn_s_barrier(); asm volatile("" ::: "memory"); }
                if constexpr (i == 28 && layer == 0) {
#pragma unroll
                    for (int nb = 0; nb < 5; ++nb) { a2[3][nb] = f32x4{zz[nb][0], zz[nb][1], zz[nb][2], zz[nb][3]}; pin(a2[3][nb]); }
                }
                if constexpr (i == 36) {
                    asm volatile("s_waitcnt lgkmcnt(0)" ::: "memory");
                    __builtin_amdgcn_s_barrier(); asm volatile("" ::: "memory");
                }
                if constexpr (STAMPS == 1 && (i == 0 || i == 9 || i == 10 || i == 15 || i == 25 || i == 35 || i == 36)) { STAMP((layer ? 51 : 41) + (i == 0 ? 0 : i == 9 ? 1 : i == 10 ? 2 : i == 15 ? 3 : i == 25 ? 4 : i == 35 ? 5 : 6)) }
                SCHED();
            });
            STAMP(layer ? 18 : 8)
        }
#pragma unroll
        for (int nb = 0; nb < 5; ++nb) a2[0][nb] = (a2[0][nb] + a2[1][nb]) + a2[2][nb];
        asm volatile("s_nop 1" : "+v"(a2[3][0]), "+v"(a2[3][1]), "+v"(a2[3][2]), "+v"(a2[3][3]), "+v"(a2[3][4]),
                     "+v"(a2[0][0]), "+v"(a2[0][1]), "+v"(a2[0][2]), "+v"(a2[0][3]), "+v"(a2[0][4]));
        float ty;
        {
            f32x4 s1, s2;
#pragma unroll
            for (int nb = 0; nb < 5; ++nb) {
                const f32x4 y = a2[3][nb] + a2[0][nb];
                xin[nb] = y;
                if (nb == 0) { s1 = y; s2 = y * y; } else { s1 += y; s2 += y * y; }
            }
            ty = xsum2h(hsum4(s1), hsum4(s2));
            st64(v2u{__float_as_uint(ty), tag0}, rn, sgs);
        }
        SCHED();
        STAMP(layer ? 19 : 9)
        if (layer == 0 && __builtin_expect(glate != 0ull, 0)) {
            asm volatile("s_waitcnt vmcnt(0)" : "+v"(gl[0]), "+v"(gl[1]), "+v"(gl[2])::"memory");
            WAVE_WAIT({ f0 = ld64(rs, fgo); ok = ISTAG(f0); })
            gld4t<0>(gl[0], glo, p.ws); gld4t<0>(gl[1], glo, gb1); gld4t<0>(gl[2], glo, gb2);
            asm volatile("s_waitcnt vmcnt(0)" : "+v"(gl[0]), "+v"(gl[1]), "+v"(gl[2])::"memory");
        }
        if (layer == 0) {
            asm volatile("s_waitcnt vmcnt(1)" : "+v"(gl[0]), "+v"(gl[1]), "+v"(gl[2])::"memory");
            *(v4u*)(smem + gw) = gl[0]; *(v4u*)(smem + gw + 1280) = gl[1]; *(v4u*)(smem + gw + 1920) = gl[2];
            asm volatile("s_waitcnt lgkmcnt(0)" ::: "memory");
            __builtin_amdgcn_s_barrier(); asm volatile("" ::: "memory");
#pragma unroll
            for (int nb = 0; nb < 5; ++nb) gt[nb] = __builtin_bit_cast(v2u, trread(smem, gr + 640 * nb));
        } else {
            ACC_TIE5(gt);
            if constexpr (OTR) {
#pragma unroll
                for (int nb = 0; nb < 5; ++nb) *(f32x4*)(smem + otw + nb * 64) = xin[nb];
#pragma unroll
                for (int nb = 0; nb < 5; ++nb) xin[nb] = *(const f32x4*)(smem + (nb < 4 ? otr0 + (nb & 1) * 8 * 336 + (nb >> 1) * 128 : otr4));
            }
        }
        SCHED();
        auto sgp = LAMBDA(p_) {
            constexpr int P = layer ? SGP1 : SGP0;
            static_for<3>(LAMBDA(k_) { constexpr int d = P >> (16 - 8 * k_.value); if constexpr (((d >> 4) & 15) == p_.value) { gran_req<32 + 8 * k_.value, d & 15>(sgo, gn); SCHED(); } });
        };
        sgp(IC(0));
        f32x4 gf[5], yg[5];
#pragma unroll
        for (int nb = 0; nb < 5; ++nb) { gf[nb] = unpk4(gt[nb]); yg[nb] = xin[nb] * gf[nb]; pin(gf[nb]); pin(yg[nb]); }
        SCHED();
        sgp(IC(1));
        int lane_o;
        asm volatile("v_mbcnt_lo_u32_b32 %0, -1, 0\n\tv_mbcnt_hi_u32_b32 %0, -1, %0" : "=v"(lane_o));
        const int lane = lane_o, c = lane & 15, q = lane >> 4;
        unsigned xs[3];
#pragma unroll
        for (int i = 0; i < 3; ++i) {
            const int ch = 10 * nt2 + 4 * i + 2 * (q & 1) + (q >> 1);
            xs[i] = N_X2 + (unsigned)((2 * (mt2 & 1) + (w >> 1)) * 5 * NB_ + (ch >> 4) * NB_ + ((c >> 2) & 1) * 4096 + (ch & 15) * 256 + (8 * (w & 1) + 4 * (c >> 3) + (c & 3)) * 16);
        }
        unsigned pso = q < 2 ? N_PS + fine + (unsigned)((((mt2 & 1) * 4 + w) * 8 + nt2) * 128 + c * 8 + q * 4) : kNone;
        unsigned fxo1 = N_FX + fine + (unsigned)(((2 * (mt2 & 1) + (w >> 1)) * 16 + nt2 * 2 + (w & 1)) * 128);
        unsigned oo = OTR ? (unsigned)(((64 * mt2 + 16 * w + (lane >> 3)) * D_ + 80 * nt2) * 4 + (nt2 & 1) * 64 + (lane & 7) * 16) : (unsigned)(((64 * mt2 + 16 * w + c) * D_ + 80 * nt2 + 4 * q) * 4);
        unsigned oo1 = oo + 8 * D_ * 4, oo4 = (unsigned)(((64 * mt2 + 16 * w + (lane >> 3) + (lane & 4) * 2) * D_ + 80 * nt2) * 4 + 256 - (nt2 & 1) * 256 + (lane & 3) * 16);
        int bpi = (lane >> 3) * 4;
        u64 up = 0xF0F0F0F0F0F0F0F0ull;
        xs[2] = (q & 1) ? kNone : xs[2];
        fxo1 = lane ? kNone : fxo1;
        if (layer == 0) { pin(xs[0]); pin(xs[1]); pin(xs[2]); pin(pso); pin(fxo1); } else { pin(oo); if (OTR) { pin(oo1); pin(oo4); pin(bpi); pins(up); } }
        SCHED();
        {
            float a, d;
            sgp(IC(2));
            room<(layer ? 900 : 1500)>(); site<9 + 10 * layer>();
            pipe_gran(a, d, sgo, gn, tag0, dead);
            STAMP(layer ? 20 : 10)
            xsum2(a, d);
            const float me = a * (1.0f / D_);
            const float ri = __builtin_amdgcn_rsqf(fmaxf(d * (1.0f / D_) - me * me, 0.f) + 1e-5f), mr = -me * ri;
            float rv[3] = {ri, ri, ri}, mv[3] = {mr, mr, mr};
            if constexpr (layer == 1 && OTR) {
#pragma unroll
                for (int i = 0; i < 2; ++i) { rv[i] = __int_as_float(__builtin_amdgcn_ds_bpermute(bpi + 32 * i, __float_as_int(ri))); mv[i] = __int_as_float(__builtin_amdgcn_ds_bpermute(bpi + 32 * i, __float_as_int(mr))); }
                SCHED();
            }
            static_for<5>(LAMBDA(n_) {
                constexpr int nb = layer ? n_.value : (n_.value + 4) % 5;
                if constexpr (layer == 1 && OTR && nb == 4) { sel1(rv[2], __float_as_uint(rv[0]), __float_as_uint(rv[1]), up); sel1(mv[2], __float_as_uint(mv[0]), __float_as_uint(mv[1]), up); }
                xin[nb] = yg[nb] * rv[nb < 4 ? nb & 1 : 2] + (gf[nb] * mv[nb < 4 ? nb & 1 : 2] + xin[nb]);
                if constexpr (layer == 1) { __builtin_amdgcn_raw_buffer_store_b128(__builtin_bit_cast(v4u, xin[nb]), ro, !OTR ? oo + 64 * nb : nb == 4 ? oo4 : ((nb & 1) ? oo1 : oo) + (nb >> 1) * 128, 0, OUTP); SCHED(); }
                else if constexpr (nb & 5) {
                    const f32x4 xa = xin[nb & 6], xb = xin[nb];
                    const auto r0 = __builtin_amdgcn_permlane16_swap(cvtpk(xa.x, xa.y), cvtpk(xb.x, xb.y), false, false);
                    const auto r1 = __builtin_amdgcn_permlane16_swap(cvtpk(xa.z, xa.w), cvtpk(xb.z, xb.w), false, false);
                    st128(v4u{r0[0], r1[0], r0[1], r1[1]}, rn, xs[nb >> 1]);
                    SCHED();
                }
            });
        }
        if constexpr (layer == 0) {
            {
                f32x4 s1, s2;
#pragma unroll
                for (int nb = 0; nb < 5; ++nb) { const f32x4 x = xin[nb]; if (nb == 0) { s1 = x; s2 = x * x; } else { s1 += x; s2 += x * x; } }
                __builtin_amdgcn_raw_buffer_store_b32(__float_as_uint(xsum2h(hsum4(s1), hsum4(s2))), rn, pso, 0, SC1);
            }
            SCHED();
            site<12>();
        int wa[2][3];
#pragma unroll
        for (int i = 0; i < 2; ++i) {
            wa[i][0] = (c >> 3) * 20480 + (c & 7) * 128 + (((4 * i + q) ^ (c & 6)) * 16); wa[i][1] = (wa[i][0] ^ 16) + 10240;
            wa[i][2] = 81920 + (c & 1) * 10240 + (c >> 1) * 128 + (((4 * i + q) ^ ((c & 1) | ((c >> 2) << 1))) * 16);
            pin(wa[i][0]); pin(wa[i][1]); pin(wa[i][2]);
        }
            unsigned fxo = N_FX + fine + (unsigned)(w * 2048 + (lane >> 2) * 128);
            unsigned xo = N_X2 + (unsigned)(w * 5 * NB_ + lane * 16), xo1 = xo + 4096u;
            const int row = 8 * q + (c & 7);
            unsigned po = N_PS + fine + (unsigned)(((2 * w + (row >> 4)) * 8 + 4 * (c >> 3)) * 128 + (row & 15) * 8);
            int rsw = l_rs(1, 0) + w * INBOX + lane * 4;
            pin(rsw); pin(fxo); pin(xo); pin(xo1); pin(po);
            v4u wf[2][5];
#pragma unroll
            for (int nb = 0; nb < 5; ++nb) wf[0][nb] = WF(nb, 0);
            SCHED();
            DRAIN();
            st64(tagv, rn, fxo1);
            if (b == 255 && w == 0) FLAG(OFF_NONCE)
            STAMP(11)
            if (w == 0) tour<4>();
            if constexpr (OTR) {
                const int sh = (nt2 & 1) * 64, r4 = (lane >> 3) + (lane & 4) * 2;
                const int a0 = c * 336 + q * 16, a1 = (lane >> 3) * 336 + sh + (lane & 7) * 16, a2_ = r4 * 336 + 256 - 4 * sh + (lane & 3) * 16;
                char* tw = smem + SC + w * INBOX + 12288;
#pragma unroll
                for (int nb = 0; nb < 5; ++nb) *(v2u*)(tw + a0 / 2 + nb * 32) = gt[nb];
#pragma unroll
                for (int nb = 0; nb < 5; ++nb) gt[nb] = *(const v2u*)(tw + (nb < 4 ? a1 / 2 + (nb & 1) * 8 * 168 + (nb >> 1) * 64 : a2_ / 2));
                otw = SC + w * INBOX + a0; otr0 = SC + w * INBOX + a1; otr4 = SC + w * INBOX + a2_;
                pin(otw); pin(otr0); pin(otr4);
            }
            addrs(std::integral_constant<int, 1>{}, lane, w, b);
            ACC_TIE5(gt);
            v2u pt[4];
            skip<SKIP5>();
            pipe_flag<KPF0>(pt, fxo, gn, tagv, dead);
            ORDER();
            STAMP(12)
            v4u ar[2 * RING1];
#define XF(n) ld128s(rn, (((n) & 1) ? xo1 : xo) + (((n) >> 1) & 3) * 1024, ((n) >> 3) * NB_)
#pragma unroll
            for (int n = 0; n < KI1; ++n) { ar[n] = XF(n); SCHED(); }
            v2u ps[4];
            float t1, t2, rinv, mri, rr_[8], rm_[8];
            static_for<20>(LAMBDA(s_) {
                constexpr int s = s_.value, cu = s & 1, nx = cu ^ 1;
                LGKM0() SCHED();
                static_for<10>(LAMBDA(g_) {
                    constexpr int g = g_.value, mb = g / 5, k = g % 5;
                    if constexpr (s == 0) mf0(acc[mb][k], ar[mb], wf[0][k]); else mf(acc[mb][k], ar[(2 * s + mb) % (2 * RING1)], wf[cu][k]);
                    SCHED();
                    if constexpr (s == 0 && g == 0) { pin(pt[0]); pin(pt[1]); pin(pt[2]); pin(pt[3]); }
                    if constexpr (s < 19 && g < 5) wf[nx][g] = WF(g, s + 1);
                    constexpr int rq = ring_req(10 * s + g, KI1, 2 * RING1, 4);
                    if constexpr (rq >= 100) ps[rq - 100] = ld64(rn, po + (rq - 100) * 128);
                    else if constexpr (rq >= 0) ar[rq % (2 * RING1)] = XF(rq);
                    constexpr int n = g >= 6 ? 4 * (s - KS1) + g - 6 : -1;
                    if constexpr (n == 0) { t1 = __uint_as_float(ps[0].x) + __uint_as_float(ps[1].x); pin(t1); t2 = __uint_as_float(ps[0].y) + __uint_as_float(ps[1].y); pin(t2); }
                    if constexpr (n == 1) { t1 += __uint_as_float(ps[2].x); pin(t1); t2 += __uint_as_float(ps[2].y); pin(t2); }
                    if constexpr (n == 2) { t1 += __uint_as_float(ps[3].x); pin(t1); t2 += __uint_as_float(ps[3].y); pin(t2); }
                    if constexpr (n == 3) { t1 += dpp<0x128>(t1); t2 += dpp<0x128>(t2); pin(t1); pin(t2); }
                    if constexpr (n == 4) { t1 *= (1.0f / D_); pin(t1); t2 *= (1.0f / D_); pin(t2); }
                    if constexpr (n == 5) { t2 = fmaxf(t2 - t1 * t1, 0.f); pin(t2); }
                    if constexpr (n == 6) { rinv = __builtin_amdgcn_rsqf(t2 + 1e-5f); pin(rinv); }
                    if constexpr (n == 8) { mri = -t1 * rinv; pin(mri); }
                    if constexpr (n == 9) { t1 = rinv; mrg8(t1, mri); }
                    if constexpr (n == 10) *(float*)(smem + rsw) = t1;
                    if constexpr (n >= 11 && n < 19) bc2b<n - 11>(rr_[n - 11], rm_[n - 11], rinv, mri);
                    if constexpr (n >= 19 && n < 39) TT_GAP(n - 19)
                    site<6000 + 10 * s + g>();
                    SCHED();
                });
            });
            rr[0] = f32x4{rr_[0], rr_[1], rr_[2], rr_[3]}; rr[1] = f32x4{rr_[4], rr_[5], rr_[6], rr_[7]};
            ACC_FENCE10(acc[0], acc[1]);
            STAMP(13)
        } else {
            STAMP(21)
            STAMP_DUMP()
            asm volatile("kpf_end:");
        }
    };
    layer_body(std::integral_constant<int, 0>{});
    layer_body(std::integral_constant<int, 1>{});
}

extern "C" void kernel_launch(void* const* d_in, const int* in_sizes, int n_in,
                              void* d_out, int out_size, void* d_ws, size_t ws_size,
                              hipStream_t stream) {
    Params p;
    p.x0 = (const u16*)d_in[0];
    p.Lre = (const u16*)d_in[1];
    p.Lim = (const u16*)d_in[2];
    p.Bre = (const u16*)d_in[3];
    p.Bim = (const u16*)d_in[4];
    p.Cre = (const u16*)d_in[5];
    p.Cim = (const u16*)d_in[6];
    p.logdt = (const u16*)d_in[7];
    p.Dskip = (const u16*)d_in[8];
    p.k = (const u16*)d_in[10];
    p.v = (const u16*)d_in[11];
    p.shk = (const u16*)d_in[12];
    p.ssmk = (const u16*)d_in[13];
    p.ssmD = (const u16*)d_in[14];
    p.ws = (char*)d_ws;
    p.out = (float*)d_out;
    k_main<<<256, 256, 0, stream>>>(p);
}
```
